```python
import torch

def _softmax(x, axis=-1):
    return torch.softmax(x, dim=axis)

def _gelu(x):
    return torch.nn.functional.gelu(x, approximate='tanh')

def _silu(x):
    return torch.nn.functional.silu(x)

def _sigmoid(x):
    return torch.sigmoid(x)

def _layernorm(x, axis=-1, eps=1e-05):
    m = x.mean(dim=axis, keepdim=True)
    v = x.var(dim=axis, keepdim=True, unbiased=False)
    return (x - m) / torch.sqrt(v + eps)

def _scatter_add(tgt, idx, src, axis=0):
    return tgt.clone().index_add_(axis, idx, src)

def _segment_sum(src, seg, n):
    out = torch.zeros((n,) + src.shape[1:], dtype=src.dtype)
    return out.index_add_(0, seg, src)

def _pad(x, pw):
    flat = tuple((v for pair in reversed(pw) for v in pair))
    return torch.nn.functional.pad(x, flat)
kernel_name = 'adaLN_cond__ssm_conv_s4__mixer_s_mlp__prompt'

def setup_inputs():

    _w = lambda a: (lambda t: t.to(torch.bfloat16) if t.is_floating_point() else t)(torch.as_tensor(a))

    def _A_setup():
        B, L, D, N = (1, 2048, 448, 256)
        A_re = -(lambda _x: torch.exp(torch.as_tensor(_x)))((lambda *s: torch.randn(*s, dtype=torch.bfloat16) * 3.2)(D, N))
        A_im = torch.pi * (lambda *s: torch.randn(*s, dtype=torch.bfloat16) * 0.1)(D, N)
        B_re = (lambda *s: torch.randn(*s, dtype=torch.bfloat16) * 0.2)(D, N)
        B_im = (lambda *s: torch.randn(*s, dtype=torch.bfloat16) * 0.2)(D, N)
        C_re = (lambda *s: torch.randn(*s, dtype=torch.bfloat16) * 0.2)(D, N)
        C_im = (lambda *s: torch.randn(*s, dtype=torch.bfloat16) * 0.2)(D, N)
        log_dt = (lambda *s: torch.randn(*s, dtype=torch.bfloat16) * 0.1)(D) * 0.5 - 4.0
        Dskip = (lambda *s: torch.randn(*s, dtype=torch.bfloat16) * 0.1)(D)
        return {'u': _w((lambda *s: torch.randn(*s, dtype=torch.bfloat16) * 0.05)(B, L, D)), 'A_re': _w(A_re), 'A_im': _w(A_im), 'B_re': _w(B_re), 'B_im': _w(B_im), 'C_re': _w(C_re), 'C_im': _w(C_im), 'log_dt': _w(log_dt), 'Dskip': _w(Dskip)}
    _A_in = _A_setup()

    def _B_setup():
        B, S, D, F = (1, 2048, 448, 1792)
        return {'x': _w((lambda *s: torch.randn(*s, dtype=torch.bfloat16) * 0.05)(B, S, D)), 'w1': _w((lambda *s: torch.randn(*s, dtype=torch.bfloat16) * 3.2)(D, D) * D ** (-0.5)), 'w2': _w((lambda *s: torch.randn(*s, dtype=torch.bfloat16) * 3.2)(D, D) * D ** (-0.5)), 'w3': _w((lambda *s: torch.randn(*s, dtype=torch.bfloat16) * 3.2)(D, F) * D ** (-0.5)), 'w4': _w((lambda *s: torch.randn(*s, dtype=torch.bfloat16) * 3.2)(F, D) * F ** (-0.5))}
    _B_in = _B_setup()
    return {'A_u': _A_in['u'], 'A_A_re': _A_in['A_re'], 'A_A_im': _A_in['A_im'], 'A_B_re': _A_in['B_re'], 'A_B_im': _A_in['B_im'], 'A_C_re': _A_in['C_re'], 'A_C_im': _A_in['C_im'], 'A_log_dt': _A_in['log_dt'], 'A_Dskip': _A_in['Dskip'], 'B_x': _B_in['x'], 'B_w1': _B_in['w1'], 'B_w2': _B_in['w2'], 'B_w3': _B_in['w3'], 'B_w4': _B_in['w4'], 'c': _w((lambda *s: torch.randn(*s, dtype=torch.bfloat16) * 0.1)(1, 448) * 8.0), 'w_mod': _w((lambda *s: torch.randn(*s, dtype=torch.bfloat16) * 0.1)(448, 6 * 448) * 448 ** (-0.5) * 8.0)}

def reference(A_u, A_A_re, A_A_im, A_B_re, A_B_im, A_C_re, A_C_im, A_log_dt, A_Dskip, B_x, B_w1, B_w2, B_w3, B_w4, c, w_mod):

    def _A_ref(u, A_re, A_im, B_re, B_im, C_re, C_im, log_dt, Dskip):
        Bsz, L, D = u.shape
        dt = torch.exp(log_dt)[:, None]
        A = A_re + 1j * A_im
        Bc = B_re + 1j * B_im
        Cc = C_re + 1j * C_im
        logdA = dt * A
        dB = dt * Bc
        l = (lambda x, dt: x.to(dt))(torch.arange(L), A_re.dtype)
        P = torch.exp(logdA[:, :, None] * l[None, None, :])
        K = torch.einsum('dn,dnl,dn->dl', Cc, P, dB).real
        L2 = 2 * L
        Kp = _pad(K, ((0, 0), (0, L)))
        up = (lambda x, *d: x.permute(*d))(u, 0, 2, 1)
        up = _pad(up, ((0, 0), (0, 0), (0, L)))
        Kf = (lambda x, axes=(-2, -1): torch.fft.rfft2(x.float(), dim=axes))(Kp[:, None, :], axes=(-2, -1))
        Uf = (lambda x, axes=(-2, -1): torch.fft.rfft2(x.float(), dim=axes))(up[:, :, None, :], axes=(-2, -1))
        Yf = Uf * Kf[None, :, :, :]
        y = (lambda x, s, axes=(-2, -1): torch.fft.irfft2(x, s=s, dim=axes).to(torch.bfloat16))(Yf, (1, L2), axes=(-2, -1))[:, :, 0, :L]
        y = (lambda x, *d: x.permute(*d))(y, 0, 2, 1) + u * Dskip[None, None, :]
        return y

    def _B_ref(x, w1, w2, w3, w4):
        B, S, D = x.shape
        h = _gelu(_layernorm(x, axis=-1) @ w1)
        c = D // 4
        g0 = _pad(h[:, :, 0 * c:1 * c], ((0, 0), (1, 0), (0, 0)))[:, :S, :]
        g1 = _pad(h[:, :, 1 * c:2 * c], ((0, 0), (0, 1), (0, 0)))[:, 1:, :]
        g2 = _pad(h[:, :, 2 * c:3 * c], ((0, 0), (2, 0), (0, 0)))[:, :S, :]
        g3 = _pad(h[:, :, 3 * c:], ((0, 0), (0, 2), (0, 0)))[:, 2:, :]
        hs = torch.concatenate([g0, g1, g2, g3], axis=-1) @ w2
        x1 = x + hs
        h2 = _gelu(_layernorm(x1, axis=-1) @ w3) @ w4
        return x1 + h2
    x = A_u
    mod = (_silu(c) @ w_mod).reshape(c.shape[0], 6, 1, -1)
    sa, ca, ga, sb, cb, gb = (mod[:, 0].clone(), mod[:, 1].clone(), mod[:, 2].clone(), mod[:, 3].clone(), mod[:, 4].clone(), mod[:, 5].clone())
    h = _layernorm(x, axis=-1) * (1 + ca) + sa
    x = x + ga * _A_ref(h, A_A_re, A_A_im, A_B_re, A_B_im, A_C_re, A_C_im, A_log_dt, A_Dskip)
    h = _layernorm(x, axis=-1) * (1 + cb) + sb
    x = x + gb * _B_ref(h, B_w1, B_w2, B_w3, B_w4)
    return x
```

```cpp
#include <hip/hip_runtime.h>
#include <hip/hip_bf16.h>
#include <stdint.h>

namespace {

typedef __attribute__((ext_vector_type(8))) short bf16x8;
typedef __attribute__((ext_vector_type(4))) float f32x4;
typedef __attribute__((ext_vector_type(2))) float f32x2;
typedef __attribute__((ext_vector_type(4))) unsigned u32x4;
typedef __attribute__((ext_vector_type(2))) unsigned u32x2;
typedef __attribute__((ext_vector_type(2))) __bf16 bf16x2v;
#define GAS __attribute__((address_space(1)))
#define LAS __attribute__((address_space(3)))
#define RLX_AGENT __ATOMIC_RELAXED, __HIP_MEMORY_SCOPE_AGENT

constexpr int L_ = 2048, D_ = 448, NS_ = 256, F_ = 1792;

constexpr size_t WS_FLAGS = 0;
constexpr size_t FLAG_BYTES = 64 * 3 * 128;
constexpr size_t WS_MOD = 32768;
constexpr size_t WS_STAT = WS_MOD + 16384;
constexpr size_t WS_XB = WS_STAT + 16384;
constexpr size_t KR_STRIDE = 4224;
constexpr size_t WS_KR = WS_XB + (size_t)64 * 448 * 64;
constexpr int NFRAG = 140;
constexpr size_t WS_WP = WS_KR + (size_t)448 * KR_STRIDE + 1024;
constexpr size_t WS_X2B = WS_WP + (size_t)28 * NFRAG * 1024;
constexpr size_t X2B_BLK = 224 * 160;
constexpr size_t WS_G = WS_X2B + 64 * X2B_BLK;
constexpr size_t WS_X1 = WS_G + (size_t)64 * 4 * 7168;
constexpr size_t WS_HID = WS_X1 + (size_t)64 * 4 * 7168;
constexpr size_t WS_END = WS_HID + (size_t)64 * 4 * 28672;

__device__ __forceinline__ float bf_lo(unsigned u) { return __builtin_bit_cast(float, u << 16); }
__device__ __forceinline__ float bf_hi(unsigned u) { return __builtin_bit_cast(float, u & 0xffff0000u); }
__device__ __forceinline__ float bf2f(unsigned short h) { return __builtin_bit_cast(float, (unsigned)h << 16); }
__device__ __forceinline__ unsigned pk_bf16(float a, float b) {
  f32x2 v = {a, b};
  bf16x2v r = __builtin_convertvector(v, bf16x2v);
  return __builtin_bit_cast(unsigned, r);
}
__device__ __forceinline__ float rbf(float a) { return bf_lo(pk_bf16(a, a)); }
__device__ __forceinline__ f32x4 mfma16(u32x4 a, u32x4 b, f32x4 c) {
  return __builtin_amdgcn_mfma_f32_16x16x32_bf16(__builtin_bit_cast(bf16x8, a), __builtin_bit_cast(bf16x8, b), c, 0, 0, 0);
}
__device__ __forceinline__ float wave_sum(float v) {
#pragma unroll
  for (int o = 32; o > 0; o >>= 1) v += __shfl_xor(v, o, 64);
  return v;
}
__device__ __forceinline__ float gelu_tanh(float x) {
  const float c1 = -2.0f * 0.7978845608028654f * 1.4426950408889634f;
  const float c2 = c1 * 0.044715f;
  float t = x * (c1 + c2 * x * x);
  float e = __builtin_amdgcn_exp2f(t);
  return x * __builtin_amdgcn_rcpf(1.0f + e);
}

struct PrepArgs {
  const unsigned short* x;
  const unsigned short* a_re;
  const unsigned short* a_im;
  const unsigned short* b_re;
  const unsigned short* b_im;
  const unsigned short* c_re;
  const unsigned short* c_im;
  const unsigned short* log_dt;
  const unsigned short* w1;
  const unsigned short* w2;
  const unsigned short* w3;
  const unsigned short* w4;
  const unsigned short* cvec;
  const unsigned short* wmod;
  unsigned char* ws;
};

constexpr int NB_LN = 64, NB_MOD = 84, NB_PACK = 123, NB_KG = 448;
constexpr int NB_PREP = NB_LN + NB_MOD + NB_PACK + NB_KG;

__device__ __forceinline__ void transpose8x8(const u32x4 (&r)[8], u32x4 (&o)[8]) {
#pragma unroll
  for (int c2 = 0; c2 < 4; ++c2) {
#pragma unroll
    for (int rp = 0; rp < 4; ++rp) {
      unsigned a = r[2 * rp][c2], b = r[2 * rp + 1][c2];
      o[2 * c2][rp] = __builtin_amdgcn_perm(b, a, 0x05040100u);
      o[2 * c2 + 1][rp] = __builtin_amdgcn_perm(b, a, 0x07060302u);
    }
  }
}

__device__ __forceinline__ int kg_base(int row) {
  if (row < 16) return (row >> 3) * 128 + (row & 7);
  if (row < 24) return 256 + ((row - 16) >> 2) * 128 + ((row - 16) & 3) * 2;
  if (row < 32) return 512 + ((row - 24) >> 1) * 128 + ((row - 24) & 1) * 4;
  return 1024 + (row - 32) * 128;
}
__device__ __forceinline__ int kg_index(int l) {
  int lr = (int)rbf((float)l);
  int c = (lr & 127) >> 3, e = lr & 7, row;
  if (lr < 256) row = (lr >> 7) * 8 + e;
  else if (lr < 512) row = 16 + ((lr - 256) >> 7) * 4 + (e >> 1);
  else if (lr < 1024) row = 24 + ((lr - 512) >> 7) * 2 + (e >> 2);
  else row = 32 + ((lr - 1024) >> 7);
  return row * 16 + c;
}

__global__ __launch_bounds__(256) void k_prep(PrepArgs p) {
  __shared__ __attribute__((aligned(16))) float sm[4 * 768 + 1024];
  const int tid = threadIdx.x, lane = tid & 63, w = tid >> 6;
  int b = blockIdx.x;
  if (b < NB_LN) {
    const int t0 = b * 32 + w * 8;
    u32x4 r[8];
    const int pl = lane < 56 ? lane : 0;
#pragma unroll
    for (int i = 0; i < 8; ++i) r[i] = *(const u32x4*)(p.x + (size_t)(t0 + i) * D_ + pl * 8);
    unsigned st = 0;
#pragma unroll
    for (int i = 0; i < 8; ++i) {
      float v[8];
#pragma unroll
      for (int k = 0; k < 4; ++k) { v[2 * k] = bf_lo(r[i][k]); v[2 * k + 1] = bf_hi(r[i][k]); }
      float s = 0.f;
#pragma unroll
      for (int k = 0; k < 8; ++k) s += v[k];
      if (lane >= 56) s = 0.f;
      const float mean = wave_sum(s) * (1.0f / D_);
      float q = 0.f;
#pragma unroll
      for (int k = 0; k < 8; ++k) { float d = v[k] - mean; q += d * d; }
      if (lane >= 56) q = 0.f;
      const float var = rbf(wave_sum(q) * (1.0f / D_));
      const float sd = rbf(sqrtf(rbf(var + 1e-5f)));
      if (lane == i) st = pk_bf16(mean, sd);
    }
    if (lane < 8) *(unsigned*)(p.ws + WS_STAT + (size_t)(t0 + lane) * 4) = st;
    u32x4 o[8];
    transpose8x8(r, o);
    if (lane < 56) {
      unsigned char* dst = p.ws + WS_XB + ((size_t)b * D_ + lane * 8) * 64 + w * 16;
#pragma unroll
      for (int i = 0; i < 8; ++i) *(u32x4*)(dst + i * 64) = o[i];
    }
    if (b == 0) {
      for (int i = tid; i < (int)(FLAG_BYTES / 16); i += 256) *(u32x4*)(p.ws + WS_FLAGS + (size_t)i * 16) = u32x4{0u, 0u, 0u, 0u};
    }
    return;
  }
  b -= NB_LN;
  if (b < NB_MOD) {
    const int rg = tid >> 2, pc = tid & 3;
    u32x4 r[7];
    float sc[7];
#pragma unroll
    for (int i = 0; i < 7; ++i) r[i] = *(const u32x4*)(p.wmod + (size_t)(rg + 64 * i) * 2688 + b * 32 + pc * 8);
#pragma unroll
    for (int i = 0; i < 7; ++i) {
      float c = bf2f(p.cvec[rg + 64 * i]);
      sc[i] = rbf(c / (1.0f + expf(-c)));
    }
    float acc[8];
#pragma unroll
    for (int k = 0; k < 8; ++k) acc[k] = 0.f;
#pragma unroll
    for (int i = 0; i < 7; ++i)
#pragma unroll
      for (int k = 0; k < 4; ++k) { acc[2 * k] += sc[i] * bf_lo(r[i][k]); acc[2 * k + 1] += sc[i] * bf_hi(r[i][k]); }
#pragma unroll
    for (int k = 0; k < 8; ++k) {
#pragma unroll
      for (int o = 4; o < 64; o <<= 1) acc[k] += __shfl_xor(acc[k], o, 64);
    }
    if (lane < 4) {
#pragma unroll
      for (int k = 0; k < 8; ++k) sm[w * 32 + lane * 8 + k] = acc[k];
    }
    __syncthreads();
    if (tid < 32) {
      float s = (sm[tid] + sm[32 + tid]) + (sm[64 + tid] + sm[96 + tid]);
      *(float*)(p.ws + WS_MOD + (size_t)(b * 32 + tid) * 4) = rbf(s);
    }
    return;
  }
  b -= NB_MOD;
  if (b < NB_PACK) {
    int g = b * 256 + tid;
    const unsigned short* W;
    int ldn, mat;
    if (g < 3136) { W = p.w1; ldn = 448; mat = 0; }
    else if (g < 6272) { W = p.w2; ldn = 448; mat = 1; g -= 3136; }
    else if (g < 18816) { W = p.w3; ldn = 1792; mat = 2; g -= 6272; }
    else if (g < 31360) { W = p.w4; ldn = 448; mat = 3; g -= 18816; }
    else return;
    const int nocts = ldn >> 3;
    const int ko = g / nocts, no = g - ko * nocts;
    u32x4 r[8], o[8];
#pragma unroll
    for (int i = 0; i < 8; ++i) r[i] = *(const u32x4*)(W + (size_t)(ko * 8 + i) * ldn + no * 8);
    transpose8x8(r, o);
    const int n = no * 8, ks = ko >> 2, q = ko & 3;
    int j, wv, f, rr;
    if (mat == 2) {
      j = n / 448; int nn = n - j * 448; wv = nn >> 6; f = 28 + ks * 4 + ((nn & 63) >> 4); rr = nn & 15;
    } else {
      j = n / 112; int nn = n - j * 112; wv = nn >> 4; rr = nn & 15;
      f = mat == 0 ? ks : (mat == 1 ? 14 + ks : 84 + ks);
    }
    unsigned char* dst = p.ws + WS_WP + ((size_t)(j * 7 + wv) * NFRAG + f) * 1024 + (q * 16 + rr) * 16;
#pragma unroll
    for (int i = 0; i < 8; ++i) *(u32x4*)(dst + i * 16) = o[i];
    return;
  }
  b -= NB_PACK;
  {
    const int d = b;
    f32x4* par = (f32x4*)(sm + 4 * 768);
    {
      const size_t o = (size_t)d * NS_ + tid;
      const float dt = rbf(expf(bf2f(p.log_dt[d])));
      const float are = bf2f(p.a_re[o]), aim = bf2f(p.a_im[o]);
      const float bre = dt * bf2f(p.b_re[o]), bim = dt * bf2f(p.b_im[o]);
      const float cre = bf2f(p.c_re[o]), cim = bf2f(p.c_im[o]);
      f32x4 v;
      v.x = (dt * are) * 1.4426950408889634f;
      v.y = (dt * aim) * 0.15915494309189535f;
      v.z = cre * bre - cim * bim;
      v.w = cre * bim + cim * bre;
      par[tid] = v;
    }
    __syncthreads();
    const int i16 = lane & 15, q = lane >> 4;
    const float b0 = (float)kg_base(i16), b1 = (float)kg_base(16 + i16), b2 = (float)kg_base(32 + i16), bt = (float)(8 * i16);
    f32x4 acc0 = {0.f, 0.f, 0.f, 0.f}, acc1 = acc0, acc2 = acc0;
#pragma unroll 4
    for (int jj = 0; jj < 16; ++jj) {
      const f32x4 v = par[w * 64 + jj * 4 + q];
      float e, ph, s, c;
      e = __builtin_amdgcn_exp2f(v.x * bt); ph = __builtin_amdgcn_fractf(v.y * bt);
      s = __builtin_amdgcn_sinf(ph) * e; c = __builtin_amdgcn_cosf(ph) * e;
      const float tre = v.z * c - v.w * s, tim = -(v.z * s + v.w * c);
      e = __builtin_amdgcn_exp2f(v.x * b0); ph = __builtin_amdgcn_fractf(v.y * b0);
      s = __builtin_amdgcn_sinf(ph) * e; c = __builtin_amdgcn_cosf(ph) * e;
      acc0 = __builtin_amdgcn_mfma_f32_16x16x4f32(c, tre, acc0, 0, 0, 0);
      acc0 = __builtin_amdgcn_mfma_f32_16x16x4f32(s, tim, acc0, 0, 0, 0);
      e = __builtin_amdgcn_exp2f(v.x * b1); ph = __builtin_amdgcn_fractf(v.y * b1);
      s = __builtin_amdgcn_sinf(ph) * e; c = __builtin_amdgcn_cosf(ph) * e;
      acc1 = __builtin_amdgcn_mfma_f32_16x16x4f32(c, tre, acc1, 0, 0, 0);
      acc1 = __builtin_amdgcn_mfma_f32_16x16x4f32(s, tim, acc1, 0, 0, 0);
      e = __builtin_amdgcn_exp2f(v.x * b2); ph = __builtin_amdgcn_fractf(v.y * b2);
      s = __builtin_amdgcn_sinf(ph) * e; c = __builtin_amdgcn_cosf(ph) * e;
      acc2 = __builtin_amdgcn_mfma_f32_16x16x4f32(c, tre, acc2, 0, 0, 0);
      acc2 = __builtin_amdgcn_mfma_f32_16x16x4f32(s, tim, acc2, 0, 0, 0);
    }
    float* red = sm + w * 768;
#pragma unroll
    for (int r = 0; r < 4; ++r) {
      red[(4 * q + r) * 16 + i16] = acc0[r];
      red[(16 + 4 * q + r) * 16 + i16] = acc1[r];
      red[(32 + 4 * q + r) * 16 + i16] = acc2[r];
    }
    __syncthreads();
    float* kd = sm + 4 * 768;
#pragma unroll
    for (int i = 0; i < 3; ++i) {
      const int o = tid + 256 * i;
      kd[o] = (sm[o] + sm[768 + o]) + (sm[1536 + o] + sm[2304 + o]);
    }
    __syncthreads();
    for (int ch = tid; ch < 264; ch += 256) {
      float v[8];
#pragma unroll
      for (int i = 0; i < 8; ++i) {
        const int l = 2048 - (ch * 8 + i);
        v[i] = (l >= 0 && l < 2048) ? kd[kg_index(l & 2047)] : 0.f;
      }
      u32x4 o = {pk_bf16(v[0], v[1]), pk_bf16(v[2], v[3]), pk_bf16(v[4], v[5]), pk_bf16(v[6], v[7])};
      *(u32x4*)(p.ws + WS_KR + (size_t)d * KR_STRIDE + ch * 16) = o;
    }
  }
}

struct ConvArgs {
  const unsigned short* dskip;
  unsigned char* ws;
};
constexpr int CV_KCOPY = 8 * 272 * 16;
constexpr int CV_UROW = 160;
constexpr int CV_U = 48 * CV_UROW;
constexpr int CV_CH = CV_KCOPY + CV_U;
constexpr int CV_LDS = 2 * CV_CH;
constexpr int CV_PROW = 272;
static_assert(4 * 32 * CV_PROW <= CV_KCOPY, "partials alias the kernel copies");

__global__ __launch_bounds__(512, 2) void k_conv(ConvArgs p) {
  extern __shared__ __attribute__((aligned(16))) unsigned char smem[];
  const int tid = threadIdx.x, lane = tid & 63;
  const int w = __builtin_amdgcn_readfirstlane(tid >> 6);
  const int dp = blockIdx.x;
  const int kc = tid >> 8, kt = tid & 255;
  const unsigned char* krp = p.ws + WS_KR + (size_t)(2 * dp + kc) * KR_STRIDE;
  u32x4 k0 = *(const u32x4*)(krp + kt * 16), k1 = *(const u32x4*)(krp + kt * 16 + 16);
  u32x4 k2 = {0u, 0u, 0u, 0u}, k3 = k2;
  if (kt < 8) {
    k2 = *(const u32x4*)(krp + (256 + kt) * 16);
    if (kt < 7) k3 = *(const u32x4*)(krp + (257 + kt) * 16);
  }
  const int tb = tid >> 3, pc = tid & 7, xc = pc >> 2, oct = pc & 3;
  const int t8 = tb * 32 + oct * 8;
  const u32x4 xv = *(const u32x4*)(p.ws + WS_XB + ((size_t)tb * D_ + 2 * dp) * 64 + pc * 16);
  const u32x4 s0 = *(const u32x4*)(p.ws + WS_STAT + (size_t)t8 * 4), s1 = *(const u32x4*)(p.ws + WS_STAT + (size_t)t8 * 4 + 16);
  const float* mod = (const float*)(p.ws + WS_MOD);
  const int dch = 2 * dp + xc;
  const float sa = mod[dch], ca1 = rbf(1.0f + mod[448 + dch]), ga = mod[896 + dch];
  const float dsk = bf2f(p.dskip[dch]);
  {
    unsigned char* base = smem + kc * CV_CH;
    const unsigned OFFS = 0x087EDA52u;
    auto put = [&](int chunk, const u32x4& a, const u32x4& bq) {
      unsigned wv[8] = {a.x, a.y, a.z, a.w, bq.x, bq.y, bq.z, bq.w};
      unsigned vv[7];
#pragma unroll
      for (int i = 0; i < 7; ++i) vv[i] = __builtin_amdgcn_alignbit(wv[i + 1], wv[i], 16);
#pragma unroll
      for (int s = 0; s < 8; ++s) {
        const int k = s >> 1;
        u32x4 o = (s & 1) ? u32x4{vv[k], vv[k + 1], vv[k + 2], vv[k + 3]} : u32x4{wv[k], wv[k + 1], wv[k + 2], wv[k + 3]};
        *(u32x4*)(base + (272 * s + ((OFFS >> (4 * s)) & 15) + chunk) * 16) = o;
      }
    };
    put(kt, k0, k1);
    if (kt < 8) put(256 + kt, k2, k3);
  }
  float xf[8], hf[8];
  {
    unsigned sv[8] = {s0.x, s0.y, s0.z, s0.w, s1.x, s1.y, s1.z, s1.w};
#pragma unroll
    for (int k = 0; k < 4; ++k) { xf[2 * k] = bf_lo(xv[k]); xf[2 * k + 1] = bf_hi(xv[k]); }
#pragma unroll
    for (int k = 0; k < 8; ++k) {
      const float dx = rbf(xf[k] - bf_lo(sv[k]));
      const float n = rbf(dx / bf_hi(sv[k]));
      hf[k] = rbf(rbf(n * ca1) + sa);
    }
    u32x4 o = {pk_bf16(hf[0], hf[1]), pk_bf16(hf[2], hf[3]), pk_bf16(hf[4], hf[5]), pk_bf16(hf[6], hf[7])};
    unsigned char* U = smem + xc * CV_CH + CV_KCOPY;
    *(u32x4*)(U + (16 + (t8 >> 6)) * CV_UROW + (t8 & 63) * 2) = o;
    if (tid < 320) {
      const int c = tid >= 160, i = tid - c * 160;
      *(u32x4*)(smem + c * CV_CH + CV_KCOPY + i * 16) = u32x4{0u, 0u, 0u, 0u};
    }
  }
  __syncthreads();
  const int ch = w >> 2, wq = w & 3;
  const int r16 = lane & 15, q = lane >> 4;
  f32x4 acc[2][4];
#pragma unroll
  for (int n = 0; n < 2; ++n)
#pragma unroll
    for (int m = 0; m < 4; ++m) acc[n][m] = f32x4{0.f, 0.f, 0.f, 0.f};
  {
    const unsigned OFFS = 0x087EDA52u;
    const int s = (8 - r16) & 7, u = (r16 + 7) >> 3;
    const unsigned char* ka = smem + ch * CV_CH + (272 * s + ((OFFS >> (4 * s)) & 15) + 256 + q - u - 2 * (16 * wq)) * 16;
    const unsigned char* ub = smem + ch * CV_CH + CV_KCOPY + (16 + r16 - 4 * wq) * CV_UROW + q * 16;
    u32x4 alo[6], ahi[6];
    alo[0] = *(const u32x4*)(ka + 2 * 32);
    alo[1] = *(const u32x4*)(ka + 1 * 32);
    ahi[0] = *(const u32x4*)(ka + 2 * 32 - 64 * 32);
    ahi[1] = *(const u32x4*)(ka + 1 * 32 - 64 * 32);
#pragma unroll
    for (int dl = 0; dl < 4; ++dl) {
#pragma unroll
      for (int i = 0; i < 4; ++i) {
        alo[2 + i] = *(const u32x4*)(ka - (4 * dl + i) * 32);
        ahi[2 + i] = *(const u32x4*)(ka - (4 * dl + i + 64) * 32);
      }
#pragma unroll
      for (int ks = 0; ks < 2; ++ks) {
        const u32x4 b0 = *(const u32x4*)(ub - dl * CV_UROW + ks * 64);
        const u32x4 b1 = *(const u32x4*)(ub + (16 - dl) * CV_UROW + ks * 64);
#pragma unroll
        for (int m = 0; m < 4; ++m) {
          acc[0][m] = mfma16(alo[m - 2 * ks + 2], b0, acc[0][m]);
          acc[1][m] = mfma16(alo[m - 2 * ks + 2], b1, acc[1][m]);
          acc[1][m] = mfma16(ahi[m - 2 * ks + 2], b0, acc[1][m]);
        }
      }
      alo[0] = alo[4]; alo[1] = alo[5]; ahi[0] = ahi[4]; ahi[1] = ahi[5];
    }
  }
  __syncthreads();
  {
    unsigned char* P = smem + ch * CV_CH + wq * (32 * CV_PROW);
#pragma unroll
    for (int n = 0; n < 2; ++n)
#pragma unroll
      for (int m = 0; m < 4; ++m) *(f32x4*)(P + (16 * n + r16) * CV_PROW + (16 * m + 4 * q) * 4) = acc[n][m];
  }
  __syncthreads();
  {
    const unsigned char* P = smem + xc * CV_CH + (t8 >> 6) * CV_PROW + (t8 & 63) * 4;
    f32x4 y0 = {0.f, 0.f, 0.f, 0.f}, y1 = y0;
#pragma unroll
    for (int k = 0; k < 4; ++k) {
      y0 += *(const f32x4*)(P + k * (32 * CV_PROW));
      y1 += *(const f32x4*)(P + k * (32 * CV_PROW) + 16);
    }
    float o[8];
#pragma unroll
    for (int k = 0; k < 8; ++k) {
      const float y = rbf(k < 4 ? y0[k & 3] : y1[k & 3]);
      const float ya = rbf(y + rbf(hf[k] * dsk));
      o[k] = xf[k] + rbf(ga * ya);
    }
    unsigned e0 = pk_bf16(o[0], o[1]), e1 = pk_bf16(o[2], o[3]), e2 = pk_bf16(o[4], o[5]), e3 = pk_bf16(o[6], o[7]);
    unsigned sx = xc ? e0 : e2, sy = xc ? e1 : e3;
    unsigned rx = __shfl_xor(sx, 4, 64), ry = __shfl_xor(sy, 4, 64);
    unsigned c0x = xc ? rx : e0, c0y = xc ? ry : e1;
    unsigned c1x = xc ? e2 : rx, c1y = xc ? e3 : ry;
    u32x4 out;
    out.x = __builtin_amdgcn_perm(c1x, c0x, 0x05040100u);
    out.y = __builtin_amdgcn_perm(c1x, c0x, 0x07060302u);
    out.z = __builtin_amdgcn_perm(c1y, c0y, 0x05040100u);
    out.w = __builtin_amdgcn_perm(c1y, c0y, 0x07060302u);
    const int tq = oct * 2 + xc;
    unsigned char* dst = p.ws + WS_X2B + (size_t)tb * X2B_BLK + dp * 160;
    *(u32x4*)(dst + (tq + 1) * 16) = out;
    if (tq == 0 && tb > 0) *(u32x4*)(dst - X2B_BLK + 9 * 16) = out;
    if (tq == 7 && tb < 63) *(u32x4*)(dst + X2B_BLK) = out;
    if (tq == 0 && tb == 0) *(u32x4*)(dst) = u32x4{0u, 0u, 0u, 0u};
    if (tq == 7 && tb == 63) *(u32x4*)(dst + 9 * 16) = u32x4{0u, 0u, 0u, 0u};
  }
}

struct GArgs {
  unsigned char* ws;
  unsigned short* out;
};
constexpr int AS = 928;
constexpr int BS = 3616;
constexpr int XS = 912;
constexpr int OS = 240;
constexpr int G_BIG = 0;
constexpr int G_X = 0;
constexpr int G_A0 = 40960;
constexpr int G_A1 = 73728;
constexpr int G_HQ = 103424;
constexpr int G_A2 = 32 * BS;
constexpr int G_OUT = G_A2 + 32 * AS;
constexpr int G_FLAG = G_OUT + 32 * OS;
constexpr int G_LDS = G_FLAG + 64;
constexpr int RING = 32;
constexpr unsigned SPIN_CAP = 1u << 16;

__device__ __forceinline__ void bar_lds() {
  asm volatile("s_waitcnt lgkmcnt(0)" ::: "memory");
  __builtin_amdgcn_s_barrier();
  asm volatile("" ::: "memory");
}

template <int NB>
__device__ __forceinline__ void publish(LAS unsigned char* L, int src, int rowbytes_pieces, int sstride, __amdgpu_buffer_rsrc_t rs, int goff,
                                        GAS unsigned* flag, int lane) {
#pragma unroll
  for (int b = 0; b < NB; b += 7) {
    u32x4 v[7];
#pragma unroll
    for (int i = 0; i < 7; ++i) {
      const int pce = (b + i) * 64 + lane, row = pce / rowbytes_pieces, c = pce - row * rowbytes_pieces;
      v[i] = *(const LAS u32x4*)(L + src + row * sstride + c * 16);
    }
#pragma unroll
    for (int i = 0; i < 7; ++i) __builtin_amdgcn_raw_buffer_store_b128(v[i], rs, goff + ((b + i) * 64 + lane) * 16, 0, 16);
    __builtin_amdgcn_sched_barrier(0);
  }
  asm volatile("s_waitcnt vmcnt(0)" ::: "memory");
  if (lane == 0) __hip_atomic_store(flag, 1u, RLX_AGENT);
}
__device__ __forceinline__ void wait_flags(GAS unsigned* flags, int lane) {
  for (unsigned spins = 0; spins < SPIN_CAP; ++spins) {
    unsigned v = 1u;
    if (lane < 4) v = __hip_atomic_load(flags + lane, RLX_AGENT);
    if (__all(v == 1u)) break;
    __builtin_amdgcn_s_sleep(1);
  }
  __builtin_amdgcn_fence(__ATOMIC_ACQUIRE, "wavefront");
}

__global__ __launch_bounds__(512, 2) void k_gchain(GArgs p) {
  extern __shared__ __attribute__((aligned(16))) unsigned char smem[];
  LAS unsigned char* L = (LAS unsigned char*)smem;
  const int tid = threadIdx.x, lane = tid & 63;
  const int w = __builtin_amdgcn_readfirstlane(tid >> 6);
  const int bid = blockIdx.x;
  const int j = bid & 3, pn = (bid >> 3) * 2 + ((bid >> 2) & 1);
  const int r16 = lane & 15, q = lane >> 4;
  const __amdgpu_buffer_rsrc_t rs = __builtin_amdgcn_make_buffer_rsrc((void*)p.ws, 0, (int)WS_END, 0x00020000);
  GAS unsigned* flags = (GAS unsigned*)(p.ws + WS_FLAGS) + pn * 96;
  const float* mod = (const float*)(p.ws + WS_MOD);
  const int wc = w < 7 ? w : 6;
  const GAS u32x4* wp = (const GAS u32x4*)(p.ws + WS_WP + (size_t)(j * 7 + wc) * NFRAG * 1024) + lane;

  u32x4 xin[5];
  {
    const unsigned char* src = p.ws + WS_X2B + (size_t)pn * X2B_BLK;
#pragma unroll
    for (int i = 0; i < 5; ++i) {
      const int pce = tid + 512 * i;
      xin[i] = *(const u32x4*)(src + (pce < 2240 ? pce : 0) * 16);
    }
  }
  u32x4 ring[RING];
  if (w < 7) {
#pragma unroll
    for (int i = 0; i < RING; ++i) ring[i] = wp[i * 64];
  }
  __builtin_amdgcn_sched_barrier(0);
  const int pl = lane < 56 ? lane : 0;
  float cb1[8], sb[8];
  {
    const f32x4 c0 = *(const f32x4*)(mod + 4 * 448 + pl * 8), c1 = *(const f32x4*)(mod + 4 * 448 + pl * 8 + 4);
    const f32x4 t0 = *(const f32x4*)(mod + 3 * 448 + pl * 8), t1 = *(const f32x4*)(mod + 3 * 448 + pl * 8 + 4);
#pragma unroll
    for (int k = 0; k < 4; ++k) { cb1[k] = rbf(1.0f + c0[k]); cb1[4 + k] = rbf(1.0f + c1[k]); sb[k] = t0[k]; sb[4 + k] = t1[k]; }
  }
  const int ncol = j * 112 + wc * 16 + q * 4;
  const f32x4 gbv = *(const f32x4*)(mod + 5 * 448 + ncol);
#pragma unroll
  for (int i = 0; i < 5; ++i) {
    const int pce = tid + 512 * i;
    if (pce < 2240) {
      const int dpi = pce / 10, tq = pce - dpi * 10;
#pragma unroll
      for (int k = 0; k < 4; ++k) *(LAS unsigned*)(L + G_X + (4 * tq + k) * XS + dpi * 4) = xin[i][k];
    }
  }
  bar_lds();
  const int sh = (j == 0) ? 1 : (j == 1) ? -1 : (j == 2) ? 2 : -2;
  {
#pragma unroll
    for (int it = 0; it < 5; ++it) {
      const int row = 2 + w + 8 * it;
      if (row < 38) {
        const u32x4 xr = *(const LAS u32x4*)(L + G_X + row * XS + pl * 16);
        float v[8];
#pragma unroll
        for (int k = 0; k < 4; ++k) { v[2 * k] = bf_lo(xr[k]); v[2 * k + 1] = bf_hi(xr[k]); }
        float s = 0.f;
#pragma unroll
        for (int k = 0; k < 8; ++k) s += v[k];
        if (lane >= 56) s = 0.f;
        float mean = wave_sum(s) * (1.0f / D_);
        float qq = 0.f;
#pragma unroll
        for (int k = 0; k < 8; ++k) { float d = v[k] - mean; qq += d * d; }
        if (lane >= 56) qq = 0.f;
        float var = rbf(wave_sum(qq) * (1.0f / D_));
        float rsd = 1.0f / rbf(sqrtf(rbf(var + 1e-5f)));
        mean = rbf(mean);
#pragma unroll
        for (int k = 0; k < 8; ++k) {
          const float n = rbf(rbf(v[k] - mean) * rsd);
          v[k] = rbf(rbf(n * cb1[k]) + sb[k]);
        }
        const int hr = row - 4;
        if (hr >= 0 && hr < 32 && lane >= j * 14 && lane < j * 14 + 14) {
          u32x4 o = {pk_bf16(v[0], v[1]), pk_bf16(v[2], v[3]), pk_bf16(v[4], v[5]), pk_bf16(v[6], v[7])};
          *(LAS u32x4*)(L + G_HQ + hr * 224 + (lane - j * 14) * 16) = o;
        }
        s = 0.f;
#pragma unroll
        for (int k = 0; k < 8; ++k) s += v[k];
        if (lane >= 56) s = 0.f;
        mean = wave_sum(s) * (1.0f / D_);
        qq = 0.f;
#pragma unroll
        for (int k = 0; k < 8; ++k) { float d = v[k] - mean; qq += d * d; }
        if (lane >= 56) qq = 0.f;
        var = rbf(wave_sum(qq) * (1.0f / D_));
        rsd = 1.0f / rbf(sqrtf(rbf(var + 1e-5f)));
        mean = rbf(mean);
#pragma unroll
        for (int k = 0; k < 8; ++k) v[k] = rbf(v[k] - mean) * rsd;
        const int ar = row - 4 + sh;
        if (ar >= 0 && ar < 32 && lane < 56) {
          u32x4 o = {pk_bf16(v[0], v[1]), pk_bf16(v[2], v[3]), pk_bf16(v[4], v[5]), pk_bf16(v[6], v[7])};
          *(LAS u32x4*)(L + G_A0 + ar * AS + lane * 16) = o;
        }
      }
    }
  }
  bar_lds();
  u32x2 h2r[2], x2r[2], x1r[2];
#pragma unroll
  for (int T = 0; T < 2; ++T) {
    h2r[T] = *(const LAS u32x2*)(L + G_HQ + (16 * T + r16) * 224 + (wc * 16 + q * 4) * 2);
    x2r[T] = *(const LAS u32x2*)(L + G_X + (4 + 16 * T + r16) * XS + ncol * 2);
  }
  if (w < 7) {
    f32x4 acc[2] = {f32x4{0.f, 0.f, 0.f, 0.f}, f32x4{0.f, 0.f, 0.f, 0.f}};
    const LAS unsigned char* ap = L + G_A0 + r16 * AS + q * 16;
#pragma unroll
    for (int ks = 0; ks < 14; ++ks) {
      const int f = ks;
      const u32x4 cur = ring[f % RING];
      if (f + RING < NFRAG) ring[f % RING] = wp[(f + RING) * 64];
      const u32x4 b0 = *(const LAS u32x4*)(ap + ks * 64), b1 = *(const LAS u32x4*)(ap + 16 * AS + ks * 64);
      acc[0] = mfma16(cur, b0, acc[0]);
      acc[1] = mfma16(cur, b1, acc[1]);
    }
#pragma unroll
    for (int T = 0; T < 2; ++T) {
      const int tsrc = pn * 32 + 16 * T + r16 - sh;
      float g[4];
#pragma unroll
      for (int k = 0; k < 4; ++k) g[k] = gelu_tanh(rbf(acc[T][k]));
      u32x2 o = {pk_bf16(g[0], g[1]), pk_bf16(g[2], g[3])};
      if (tsrc < 0 || tsrc >= L_) o = u32x2{0u, 0u};
      *(LAS u32x2*)(L + G_OUT + (16 * T + r16) * OS + (w * 16 + q * 4) * 2) = o;
    }
  }
  bar_lds();
  if (w == 7) {
    publish<7>(L, G_OUT, 14, OS, rs, (int)(WS_G + (size_t)(pn * 4 + j) * 7168), flags + j, lane);
    wait_flags(flags, lane);
  }
  bar_lds();
  {
    u32x4 v[4];
#pragma unroll
    for (int i = 0; i < 4; ++i) {
      const int pce = tid + 512 * i;
      v[i] = __builtin_amdgcn_raw_buffer_load_b128(rs, (int)(WS_G + (size_t)pn * 4 * 7168) + (pce < 1792 ? pce : 0) * 16, 0, 16);
    }
#pragma unroll
    for (int i = 0; i < 4; ++i) {
      const int pce = tid + 512 * i;
      if (pce < 1792) {
        const int jq = pce / 448, rem = pce - jq * 448, row = rem / 14, c = rem - row * 14;
        *(LAS u32x4*)(L + G_A1 + row * AS + (jq * 14 + c) * 16) = v[i];
      }
    }
  }
  bar_lds();
  if (w < 7) {
    f32x4 acc[2] = {f32x4{0.f, 0.f, 0.f, 0.f}, f32x4{0.f, 0.f, 0.f, 0.f}};
    const LAS unsigned char* ap = L + G_A1 + r16 * AS + q * 16;
#pragma unroll
    for (int ks = 0; ks < 14; ++ks) {
      const int f = 14 + ks;
      const u32x4 cur = ring[f % RING];
      if (f + RING < NFRAG) ring[f % RING] = wp[(f + RING) * 64];
      const u32x4 b0 = *(const LAS u32x4*)(ap + ks * 64), b1 = *(const LAS u32x4*)(ap + 16 * AS + ks * 64);
      acc[0] = mfma16(cur, b0, acc[0]);
      acc[1] = mfma16(cur, b1, acc[1]);
    }
#pragma unroll
    for (int T = 0; T < 2; ++T) {
      x1r[T].x = pk_bf16(bf_lo(h2r[T].x) + rbf(acc[T][0]), bf_hi(h2r[T].x) + rbf(acc[T][1]));
      x1r[T].y = pk_bf16(bf_lo(h2r[T].y) + rbf(acc[T][2]), bf_hi(h2r[T].y) + rbf(acc[T][3]));
      *(LAS u32x2*)(L + G_OUT + (16 * T + r16) * OS + (w * 16 + q * 4) * 2) = x1r[T];
    }
  }
  bar_lds();
  if (w == 7) {
    publish<7>(L, G_OUT, 14, OS, rs, (int)(WS_X1 + (size_t)(pn * 4 + j) * 7168), flags + 32 + j, lane);
    wait_flags(flags + 32, lane);
  }
  bar_lds();
  {
    u32x4 v4[4];
    const int jq = pl / 14, c = pl - jq * 14;
#pragma unroll
    for (int i = 0; i < 4; ++i)
      v4[i] = __builtin_amdgcn_raw_buffer_load_b128(rs, (int)(WS_X1 + (size_t)pn * 4 * 7168) + jq * 7168 + (w * 4 + i) * 224 + c * 16, 0, 16);
#pragma unroll
    for (int i = 0; i < 4; ++i) {
      float v[8];
#pragma unroll
      for (int k = 0; k < 4; ++k) { v[2 * k] = bf_lo(v4[i][k]); v[2 * k + 1] = bf_hi(v4[i][k]); }
      float s = 0.f;
#pragma unroll
      for (int k = 0; k < 8; ++k) s += v[k];
      if (lane >= 56) s = 0.f;
      float mean = wave_sum(s) * (1.0f / D_);
      float qq = 0.f;
#pragma unroll
      for (int k = 0; k < 8; ++k) { float d = v[k] - mean; qq += d * d; }
      if (lane >= 56) qq = 0.f;
      const float var = rbf(wave_sum(qq) * (1.0f / D_));
      const float rsd = 1.0f / rbf(sqrtf(rbf(var + 1e-5f)));
      mean = rbf(mean);
#pragma unroll
      for (int k = 0; k < 8; ++k) v[k] = rbf(v[k] - mean) * rsd;
      if (lane < 56) {
        u32x4 o = {pk_bf16(v[0], v[1]), pk_bf16(v[2], v[3]), pk_bf16(v[4], v[5]), pk_bf16(v[6], v[7])};
        *(LAS u32x4*)(L + G_A2 + (w * 4 + i) * AS + lane * 16) = o;
      }
    }
  }
  bar_lds();
  if (w < 7) {
    f32x4 acc[4][2];
#pragma unroll
    for (int i = 0; i < 4; ++i) { acc[i][0] = f32x4{0.f, 0.f, 0.f, 0.f}; acc[i][1] = acc[i][0]; }
    const LAS unsigned char* ap = L + G_A2 + r16 * AS + q * 16;
#pragma unroll
    for (int ks = 0; ks < 14; ++ks) {
      const u32x4 b0 = *(const LAS u32x4*)(ap + ks * 64), b1 = *(const LAS u32x4*)(ap + 16 * AS + ks * 64);
#pragma unroll
      for (int i = 0; i < 4; ++i) {
        const int f = 28 + ks * 4 + i;
        const u32x4 cur = ring[f % RING];
        if (f + RING < NFRAG) ring[f % RING] = wp[(f + RING) * 64];
        acc[i][0] = mfma16(cur, b0, acc[i][0]);
        acc[i][1] = mfma16(cur, b1, acc[i][1]);
      }
    }
#pragma unroll
    for (int i = 0; i < 4; ++i)
#pragma unroll
      for (int T = 0; T < 2; ++T) {
        float g[4];
#pragma unroll
        for (int k = 0; k < 4; ++k) g[k] = gelu_tanh(rbf(acc[i][T][k]));
        u32x2 o = {pk_bf16(g[0], g[1]), pk_bf16(g[2], g[3])};
        *(LAS u32x2*)(L + G_BIG + (16 * T + r16) * BS + (j * 448 + w * 64 + i * 16 + q * 4) * 2) = o;
      }
  }
  bar_lds();
  if (w == 7) {
    publish<28>(L, G_BIG + j * 896, 56, BS, rs, (int)(WS_HID + (size_t)(pn * 4 + j) * 28672), flags + 64 + j, lane);
    wait_flags(flags + 64, lane);
  }
  bar_lds();
  {
    u32x4 v[11];
#pragma unroll
    for (int i = 0; i < 11; ++i) {
      int pce = tid + 512 * i;
      if (pce >= 5376) pce = 0;
      int jq = pce / 1792;
      const int rem = pce - jq * 1792;
      jq += (jq >= j);
      v[i] = __builtin_amdgcn_raw_buffer_load_b128(rs, (int)(WS_HID + (size_t)pn * 4 * 28672) + jq * 28672 + rem * 16, 0, 16);
    }
#pragma unroll
    for (int i = 0; i < 11; ++i) {
      const int pce = tid + 512 * i;
      if (pce < 5376) {
        int jq = pce / 1792;
        const int rem = pce - jq * 1792, row = rem / 56, c = rem - row * 56;
        jq += (jq >= j);
        *(LAS u32x4*)(L + G_BIG + row * BS + (jq * 56 + c) * 16) = v[i];
      }
    }
  }
  bar_lds();
  if (w < 7) {
    f32x4 acc[2] = {f32x4{0.f, 0.f, 0.f, 0.f}, f32x4{0.f, 0.f, 0.f, 0.f}};
    const LAS unsigned char* ap = L + G_BIG + r16 * BS + q * 16;
#pragma unroll
    for (int ks = 0; ks < 56; ++ks) {
      const int f = 84 + ks;
      const u32x4 cur = ring[f % RING];
      if (f + RING < NFRAG) ring[f % RING] = wp[(f + RING) * 64];
      const u32x4 b0 = *(const LAS u32x4*)(ap + ks * 64), b1 = *(const LAS u32x4*)(ap + 16 * BS + ks * 64);
      acc[0] = mfma16(cur, b0, acc[0]);
      acc[1] = mfma16(cur, b1, acc[1]);
    }
#pragma unroll
    for (int T = 0; T < 2; ++T) {
      const float b0 = rbf(bf_lo(x1r[T].x) + rbf(acc[T][0])), b1 = rbf(bf_hi(x1r[T].x) + rbf(acc[T][1]));
      const float b2 = rbf(bf_lo(x1r[T].y) + rbf(acc[T][2])), b3 = rbf(bf_hi(x1r[T].y) + rbf(acc[T][3]));
      u32x2 o;
      o.x = pk_bf16(bf_lo(x2r[T].x) + rbf(gbv[0] * b0), bf_hi(x2r[T].x) + rbf(gbv[1] * b1));
      o.y = pk_bf16(bf_lo(x2r[T].y) + rbf(gbv[2] * b2), bf_hi(x2r[T].y) + rbf(gbv[3] * b3));
      *(u32x2*)(p.out + (size_t)(pn * 32 + 16 * T + r16) * D_ + ncol) = o;
    }
  }
}

}

extern "C" void kernel_launch(void* const* d_in, const int* in_sizes, int n_in, void* d_out, int out_size, void* d_ws,
                              size_t ws_size, hipStream_t stream) {
  (void)in_sizes; (void)n_in; (void)out_size; (void)ws_size;
  PrepArgs pa;
  pa.x = (const unsigned short*)d_in[0];
  pa.a_re = (const unsigned short*)d_in[1];
  pa.a_im = (const unsigned short*)d_in[2];
  pa.b_re = (const unsigned short*)d_in[3];
  pa.b_im = (const unsigned short*)d_in[4];
  pa.c_re = (const unsigned short*)d_in[5];
  pa.c_im = (const unsigned short*)d_in[6];
  pa.log_dt = (const unsigned short*)d_in[7];
  pa.w1 = (const unsigned short*)d_in[10];
  pa.w2 = (const unsigned short*)d_in[11];
  pa.w3 = (const unsigned short*)d_in[12];
  pa.w4 = (const unsigned short*)d_in[13];
  pa.cvec = (const unsigned short*)d_in[14];
  pa.wmod = (const unsigned short*)d_in[15];
  pa.ws = (unsigned char*)d_ws;
  k_prep<<<NB_PREP, 256, 0, stream>>>(pa);
  ConvArgs ca;
  ca.dskip = (const unsigned short*)d_in[8];
  ca.ws = (unsigned char*)d_ws;
  (void)hipFuncSetAttribute((const void*)k_conv, hipFuncAttributeMaxDynamicSharedMemorySize, CV_LDS);
  k_conv<<<224, 512, CV_LDS, stream>>>(ca);
  GArgs ga;
  ga.ws = (unsigned char*)d_ws;
  ga.out = (unsigned short*)d_out;
  (void)hipFuncSetAttribute((const void*)k_gchain, hipFuncAttributeMaxDynamicSharedMemorySize, G_LDS);
  k_gchain<<<256, 512, G_LDS, stream>>>(ga);
}
```

```cpp
#include <hip/hip_runtime.h>
#include <hip/hip_bf16.h>
#include <stdint.h>

namespace {

typedef __attribute__((ext_vector_type(8))) short bf16x8;
typedef __attribute__((ext_vector_type(8))) _Float16 f16x8;
typedef __attribute__((ext_vector_type(2))) _Float16 f16x2;
typedef __attribute__((ext_vector_type(4))) float f32x4;
typedef __attribute__((ext_vector_type(2))) float f32x2;
typedef __attribute__((ext_vector_type(4))) unsigned u32x4;
typedef __attribute__((ext_vector_type(2))) unsigned u32x2;
typedef __attribute__((ext_vector_type(2))) __bf16 bf16x2v;
#define GAS __attribute__((address_space(1)))
#define LAS __attribute__((address_space(3)))

constexpr int L_ = 2048, D_ = 448, NS_ = 256;

constexpr size_t WS_MOD = 0;
constexpr size_t WS_STAT = WS_MOD + 16384;
constexpr size_t WS_XB = WS_STAT + 16384;
constexpr size_t KR_STRIDE = 4224;
constexpr size_t WS_KR = WS_XB + (size_t)64 * 448 * 64;
constexpr int NFRAG = 140;
constexpr size_t WS_WP = WS_KR + (size_t)448 * KR_STRIDE + 1024;
constexpr size_t WS_X2B = WS_WP + (size_t)28 * NFRAG * 1024;
constexpr size_t X2B_BLK = 224 * 128;
constexpr size_t WS_G = WS_X2B + 64 * X2B_BLK;
constexpr size_t WS_X1 = WS_G + (size_t)64 * 4 * 7168;
constexpr size_t WS_P = WS_X1 + (size_t)64 * 4 * 7168;
constexpr size_t WS_END = WS_P + (size_t)64 * 12 * 7168;

#define STAMP(base, k) do { } while (0)

__device__ __forceinline__ float bf_lo(unsigned u) { return __builtin_bit_cast(float, u << 16); }
__device__ __forceinline__ float bf_hi(unsigned u) { return __builtin_bit_cast(float, u & 0xffff0000u); }
__device__ __forceinline__ float bf2f(unsigned short h) { return __builtin_bit_cast(float, (unsigned)h << 16); }
__device__ __forceinline__ unsigned pk_bf16(float a, float b) {
  f32x2 v = {a, b};
  bf16x2v r = __builtin_convertvector(v, bf16x2v);
  return __builtin_bit_cast(unsigned, r);
}
__device__ __forceinline__ unsigned pk_f16(f32x2 v) {
  f16x2 r = __builtin_convertvector(v, f16x2);
  return __builtin_bit_cast(unsigned, r);
}
__device__ __forceinline__ float rbf(float a) { return bf_lo(pk_bf16(a, a)); }
__device__ __forceinline__ f32x4 mfma16(u32x4 a, u32x4 b, f32x4 c) {
  return __builtin_amdgcn_mfma_f32_16x16x32_bf16(__builtin_bit_cast(bf16x8, a), __builtin_bit_cast(bf16x8, b), c, 0, 0, 0);
}
__device__ __forceinline__ f32x4 mfma16h(u32x4 a, u32x4 b, f32x4 c) {
  return __builtin_amdgcn_mfma_f32_16x16x32_f16(__builtin_bit_cast(f16x8, a), __builtin_bit_cast(f16x8, b), c, 0, 0, 0);
}
template <int CTRL>
__device__ __forceinline__ float dpp_f(float v) {
  return __builtin_bit_cast(float, __builtin_amdgcn_update_dpp(0, __builtin_bit_cast(int, v), CTRL, 0xf, 0xf, true));
}
__device__ __forceinline__ float row16_sum(float v) {
  v += dpp_f<0xB1>(v);
  v += dpp_f<0x4E>(v);
  v += dpp_f<0x141>(v);
  v += dpp_f<0x140>(v);
  return v;
}
__device__ __forceinline__ float row16_max(float v) {
  v = fmaxf(v, dpp_f<0xB1>(v));
  v = fmaxf(v, dpp_f<0x4E>(v));
  v = fmaxf(v, dpp_f<0x141>(v));
  v = fmaxf(v, dpp_f<0x140>(v));
  return v;
}
__device__ __forceinline__ void swap16(float v, float& a, float& b) {
  const unsigned u = __builtin_bit_cast(unsigned, v), u2 = u;
  auto r = __builtin_amdgcn_permlane16_swap(u, u2, false, false);
  const unsigned r0 = r[0], r1 = r[1];
  a = __builtin_bit_cast(float, r0); b = __builtin_bit_cast(float, r1);
}
__device__ __forceinline__ void swap32(float v, float& a, float& b) {
  const unsigned u = __builtin_bit_cast(unsigned, v), u2 = u;
  auto r = __builtin_amdgcn_permlane32_swap(u, u2, false, false);
  const unsigned r0 = r[0], r1 = r[1];
  a = __builtin_bit_cast(float, r0); b = __builtin_bit_cast(float, r1);
}
__device__ __forceinline__ float rows_sum(float v) {
  float a, b;
  swap16(v, a, b); v = a + b;
  swap32(v, a, b);
  return a + b;
}
__device__ __forceinline__ float rows_max(float v) {
  float a, b;
  swap16(v, a, b); v = fmaxf(a, b);
  swap32(v, a, b);
  return fmaxf(a, b);
}
__device__ __forceinline__ float wave_sum(float v) { return rows_sum(row16_sum(v)); }
__device__ __forceinline__ float gelu_tanh(float x) {
  const float c1 = -2.0f * 0.7978845608028654f * 1.4426950408889634f;
  const float c2 = c1 * 0.044715f;
  float t = x * (c1 + c2 * x * x);
  float e = __builtin_amdgcn_exp2f(t);
  return x * __builtin_amdgcn_rcpf(1.0f + e);
}
__device__ __forceinline__ float ln_sd(float var) { return rbf(__builtin_amdgcn_sqrtf(rbf(rbf(var) + 1e-5f))); }
__device__ __forceinline__ void lds_fence() { asm volatile("" ::: "memory"); }
__device__ __forceinline__ void bar_lds() {
  asm volatile("s_waitcnt lgkmcnt(0)" ::: "memory");
  __builtin_amdgcn_s_barrier();
  asm volatile("" ::: "memory");
}

__device__ __forceinline__ void transpose8x8(const u32x4 (&r)[8], u32x4 (&o)[8]) {
#pragma unroll
  for (int c2 = 0; c2 < 4; ++c2) {
#pragma unroll
    for (int rp = 0; rp < 4; ++rp) {
      unsigned a = r[2 * rp][c2], b = r[2 * rp + 1][c2];
      o[2 * c2][rp] = __builtin_amdgcn_perm(b, a, 0x05040100u);
      o[2 * c2 + 1][rp] = __builtin_amdgcn_perm(b, a, 0x07060302u);
    }
  }
}
__device__ __forceinline__ f32x2 cmul(f32x2 a, f32x2 b) {
  f32x2 r, t;
  asm("v_pk_mul_f32 %0, %1, %2 op_sel_hi:[0,1]" : "=v"(t) : "v"(a), "v"(b));
  asm("v_pk_fma_f32 %0, %1, %2, %3 op_sel:[1,1,0] op_sel_hi:[1,0,1] neg_lo:[1,0,0]" : "=v"(r) : "v"(a), "v"(b), "v"(t));
  return r;
}
__device__ __forceinline__ f32x2 cexp_tab(float a, float b, float l) {
  const float e = __builtin_amdgcn_exp2f(a * l), ph = __builtin_amdgcn_fractf(b * l);
  f32x2 r;
  r.x = e * __builtin_amdgcn_cosf(ph);
  r.y = e * __builtin_amdgcn_sinf(ph);
  return r;
}

struct PrepArgs {
  const unsigned short* x;
  const unsigned short* a_re;
  const unsigned short* a_im;
  const unsigned short* b_re;
  const unsigned short* b_im;
  const unsigned short* c_re;
  const unsigned short* c_im;
  const unsigned short* log_dt;
  const unsigned short* w1;
  const unsigned short* w2;
  const unsigned short* w3;
  const unsigned short* w4;
  const unsigned short* cvec;
  const unsigned short* wmod;
  unsigned char* ws;
};

constexpr int NB_LN = 64, NB_MOD = 84, NB_PACK = 140, NB_KG = 448;
constexpr int NB_PREP = NB_LN + NB_MOD + NB_PACK + NB_KG;
constexpr int KG_ROW = 288;
constexpr int KG_BUF = 16 * KG_ROW;
constexpr int PREP_LDS = 4 * 2 * KG_BUF;

__global__ __launch_bounds__(256) void k_prep(PrepArgs p) {
  __shared__ __attribute__((aligned(16))) unsigned char smem[PREP_LDS];
  LAS unsigned char* L = (LAS unsigned char*)smem;
  const int tid = threadIdx.x, lane = tid & 63;
  const int w = __builtin_amdgcn_readfirstlane(tid >> 6);
  int b = blockIdx.x >= NB_KG ? blockIdx.x - NB_KG : blockIdx.x + (NB_PREP - NB_KG);
  STAMP(1 << 20, 0);
  if (b < NB_LN) {
    const int t0 = b * 32 + w * 8;
    u32x4 r[8];
    const bool act = lane < 56;
    const int pl = act ? lane : 0;
#pragma unroll
    for (int i = 0; i < 8; ++i) r[i] = *(const u32x4*)(p.x + (size_t)(t0 + i) * D_ + pl * 8);
#pragma unroll
    for (int i = 0; i < 7; ++i) {
      *(u32x4*)(p.ws + WS_X2B + (size_t)b * X2B_BLK + (tid + 256 * i) * 16) = u32x4{~0u, ~0u, ~0u, ~0u};
      *(u32x4*)(p.ws + WS_G + (size_t)b * X2B_BLK + (tid + 256 * i) * 16) = u32x4{~0u, ~0u, ~0u, ~0u};
    }
    const float msk = act ? 1.0f : 0.0f;
    u32x4 o[8];
    transpose8x8(r, o);
#pragma unroll
    for (int i = 0; i < 8; ++i) *(LAS u32x4*)(L + (pl * 8 + i) * 64 + ((w ^ (pl & 3)) * 16) + (act ? 0 : 30000 - i * 64)) = o[i];
    unsigned st = 0;
#pragma unroll
    for (int i = 0; i < 8; ++i) {
      float v[8];
#pragma unroll
      for (int k = 0; k < 4; ++k) { v[2 * k] = bf_lo(r[i][k]); v[2 * k + 1] = bf_hi(r[i][k]); }
      float s = ((v[0] + v[1]) + (v[2] + v[3])) + ((v[4] + v[5]) + (v[6] + v[7]));
      const float mean = wave_sum(s * msk) * (1.0f / D_);
      float q = 0.f;
#pragma unroll
      for (int k = 0; k < 8; ++k) { float d = v[k] - mean; q += d * d; }
      const float sd = ln_sd(wave_sum(q * msk) * (1.0f / D_));
      const unsigned pk = pk_bf16(mean, sd);
      st = (lane == i) ? pk : st;
    }
    if (lane < 8) *(unsigned*)(p.ws + WS_STAT + (size_t)(t0 + lane) * 4) = st;
    bar_lds();
    unsigned char* dst = p.ws + WS_XB + (size_t)b * (D_ * 64);
#pragma unroll
    for (int i = 0; i < 7; ++i) {
      const int pce = tid + 256 * i, d = pce >> 2, s = pce & 3;
      const u32x4 v = *(const LAS u32x4*)(L + d * 64 + ((s ^ ((d >> 3) & 3)) * 16));
      *(u32x4*)(dst + pce * 16) = v;
    }
    STAMP(1 << 20, 1);
    return;
  }
  b -= NB_LN;
  if (b < NB_MOD) {
    const int rg = tid >> 2, pc = tid & 3;
    u32x4 r[7];
    float sc[7];
#pragma unroll
    for (int i = 0; i < 7; ++i) r[i] = *(const u32x4*)(p.wmod + (size_t)(rg + 64 * i) * 2688 + b * 32 + pc * 8);
#pragma unroll
    for (int i = 0; i < 7; ++i) {
      float c = bf2f(p.cvec[rg + 64 * i]);
      sc[i] = rbf(c / (1.0f + expf(-c)));
    }
    float acc[8];
#pragma unroll
    for (int k = 0; k < 8; ++k) acc[k] = 0.f;
#pragma unroll
    for (int i = 0; i < 7; ++i)
#pragma unroll
      for (int k = 0; k < 4; ++k) { acc[2 * k] += sc[i] * bf_lo(r[i][k]); acc[2 * k + 1] += sc[i] * bf_hi(r[i][k]); }
    LAS float* sm = (LAS float*)L;
#pragma unroll
    for (int k = 0; k < 8; ++k) sm[rg * 33 + pc * 8 + k] = acc[k];
    bar_lds();
    {
      const int col = tid & 31, part = tid >> 5;
      float s = 0.f;
#pragma unroll
      for (int k = 0; k < 8; ++k) s += sm[(part * 8 + k) * 33 + col];
      sm[64 * 33 + part * 32 + col] = s;
    }
    bar_lds();
    if (tid < 32) {
      float s = 0.f;
#pragma unroll
      for (int k = 0; k < 8; ++k) s += sm[64 * 33 + k * 32 + tid];
      s = rbf(s);
      const int sec = b / 14;
      if (sec == 1 || sec == 4) s += 1.0f;
      *(unsigned short*)(p.ws + WS_MOD + (size_t)(b * 32 + tid) * 2) = (unsigned short)pk_bf16(s, s);
    }
    STAMP(1 << 20, 2);
    return;
  }
  b -= NB_MOD;
  if (b < NB_PACK) {
    int g = b * 4 + w;
    const unsigned short* W;
    int ldn, mat;
    if (g < 56) { W = p.w1; ldn = 448; mat = 0; }
    else if (g < 112) { W = p.w2; ldn = 448; mat = 1; g -= 56; }
    else if (g < 336) { W = p.w3; ldn = 1792; mat = 2; g -= 112; }
    else { W = p.w4; ldn = 448; mat = 3; g -= 336; }
    const int ncb = ldn / 112;
    const int ks = g / ncb, cb = g - ks * ncb;
    const int jn = lane & 15, q = lane >> 4;
    const bool act = jn < 14;
    const int jl = act ? jn : 0;
    u32x4 r[8], o[8];
#pragma unroll
    for (int i = 0; i < 8; ++i) r[i] = *(const u32x4*)(W + (size_t)(ks * 32 + q * 8 + i) * ldn + cb * 112 + jl * 8);
    transpose8x8(r, o);
    LAS unsigned char* Lw = L + w * 9216;
#pragma unroll
    for (int i = 0; i < 8; ++i) *(LAS u32x4*)(Lw + (jn >> 1) * 1024 + (q * 16 + (jn & 1) * 8 + (i ^ (jn >> 1))) * 16) = o[i];
    lds_fence();
#pragma unroll
    for (int m = 0; m < 7; ++m) {
      const u32x4 v = *(const LAS u32x4*)(Lw + m * 1024 + (lane ^ m) * 16);
      int j, wv, f;
      if (mat == 2) { const int nt = cb * 7 + m; j = nt / 28; const int ntl = nt - j * 28; wv = ntl >> 2; f = 28 + ks * 4 + (ntl & 3); }
      else if (mat == 3) { j = ks / 14; const int ksl = ks - j * 14; wv = m; const int sl = (cb - j - 1) & 3; f = sl < 3 ? 84 + ksl * 3 + sl : 126 + ksl; }
      else { j = cb; wv = m; f = mat * 14 + ks; }
      *(u32x4*)(p.ws + WS_WP + ((size_t)(j * 7 + wv) * NFRAG + f) * 1024 + lane * 16) = v;
    }
    STAMP(1 << 20, 3);
    return;
  }
  b -= NB_PACK;
  {
    const int d = b;
    const size_t o = (size_t)d * NS_ + tid;
    const float are = bf2f(p.a_re[o]), aim = bf2f(p.a_im[o]);
    const float b_re = bf2f(p.b_re[o]), b_im = bf2f(p.b_im[o]);
    const float cre = bf2f(p.c_re[o]), cim = bf2f(p.c_im[o]);
    const float dt = rbf(expf(bf2f(p.log_dt[d])));
    const float ea = (dt * are) * 1.4426950408889634f, eb = (dt * aim) * 0.15915494309189535f;
    const float bre = dt * b_re, bim = dt * b_im;
    f32x2 wc;
    wc.x = cre * bre - cim * bim;
    wc.y = -(cre * bim + cim * bre);
    const float mx = rows_max(row16_max(fmaxf(fabsf(wc.x), fabsf(wc.y))));
    int ex = mx > 0.f ? __builtin_amdgcn_frexp_expf(mx) : 0;
    ex = ex < -120 ? -120 : (ex > 120 ? 120 : ex);
    const float scale = __builtin_amdgcn_ldexpf(1.0f, ex), iscale = __builtin_amdgcn_ldexpf(1.0f, -ex);
    wc.x *= iscale; wc.y *= iscale;
    STAMP(1 << 20, 4);
    LAS unsigned char* B0 = L + w * (2 * KG_BUF);
    LAS unsigned char* B1 = B0 + KG_BUF;
    const int wo = lane * 4;
    const int ro = (lane & 15) * KG_ROW + (lane >> 4) * 16;
    f32x2 z8c = cexp_tab(ea, eb, 8.0f);
    z8c.y = -z8c.y;
    const f32x2 z1 = cexp_tab(ea, eb, 1.0f), z128 = cexp_tab(ea, eb, 128.0f), z1024 = cexp_tab(ea, eb, 1024.0f);
    {
      const f32x2 z16c = cmul(z8c, z8c);
      f32x2 t0 = wc, t1 = cmul(wc, z8c);
#pragma unroll
      for (int c = 0; c < 16; c += 2) {
        *(LAS unsigned*)(B0 + c * KG_ROW + wo) = pk_f16(t0);
        *(LAS unsigned*)(B0 + (c + 1) * KG_ROW + wo) = pk_f16(t1);
        if (c < 14) { t0 = cmul(t0, z16c); t1 = cmul(t1, z16c); }
      }
    }
    f32x2 ze[8];
    ze[0] = f32x2{1.0f, 0.0f}; ze[1] = z1; ze[2] = cmul(z1, z1); ze[3] = cmul(ze[2], z1); ze[4] = cmul(ze[2], ze[2]);
    ze[5] = cmul(ze[4], z1); ze[6] = cmul(ze[4], ze[2]); ze[7] = cmul(ze[4], ze[3]);
#pragma unroll
    for (int e = 0; e < 8; ++e) {
      *(LAS unsigned*)(B1 + e * KG_ROW + wo) = pk_f16(ze[e]);
      *(LAS unsigned*)(B1 + (8 + e) * KG_ROW + wo) = pk_f16(e == 0 ? z128 : cmul(z128, ze[e]));
    }
    lds_fence();
    u32x4 tf[4];
#pragma unroll
    for (int ks = 0; ks < 4; ++ks) tf[ks] = *(const LAS u32x4*)(B0 + ro + ks * 64);
    u32x4 rf[4];
#pragma unroll
    for (int ks = 0; ks < 4; ++ks) rf[ks] = *(const LAS u32x4*)(B1 + ro + ks * 64);
    lds_fence();
    const f32x2 p2 = cmul(z128, z128), p3 = cmul(p2, z128), p4 = cmul(p2, p2), p5 = cmul(p4, z128), p6 = cmul(p4, p2), p7 = cmul(p4, p3);
    {
#pragma unroll
      for (int e = 0; e < 4; ++e) {
        *(LAS unsigned*)(B0 + e * KG_ROW + wo) = pk_f16(e == 0 ? p2 : cmul(p2, ze[2 * e]));
        *(LAS unsigned*)(B0 + (4 + e) * KG_ROW + wo) = pk_f16(e == 0 ? p3 : cmul(p3, ze[2 * e]));
      }
      *(LAS unsigned*)(B0 + 8 * KG_ROW + wo) = pk_f16(p4);
      *(LAS unsigned*)(B0 + 9 * KG_ROW + wo) = pk_f16(cmul(p4, ze[4]));
      *(LAS unsigned*)(B0 + 10 * KG_ROW + wo) = pk_f16(p5);
      *(LAS unsigned*)(B0 + 11 * KG_ROW + wo) = pk_f16(cmul(p5, ze[4]));
      *(LAS unsigned*)(B0 + 12 * KG_ROW + wo) = pk_f16(p6);
      *(LAS unsigned*)(B0 + 13 * KG_ROW + wo) = pk_f16(cmul(p6, ze[4]));
      *(LAS unsigned*)(B0 + 14 * KG_ROW + wo) = pk_f16(p7);
      *(LAS unsigned*)(B0 + 15 * KG_ROW + wo) = pk_f16(cmul(p7, ze[4]));
    }
    f32x4 acc0 = {0.f, 0.f, 0.f, 0.f}, acc1 = acc0, acc2 = acc0;
#pragma unroll
    for (int ks = 0; ks < 4; ++ks) acc0 = mfma16h(rf[ks], tf[ks], acc0);
    {
      *(LAS unsigned*)(B1 + 0 * KG_ROW + wo) = pk_f16(z1024);
      *(LAS unsigned*)(B1 + 1 * KG_ROW + wo) = pk_f16(cmul(z1024, z128));
      *(LAS unsigned*)(B1 + 2 * KG_ROW + wo) = pk_f16(cmul(z1024, p2));
      *(LAS unsigned*)(B1 + 3 * KG_ROW + wo) = pk_f16(cmul(z1024, p3));
      *(LAS unsigned*)(B1 + 4 * KG_ROW + wo) = pk_f16(cmul(z1024, p4));
      *(LAS unsigned*)(B1 + 5 * KG_ROW + wo) = pk_f16(cmul(z1024, p5));
      *(LAS unsigned*)(B1 + 6 * KG_ROW + wo) = pk_f16(cmul(z1024, p6));
      *(LAS unsigned*)(B1 + 7 * KG_ROW + wo) = pk_f16(cmul(z1024, p7));
      *(LAS unsigned*)(B1 + 8 * KG_ROW + wo) = pk_f16(cmul(z1024, z1024));
    }
    lds_fence();
#pragma unroll
    for (int ks = 0; ks < 4; ++ks) acc1 = mfma16h(*(const LAS u32x4*)(B0 + ro + ks * 64), tf[ks], acc1);
#pragma unroll
    for (int ks = 0; ks < 4; ++ks) acc2 = mfma16h(*(const LAS u32x4*)(B1 + ro + ks * 64), tf[ks], acc2);
    lds_fence();
    STAMP(1 << 20, 5);
    {
      LAS float* red = (LAS float*)B0;
      const int c = lane & 15, q = lane >> 4;
#pragma unroll
      for (int r = 0; r < 4; ++r) {
        const int row = 4 * q + r;
        red[(row >> 3) * 128 + 8 * c + (row & 7)] = acc0[r] * scale;
        const int i1 = row < 8 ? 256 + (row >> 2) * 64 + 4 * c + (row & 3) : 384 + ((row - 8) >> 1) * 32 + 2 * c + (row & 1);
        red[i1] = acc1[r] * scale;
        const int i2 = row < 9 ? 512 + row * 16 + c : 700;
        red[i2] = acc2[r] * scale;
      }
    }
    bar_lds();
    LAS float* kd = (LAS float*)(L + KG_BUF);
#pragma unroll
    for (int i = 0; i < 3; ++i) {
      const int oo = tid + 256 * i;
      if (oo < 648) {
        const LAS float* s = (const LAS float*)L + oo;
        kd[oo] = (s[0] + s[2 * KG_BUF / 4]) + (s[4 * KG_BUF / 4] + s[6 * KG_BUF / 4]);
      }
    }
    if (tid == 0) kd[648] = 0.f;
    bar_lds();
    {
      float v[8];
#pragma unroll
      for (int i = 0; i < 8; i += 2) {
        const int l0 = 2048 - (tid * 8 + i), l1 = l0 - 1;
        const unsigned bits = pk_bf16((float)l0, (float)l1);
        int i0 = l0 < 128 ? l0 : (int)(bits & 0xffffu) - 17024;
        const int i1 = l1 < 128 ? l1 : (int)(bits >> 16) - 17024;
        if (i == 0) i0 = tid == 0 ? 648 : i0;
        v[i] = kd[i0];
        v[i + 1] = kd[i1];
      }
      u32x4 ov = {pk_bf16(v[0], v[1]), pk_bf16(v[2], v[3]), pk_bf16(v[4], v[5]), pk_bf16(v[6], v[7])};
      unsigned char* dst = p.ws + WS_KR + (size_t)d * KR_STRIDE;
      *(u32x4*)(dst + tid * 16) = ov;
      if (tid < 8) {
        u32x4 z = {0u, 0u, 0u, 0u};
        if (tid == 0) z.x = pk_bf16(kd[0], 0.f);
        *(u32x4*)(dst + (256 + tid) * 16) = z;
      }
    }
    STAMP(1 << 20, 6);
  }
}

constexpr int CV_KCOPY = 8 * 272 * 16;
constexpr int CV_UROW = 160;
constexpr int CV_U = 48 * CV_UROW;
constexpr int CV_CH = CV_KCOPY + CV_U;
constexpr int CV_LDS = 2 * CV_CH;
constexpr int CV_PROW = 272;
static_assert(4 * 32 * CV_PROW <= CV_KCOPY, "partials alias the kernel copies");

struct GArgs {
  unsigned char* ws;
  unsigned short* out;
  const unsigned short* dskip;
};
constexpr int AS = 928;
constexpr int G_T0 = 0;
constexpr int G_T1 = G_T0 + 32 * AS;
constexpr int G_X = G_T1;
constexpr int G_HQ = G_X + 32768;
constexpr int G_WL = G_HQ + 32 * 240;
constexpr int NLDS = 12;
constexpr int NRING = NFRAG - NLDS;
constexpr int G_MOD = G_WL + 7 * NLDS * 1024;
constexpr int G_FLAG = G_MOD + 2 * 448 * 4;
constexpr int G_LDS = G_FLAG + 16;
static_assert(G_MOD >= CV_LDS && G_LDS <= 163840, "LDS map");
constexpr int RING = 40;
#define RINGA_ 0
constexpr int RINGA = RINGA_;
constexpr int RINGB = RINGA + 16;
constexpr int RING0 = RINGB + 6;
constexpr int CV_P = CV_LDS;
static_assert(CV_P + 8 * 32 * CV_PROW <= G_MOD, "partials");
constexpr unsigned SPIN_CAP = 1u << 14;
constexpr int AUX_SC1 = 16;
#define WARM 0
#define EXP_REFILL true
#define POLL_SLEEP 6

__device__ __forceinline__ unsigned sane(unsigned v) { return v < 0xFFFFFFFEu ? v : 0xFFFFFFFEu; }
__device__ __forceinline__ bool bad4(const u32x4& v) { return ((v.x == ~0u) | (v.y == ~0u)) | ((v.z == ~0u) | (v.w == ~0u)); }
__device__ __forceinline__ u32x4 make_piece(u32x2 a, u32x2 b) {
  auto rx = __builtin_amdgcn_permlane16_swap(a.x, b.x, false, false);
  auto ry = __builtin_amdgcn_permlane16_swap(a.y, b.y, false, false);
  return u32x4{sane(rx[0]), sane(ry[0]), sane(rx[1]), sane(ry[1])};
}
template <int N>
__device__ __forceinline__ void poll_issue(__amdgpu_buffer_rsrc_t r, const int (&off)[N], u32x4 (&v)[N]) {
#pragma unroll
  for (int i = 0; i < N; ++i) v[i] = __builtin_amdgcn_raw_buffer_load_b128(r, off[i], 0, AUX_SC1);
  __builtin_amdgcn_sched_barrier(0);
}
template <int N>
__device__ __forceinline__ void poll_finish(__amdgpu_buffer_rsrc_t r, const int (&off)[N], u32x4 (&v)[N]) {
  for (unsigned spins = 0; spins < SPIN_CAP; ++spins) {
    bool any = false;
#pragma unroll
    for (int i = 0; i < N; ++i) any |= bad4(v[i]);
    if (!__any(any)) break;
    __builtin_amdgcn_s_sleep(POLL_SLEEP);
    asm volatile("" ::: "memory");
#pragma unroll
    for (int i = 0; i < N; ++i)
      if (bad4(v[i])) v[i] = __builtin_amdgcn_raw_buffer_load_b128(r, off[i], 0, AUX_SC1);
  }
}
template <int N>
__device__ __forceinline__ void poll_load(__amdgpu_buffer_rsrc_t r, const int (&off)[N], u32x4 (&v)[N]) {
  poll_issue<N>(r, off, v);
  poll_finish<N>(r, off, v);
}
__device__ __forceinline__ float dot2bf(unsigned a, unsigned b, float c) {
  return __builtin_amdgcn_fdot2_f32_bf16(__builtin_bit_cast(bf16x2v, a), __builtin_bit_cast(bf16x2v, b), c, false);
}
__device__ __forceinline__ void ln_stats(const unsigned (&x)[16], float lmsk, float& r, float& mr) {
  float s0 = 0.f, s1 = 0.f, q0 = 0.f, q1 = 0.f;
#pragma unroll
  for (int k = 0; k < 16; k += 2) {
    s0 = dot2bf(x[k], 0x3f803f80u, s0); q0 = dot2bf(x[k], x[k], q0);
    s1 = dot2bf(x[k + 1], 0x3f803f80u, s1); q1 = dot2bf(x[k + 1], x[k + 1], q1);
  }
  const float mean = row16_sum((s0 + s1) * lmsk) * (1.0f / D_);
  const float msq = row16_sum((q0 + q1) * lmsk) * (1.0f / D_);
  const float var = fmaxf(msq - mean * mean, 0.f);
  r = __builtin_amdgcn_rcpf(ln_sd(var));
  mr = -rbf(mean) * r;
}

#define SB0() __builtin_amdgcn_sched_barrier(0)
#define WLOAD(f) __builtin_amdgcn_raw_buffer_load_b128(rw, voff, (f) * 1024, 0)
template <int NT, int F0, int LSTART, int LNUM, int LEVERY, int PD>
__device__ __forceinline__ void gemm14(const LAS unsigned char* ap, u32x4 (&ring)[RING], __amdgpu_buffer_rsrc_t rw, int voff, f32x4 (&acc)[NT][2]) {
  u32x4 bq[PD][2];
#pragma unroll
  for (int i = 0; i < PD; ++i) { bq[i][0] = *(const LAS u32x4*)(ap + i * 64); bq[i][1] = *(const LAS u32x4*)(ap + 16 * AS + i * 64); }
#pragma unroll
  for (int i = 0; i < NT; ++i) { acc[i][0] = f32x4{0.f, 0.f, 0.f, 0.f}; acc[i][1] = acc[i][0]; }
  SB0();
#pragma unroll
  for (int ks = 0; ks < 14; ++ks) {
#pragma unroll
    for (int i = 0; i < NT; ++i) {
      const int st = ks * NT + i, f = F0 + st;
      acc[i][0] = mfma16(ring[f % RING], bq[ks % PD][0], acc[i][0]);
      acc[i][1] = mfma16(ring[f % RING], bq[ks % PD][1], acc[i][1]);
      if (st % LEVERY == 0 && st / LEVERY < LNUM) {
        static_assert(LNUM == 0 || LSTART - RING <= F0, "slot still in use");
        const int g = LSTART + st / LEVERY;
        ring[g % RING] = WLOAD(g);
      }
      if (i == NT - 1 && ks + PD < 14) {
        bq[ks % PD][0] = *(const LAS u32x4*)(ap + (ks + PD) * 64);
        bq[ks % PD][1] = *(const LAS u32x4*)(ap + 16 * AS + (ks + PD) * 64);
      }
      SB0();
    }
  }
}
template <int F0, int N>
__device__ __forceinline__ void ring_fill(u32x4 (&ring)[RING], __amdgpu_buffer_rsrc_t rw, int voff) {
#pragma unroll
  for (int f = F0; f < F0 + N; ++f) { ring[f % RING] = WLOAD(f); SB0(); }
}

__device__ __forceinline__ void gather_x2(const GArgs& p, LAS unsigned char* L, __amdgpu_buffer_rsrc_t rs, int pn, int tid) {
  const int lane = tid & 63, w = tid >> 6; (void)lane; (void)w; (void)p;
  u32x4 xin[4];
  int xo[4];
#pragma unroll
  for (int i = 0; i < 4; ++i) {
    const int pce = tid + 512 * i;
    xo[i] = pce < 1792 ? (int)WS_X2B + pn * (int)X2B_BLK + pce * 16 : (int)WS_END;
  }
  STAMP(0, 27);
  poll_issue<4>(rs, xo, xin);
  STAMP(0, 28);
  poll_finish<4>(rs, xo, xin);
  STAMP(0, 26);
  bar_lds();
#pragma unroll
  for (int i = 0; i < 4; ++i) {
    const int pce = tid + 512 * i;
    if (i < 3 || pce < 1792) {
      const int dpi = pce >> 3, tq = pce & 7;
      LAS unsigned char* dst = L + G_X + tq * 4096 + (((dpi >> 2) ^ tq) * 16) + (dpi & 3) * 4;
#pragma unroll
      for (int k = 0; k < 4; ++k) *(LAS unsigned*)(dst + k * 1024) = xin[i][k];
    }
  }
}
__device__ __forceinline__ void ln_phase_a(LAS unsigned char* L, int j, int w, int r16, int q) {
  const int c16 = r16 < 14 ? r16 : 0;
  const float lmsk = r16 < 14 ? 1.0f : 0.0f;
  const int row = 4 * w + q;
  unsigned x[16];
#pragma unroll
  for (int jq = 0; jq < 4; ++jq) {
    const u32x4 t = *(const LAS u32x4*)(L + G_X + row * 1024 + (((((jq + j) & 3) * 14 + c16) ^ w) * 16));
    x[4 * jq] = t.x; x[4 * jq + 1] = t.y; x[4 * jq + 2] = t.z; x[4 * jq + 3] = t.w;
  }
  float r, mr;
  ln_stats(x, lmsk, r, mr);
  f32x2 h[16];
#pragma unroll
  for (int jq = 0; jq < 4; ++jq) {
    const LAS unsigned char* mp = L + G_MOD + (((jq + j) & 3) * 112 + c16 * 8) * 4;
    const f32x4 sb0 = *(const LAS f32x4*)mp, sb1 = *(const LAS f32x4*)(mp + 16);
    const f32x4 c10 = *(const LAS f32x4*)(mp + 1792), c11 = *(const LAS f32x4*)(mp + 1792 + 16);
#pragma unroll
    for (int k = 0; k < 4; ++k) {
      const f32x2 xv2 = {bf_lo(x[4 * jq + k]), bf_hi(x[4 * jq + k])};
      const f32x2 c2 = k < 2 ? f32x2{c10[2 * k], c10[2 * k + 1]} : f32x2{c11[2 * k - 4], c11[2 * k - 3]};
      const f32x2 sb2 = k < 2 ? f32x2{sb0[2 * k], sb0[2 * k + 1]} : f32x2{sb1[2 * k - 4], sb1[2 * k - 3]};
      const f32x2 n = xv2 * r + mr;
      h[4 * jq + k] = n * c2 + sb2;
      x[4 * jq + k] = pk_bf16(h[4 * jq + k].x, h[4 * jq + k].y);
    }
  }
  *(LAS u32x4*)(L + G_HQ + row * 240 + (r16 < 14 ? r16 : 14) * 16) = u32x4{x[0], x[1], x[2], x[3]};
  ln_stats(x, lmsk, r, mr);
#pragma unroll
  for (int jq = 0; jq < 4; ++jq) {
    u32x4 o;
#pragma unroll
    for (int k = 0; k < 4; ++k) {
      const f32x2 n = h[4 * jq + k] * r + mr;
      o[k] = pk_bf16(n.x, n.y);
    }
    *(LAS u32x4*)(L + G_T0 + row * AS + (r16 < 14 ? (((jq + j) & 3) * 14 + r16) * 16 : 896)) = o;
  }
}
__device__ __forceinline__ void x1_offsets(int pn, int j, int w, int r16, int q, int (&gof)[3]) {
  const int c16 = r16 < 14 ? r16 : 0;
  const int go = (int)WS_X1 + pn * 4 * 7168 + ((c16 >> 1) * 64 + w * 8 + (c16 & 1) * 4 + q) * 16;
#pragma unroll
  for (int i = 0; i < 3; ++i) gof[i] = go + ((j + 1 + i) & 3) * 7168;
}
__device__ __forceinline__ void ln_phase_c(LAS unsigned char* L, const u32x4 (&xr)[3], int j, int w, int r16, int q) {
  const int c16 = r16 < 14 ? r16 : 0;
  const float lmsk = r16 < 14 ? 1.0f : 0.0f;
  const int row = 4 * w + q;
  const u32x4 own = *(const LAS u32x4*)(L + G_HQ + row * 240 + c16 * 16);
  unsigned x[16];
#pragma unroll
  for (int k = 0; k < 4; ++k) x[k] = own[k];
#pragma unroll
  for (int k = 4; k < 16; ++k) x[k] = xr[(k >> 2) - 1][k & 3];
  float r, mr;
  ln_stats(x, lmsk, r, mr);
#pragma unroll
  for (int jq = 0; jq < 4; ++jq) {
    u32x4 o;
#pragma unroll
    for (int k = 0; k < 4; ++k) {
      const f32x2 n = f32x2{bf_lo(x[4 * jq + k]), bf_hi(x[4 * jq + k])} * r + mr;
      o[k] = pk_bf16(n.x, n.y);
    }
    *(LAS u32x4*)(L + G_T0 + row * AS + (r16 < 14 ? (((jq + j) & 3) * 14 + r16) * 16 : 896)) = o;
  }
}
__device__ __forceinline__ u32x2 gelu4(f32x4 a) {
  const unsigned u = pk_bf16(a[0], a[1]), v = pk_bf16(a[2], a[3]);
  u32x2 o;
  o.x = pk_bf16(gelu_tanh(bf_lo(u)), gelu_tanh(bf_hi(u)));
  o.y = pk_bf16(gelu_tanh(bf_lo(v)), gelu_tanh(bf_hi(v)));
  return o;
}

__global__ __launch_bounds__(512, 2) void k_main(GArgs p) {
  extern __shared__ __attribute__((aligned(16))) unsigned char smem[];
  LAS unsigned char* L = (LAS unsigned char*)smem;
  const int tid = threadIdx.x, lane = tid & 63;
  const int w = __builtin_amdgcn_readfirstlane(tid >> 6);
  const int bid = blockIdx.x;
  const int j = bid & 3, pn = (bid >> 3) * 2 + ((bid >> 2) & 1);
  const int r16 = lane & 15, q = lane >> 4;
  const bool cw = w < 7;
  const int wc = cw ? w : 6;
  const bool convwg = bid < 224;
  const int dp = convwg ? bid : bid - 32;
  const __amdgpu_buffer_rsrc_t rs = __builtin_amdgcn_make_buffer_rsrc((void*)p.ws, 0, (int)WS_END, 0x00020000);
  const __amdgpu_buffer_rsrc_t rcv = __builtin_amdgcn_make_buffer_rsrc((void*)p.ws, 0, convwg ? (int)WS_END : 0, 0x00020000);
  const __amdgpu_buffer_rsrc_t rst = __builtin_amdgcn_make_buffer_rsrc((void*)p.ws, 0, cw ? (int)WS_END : 0, 0x00020000);
  const __amdgpu_buffer_rsrc_t rw =
      __builtin_amdgcn_make_buffer_rsrc((void*)(p.ws + WS_WP + (size_t)(j * 7 + wc) * NFRAG * 1024), 0, cw ? NFRAG * 1024 : 0, 0x00020000);
  const unsigned short* mod = (const unsigned short*)(p.ws + WS_MOD);
  const int voff = lane * 16;
  const int pslot = (wc * 64 + lane) * 16;
  STAMP(0, 0);
  const int kc = tid >> 8, kt = tid & 255;
  const unsigned char* krp = p.ws + WS_KR + (size_t)(2 * dp + kc) * KR_STRIDE;
  const u32x4 k0 = *(const u32x4*)(krp + kt * 16), k1 = *(const u32x4*)(krp + kt * 16 + 16);
  const int tb = tid >> 3, pc = tid & 7, xc = pc >> 2, oct = pc & 3;
  const int t8 = tb * 32 + oct * 8;
  const u32x4 xv = *(const u32x4*)(p.ws + WS_XB + ((size_t)tb * D_ + 2 * dp) * 64 + pc * 16);
  const u32x4 s0 = *(const u32x4*)(p.ws + WS_STAT + (size_t)t8 * 4), s1 = *(const u32x4*)(p.ws + WS_STAT + (size_t)t8 * 4 + 16);
  const int dch = 2 * dp + xc;
  const float sa = bf2f(mod[dch]), ca1 = bf2f(mod[448 + dch]), ga = bf2f(mod[896 + dch]);
  const float dsk = bf2f(p.dskip[dch]);
  const unsigned mtab = *(const unsigned*)(mod + 3 * 448 + (tid < 448 ? tid : 0) * 2);
  const int ncol = j * 112 + wc * 16 + q * 4;
  const u32x2 gbp = *(const u32x2*)(mod + 5 * 448 + ncol);
  SB0();
  __builtin_amdgcn_s_barrier();
  u32x4 ring[RING];
  ring_fill<0, RINGA>(ring, rw, voff);
  const unsigned OFFS = 0x087EDA52u;
  {
    const int c = tid >> 8, i = tid & 255;
    int off = CV_KCOPY + i * 16;
    if (i >= 160) { const int k = i - 160, s = k >> 3, ch = (k & 7) == 7 ? 6 : (k & 7); off = (272 * (s & 7) + ((OFFS >> (4 * (s & 7))) & 15) + 257 + ch) * 16; }
    *(LAS u32x4*)(L + c * CV_CH + off) = u32x4{0u, 0u, 0u, 0u};
  }
  {
    LAS unsigned char* base = L + kc * CV_CH;
    unsigned wv[8] = {k0.x, k0.y, k0.z, k0.w, k1.x, k1.y, k1.z, k1.w};
    unsigned vv[7];
#pragma unroll
    for (int i = 0; i < 7; ++i) vv[i] = __builtin_amdgcn_alignbit(wv[i + 1], wv[i], 16);
#pragma unroll
    for (int s = 0; s < 8; ++s) {
      const int k = s >> 1;
      u32x4 o = (s & 1) ? u32x4{vv[k], vv[k + 1], vv[k + 2], vv[k + 3]} : u32x4{wv[k], wv[k + 1], wv[k + 2], wv[k + 3]};
      *(LAS u32x4*)(base + (272 * s + ((OFFS >> (4 * s)) & 15) + kt) * 16) = o;
    }
    if (kt == 255) {
#pragma unroll
      for (int s = 0; s < 8; ++s)
        *(LAS u32x4*)(base + (272 * s + ((OFFS >> (4 * s)) & 15) + 256) * 16) = u32x4{s == 0 ? k1.x : 0u, 0u, 0u, 0u};
    }
  }
  u32x4 hv;
  {
    unsigned sv[8] = {s0.x, s0.y, s0.z, s0.w, s1.x, s1.y, s1.z, s1.w};
#pragma unroll
    for (int k = 0; k < 4; ++k) {
      unsigned t = pk_bf16(bf_lo(xv[k]) - bf_lo(sv[2 * k]), bf_hi(xv[k]) - bf_lo(sv[2 * k + 1]));
      t = pk_bf16(bf_lo(t) * __builtin_amdgcn_rcpf(bf_hi(sv[2 * k])), bf_hi(t) * __builtin_amdgcn_rcpf(bf_hi(sv[2 * k + 1])));
      t = pk_bf16(bf_lo(t) * ca1, bf_hi(t) * ca1);
      hv[k] = pk_bf16(bf_lo(t) + sa, bf_hi(t) + sa);
    }
    *(LAS u32x4*)(L + xc * CV_CH + CV_KCOPY + (16 + (t8 >> 6)) * CV_UROW + (t8 & 63) * 2) = hv;
    if (tid < 448) *(LAS f32x2*)(L + G_MOD + tid * 8) = f32x2{bf_lo(mtab), bf_hi(mtab)};
  }
  STAMP(0, 21);
  bar_lds();
  __builtin_amdgcn_raw_buffer_store_b128(u32x4{~0u, ~0u, ~0u, ~0u}, rst, (int)WS_X1 + (pn * 4 + j) * 7168 + pslot, 0, AUX_SC1);
#pragma unroll
  for (int i = 0; i < 3; ++i)
    __builtin_amdgcn_raw_buffer_store_b128(u32x4{~0u, ~0u, ~0u, ~0u}, rst, (int)WS_P + ((pn * 4 + j) * 3 + i) * 7168 + pslot, 0, AUX_SC1);
  SB0();
  STAMP(0, 22);
  {
    const int ch = w >> 2, wq = w & 3;
    f32x4 acc[2][4];
#pragma unroll
    for (int n = 0; n < 2; ++n)
#pragma unroll
      for (int m = 0; m < 4; ++m) acc[n][m] = f32x4{0.f, 0.f, 0.f, 0.f};
    const int s = (8 - r16) & 7, u = (r16 + 7) >> 3;
    const LAS unsigned char* ka = L + ch * CV_CH + (272 * s + ((OFFS >> (4 * s)) & 15) + 256 + q - u - 2 * (16 * wq) - 2 * 79) * 16;
    const LAS unsigned char* ub = L + ch * CV_CH + CV_KCOPY + (16 + r16 - 4 * wq - 3) * CV_UROW + q * 16;
#define KFRAG(f) (*(const LAS u32x4*)(ka + (79 - (f)) * 32))
    u32x4 alo[6], ahi[6];
    alo[0] = KFRAG(-2); alo[1] = KFRAG(-1); ahi[0] = KFRAG(62); ahi[1] = KFRAG(63);
#pragma unroll
    for (int dl = 0; dl < 4; ++dl) {
#pragma unroll
      for (int i = 0; i < 4; ++i) { alo[2 + i] = KFRAG(4 * dl + i); ahi[2 + i] = KFRAG(4 * dl + i + 64); }
#pragma unroll
      for (int ks = 0; ks < 2; ++ks) {
        const u32x4 b0 = *(const LAS u32x4*)(ub + (3 - dl) * CV_UROW + ks * 64);
        const u32x4 b1 = *(const LAS u32x4*)(ub + (19 - dl) * CV_UROW + ks * 64);
#pragma unroll
        for (int m = 0; m < 4; ++m) {
          acc[0][m] = mfma16(alo[m - 2 * ks + 2], b0, acc[0][m]);
          acc[1][m] = mfma16(alo[m - 2 * ks + 2], b1, acc[1][m]);
          acc[1][m] = mfma16(ahi[m - 2 * ks + 2], b0, acc[1][m]);
          const int f = RINGA + (dl * 2 + ks) * 2 + (m >> 1);
          if ((m & 1) == 0 && f < RINGB) ring[f] = WLOAD(f);
          __builtin_amdgcn_sched_barrier(0x106);
        }
      }
      alo[0] = alo[4]; alo[1] = alo[5]; ahi[0] = ahi[4]; ahi[1] = ahi[5];
    }
#undef KFRAG
    STAMP(0, 23);
    LAS unsigned char* P = L + CV_P + w * (32 * CV_PROW);
#pragma unroll
    for (int n = 0; n < 2; ++n)
#pragma unroll
      for (int m = 0; m < 4; ++m) *(LAS f32x4*)(P + (16 * n + r16) * CV_PROW + (16 * m + 4 * q) * 4) = acc[n][m];
    SB0();
    ring_fill<RINGB, RING0 - RINGB>(ring, rw, voff);
  }
  bar_lds();
  STAMP(0, 24);
  {
    const LAS unsigned char* P = L + CV_P + xc * (4 * 32 * CV_PROW) + (t8 >> 6) * CV_PROW + (t8 & 63) * 4;
    f32x4 y0 = {0.f, 0.f, 0.f, 0.f}, y1 = y0;
#pragma unroll
    for (int k = 0; k < 4; ++k) {
      y0 += *(const LAS f32x4*)(P + k * (32 * CV_PROW));
      y1 += *(const LAS f32x4*)(P + k * (32 * CV_PROW) + 16);
    }
    unsigned e[4];
#pragma unroll
    for (int k = 0; k < 4; ++k) {
      const float ya = k < 2 ? y0[2 * k] : y1[2 * k - 4], yb = k < 2 ? y0[2 * k + 1] : y1[2 * k - 3];
      const unsigned yy = pk_bf16(ya, yb), hd = pk_bf16(bf_lo(hv[k]) * dsk, bf_hi(hv[k]) * dsk);
      unsigned t = pk_bf16(bf_lo(yy) + bf_lo(hd), bf_hi(yy) + bf_hi(hd));
      t = pk_bf16(ga * bf_lo(t), ga * bf_hi(t));
      e[k] = pk_bf16(bf_lo(xv[k]) + bf_lo(t), bf_hi(xv[k]) + bf_hi(t));
    }
    const unsigned sx = xc ? e[0] : e[2], sy = xc ? e[1] : e[3];
    const unsigned rx = __builtin_amdgcn_ds_swizzle(sx, 0x101F), ry = __builtin_amdgcn_ds_swizzle(sy, 0x101F);
    const unsigned c0x = xc ? rx : e[0], c0y = xc ? ry : e[1];
    const unsigned c1x = xc ? e[2] : rx, c1y = xc ? e[3] : ry;
    u32x4 out;
    out.x = sane(__builtin_amdgcn_perm(c1x, c0x, 0x05040100u));
    out.y = sane(__builtin_amdgcn_perm(c1x, c0x, 0x07060302u));
    out.z = sane(__builtin_amdgcn_perm(c1y, c0y, 0x05040100u));
    out.w = sane(__builtin_amdgcn_perm(c1y, c0y, 0x07060302u));
    const int tq = oct * 2 + xc;
    __builtin_amdgcn_raw_buffer_store_b128(out, rcv, (int)WS_X2B + tb * (int)X2B_BLK + dp * 128 + tq * 16, 0, AUX_SC1);
  }
  STAMP(0, 25);
  if (!cw) {
    gather_x2(p, L, rs, pn, tid);
    STAMP(0, 1);
    bar_lds();
    ln_phase_a(L, j, w, r16, q);
    STAMP(0, 2);
    bar_lds();
    {
      const unsigned char* src = p.ws + WS_WP + (size_t)(j * 7) * NFRAG * 1024 + NRING * 1024 + voff;
#pragma unroll
      for (int ww = 0; ww < 7; ++ww)
#pragma unroll
        for (int i = 0; i < NLDS; ++i)
          __builtin_amdgcn_global_load_lds((const GAS unsigned*)(src + (size_t)ww * NFRAG * 1024 + i * 1024), (LAS unsigned*)(L + G_WL + (ww * NLDS + i) * 1024), 16, 0, 0);
    }
    bar_lds();
    STAMP(0, 4);
    bar_lds();
    bar_lds();
    {
      int gof[3];
      u32x4 xr[3];
      x1_offsets(pn, j, w, r16, q, gof);
      poll_load<3>(rs, gof, xr);
      ln_phase_c(L, xr, j, w, r16, q);
    }
    asm volatile("s_waitcnt vmcnt(0)" ::: "memory");
    STAMP(0, 7);
    bar_lds();
    bar_lds();
    return;
  }
  SB0();
  gather_x2(p, L, rs, pn, tid);
  SB0();
  ring_fill<RING0, 8>(ring, rw, voff);
  STAMP(0, 1);
  bar_lds();
  u32x2 h2r[2], x2r[2], x1r[2];
#pragma unroll
  for (int T = 0; T < 2; ++T) {
    const int row = 16 * T + r16;
    x2r[T] = *(const LAS u32x2*)(L + G_X + row * 1024 + (((ncol >> 3) ^ (row >> 2)) * 16) + (ncol & 4) * 2);
  }
  ln_phase_a(L, j, w, r16, q);
  SB0();
  ring_fill<RING0 + 8, RING - RING0 - 8>(ring, rw, voff);
  STAMP(0, 2);
  bar_lds();
#pragma unroll
  for (int T = 0; T < 2; ++T) h2r[T] = *(const LAS u32x2*)(L + G_HQ + (16 * T + r16) * 240 + (w * 16 + q * 4) * 2);
  const LAS unsigned char* ap0 = L + G_T0 + r16 * AS + q * 16;
  const LAS unsigned char* ap1 = L + G_T1 + r16 * AS + q * 16;
  const int tok = (q & 1) * 16 + r16, octc = 2 * w + (q >> 1);
  {
    f32x4 acc[1][2];
    STAMP(0, 16);
    gemm14<1, 0, 0, 0, 1, 5>(ap0, ring, rw, voff, acc);
    STAMP(0, 17);
    const u32x4 own = make_piece(gelu4(acc[0][0]), gelu4(acc[0][1]));
    __builtin_amdgcn_raw_buffer_store_b128(own, rs, (int)WS_G + (pn * 4 + j) * 7168 + pslot, 0, AUX_SC1);
    SB0();
    STAMP(0, 18);
    ring_fill<RING, 7>(ring, rw, voff);
    int goff[4], loff[4];
    bool wrap0;
#pragma unroll
    for (int i = 0; i < 4; ++i) {
      const int jq = (j + i) & 3;
      const int sh = (jq & 1) ? -1 - (jq >> 1) : 1 + (jq >> 1);
      const int it = tok + sh;
      const int sp = pn + (it >= 32 ? -1 : (it < 0 ? 1 : 0));
      if (i == 0) wrap0 = sp != pn;
      goff[i] = (sp < 0 || sp > 63 || (i == 0 && sp == pn)) ? (int)WS_END : (int)WS_G + (sp * 4 + jq) * 7168 + pslot;
      loff[i] = G_T1 + (it & 31) * AS + (jq * 14 + octc) * 16;
    }
    STAMP(0, 3);
    bar_lds();
    u32x4 v[4];
    poll_issue<4>(rs, goff, v);
    ring_fill<RING + 7, 7>(ring, rw, voff);
    poll_finish<4>(rs, goff, v);
    *(LAS u32x4*)(L + loff[0]) = wrap0 ? v[0] : own;
#pragma unroll
    for (int i = 1; i < 4; ++i) *(LAS u32x4*)(L + loff[i]) = v[i];
  }
  STAMP(0, 4);
  bar_lds();
  {
    f32x4 acc[1][2];
    gemm14<1, 14, 0, 0, 1, 5>(ap1, ring, rw, voff, acc);
#pragma unroll
    for (int T = 0; T < 2; ++T) {
      const unsigned a = pk_bf16(acc[0][T][0], acc[0][T][1]), b = pk_bf16(acc[0][T][2], acc[0][T][3]);
      x1r[T].x = pk_bf16(bf_lo(h2r[T].x) + bf_lo(a), bf_hi(h2r[T].x) + bf_hi(a));
      x1r[T].y = pk_bf16(bf_lo(h2r[T].y) + bf_lo(b), bf_hi(h2r[T].y) + bf_hi(b));
    }
    const u32x4 own = make_piece(x1r[0], x1r[1]);
    __builtin_amdgcn_raw_buffer_store_b128(own, rs, (int)WS_X1 + (pn * 4 + j) * 7168 + (w * 64 + (tok >> 2) * 8 + (q >> 1) * 4 + (tok & 3)) * 16, 0, AUX_SC1);
    *(LAS u32x4*)(L + G_HQ + tok * 240 + octc * 16) = own;
    SB0();
    STAMP(0, 19);
    ring_fill<RING + 14, 7>(ring, rw, voff);
    int gof[3];
    u32x4 xr[3];
    x1_offsets(pn, j, w, r16, q, gof);
    STAMP(0, 5);
    bar_lds();
    poll_issue<3>(rs, gof, xr);
    ring_fill<RING + 21, 7>(ring, rw, voff);
    poll_finish<3>(rs, gof, xr);
    STAMP(0, 6);
    ln_phase_c(L, xr, j, w, r16, q);
  }
  STAMP(0, 7);
  bar_lds();
  {
    f32x4 acc[4][2];
    STAMP(0, 12);
    gemm14<4, 28, RING + 28, 28, 2, 2>(ap0, ring, rw, voff, acc);
    STAMP(0, 13);
#pragma unroll
    for (int i = 0; i < 4; ++i)
#pragma unroll
      for (int T = 0; T < 2; ++T) {
        const int g = RING + 56 + (i * 2 + T) * 2;
        ring[g % RING] = WLOAD(g);
        ring[(g + 1) % RING] = WLOAD(g + 1);
        SB0();
        *(LAS u32x2*)(L + G_T1 + (16 * T + r16) * AS + (w * 64 + i * 16 + q * 4) * 2) = gelu4(acc[i][T]);
        SB0();
      }
  }
  STAMP(0, 8);
  bar_lds();
  {
    {
      f32x4 acc[3][2];
      STAMP(0, 14);
      gemm14<3, 84, RING + 72, NRING - RING - 72, 1, 3>(ap1, ring, rw, voff, acc);
      STAMP(0, 15);
#pragma unroll
      for (int i = 0; i < 3; ++i) {
        const int jd = (j + 1 + i) & 3;
        const u32x4 o = {sane(pk_bf16(acc[i][0][0], acc[i][0][1])), sane(pk_bf16(acc[i][0][2], acc[i][0][3])),
                         sane(pk_bf16(acc[i][1][0], acc[i][1][1])), sane(pk_bf16(acc[i][1][2], acc[i][1][3]))};
        __builtin_amdgcn_raw_buffer_store_b128(o, rs, (int)WS_P + ((pn * 4 + jd) * 3 + (2 - i)) * 7168 + pslot, 0, AUX_SC1);
      }
      SB0();
    }
    STAMP(0, 9);
    f32x4 acc0 = {0.f, 0.f, 0.f, 0.f}, acc1 = acc0;
    {
      const LAS unsigned char* wl = L + G_WL + w * (NLDS * 1024) + voff;
#pragma unroll
      for (int ks = 0; ks < 14; ++ks) {
        const int f = 126 + ks;
        const u32x4 a = f < NRING ? ring[f % RING] : *(const LAS u32x4*)(wl + (f - NRING) * 1024);
        acc0 = mfma16(a, *(const LAS u32x4*)(ap1 + ks * 64), acc0);
        acc1 = mfma16(a, *(const LAS u32x4*)(ap1 + 16 * AS + ks * 64), acc1);
      }
    }
    STAMP(0, 10);
    u32x4 v[3];
    const int go = (int)WS_P + (pn * 4 + j) * 3 * 7168 + pslot;
    const int gof[3] = {go, go + 7168, go + 2 * 7168};
    poll_load<3>(rs, gof, v);
    STAMP(0, 11);
#pragma unroll
    for (int T = 0; T < 2; ++T) {
      const f32x4 ac = T ? acc1 : acc0;
      float hh[4];
#pragma unroll
      for (int k = 0; k < 2; ++k) {
        hh[2 * k] = ac[2 * k] + ((bf_lo(v[0][2 * T + k]) + bf_lo(v[1][2 * T + k])) + bf_lo(v[2][2 * T + k]));
        hh[2 * k + 1] = ac[2 * k + 1] + ((bf_hi(v[0][2 * T + k]) + bf_hi(v[1][2 * T + k])) + bf_hi(v[2][2 * T + k]));
      }
      unsigned a = pk_bf16(hh[0], hh[1]), b = pk_bf16(hh[2], hh[3]);
      a = pk_bf16(bf_lo(x1r[T].x) + bf_lo(a), bf_hi(x1r[T].x) + bf_hi(a));
      b = pk_bf16(bf_lo(x1r[T].y) + bf_lo(b), bf_hi(x1r[T].y) + bf_hi(b));
      a = pk_bf16(bf_lo(gbp.x) * bf_lo(a), bf_hi(gbp.x) * bf_hi(a));
      b = pk_bf16(bf_lo(gbp.y) * bf_lo(b), bf_hi(gbp.y) * bf_hi(b));
      u32x2 o;
      o.x = pk_bf16(bf_lo(x2r[T].x) + bf_lo(a), bf_hi(x2r[T].x) + bf_hi(a));
      o.y = pk_bf16(bf_lo(x2r[T].y) + bf_lo(b), bf_hi(x2r[T].y) + bf_hi(b));
      *(u32x2*)(p.out + (size_t)(pn * 32 + 16 * T + r16) * D_ + ncol) = o;
    }
  }
  STAMP(0, 20);
}

}

extern "C" void kernel_launch(void* const* d_in, const int* in_sizes, int n_in, void* d_out, int out_size, void* d_ws,
                              size_t ws_size, hipStream_t stream) {
  (void)in_sizes; (void)n_in; (void)out_size; (void)ws_size;
  PrepArgs pa;
  pa.x = (const unsigned short*)d_in[0];
  pa.a_re = (const unsigned short*)d_in[1];
  pa.a_im = (const unsigned short*)d_in[2];
  pa.b_re = (const unsigned short*)d_in[3];
  pa.b_im = (const unsigned short*)d_in[4];
  pa.c_re = (const unsigned short*)d_in[5];
  pa.c_im = (const unsigned short*)d_in[6];
  pa.log_dt = (const unsigned short*)d_in[7];
  pa.w1 = (const unsigned short*)d_in[10];
  pa.w2 = (const unsigned short*)d_in[11];
  pa.w3 = (const unsigned short*)d_in[12];
  pa.w4 = (const unsigned short*)d_in[13];
  pa.cvec = (const unsigned short*)d_in[14];
  pa.wmod = (const unsigned short*)d_in[15];
  pa.ws = (unsigned char*)d_ws;
  k_prep<<<NB_PREP, 256, 0, stream>>>(pa);
  GArgs ga;
  ga.ws = (unsigned char*)d_ws;
  ga.out = (unsigned short*)d_out;
  ga.dskip = (const unsigned short*)d_in[8];
  (void)hipFuncSetAttribute((const void*)k_main, hipFuncAttributeMaxDynamicSharedMemorySize, G_LDS);
  k_main<<<256, 512, G_LDS, stream>>>(ga);
}
```

```cpp
#include <hip/hip_runtime.h>
#include <hip/hip_bf16.h>
#include <stdint.h>

namespace {

typedef __attribute__((ext_vector_type(8))) short bf16x8;
typedef __attribute__((ext_vector_type(8))) _Float16 f16x8;
typedef __attribute__((ext_vector_type(2))) _Float16 f16x2;
typedef __attribute__((ext_vector_type(4))) float f32x4;
typedef __attribute__((ext_vector_type(2))) float f32x2;
typedef __attribute__((ext_vector_type(4))) unsigned u32x4;
typedef __attribute__((ext_vector_type(2))) unsigned u32x2;
typedef __attribute__((ext_vector_type(2))) __bf16 bf16x2v;
#define GAS __attribute__((address_space(1)))
#define LAS __attribute__((address_space(3)))

constexpr int L_ = 2048, D_ = 448, NS_ = 256;

constexpr size_t WS_MOD = 0;
constexpr size_t WS_STAT = WS_MOD + 16384;
constexpr size_t WS_XB = WS_STAT + 16384;
constexpr size_t KR_STRIDE = 4224;
constexpr size_t WS_KR = WS_XB + (size_t)64 * 448 * 64;
constexpr int NFRAG = 140;
constexpr size_t WS_WP = WS_KR + (size_t)448 * KR_STRIDE + 1024;
constexpr size_t WS_X2B = WS_WP + (size_t)28 * NFRAG * 1024;
constexpr size_t X2B_BLK = 224 * 128;
constexpr size_t WS_G = WS_X2B + 64 * X2B_BLK;
constexpr size_t WS_X1 = WS_G + (size_t)64 * 4 * 7168;
constexpr size_t WS_P = WS_X1 + (size_t)64 * 4 * 7168;
constexpr size_t WS_END = WS_P + (size_t)64 * 12 * 7168;

#define STAMP(base, k) do { } while (0)

__device__ __forceinline__ float bf_lo(unsigned u) { return __builtin_bit_cast(float, u << 16); }
__device__ __forceinline__ float bf_hi(unsigned u) { return __builtin_bit_cast(float, u & 0xffff0000u); }
__device__ __forceinline__ float bf2f(unsigned short h) { return __builtin_bit_cast(float, (unsigned)h << 16); }
__device__ __forceinline__ unsigned pk_bf16(float a, float b) {
  f32x2 v = {a, b};
  bf16x2v r = __builtin_convertvector(v, bf16x2v);
  return __builtin_bit_cast(unsigned, r);
}
__device__ __forceinline__ unsigned pk_f16(f32x2 v) {
  f16x2 r = __builtin_convertvector(v, f16x2);
  return __builtin_bit_cast(unsigned, r);
}
__device__ __forceinline__ float rbf(float a) { return bf_lo(pk_bf16(a, a)); }
__device__ __forceinline__ f32x4 mfma16(u32x4 a, u32x4 b, f32x4 c) {
  return __builtin_amdgcn_mfma_f32_16x16x32_bf16(__builtin_bit_cast(bf16x8, a), __builtin_bit_cast(bf16x8, b), c, 0, 0, 0);
}
__device__ __forceinline__ f32x4 mfma16h(u32x4 a, u32x4 b, f32x4 c) {
  return __builtin_amdgcn_mfma_f32_16x16x32_f16(__builtin_bit_cast(f16x8, a), __builtin_bit_cast(f16x8, b), c, 0, 0, 0);
}
template <int CTRL>
__device__ __forceinline__ float dpp_f(float v) {
  return __builtin_bit_cast(float, __builtin_amdgcn_update_dpp(0, __builtin_bit_cast(int, v), CTRL, 0xf, 0xf, true));
}
__device__ __forceinline__ float row16_sum(float v) {
  v += dpp_f<0xB1>(v);
  v += dpp_f<0x4E>(v);
  v += dpp_f<0x141>(v);
  v += dpp_f<0x140>(v);
  return v;
}
__device__ __forceinline__ float row16_max(float v) {
  v = fmaxf(v, dpp_f<0xB1>(v));
  v = fmaxf(v, dpp_f<0x4E>(v));
  v = fmaxf(v, dpp_f<0x141>(v));
  v = fmaxf(v, dpp_f<0x140>(v));
  return v;
}
__device__ __forceinline__ void swap16(float v, float& a, float& b) {
  const unsigned u = __builtin_bit_cast(unsigned, v), u2 = u;
  auto r = __builtin_amdgcn_permlane16_swap(u, u2, false, false);
  const unsigned r0 = r[0], r1 = r[1];
  a = __builtin_bit_cast(float, r0); b = __builtin_bit_cast(float, r1);
}
__device__ __forceinline__ void swap32(float v, float& a, float& b) {
  const unsigned u = __builtin_bit_cast(unsigned, v), u2 = u;
  auto r = __builtin_amdgcn_permlane32_swap(u, u2, false, false);
  const unsigned r0 = r[0], r1 = r[1];
  a = __builtin_bit_cast(float, r0); b = __builtin_bit_cast(float, r1);
}
__device__ __forceinline__ float rows_sum(float v) {
  float a, b;
  swap16(v, a, b); v = a + b;
  swap32(v, a, b);
  return a + b;
}
__device__ __forceinline__ float rows_max(float v) {
  float a, b;
  swap16(v, a, b); v = fmaxf(a, b);
  swap32(v, a, b);
  return fmaxf(a, b);
}
__device__ __forceinline__ float wave_sum(float v) { return rows_sum(row16_sum(v)); }
__device__ __forceinline__ float gelu_tanh(float x) {
  const float c1 = -2.0f * 0.7978845608028654f * 1.4426950408889634f;
  const float c2 = c1 * 0.044715f;
  float t = x * (c1 + c2 * x * x);
  float e = __builtin_amdgcn_exp2f(t);
  return x * __builtin_amdgcn_rcpf(1.0f + e);
}
__device__ __forceinline__ float ln_sd(float var) { return rbf(__builtin_amdgcn_sqrtf(rbf(rbf(var) + 1e-5f))); }
__device__ __forceinline__ void lds_fence() { asm volatile("" ::: "memory"); }
__device__ __forceinline__ void bar_lds() {
  asm volatile("s_waitcnt lgkmcnt(0)" ::: "memory");
  __builtin_amdgcn_s_barrier();
  asm volatile("" ::: "memory");
}

__device__ __forceinline__ void transpose8x8(const u32x4 (&r)[8], u32x4 (&o)[8]) {
#pragma unroll
  for (int c2 = 0; c2 < 4; ++c2) {
#pragma unroll
    for (int rp = 0; rp < 4; ++rp) {
      unsigned a = r[2 * rp][c2], b = r[2 * rp + 1][c2];
      o[2 * c2][rp] = __builtin_amdgcn_perm(b, a, 0x05040100u);
      o[2 * c2 + 1][rp] = __builtin_amdgcn_perm(b, a, 0x07060302u);
    }
  }
}
__device__ __forceinline__ f32x2 cmul(f32x2 a, f32x2 b) {
  f32x2 r, t;
  asm("v_pk_mul_f32 %0, %1, %2 op_sel_hi:[0,1]" : "=v"(t) : "v"(a), "v"(b));
  asm("v_pk_fma_f32 %0, %1, %2, %3 op_sel:[1,1,0] op_sel_hi:[1,0,1] neg_lo:[1,0,0]" : "=v"(r) : "v"(a), "v"(b), "v"(t));
  return r;
}
__device__ __forceinline__ f32x2 cexp_tab(float a, float b, float l) {
  const float e = __builtin_amdgcn_exp2f(a * l), ph = __builtin_amdgcn_fractf(b * l);
  f32x2 r;
  r.x = e * __builtin_amdgcn_cosf(ph);
  r.y = e * __builtin_amdgcn_sinf(ph);
  return r;
}

struct PrepArgs {
  const unsigned short* x;
  const unsigned short* a_re;
  const unsigned short* a_im;
  const unsigned short* b_re;
  const unsigned short* b_im;
  const unsigned short* c_re;
  const unsigned short* c_im;
  const unsigned short* log_dt;
  const unsigned short* w1;
  const unsigned short* w2;
  const unsigned short* w3;
  const unsigned short* w4;
  const unsigned short* cvec;
  const unsigned short* wmod;
  unsigned char* ws;
};

constexpr int NB_LN = 64, NB_MOD = 84, NB_PACK = 140, NB_KG = 448;
constexpr int NB_PREP = NB_LN + NB_MOD + NB_PACK + NB_KG;
constexpr int KG_ROW = 288;
constexpr int KG_BUF = 16 * KG_ROW;
constexpr int PREP_LDS = 4 * 2 * KG_BUF;

#define K1_AUX 16
#define ST16(off, v) __builtin_amdgcn_raw_buffer_store_b128(v, rs, (int)(off), 0, K1_AUX)
__global__ __launch_bounds__(256) void k_prep(PrepArgs p) {
  __shared__ __attribute__((aligned(16))) unsigned char smem[PREP_LDS];
  const __amdgpu_buffer_rsrc_t rs = __builtin_amdgcn_make_buffer_rsrc((void*)p.ws, 0, (int)WS_END, 0x00020000);
  LAS unsigned char* L = (LAS unsigned char*)smem;
  const int tid = threadIdx.x, lane = tid & 63;
  const int w = __builtin_amdgcn_readfirstlane(tid >> 6);
  int b = blockIdx.x >= NB_KG ? blockIdx.x - NB_KG : blockIdx.x + (NB_PREP - NB_KG);
  STAMP(1 << 20, 0);
  if (b < NB_LN) {
    const int t0 = b * 32 + w * 8;
    u32x4 r[8];
    const bool act = lane < 56;
    const int pl = act ? lane : 0;
#pragma unroll
    for (int i = 0; i < 8; ++i) r[i] = *(const u32x4*)(p.x + (size_t)(t0 + i) * D_ + pl * 8);
#pragma unroll
    for (int i = 0; i < 7; ++i) {
      ST16(WS_X2B + (size_t)b * X2B_BLK + (tid + 256 * i) * 16, (u32x4{~0u, ~0u, ~0u, ~0u}));
      ST16(WS_G + (size_t)b * X2B_BLK + (tid + 256 * i) * 16, (u32x4{~0u, ~0u, ~0u, ~0u}));
    }
    const float msk = act ? 1.0f : 0.0f;
    u32x4 o[8];
    transpose8x8(r, o);
#pragma unroll
    for (int i = 0; i < 8; ++i) *(LAS u32x4*)(L + (pl * 8 + i) * 64 + ((w ^ (pl & 3)) * 16) + (act ? 0 : 30000 - i * 64)) = o[i];
    unsigned st = 0;
#pragma unroll
    for (int i = 0; i < 8; ++i) {
      float v[8];
#pragma unroll
      for (int k = 0; k < 4; ++k) { v[2 * k] = bf_lo(r[i][k]); v[2 * k + 1] = bf_hi(r[i][k]); }
      float s = ((v[0] + v[1]) + (v[2] + v[3])) + ((v[4] + v[5]) + (v[6] + v[7]));
      const float mean = wave_sum(s * msk) * (1.0f / D_);
      float q = 0.f;
#pragma unroll
      for (int k = 0; k < 8; ++k) { float d = v[k] - mean; q += d * d; }
      const float sd = ln_sd(wave_sum(q * msk) * (1.0f / D_));
      const unsigned pk = pk_bf16(mean, sd);
      st = (lane == i) ? pk : st;
    }
    if (lane < 8) *(unsigned*)(p.ws + WS_STAT + (size_t)(t0 + lane) * 4) = st;
    bar_lds();
#pragma unroll
    for (int i = 0; i < 7; ++i) {
      const int pce = tid + 256 * i, d = pce >> 2, s = pce & 3;
      const u32x4 v = *(const LAS u32x4*)(L + d * 64 + ((s ^ ((d >> 3) & 3)) * 16));
      ST16(WS_XB + (size_t)b * (D_ * 64) + pce * 16, v);
    }
    STAMP(1 << 20, 1);
    return;
  }
  b -= NB_LN;
  if (b < NB_MOD) {
    const int rg = tid >> 2, pc = tid & 3;
    u32x4 r[7];
    float sc[7];
#pragma unroll
    for (int i = 0; i < 7; ++i) r[i] = *(const u32x4*)(p.wmod + (size_t)(rg + 64 * i) * 2688 + b * 32 + pc * 8);
#pragma unroll
    for (int i = 0; i < 7; ++i) {
      float c = bf2f(p.cvec[rg + 64 * i]);
      sc[i] = rbf(c / (1.0f + expf(-c)));
    }
    float acc[8];
#pragma unroll
    for (int k = 0; k < 8; ++k) acc[k] = 0.f;
#pragma unroll
    for (int i = 0; i < 7; ++i)
#pragma unroll
      for (int k = 0; k < 4; ++k) { acc[2 * k] += sc[i] * bf_lo(r[i][k]); acc[2 * k + 1] += sc[i] * bf_hi(r[i][k]); }
    LAS float* sm = (LAS float*)L;
#pragma unroll
    for (int k = 0; k < 8; ++k) sm[rg * 33 + pc * 8 + k] = acc[k];
    bar_lds();
    {
      const int col = tid & 31, part = tid >> 5;
      float s = 0.f;
#pragma unroll
      for (int k = 0; k < 8; ++k) s += sm[(part * 8 + k) * 33 + col];
      sm[64 * 33 + part * 32 + col] = s;
    }
    bar_lds();
    if (tid < 32) {
      float s = 0.f;
#pragma unroll
      for (int k = 0; k < 8; ++k) s += sm[64 * 33 + k * 32 + tid];
      s = rbf(s);
      const int sec = b / 14;
      if (sec == 1 || sec == 4) s += 1.0f;
      *(unsigned short*)(p.ws + WS_MOD + (size_t)(b * 32 + tid) * 2) = (unsigned short)pk_bf16(s, s);
    }
    STAMP(1 << 20, 2);
    return;
  }
  b -= NB_MOD;
  if (b < NB_PACK) {
    int g = b * 4 + w;
    const unsigned short* W;
    int ldn, mat;
    if (g < 56) { W = p.w1; ldn = 448; mat = 0; }
    else if (g < 112) { W = p.w2; ldn = 448; mat = 1; g -= 56; }
    else if (g < 336) { W = p.w3; ldn = 1792; mat = 2; g -= 112; }
    else { W = p.w4; ldn = 448; mat = 3; g -= 336; }
    const int ncb = ldn / 112;
    const int ks = g / ncb, cb = g - ks * ncb;
    const int jn = lane & 15, q = lane >> 4;
    const bool act = jn < 14;
    const int jl = act ? jn : 0;
    u32x4 r[8], o[8];
#pragma unroll
    for (int i = 0; i < 8; ++i) r[i] = *(const u32x4*)(W + (size_t)(ks * 32 + q * 8 + i) * ldn + cb * 112 + jl * 8);
    transpose8x8(r, o);
    LAS unsigned char* Lw = L + w * 9216;
#pragma unroll
    for (int i = 0; i < 8; ++i) *(LAS u32x4*)(Lw + (jn >> 1) * 1024 + (q * 16 + (jn & 1) * 8 + (i ^ (jn >> 1))) * 16) = o[i];
    lds_fence();
#pragma unroll
    for (int m = 0; m < 7; ++m) {
      const u32x4 v = *(const LAS u32x4*)(Lw + m * 1024 + (lane ^ m) * 16);
      int j, wv, f;
      if (mat == 2) { const int nt = cb * 7 + m; j = nt / 28; const int ntl = nt - j * 28; wv = ntl >> 2; f = 28 + ks * 4 + (ntl & 3); }
      else if (mat == 3) { j = ks / 14; const int ksl = ks - j * 14; wv = m; const int sl = (cb - j - 1) & 3; f = sl < 3 ? 84 + ksl * 3 + sl : 126 + ksl; }
      else { j = cb; wv = m; f = mat * 14 + ks; }
      ST16(WS_WP + ((size_t)(j * 7 + wv) * NFRAG + f) * 1024 + lane * 16, v);
    }
    STAMP(1 << 20, 3);
    return;
  }
  b -= NB_PACK;
  {
    const int d = b;
    const size_t o = (size_t)d * NS_ + tid;
    const float are = bf2f(p.a_re[o]), aim = bf2f(p.a_im[o]);
    const float b_re = bf2f(p.b_re[o]), b_im = bf2f(p.b_im[o]);
    const float cre = bf2f(p.c_re[o]), cim = bf2f(p.c_im[o]);
    const float dt = rbf(expf(bf2f(p.log_dt[d])));
    const float ea = (dt * are) * 1.4426950408889634f, eb = (dt * aim) * 0.15915494309189535f;
    const float bre = dt * b_re, bim = dt * b_im;
    f32x2 wc;
    wc.x = cre * bre - cim * bim;
    wc.y = -(cre * bim + cim * bre);
    const float mx = rows_max(row16_max(fmaxf(fabsf(wc.x), fabsf(wc.y))));
    int ex = mx > 0.f ? __builtin_amdgcn_frexp_expf(mx) : 0;
    ex = ex < -120 ? -120 : (ex > 120 ? 120 : ex);
    const float scale = __builtin_amdgcn_ldexpf(1.0f, ex), iscale = __builtin_amdgcn_ldexpf(1.0f, -ex);
    wc.x *= iscale; wc.y *= iscale;
    STAMP(1 << 20, 4);
    LAS unsigned char* B0 = L + w * (2 * KG_BUF);
    LAS unsigned char* B1 = B0 + KG_BUF;
    const int wo = lane * 4;
    const int ro = (lane & 15) * KG_ROW + (lane >> 4) * 16;
    f32x2 z8c = cexp_tab(ea, eb, 8.0f);
    z8c.y = -z8c.y;
    const f32x2 z1 = cexp_tab(ea, eb, 1.0f), z128 = cexp_tab(ea, eb, 128.0f), z1024 = cexp_tab(ea, eb, 1024.0f);
    {
      const f32x2 z16c = cmul(z8c, z8c);
      f32x2 t0 = wc, t1 = cmul(wc, z8c);
#pragma unroll
      for (int c = 0; c < 16; c += 2) {
        *(LAS unsigned*)(B0 + c * KG_ROW + wo) = pk_f16(t0);
        *(LAS unsigned*)(B0 + (c + 1) * KG_ROW + wo) = pk_f16(t1);
        if (c < 14) { t0 = cmul(t0, z16c); t1 = cmul(t1, z16c); }
      }
    }
    f32x2 ze[8];
    ze[0] = f32x2{1.0f, 0.0f}; ze[1] = z1; ze[2] = cmul(z1, z1); ze[3] = cmul(ze[2], z1); ze[4] = cmul(ze[2], ze[2]);
    ze[5] = cmul(ze[4], z1); ze[6] = cmul(ze[4], ze[2]); ze[7] = cmul(ze[4], ze[3]);
#pragma unroll
    for (int e = 0; e < 8; ++e) {
      *(LAS unsigned*)(B1 + e * KG_ROW + wo) = pk_f16(ze[e]);
      *(LAS unsigned*)(B1 + (8 + e) * KG_ROW + wo) = pk_f16(e == 0 ? z128 : cmul(z128, ze[e]));
    }
    lds_fence();
    u32x4 tf[4];
#pragma unroll
    for (int ks = 0; ks < 4; ++ks) tf[ks] = *(const LAS u32x4*)(B0 + ro + ks * 64);
    u32x4 rf[4];
#pragma unroll
    for (int ks = 0; ks < 4; ++ks) rf[ks] = *(const LAS u32x4*)(B1 + ro + ks * 64);
    lds_fence();
    const f32x2 p2 = cmul(z128, z128), p3 = cmul(p2, z128), p4 = cmul(p2, p2), p5 = cmul(p4, z128), p6 = cmul(p4, p2), p7 = cmul(p4, p3);
    {
#pragma unroll
      for (int e = 0; e < 4; ++e) {
        *(LAS unsigned*)(B0 + e * KG_ROW + wo) = pk_f16(e == 0 ? p2 : cmul(p2, ze[2 * e]));
        *(LAS unsigned*)(B0 + (4 + e) * KG_ROW + wo) = pk_f16(e == 0 ? p3 : cmul(p3, ze[2 * e]));
      }
      *(LAS unsigned*)(B0 + 8 * KG_ROW + wo) = pk_f16(p4);
      *(LAS unsigned*)(B0 + 9 * KG_ROW + wo) = pk_f16(cmul(p4, ze[4]));
      *(LAS unsigned*)(B0 + 10 * KG_ROW + wo) = pk_f16(p5);
      *(LAS unsigned*)(B0 + 11 * KG_ROW + wo) = pk_f16(cmul(p5, ze[4]));
      *(LAS unsigned*)(B0 + 12 * KG_ROW + wo) = pk_f16(p6);
      *(LAS unsigned*)(B0 + 13 * KG_ROW + wo) = pk_f16(cmul(p6, ze[4]));
      *(LAS unsigned*)(B0 + 14 * KG_ROW + wo) = pk_f16(p7);
      *(LAS unsigned*)(B0 + 15 * KG_ROW + wo) = pk_f16(cmul(p7, ze[4]));
    }
    f32x4 acc0 = {0.f, 0.f, 0.f, 0.f}, acc1 = acc0, acc2 = acc0;
#pragma unroll
    for (int ks = 0; ks < 4; ++ks) acc0 = mfma16h(rf[ks], tf[ks], acc0);
    {
      *(LAS unsigned*)(B1 + 0 * KG_ROW + wo) = pk_f16(z1024);
      *(LAS unsigned*)(B1 + 1 * KG_ROW + wo) = pk_f16(cmul(z1024, z128));
      *(LAS unsigned*)(B1 + 2 * KG_ROW + wo) = pk_f16(cmul(z1024, p2));
      *(LAS unsigned*)(B1 + 3 * KG_ROW + wo) = pk_f16(cmul(z1024, p3));
      *(LAS unsigned*)(B1 + 4 * KG_ROW + wo) = pk_f16(cmul(z1024, p4));
      *(LAS unsigned*)(B1 + 5 * KG_ROW + wo) = pk_f16(cmul(z1024, p5));
      *(LAS unsigned*)(B1 + 6 * KG_ROW + wo) = pk_f16(cmul(z1024, p6));
      *(LAS unsigned*)(B1 + 7 * KG_ROW + wo) = pk_f16(cmul(z1024, p7));
      *(LAS unsigned*)(B1 + 8 * KG_ROW + wo) = pk_f16(cmul(z1024, z1024));
    }
    lds_fence();
#pragma unroll
    for (int ks = 0; ks < 4; ++ks) acc1 = mfma16h(*(const LAS u32x4*)(B0 + ro + ks * 64), tf[ks], acc1);
#pragma unroll
    for (int ks = 0; ks < 4; ++ks) acc2 = mfma16h(*(const LAS u32x4*)(B1 + ro + ks * 64), tf[ks], acc2);
    lds_fence();
    STAMP(1 << 20, 5);
    {
      LAS float* red = (LAS float*)B0;
      const int c = lane & 15, q = lane >> 4;
#pragma unroll
      for (int r = 0; r < 4; ++r) {
        const int row = 4 * q + r;
        red[(row >> 3) * 128 + 8 * c + (row & 7)] = acc0[r] * scale;
        const int i1 = row < 8 ? 256 + (row >> 2) * 64 + 4 * c + (row & 3) : 384 + ((row - 8) >> 1) * 32 + 2 * c + (row & 1);
        red[i1] = acc1[r] * scale;
        const int i2 = row < 9 ? 512 + row * 16 + c : 700;
        red[i2] = acc2[r] * scale;
      }
    }
    bar_lds();
    LAS float* kd = (LAS float*)(L + KG_BUF);
#pragma unroll
    for (int i = 0; i < 3; ++i) {
      const int oo = tid + 256 * i;
      if (oo < 648) {
        const LAS float* s = (const LAS float*)L + oo;
        kd[oo] = (s[0] + s[2 * KG_BUF / 4]) + (s[4 * KG_BUF / 4] + s[6 * KG_BUF / 4]);
      }
    }
    if (tid == 0) kd[648] = 0.f;
    bar_lds();
    {
      float v[8];
#pragma unroll
      for (int i = 0; i < 8; i += 2) {
        const int l0 = 2048 - (tid * 8 + i), l1 = l0 - 1;
        const unsigned bits = pk_bf16((float)l0, (float)l1);
        int i0 = l0 < 128 ? l0 : (int)(bits & 0xffffu) - 17024;
        const int i1 = l1 < 128 ? l1 : (int)(bits >> 16) - 17024;
        if (i == 0) i0 = tid == 0 ? 648 : i0;
        v[i] = kd[i0];
        v[i + 1] = kd[i1];
      }
      u32x4 ov = {pk_bf16(v[0], v[1]), pk_bf16(v[2], v[3]), pk_bf16(v[4], v[5]), pk_bf16(v[6], v[7])};
      ST16(WS_KR + (size_t)d * KR_STRIDE + tid * 16, ov);
      if (tid < 8) {
        u32x4 z = {0u, 0u, 0u, 0u};
        if (tid == 0) z.x = pk_bf16(kd[0], 0.f);
        ST16(WS_KR + (size_t)d * KR_STRIDE + (256 + tid) * 16, z);
      }
    }
    STAMP(1 << 20, 6);
  }
}

constexpr int CV_KCOPY = 8 * 272 * 16;
constexpr int CV_UROW = 160;
constexpr int CV_U = 48 * CV_UROW;
constexpr int CV_CH = CV_KCOPY + CV_U;
constexpr int CV_LDS = 2 * CV_CH;
constexpr int CV_PROW = 272;
static_assert(4 * 32 * CV_PROW <= CV_KCOPY, "partials alias the kernel copies");

struct GArgs {
  unsigned char* ws;
  unsigned short* out;
  const unsigned short* dskip;
};
constexpr int AS = 928;
constexpr int G_T0 = 0;
constexpr int G_T1 = G_T0 + 32 * AS;
constexpr int G_X = G_T1;
constexpr int G_HQ = G_X + 32768;
constexpr int G_WL = G_HQ + 32 * 240;
constexpr int NLDS = 12;
constexpr int NRING = NFRAG - NLDS;
constexpr int G_MOD = G_WL + 7 * NLDS * 1024;
constexpr int G_FLAG = G_MOD + 2 * 448 * 4;
constexpr int G_LDS = G_FLAG + 16;
static_assert(G_MOD >= CV_LDS && G_LDS <= 163840, "LDS map");
constexpr int RING = 40;
#define RINGA_ 0
constexpr int RINGA = RINGA_;
constexpr int RINGB = RINGA + 16;
constexpr int RING0 = RINGB + 6;
constexpr int CV_P = CV_LDS;
static_assert(CV_P + 8 * 32 * CV_PROW <= G_MOD, "partials");
constexpr unsigned SPIN_CAP = 1u << 14;
constexpr int AUX_SC1 = 16;
#define WARM 0
#define EXP_REFILL true
#define POLL_SLEEP 6

__device__ __forceinline__ unsigned sane(unsigned v) { return v < 0xFFFFFFFEu ? v : 0xFFFFFFFEu; }
__device__ __forceinline__ bool bad4(const u32x4& v) { return ((v.x == ~0u) | (v.y == ~0u)) | ((v.z == ~0u) | (v.w == ~0u)); }
__device__ __forceinline__ u32x4 make_piece(u32x2 a, u32x2 b) {
  auto rx = __builtin_amdgcn_permlane16_swap(a.x, b.x, false, false);
  auto ry = __builtin_amdgcn_permlane16_swap(a.y, b.y, false, false);
  return u32x4{sane(rx[0]), sane(ry[0]), sane(rx[1]), sane(ry[1])};
}
template <int N>
__device__ __forceinline__ void poll_issue(__amdgpu_buffer_rsrc_t r, const int (&off)[N], u32x4 (&v)[N]) {
#pragma unroll
  for (int i = 0; i < N; ++i) v[i] = __builtin_amdgcn_raw_buffer_load_b128(r, off[i], 0, AUX_SC1);
  __builtin_amdgcn_sched_barrier(0);
}
template <int N>
__device__ __forceinline__ void poll_finish(__amdgpu_buffer_rsrc_t r, const int (&off)[N], u32x4 (&v)[N]) {
  for (unsigned spins = 0; spins < SPIN_CAP; ++spins) {
    bool any = false;
#pragma unroll
    for (int i = 0; i < N; ++i) any |= bad4(v[i]);
    if (!__any(any)) break;
    __builtin_amdgcn_s_sleep(POLL_SLEEP);
    asm volatile("" ::: "memory");
#pragma unroll
    for (int i = 0; i < N; ++i)
      if (bad4(v[i])) v[i] = __builtin_amdgcn_raw_buffer_load_b128(r, off[i], 0, AUX_SC1);
  }
}
template <int N>
__device__ __forceinline__ void poll_load(__amdgpu_buffer_rsrc_t r, const int (&off)[N], u32x4 (&v)[N]) {
  poll_issue<N>(r, off, v);
  poll_finish<N>(r, off, v);
}
__device__ __forceinline__ float dot2bf(unsigned a, unsigned b, float c) {
  return __builtin_amdgcn_fdot2_f32_bf16(__builtin_bit_cast(bf16x2v, a), __builtin_bit_cast(bf16x2v, b), c, false);
}
__device__ __forceinline__ void ln_stats(const unsigned (&x)[16], float lmsk, float& r, float& mr) {
  float s0 = 0.f, s1 = 0.f, q0 = 0.f, q1 = 0.f;
#pragma unroll
  for (int k = 0; k < 16; k += 2) {
    s0 = dot2bf(x[k], 0x3f803f80u, s0); q0 = dot2bf(x[k], x[k], q0);
    s1 = dot2bf(x[k + 1], 0x3f803f80u, s1); q1 = dot2bf(x[k + 1], x[k + 1], q1);
  }
  const float mean = row16_sum((s0 + s1) * lmsk) * (1.0f / D_);
  const float msq = row16_sum((q0 + q1) * lmsk) * (1.0f / D_);
  const float var = fmaxf(msq - mean * mean, 0.f);
  r = __builtin_amdgcn_rcpf(ln_sd(var));
  mr = -rbf(mean) * r;
}

#define SB0() __builtin_amdgcn_sched_barrier(0)
#define WLOAD(f) __builtin_amdgcn_raw_buffer_load_b128(rw, voff, (f) * 1024, 0)
template <int NT, int F0, int LSTART, int LNUM, int LEVERY, int PD>
__device__ __forceinline__ void gemm14(const LAS unsigned char* ap, u32x4 (&ring)[RING], __amdgpu_buffer_rsrc_t rw, int voff, f32x4 (&acc)[NT][2]) {
  u32x4 bq[PD][2];
#pragma unroll
  for (int i = 0; i < PD; ++i) { bq[i][0] = *(const LAS u32x4*)(ap + i * 64); bq[i][1] = *(const LAS u32x4*)(ap + 16 * AS + i * 64); }
#pragma unroll
  for (int i = 0; i < NT; ++i) { acc[i][0] = f32x4{0.f, 0.f, 0.f, 0.f}; acc[i][1] = acc[i][0]; }
  SB0();
#pragma unroll
  for (int ks = 0; ks < 14; ++ks) {
#pragma unroll
    for (int i = 0; i < NT; ++i) {
      const int st = ks * NT + i, f = F0 + st;
      acc[i][0] = mfma16(ring[f % RING], bq[ks % PD][0], acc[i][0]);
      acc[i][1] = mfma16(ring[f % RING], bq[ks % PD][1], acc[i][1]);
      if (st % LEVERY == 0 && st / LEVERY < LNUM) {
        static_assert(LNUM == 0 || LSTART - RING <= F0, "slot still in use");
        const int g = LSTART + st / LEVERY;
        ring[g % RING] = WLOAD(g);
      }
      if (i == NT - 1 && ks + PD < 14) {
        bq[ks % PD][0] = *(const LAS u32x4*)(ap + (ks + PD) * 64);
        bq[ks % PD][1] = *(const LAS u32x4*)(ap + 16 * AS + (ks + PD) * 64);
      }
      SB0();
    }
  }
}
template <int F0, int N>
__device__ __forceinline__ void ring_fill(u32x4 (&ring)[RING], __amdgpu_buffer_rsrc_t rw, int voff) {
#pragma unroll
  for (int f = F0; f < F0 + N; ++f) { ring[f % RING] = WLOAD(f); SB0(); }
}

__device__ __forceinline__ void gather_x2(const GArgs& p, LAS unsigned char* L, __amdgpu_buffer_rsrc_t rs, int pn, int tid) {
  const int lane = tid & 63, w = tid >> 6; (void)lane; (void)w; (void)p;
  u32x4 xin[4];
  int xo[4];
#pragma unroll
  for (int i = 0; i < 4; ++i) {
    const int pce = tid + 512 * i;
    xo[i] = pce < 1792 ? (int)WS_X2B + pn * (int)X2B_BLK + pce * 16 : (int)WS_END;
  }
  STAMP(0, 27);
  poll_issue<4>(rs, xo, xin);
  STAMP(0, 28);
  poll_finish<4>(rs, xo, xin);
  STAMP(0, 26);
  bar_lds();
#pragma unroll
  for (int i = 0; i < 4; ++i) {
    const int pce = tid + 512 * i;
    if (i < 3 || pce < 1792) {
      const int dpi = pce >> 3, tq = pce & 7;
      LAS unsigned char* dst = L + G_X + tq * 4096 + (((dpi >> 2) ^ tq) * 16) + (dpi & 3) * 4;
#pragma unroll
      for (int k = 0; k < 4; ++k) *(LAS unsigned*)(dst + k * 1024) = xin[i][k];
    }
  }
}
__device__ __forceinline__ void ln_phase_a(LAS unsigned char* L, int j, int w, int r16, int q) {
  const int c16 = r16 < 14 ? r16 : 0;
  const float lmsk = r16 < 14 ? 1.0f : 0.0f;
  const int row = 4 * w + q;
  unsigned x[16];
#pragma unroll
  for (int jq = 0; jq < 4; ++jq) {
    const u32x4 t = *(const LAS u32x4*)(L + G_X + row * 1024 + (((((jq + j) & 3) * 14 + c16) ^ w) * 16));
    x[4 * jq] = t.x; x[4 * jq + 1] = t.y; x[4 * jq + 2] = t.z; x[4 * jq + 3] = t.w;
  }
  float r, mr;
  ln_stats(x, lmsk, r, mr);
  f32x2 h[16];
#pragma unroll
  for (int jq = 0; jq < 4; ++jq) {
    const LAS unsigned char* mp = L + G_MOD + (((jq + j) & 3) * 112 + c16 * 8) * 4;
    const f32x4 sb0 = *(const LAS f32x4*)mp, sb1 = *(const LAS f32x4*)(mp + 16);
    const f32x4 c10 = *(const LAS f32x4*)(mp + 1792), c11 = *(const LAS f32x4*)(mp + 1792 + 16);
#pragma unroll
    for (int k = 0; k < 4; ++k) {
      const f32x2 xv2 = {bf_lo(x[4 * jq + k]), bf_hi(x[4 * jq + k])};
      const f32x2 c2 = k < 2 ? f32x2{c10[2 * k], c10[2 * k + 1]} : f32x2{c11[2 * k - 4], c11[2 * k - 3]};
      const f32x2 sb2 = k < 2 ? f32x2{sb0[2 * k], sb0[2 * k + 1]} : f32x2{sb1[2 * k - 4], sb1[2 * k - 3]};
      const f32x2 n = xv2 * r + mr;
      h[4 * jq + k] = n * c2 + sb2;
      x[4 * jq + k] = pk_bf16(h[4 * jq + k].x, h[4 * jq + k].y);
    }
  }
  *(LAS u32x4*)(L + G_HQ + row * 240 + (r16 < 14 ? r16 : 14) * 16) = u32x4{x[0], x[1], x[2], x[3]};
  ln_stats(x, lmsk, r, mr);
#pragma unroll
  for (int jq = 0; jq < 4; ++jq) {
    u32x4 o;
#pragma unroll
    for (int k = 0; k < 4; ++k) {
      const f32x2 n = h[4 * jq + k] * r + mr;
      o[k] = pk_bf16(n.x, n.y);
    }
    *(LAS u32x4*)(L + G_T0 + row * AS + (r16 < 14 ? (((jq + j) & 3) * 14 + r16) * 16 : 896)) = o;
  }
}
__device__ __forceinline__ void x1_offsets(int pn, int j, int w, int r16, int q, int (&gof)[3]) {
  const int c16 = r16 < 14 ? r16 : 0;
  const int go = (int)WS_X1 + pn * 4 * 7168 + ((c16 >> 1) * 64 + w * 8 + (c16 & 1) * 4 + q) * 16;
#pragma unroll
  for (int i = 0; i < 3; ++i) gof[i] = go + ((j + 1 + i) & 3) * 7168;
}
__device__ __forceinline__ void ln_phase_c(LAS unsigned char* L, const u32x4 (&xr)[3], int j, int w, int r16, int q) {
  const int c16 = r16 < 14 ? r16 : 0;
  const float lmsk = r16 < 14 ? 1.0f : 0.0f;
  const int row = 4 * w + q;
  const u32x4 own = *(const LAS u32x4*)(L + G_HQ + row * 240 + c16 * 16);
  unsigned x[16];
#pragma unroll
  for (int k = 0; k < 4; ++k) x[k] = own[k];
#pragma unroll
  for (int k = 4; k < 16; ++k) x[k] = xr[(k >> 2) - 1][k & 3];
  float r, mr;
  ln_stats(x, lmsk, r, mr);
#pragma unroll
  for (int jq = 0; jq < 4; ++jq) {
    u32x4 o;
#pragma unroll
    for (int k = 0; k < 4; ++k) {
      const f32x2 n = f32x2{bf_lo(x[4 * jq + k]), bf_hi(x[4 * jq + k])} * r + mr;
      o[k] = pk_bf16(n.x, n.y);
    }
    *(LAS u32x4*)(L + G_T0 + row * AS + (r16 < 14 ? (((jq + j) & 3) * 14 + r16) * 16 : 896)) = o;
  }
}
__device__ __forceinline__ u32x2 gelu4(f32x4 a) {
  const unsigned u = pk_bf16(a[0], a[1]), v = pk_bf16(a[2], a[3]);
  u32x2 o;
  o.x = pk_bf16(gelu_tanh(bf_lo(u)), gelu_tanh(bf_hi(u)));
  o.y = pk_bf16(gelu_tanh(bf_lo(v)), gelu_tanh(bf_hi(v)));
  return o;
}

__global__ __launch_bounds__(512, 2) void k_main(GArgs p) {
  extern __shared__ __attribute__((aligned(16))) unsigned char smem[];
  LAS unsigned char* L = (LAS unsigned char*)smem;
  const int tid = threadIdx.x, lane = tid & 63;
  const int w = __builtin_amdgcn_readfirstlane(tid >> 6);
  const int bid = blockIdx.x;
  const int j = bid & 3, pn = (bid >> 3) * 2 + ((bid >> 2) & 1);
  const int r16 = lane & 15, q = lane >> 4;
  const bool cw = w < 7;
  const int wc = cw ? w : 6;
  const bool convwg = bid < 224;
  const int dp = convwg ? bid : bid - 32;
  const __amdgpu_buffer_rsrc_t rs = __builtin_amdgcn_make_buffer_rsrc((void*)p.ws, 0, (int)WS_END, 0x00020000);
  const __amdgpu_buffer_rsrc_t rcv = __builtin_amdgcn_make_buffer_rsrc((void*)p.ws, 0, convwg ? (int)WS_END : 0, 0x00020000);
  const __amdgpu_buffer_rsrc_t rst = __builtin_amdgcn_make_buffer_rsrc((void*)p.ws, 0, cw ? (int)WS_END : 0, 0x00020000);
  const __amdgpu_buffer_rsrc_t rw =
      __builtin_amdgcn_make_buffer_rsrc((void*)(p.ws + WS_WP + (size_t)(j * 7 + wc) * NFRAG * 1024), 0, cw ? NFRAG * 1024 : 0, 0x00020000);
  const unsigned short* mod = (const unsigned short*)(p.ws + WS_MOD);
  const int voff = lane * 16;
  const int pslot = (wc * 64 + lane) * 16;
  STAMP(0, 0);
  const int kc = tid >> 8, kt = tid & 255;
  const unsigned char* krp = p.ws + WS_KR + (size_t)(2 * dp + kc) * KR_STRIDE;
  const u32x4 k0 = *(const u32x4*)(krp + kt * 16), k1 = *(const u32x4*)(krp + kt * 16 + 16);
  const int tb = tid >> 3, pc = tid & 7, xc = pc >> 2, oct = pc & 3;
  const int t8 = tb * 32 + oct * 8;
  const u32x4 xv = *(const u32x4*)(p.ws + WS_XB + ((size_t)tb * D_ + 2 * dp) * 64 + pc * 16);
  const u32x4 s0 = *(const u32x4*)(p.ws + WS_STAT + (size_t)t8 * 4), s1 = *(const u32x4*)(p.ws + WS_STAT + (size_t)t8 * 4 + 16);
  const int dch = 2 * dp + xc;
  const float sa = bf2f(mod[dch]), ca1 = bf2f(mod[448 + dch]), ga = bf2f(mod[896 + dch]);
  const float dsk = bf2f(p.dskip[dch]);
  const unsigned mtab = *(const unsigned*)(mod + 3 * 448 + (tid < 448 ? tid : 0) * 2);
  const int ncol = j * 112 + wc * 16 + q * 4;
  const u32x2 gbp = *(const u32x2*)(mod + 5 * 448 + ncol);
  SB0();
  __builtin_amdgcn_s_barrier();
  u32x4 ring[RING];
  ring_fill<0, RINGA>(ring, rw, voff);
  const unsigned OFFS = 0x087EDA52u;
  {
    const int c = tid >> 8, i = tid & 255;
    int off = CV_KCOPY + i * 16;
    if (i >= 160) { const int k = i - 160, s = k >> 3, ch = (k & 7) == 7 ? 6 : (k & 7); off = (272 * (s & 7) + ((OFFS >> (4 * (s & 7))) & 15) + 257 + ch) * 16; }
    *(LAS u32x4*)(L + c * CV_CH + off) = u32x4{0u, 0u, 0u, 0u};
  }
  {
    LAS unsigned char* base = L + kc * CV_CH;
    unsigned wv[8] = {k0.x, k0.y, k0.z, k0.w, k1.x, k1.y, k1.z, k1.w};
    unsigned vv[7];
#pragma unroll
    for (int i = 0; i < 7; ++i) vv[i] = __builtin_amdgcn_alignbit(wv[i + 1], wv[i], 16);
#pragma unroll
    for (int s = 0; s < 8; ++s) {
      const int k = s >> 1;
      u32x4 o = (s & 1) ? u32x4{vv[k], vv[k + 1], vv[k + 2], vv[k + 3]} : u32x4{wv[k], wv[k + 1], wv[k + 2], wv[k + 3]};
      *(LAS u32x4*)(base + (272 * s + ((OFFS >> (4 * s)) & 15) + kt) * 16) = o;
    }
    if (kt == 255) {
#pragma unroll
      for (int s = 0; s < 8; ++s)
        *(LAS u32x4*)(base + (272 * s + ((OFFS >> (4 * s)) & 15) + 256) * 16) = u32x4{s == 0 ? k1.x : 0u, 0u, 0u, 0u};
    }
  }
  u32x4 hv;
  {
    unsigned sv[8] = {s0.x, s0.y, s0.z, s0.w, s1.x, s1.y, s1.z, s1.w};
#pragma unroll
    for (int k = 0; k < 4; ++k) {
      unsigned t = pk_bf16(bf_lo(xv[k]) - bf_lo(sv[2 * k]), bf_hi(xv[k]) - bf_lo(sv[2 * k + 1]));
      t = pk_bf16(bf_lo(t) * __builtin_amdgcn_rcpf(bf_hi(sv[2 * k])), bf_hi(t) * __builtin_amdgcn_rcpf(bf_hi(sv[2 * k + 1])));
      t = pk_bf16(bf_lo(t) * ca1, bf_hi(t) * ca1);
      hv[k] = pk_bf16(bf_lo(t) + sa, bf_hi(t) + sa);
    }
    *(LAS u32x4*)(L + xc * CV_CH + CV_KCOPY + (16 + (t8 >> 6)) * CV_UROW + (t8 & 63) * 2) = hv;
    if (tid < 448) *(LAS f32x2*)(L + G_MOD + tid * 8) = f32x2{bf_lo(mtab), bf_hi(mtab)};
  }
  STAMP(0, 21);
  bar_lds();
  __builtin_amdgcn_raw_buffer_store_b128(u32x4{~0u, ~0u, ~0u, ~0u}, rst, (int)WS_X1 + (pn * 4 + j) * 7168 + pslot, 0, AUX_SC1);
#pragma unroll
  for (int i = 0; i < 3; ++i)
    __builtin_amdgcn_raw_buffer_store_b128(u32x4{~0u, ~0u, ~0u, ~0u}, rst, (int)WS_P + ((pn * 4 + j) * 3 + i) * 7168 + pslot, 0, AUX_SC1);
  SB0();
  STAMP(0, 22);
  {
    const int ch = w >> 2, wq = w & 3;
    f32x4 acc[2][4];
#pragma unroll
    for (int n = 0; n < 2; ++n)
#pragma unroll
      for (int m = 0; m < 4; ++m) acc[n][m] = f32x4{0.f, 0.f, 0.f, 0.f};
    const int s = (8 - r16) & 7, u = (r16 + 7) >> 3;
    const LAS unsigned char* ka = L + ch * CV_CH + (272 * s + ((OFFS >> (4 * s)) & 15) + 256 + q - u - 2 * (16 * wq) - 2 * 79) * 16;
    const LAS unsigned char* ub = L + ch * CV_CH + CV_KCOPY + (16 + r16 - 4 * wq - 3) * CV_UROW + q * 16;
#define KFRAG(f) (*(const LAS u32x4*)(ka + (79 - (f)) * 32))
    u32x4 alo[6], ahi[6];
    alo[0] = KFRAG(-2); alo[1] = KFRAG(-1); ahi[0] = KFRAG(62); ahi[1] = KFRAG(63);
#pragma unroll
    for (int dl = 0; dl < 4; ++dl) {
#pragma unroll
      for (int i = 0; i < 4; ++i) { alo[2 + i] = KFRAG(4 * dl + i); ahi[2 + i] = KFRAG(4 * dl + i + 64); }
#pragma unroll
      for (int ks = 0; ks < 2; ++ks) {
        const u32x4 b0 = *(const LAS u32x4*)(ub + (3 - dl) * CV_UROW + ks * 64);
        const u32x4 b1 = *(const LAS u32x4*)(ub + (19 - dl) * CV_UROW + ks * 64);
#pragma unroll
        for (int m = 0; m < 4; ++m) {
          acc[0][m] = mfma16(alo[m - 2 * ks + 2], b0, acc[0][m]);
          acc[1][m] = mfma16(alo[m - 2 * ks + 2], b1, acc[1][m]);
          acc[1][m] = mfma16(ahi[m - 2 * ks + 2], b0, acc[1][m]);
          const int f = RINGA + (dl * 2 + ks) * 2 + (m >> 1);
          if ((m & 1) == 0 && f < RINGB) ring[f] = WLOAD(f);
          __builtin_amdgcn_sched_barrier(0x106);
        }
      }
      alo[0] = alo[4]; alo[1] = alo[5]; ahi[0] = ahi[4]; ahi[1] = ahi[5];
    }
#undef KFRAG
    STAMP(0, 23);
    LAS unsigned char* P = L + CV_P + w * (32 * CV_PROW);
#pragma unroll
    for (int n = 0; n < 2; ++n)
#pragma unroll
      for (int m = 0; m < 4; ++m) *(LAS f32x4*)(P + (16 * n + r16) * CV_PROW + (16 * m + 4 * q) * 4) = acc[n][m];
    SB0();
    ring_fill<RINGB, RING0 - RINGB>(ring, rw, voff);
  }
  bar_lds();
  STAMP(0, 24);
  {
    const LAS unsigned char* P = L + CV_P + xc * (4 * 32 * CV_PROW) + (t8 >> 6) * CV_PROW + (t8 & 63) * 4;
    f32x4 y0 = {0.f, 0.f, 0.f, 0.f}, y1 = y0;
#pragma unroll
    for (int k = 0; k < 4; ++k) {
      y0 += *(const LAS f32x4*)(P + k * (32 * CV_PROW));
      y1 += *(const LAS f32x4*)(P + k * (32 * CV_PROW) + 16);
    }
    unsigned e[4];
#pragma unroll
    for (int k = 0; k < 4; ++k) {
      const float ya = k < 2 ? y0[2 * k] : y1[2 * k - 4], yb = k < 2 ? y0[2 * k + 1] : y1[2 * k - 3];
      const unsigned yy = pk_bf16(ya, yb), hd = pk_bf16(bf_lo(hv[k]) * dsk, bf_hi(hv[k]) * dsk);
      unsigned t = pk_bf16(bf_lo(yy) + bf_lo(hd), bf_hi(yy) + bf_hi(hd));
      t = pk_bf16(ga * bf_lo(t), ga * bf_hi(t));
      e[k] = pk_bf16(bf_lo(xv[k]) + bf_lo(t), bf_hi(xv[k]) + bf_hi(t));
    }
    const unsigned sx = xc ? e[0] : e[2], sy = xc ? e[1] : e[3];
    const unsigned rx = __builtin_amdgcn_ds_swizzle(sx, 0x101F), ry = __builtin_amdgcn_ds_swizzle(sy, 0x101F);
    const unsigned c0x = xc ? rx : e[0], c0y = xc ? ry : e[1];
    const unsigned c1x = xc ? e[2] : rx, c1y = xc ? e[3] : ry;
    u32x4 out;
    out.x = sane(__builtin_amdgcn_perm(c1x, c0x, 0x05040100u));
    out.y = sane(__builtin_amdgcn_perm(c1x, c0x, 0x07060302u));
    out.z = sane(__builtin_amdgcn_perm(c1y, c0y, 0x05040100u));
    out.w = sane(__builtin_amdgcn_perm(c1y, c0y, 0x07060302u));
    const int tq = oct * 2 + xc;
    __builtin_amdgcn_raw_buffer_store_b128(out, rcv, (int)WS_X2B + tb * (int)X2B_BLK + dp * 128 + tq * 16, 0, AUX_SC1);
  }
  STAMP(0, 25);
  if (!cw) {
    gather_x2(p, L, rs, pn, tid);
    STAMP(0, 1);
    bar_lds();
    ln_phase_a(L, j, w, r16, q);
    STAMP(0, 2);
    bar_lds();
    {
      const unsigned char* src = p.ws + WS_WP + (size_t)(j * 7) * NFRAG * 1024 + NRING * 1024 + voff;
#pragma unroll
      for (int ww = 0; ww < 7; ++ww)
#pragma unroll
        for (int i = 0; i < NLDS; ++i)
          __builtin_amdgcn_global_load_lds((const GAS unsigned*)(src + (size_t)ww * NFRAG * 1024 + i * 1024), (LAS unsigned*)(L + G_WL + (ww * NLDS + i) * 1024), 16, 0, 0);
    }
    bar_lds();
    STAMP(0, 4);
    bar_lds();
    bar_lds();
    {
      int gof[3];
      u32x4 xr[3];
      x1_offsets(pn, j, w, r16, q, gof);
      poll_load<3>(rs, gof, xr);
      ln_phase_c(L, xr, j, w, r16, q);
    }
    asm volatile("s_waitcnt vmcnt(0)" ::: "memory");
    STAMP(0, 7);
    bar_lds();
    bar_lds();
    return;
  }
  SB0();
  gather_x2(p, L, rs, pn, tid);
  SB0();
  ring_fill<RING0, 8>(ring, rw, voff);
  STAMP(0, 1);
  bar_lds();
  u32x2 h2r[2], x2r[2], x1r[2];
#pragma unroll
  for (int T = 0; T < 2; ++T) {
    const int row = 16 * T + r16;
    x2r[T] = *(const LAS u32x2*)(L + G_X + row * 1024 + (((ncol >> 3) ^ (row >> 2)) * 16) + (ncol & 4) * 2);
  }
  ln_phase_a(L, j, w, r16, q);
  SB0();
  ring_fill<RING0 + 8, RING - RING0 - 8>(ring, rw, voff);
  STAMP(0, 2);
  bar_lds();
#pragma unroll
  for (int T = 0; T < 2; ++T) h2r[T] = *(const LAS u32x2*)(L + G_HQ + (16 * T + r16) * 240 + (w * 16 + q * 4) * 2);
  const LAS unsigned char* ap0 = L + G_T0 + r16 * AS + q * 16;
  const LAS unsigned char* ap1 = L + G_T1 + r16 * AS + q * 16;
  const int tok = (q & 1) * 16 + r16, octc = 2 * w + (q >> 1);
  {
    f32x4 acc[1][2];
    STAMP(0, 16);
    gemm14<1, 0, 0, 0, 1, 5>(ap0, ring, rw, voff, acc);
    STAMP(0, 17);
    const u32x4 own = make_piece(gelu4(acc[0][0]), gelu4(acc[0][1]));
    __builtin_amdgcn_raw_buffer_store_b128(own, rs, (int)WS_G + (pn * 4 + j) * 7168 + pslot, 0, AUX_SC1);
    SB0();
    STAMP(0, 18);
    ring_fill<RING, 7>(ring, rw, voff);
    int goff[4], loff[4];
    bool wrap0;
#pragma unroll
    for (int i = 0; i < 4; ++i) {
      const int jq = (j + i) & 3;
      const int sh = (jq & 1) ? -1 - (jq >> 1) : 1 + (jq >> 1);
      const int it = tok + sh;
      const int sp = pn + (it >= 32 ? -1 : (it < 0 ? 1 : 0));
      if (i == 0) wrap0 = sp != pn;
      goff[i] = (sp < 0 || sp > 63 || (i == 0 && sp == pn)) ? (int)WS_END : (int)WS_G + (sp * 4 + jq) * 7168 + pslot;
      loff[i] = G_T1 + (it & 31) * AS + (jq * 14 + octc) * 16;
    }
    STAMP(0, 3);
    bar_lds();
    u32x4 v[4];
    poll_issue<4>(rs, goff, v);
    ring_fill<RING + 7, 7>(ring, rw, voff);
    poll_finish<4>(rs, goff, v);
    *(LAS u32x4*)(L + loff[0]) = wrap0 ? v[0] : own;
#pragma unroll
    for (int i = 1; i < 4; ++i) *(LAS u32x4*)(L + loff[i]) = v[i];
  }
  STAMP(0, 4);
  bar_lds();
  {
    f32x4 acc[1][2];
    gemm14<1, 14, 0, 0, 1, 5>(ap1, ring, rw, voff, acc);
#pragma unroll
    for (int T = 0; T < 2; ++T) {
      const unsigned a = pk_bf16(acc[0][T][0], acc[0][T][1]), b = pk_bf16(acc[0][T][2], acc[0][T][3]);
      x1r[T].x = pk_bf16(bf_lo(h2r[T].x) + bf_lo(a), bf_hi(h2r[T].x) + bf_hi(a));
      x1r[T].y = pk_bf16(bf_lo(h2r[T].y) + bf_lo(b), bf_hi(h2r[T].y) + bf_hi(b));
    }
    const u32x4 own = make_piece(x1r[0], x1r[1]);
    __builtin_amdgcn_raw_buffer_store_b128(own, rs, (int)WS_X1 + (pn * 4 + j) * 7168 + (w * 64 + (tok >> 2) * 8 + (q >> 1) * 4 + (tok & 3)) * 16, 0, AUX_SC1);
    *(LAS u32x4*)(L + G_HQ + tok * 240 + octc * 16) = own;
    SB0();
    STAMP(0, 19);
    ring_fill<RING + 14, 7>(ring, rw, voff);
    int gof[3];
    u32x4 xr[3];
    x1_offsets(pn, j, w, r16, q, gof);
    STAMP(0, 5);
    bar_lds();
    poll_issue<3>(rs, gof, xr);
    ring_fill<RING + 21, 7>(ring, rw, voff);
    poll_finish<3>(rs, gof, xr);
    STAMP(0, 6);
    ln_phase_c(L, xr, j, w, r16, q);
  }
  STAMP(0, 7);
  bar_lds();
  {
    f32x4 acc[4][2];
    STAMP(0, 12);
    gemm14<4, 28, RING + 28, 28, 2, 2>(ap0, ring, rw, voff, acc);
    STAMP(0, 13);
#pragma unroll
    for (int i = 0; i < 4; ++i)
#pragma unroll
      for (int T = 0; T < 2; ++T) {
        const int g = RING + 56 + (i * 2 + T) * 2;
        ring[g % RING] = WLOAD(g);
        ring[(g + 1) % RING] = WLOAD(g + 1);
        SB0();
        *(LAS u32x2*)(L + G_T1 + (16 * T + r16) * AS + (w * 64 + i * 16 + q * 4) * 2) = gelu4(acc[i][T]);
        SB0();
      }
  }
  STAMP(0, 8);
  bar_lds();
  {
    {
      f32x4 acc[3][2];
      STAMP(0, 14);
      gemm14<3, 84, RING + 72, NRING - RING - 72, 1, 3>(ap1, ring, rw, voff, acc);
      STAMP(0, 15);
#pragma unroll
      for (int i = 0; i < 3; ++i) {
        const int jd = (j + 1 + i) & 3;
        const u32x4 o = {sane(pk_bf16(acc[i][0][0], acc[i][0][1])), sane(pk_bf16(acc[i][0][2], acc[i][0][3])),
                         sane(pk_bf16(acc[i][1][0], acc[i][1][1])), sane(pk_bf16(acc[i][1][2], acc[i][1][3]))};
        __builtin_amdgcn_raw_buffer_store_b128(o, rs, (int)WS_P + ((pn * 4 + jd) * 3 + (2 - i)) * 7168 + pslot, 0, AUX_SC1);
      }
      SB0();
    }
    STAMP(0, 9);
    f32x4 acc0 = {0.f, 0.f, 0.f, 0.f}, acc1 = acc0;
    {
      const LAS unsigned char* wl = L + G_WL + w * (NLDS * 1024) + voff;
#pragma unroll
      for (int ks = 0; ks < 14; ++ks) {
        const int f = 126 + ks;
        const u32x4 a = f < NRING ? ring[f % RING] : *(const LAS u32x4*)(wl + (f - NRING) * 1024);
        acc0 = mfma16(a, *(const LAS u32x4*)(ap1 + ks * 64), acc0);
        acc1 = mfma16(a, *(const LAS u32x4*)(ap1 + 16 * AS + ks * 64), acc1);
      }
    }
    STAMP(0, 10);
    u32x4 v[3];
    const int go = (int)WS_P + (pn * 4 + j) * 3 * 7168 + pslot;
    const int gof[3] = {go, go + 7168, go + 2 * 7168};
    poll_load<3>(rs, gof, v);
    STAMP(0, 11);
#pragma unroll
    for (int T = 0; T < 2; ++T) {
      const f32x4 ac = T ? acc1 : acc0;
      float hh[4];
#pragma unroll
      for (int k = 0; k < 2; ++k) {
        hh[2 * k] = ac[2 * k] + ((bf_lo(v[0][2 * T + k]) + bf_lo(v[1][2 * T + k])) + bf_lo(v[2][2 * T + k]));
        hh[2 * k + 1] = ac[2 * k + 1] + ((bf_hi(v[0][2 * T + k]) + bf_hi(v[1][2 * T + k])) + bf_hi(v[2][2 * T + k]));
      }
      unsigned a = pk_bf16(hh[0], hh[1]), b = pk_bf16(hh[2], hh[3]);
      a = pk_bf16(bf_lo(x1r[T].x) + bf_lo(a), bf_hi(x1r[T].x) + bf_hi(a));
      b = pk_bf16(bf_lo(x1r[T].y) + bf_lo(b), bf_hi(x1r[T].y) + bf_hi(b));
      a = pk_bf16(bf_lo(gbp.x) * bf_lo(a), bf_hi(gbp.x) * bf_hi(a));
      b = pk_bf16(bf_lo(gbp.y) * bf_lo(b), bf_hi(gbp.y) * bf_hi(b));
      u32x2 o;
      o.x = pk_bf16(bf_lo(x2r[T].x) + bf_lo(a), bf_hi(x2r[T].x) + bf_hi(a));
      o.y = pk_bf16(bf_lo(x2r[T].y) + bf_lo(b), bf_hi(x2r[T].y) + bf_hi(b));
      *(u32x2*)(p.out + (size_t)(pn * 32 + 16 * T + r16) * D_ + ncol) = o;
    }
  }
  STAMP(0, 20);
}

}

extern "C" void kernel_launch(void* const* d_in, const int* in_sizes, int n_in, void* d_out, int out_size, void* d_ws,
                              size_t ws_size, hipStream_t stream) {
  (void)in_sizes; (void)n_in; (void)out_size; (void)ws_size;
  PrepArgs pa;
  pa.x = (const unsigned short*)d_in[0];
  pa.a_re = (const unsigned short*)d_in[1];
  pa.a_im = (const unsigned short*)d_in[2];
  pa.b_re = (const unsigned short*)d_in[3];
  pa.b_im = (const unsigned short*)d_in[4];
  pa.c_re = (const unsigned short*)d_in[5];
  pa.c_im = (const unsigned short*)d_in[6];
  pa.log_dt = (const unsigned short*)d_in[7];
  pa.w1 = (const unsigned short*)d_in[10];
  pa.w2 = (const unsigned short*)d_in[11];
  pa.w3 = (const unsigned short*)d_in[12];
  pa.w4 = (const unsigned short*)d_in[13];
  pa.cvec = (const unsigned short*)d_in[14];
  pa.wmod = (const unsigned short*)d_in[15];
  pa.ws = (unsigned char*)d_ws;
  k_prep<<<NB_PREP, 256, 0, stream>>>(pa);
  GArgs ga;
  ga.ws = (unsigned char*)d_ws;
  ga.out = (unsigned short*)d_out;
  ga.dskip = (const unsigned short*)d_in[8];
  (void)hipFuncSetAttribute((const void*)k_main, hipFuncAttributeMaxDynamicSharedMemorySize, G_LDS);
  k_main<<<256, 512, G_LDS, stream>>>(ga);
}
```

```cpp
#include <hip/hip_runtime.h>
#include <hip/hip_bf16.h>
#include <stdint.h>

namespace {

typedef __attribute__((ext_vector_type(8))) short bf16x8;
typedef __attribute__((ext_vector_type(8))) _Float16 f16x8;
typedef __attribute__((ext_vector_type(2))) _Float16 f16x2;
typedef __attribute__((ext_vector_type(4))) float f32x4;
typedef __attribute__((ext_vector_type(2))) float f32x2;
typedef __attribute__((ext_vector_type(4))) unsigned u32x4;
typedef __attribute__((ext_vector_type(2))) unsigned u32x2;
typedef __attribute__((ext_vector_type(2))) __bf16 bf16x2v;
#define GAS __attribute__((address_space(1)))
#define LAS __attribute__((address_space(3)))

constexpr int L_ = 2048, D_ = 448, NS_ = 256;

constexpr size_t WS_EPOCH = 0;
constexpr size_t WS_FLAGA = 1024;
constexpr size_t WS_FLAGC = 4608;
constexpr size_t WS_MOD = 8192;
constexpr size_t WS_MODG = 16384;
constexpr size_t WS_XN = 32768;
constexpr size_t XN_BLK = 224 * 64;
constexpr int NFRAG = 140;
constexpr size_t WS_WP = WS_XN + 256 * XN_BLK;
constexpr size_t WS_X2B = WS_WP + (size_t)28 * NFRAG * 1024;
constexpr size_t X2B_BLK = 224 * 128;
constexpr size_t WS_G = WS_X2B + 64 * X2B_BLK;
constexpr size_t WS_X1 = WS_G + (size_t)64 * 4 * 7168;
constexpr size_t WS_P = WS_X1 + (size_t)64 * 4 * 7168;
constexpr size_t WS_END = WS_P + (size_t)64 * 12 * 7168;

#define STAMP(base, k) do { } while (0)

__device__ __forceinline__ float bf_lo(unsigned u) { return __builtin_bit_cast(float, u << 16); }
__device__ __forceinline__ float bf_hi(unsigned u) { return __builtin_bit_cast(float, u & 0xffff0000u); }
__device__ __forceinline__ float bf2f(unsigned short h) { return __builtin_bit_cast(float, (unsigned)h << 16); }
__device__ __forceinline__ unsigned pk_bf16(float a, float b) {
  f32x2 v = {a, b};
  bf16x2v r = __builtin_convertvector(v, bf16x2v);
  return __builtin_bit_cast(unsigned, r);
}
__device__ __forceinline__ unsigned pk_f16(f32x2 v) {
  f16x2 r = __builtin_convertvector(v, f16x2);
  return __builtin_bit_cast(unsigned, r);
}
__device__ __forceinline__ float rbf(float a) { return bf_lo(pk_bf16(a, a)); }
__device__ __forceinline__ f32x4 mfma16(u32x4 a, u32x4 b, f32x4 c) {
  return __builtin_amdgcn_mfma_f32_16x16x32_bf16(__builtin_bit_cast(bf16x8, a), __builtin_bit_cast(bf16x8, b), c, 0, 0, 0);
}
__device__ __forceinline__ f32x4 mfma16h(u32x4 a, u32x4 b, f32x4 c) {
  return __builtin_amdgcn_mfma_f32_16x16x32_f16(__builtin_bit_cast(f16x8, a), __builtin_bit_cast(f16x8, b), c, 0, 0, 0);
}
template <int CTRL>
__device__ __forceinline__ float dpp_f(float v) {
  return __builtin_bit_cast(float, __builtin_amdgcn_update_dpp(0, __builtin_bit_cast(int, v), CTRL, 0xf, 0xf, true));
}
__device__ __forceinline__ float row16_sum(float v) {
  v += dpp_f<0xB1>(v);
  v += dpp_f<0x4E>(v);
  v += dpp_f<0x141>(v);
  v += dpp_f<0x140>(v);
  return v;
}
__device__ __forceinline__ float row16_max(float v) {
  v = fmaxf(v, dpp_f<0xB1>(v));
  v = fmaxf(v, dpp_f<0x4E>(v));
  v = fmaxf(v, dpp_f<0x141>(v));
  v = fmaxf(v, dpp_f<0x140>(v));
  return v;
}
__device__ __forceinline__ void swap16(float v, float& a, float& b) {
  const unsigned u = __builtin_bit_cast(unsigned, v), u2 = u;
  auto r = __builtin_amdgcn_permlane16_swap(u, u2, false, false);
  const unsigned r0 = r[0], r1 = r[1];
  a = __builtin_bit_cast(float, r0); b = __builtin_bit_cast(float, r1);
}
__device__ __forceinline__ void swap32(float v, float& a, float& b) {
  const unsigned u = __builtin_bit_cast(unsigned, v), u2 = u;
  auto r = __builtin_amdgcn_permlane32_swap(u, u2, false, false);
  const unsigned r0 = r[0], r1 = r[1];
  a = __builtin_bit_cast(float, r0); b = __builtin_bit_cast(float, r1);
}
__device__ __forceinline__ float rows_sum(float v) {
  float a, b;
  swap16(v, a, b); v = a + b;
  swap32(v, a, b);
  return a + b;
}
__device__ __forceinline__ float rows_max(float v) {
  float a, b;
  swap16(v, a, b); v = fmaxf(a, b);
  swap32(v, a, b);
  return fmaxf(a, b);
}
__device__ __forceinline__ float wave_sum(float v) { return rows_sum(row16_sum(v)); }
__device__ __forceinline__ float gelu_tanh(float x) {
  const float c1 = -2.0f * 0.7978845608028654f * 1.4426950408889634f;
  const float c2 = c1 * 0.044715f;
  float t = x * (c1 + c2 * x * x);
  float e = __builtin_amdgcn_exp2f(t);
  return x * __builtin_amdgcn_rcpf(1.0f + e);
}
__device__ __forceinline__ float ln_sd(float var) { return rbf(__builtin_amdgcn_sqrtf(rbf(rbf(var) + 1e-5f))); }
__device__ __forceinline__ void lds_fence() { asm volatile("" ::: "memory"); }
__device__ __forceinline__ void bar_lds() {
  asm volatile("s_waitcnt lgkmcnt(0)" ::: "memory");
  __builtin_amdgcn_s_barrier();
  asm volatile("" ::: "memory");
}

__device__ __forceinline__ void transpose8x8(const u32x4 (&r)[8], u32x4 (&o)[8]) {
#pragma unroll
  for (int c2 = 0; c2 < 4; ++c2) {
#pragma unroll
    for (int rp = 0; rp < 4; ++rp) {
      unsigned a = r[2 * rp][c2], b = r[2 * rp + 1][c2];
      o[2 * c2][rp] = __builtin_amdgcn_perm(b, a, 0x05040100u);
      o[2 * c2 + 1][rp] = __builtin_amdgcn_perm(b, a, 0x07060302u);
    }
  }
}
__device__ __forceinline__ f32x2 cmul(f32x2 a, f32x2 b) {
  f32x2 r, t;
  asm("v_pk_mul_f32 %0, %1, %2 op_sel_hi:[0,1]" : "=v"(t) : "v"(a), "v"(b));
  asm("v_pk_fma_f32 %0, %1, %2, %3 op_sel:[1,1,0] op_sel_hi:[1,0,1] neg_lo:[1,0,0]" : "=v"(r) : "v"(a), "v"(b), "v"(t));
  return r;
}
__device__ __forceinline__ f32x2 cexp_tab(float a, float b, float l) {
  const float e = __builtin_amdgcn_exp2f(a * l), ph = __builtin_amdgcn_fractf(b * l);
  f32x2 r;
  r.x = e * __builtin_amdgcn_cosf(ph);
  r.y = e * __builtin_amdgcn_sinf(ph);
  return r;
}

constexpr int CV_KCOPY = 8 * 272 * 16;
constexpr int CV_UROW = 160;
constexpr int CV_U = 48 * CV_UROW;
constexpr int CV_CH = CV_KCOPY + CV_U;
constexpr int CV_LDS = 2 * CV_CH;
constexpr int CV_PROW = 272;
static_assert(4 * 32 * CV_PROW <= CV_KCOPY, "partials alias the kernel copies");

struct GArgs {
  const unsigned short* x;
  const unsigned short* a_re;
  const unsigned short* a_im;
  const unsigned short* b_re;
  const unsigned short* b_im;
  const unsigned short* c_re;
  const unsigned short* c_im;
  const unsigned short* log_dt;
  const unsigned short* dskip;
  const unsigned short* w1;
  const unsigned short* w2;
  const unsigned short* w3;
  const unsigned short* w4;
  const unsigned short* cvec;
  const unsigned short* wmod;
  unsigned char* ws;
  unsigned short* out;
};
constexpr int AS = 928;
constexpr int G_T0 = 0;
constexpr int G_T1 = G_T0 + 32 * AS;
constexpr int G_X = G_T1;
constexpr int G_HQ = G_X + 32768;
constexpr int G_WL = G_HQ + 32 * 240;
constexpr int NLDS = 12;
constexpr int NRING = NFRAG - NLDS;
constexpr int G_MOD = G_WL + 7 * NLDS * 1024;
constexpr int G_CNT = G_MOD + 2 * 448 * 4;
constexpr int P0_MS = G_CNT + 16;
constexpr int G_LDS = P0_MS + 256;
constexpr int KG_ROW = 288;
constexpr int KG_BUF = 16 * KG_ROW;
constexpr int P0_KG = CV_LDS;
constexpr int P0_LN = 0;
constexpr int LNS = 1856;
constexpr int P0_PK = 16384;
static_assert(P0_KG + 16 * KG_BUF <= G_CNT, "tables");
static_assert(G_MOD >= CV_LDS && G_LDS <= 163840, "LDS map");
constexpr int RING = 40;
constexpr int RINGB = 16;
constexpr int RING0 = RINGB + 6;
constexpr int CV_P = CV_LDS;
static_assert(CV_P + 8 * 32 * CV_PROW <= G_MOD, "partials");
constexpr unsigned SPIN_CAP = 1u << 14;
constexpr int AUX_SC1 = 16;
constexpr int CODE_LINES = 480;
#define PRETOUCH 1
#define POLL_SLEEP 6

__device__ __forceinline__ unsigned sane(unsigned v) { return v < 0xFFFFFFFEu ? v : 0xFFFFFFFEu; }
__device__ __forceinline__ unsigned umax4(const u32x4& v) { return max(max(v.x, v.y), max(v.z, v.w)); }
__device__ __forceinline__ bool bad4(const u32x4& v) { return umax4(v) == ~0u; }
__device__ __forceinline__ u32x4 make_piece(u32x2 a, u32x2 b) {
  auto rx = __builtin_amdgcn_permlane16_swap(a.x, b.x, false, false);
  auto ry = __builtin_amdgcn_permlane16_swap(a.y, b.y, false, false);
  return u32x4{sane(rx[0]), sane(ry[0]), sane(rx[1]), sane(ry[1])};
}
template <int N>
__device__ __forceinline__ void poll_issue(__amdgpu_buffer_rsrc_t r, const int (&off)[N], u32x4 (&v)[N]) {
#pragma unroll
  for (int i = 0; i < N; ++i) v[i] = __builtin_amdgcn_raw_buffer_load_b128(r, off[i], 0, AUX_SC1);
  __builtin_amdgcn_sched_barrier(0);
}
template <int N>
__device__ __forceinline__ void poll_finish(__amdgpu_buffer_rsrc_t r, const int (&off)[N], u32x4 (&v)[N]) {
  for (unsigned spins = 0; spins < SPIN_CAP; ++spins) {
    unsigned m = 0u;
#pragma unroll
    for (int i = 0; i < N; ++i) m = max(m, umax4(v[i]));
    if (!__any(m == ~0u)) break;
    __builtin_amdgcn_s_sleep(POLL_SLEEP);
    asm volatile("" ::: "memory");
#pragma unroll
    for (int i = 0; i < N; ++i)
      if (bad4(v[i])) v[i] = __builtin_amdgcn_raw_buffer_load_b128(r, off[i], 0, AUX_SC1);
  }
}
template <int N>
__device__ __forceinline__ void poll_load(__amdgpu_buffer_rsrc_t r, const int (&off)[N], u32x4 (&v)[N]) {
  poll_issue<N>(r, off, v);
  poll_finish<N>(r, off, v);
}
__device__ __forceinline__ float dot2bf(unsigned a, unsigned b, float c) {
  return __builtin_amdgcn_fdot2_f32_bf16(__builtin_bit_cast(bf16x2v, a), __builtin_bit_cast(bf16x2v, b), c, false);
}
__device__ __forceinline__ void ln_stats(const unsigned (&x)[16], float lmsk, float& r, float& mr) {
  float s0 = 0.f, s1 = 0.f, q0 = 0.f, q1 = 0.f;
#pragma unroll
  for (int k = 0; k < 16; k += 2) {
    s0 = dot2bf(x[k], 0x3f803f80u, s0); q0 = dot2bf(x[k], x[k], q0);
    s1 = dot2bf(x[k + 1], 0x3f803f80u, s1); q1 = dot2bf(x[k + 1], x[k + 1], q1);
  }
  const float mean = row16_sum((s0 + s1) * lmsk) * (1.0f / D_);
  const float msq = row16_sum((q0 + q1) * lmsk) * (1.0f / D_);
  const float var = fmaxf(msq - mean * mean, 0.f);
  r = __builtin_amdgcn_rcpf(ln_sd(var));
  mr = -rbf(mean) * r;
}

#define SB0() __builtin_amdgcn_sched_barrier(0)
#define WLOAD(f) __builtin_amdgcn_raw_buffer_load_b128(rw, voff + ((f) & 3) * 1024, ((f) >> 2) * 4096, AUX_SC1)
template <int NT, int F0, int LSTART, int LNUM, int LEVERY, int PD>
__device__ __forceinline__ void gemm14(const LAS unsigned char* ap, u32x4 (&ring)[RING], __amdgpu_buffer_rsrc_t rw, int voff, f32x4 (&acc)[NT][2]) {
  u32x4 bq[PD][2];
#pragma unroll
  for (int i = 0; i < PD; ++i) { bq[i][0] = *(const LAS u32x4*)(ap + i * 64); bq[i][1] = *(const LAS u32x4*)(ap + 16 * AS + i * 64); }
#pragma unroll
  for (int i = 0; i < NT; ++i) { acc[i][0] = f32x4{0.f, 0.f, 0.f, 0.f}; acc[i][1] = acc[i][0]; }
  SB0();
#pragma unroll
  for (int ks = 0; ks < 14; ++ks) {
#pragma unroll
    for (int i = 0; i < NT; ++i) {
      const int st = ks * NT + i, f = F0 + st;
      acc[i][0] = mfma16(ring[f % RING], bq[ks % PD][0], acc[i][0]);
      acc[i][1] = mfma16(ring[f % RING], bq[ks % PD][1], acc[i][1]);
      if (st % LEVERY == 0 && st / LEVERY < LNUM) {
        const int g = LSTART + st / LEVERY;
        ring[g % RING] = WLOAD(g);
      }
      if (i == NT - 1 && ks + PD < 14) {
        bq[ks % PD][0] = *(const LAS u32x4*)(ap + (ks + PD) * 64);
        bq[ks % PD][1] = *(const LAS u32x4*)(ap + 16 * AS + (ks + PD) * 64);
      }
      SB0();
    }
  }
}
template <int F0, int N>
__device__ __forceinline__ void ring_fill(u32x4 (&ring)[RING], __amdgpu_buffer_rsrc_t rw, int voff) {
#pragma unroll
  for (int f = F0; f < F0 + N; ++f) { ring[f % RING] = WLOAD(f); SB0(); }
}

__device__ __forceinline__ void gather_x2(const GArgs& p, LAS unsigned char* L, __amdgpu_buffer_rsrc_t rs, int pn, int tid) {
  const int lane = tid & 63, w = tid >> 6; (void)lane; (void)w; (void)p;
  u32x4 xin[4];
  int xo[4];
#pragma unroll
  for (int i = 0; i < 4; ++i) {
    const int pce = tid + 512 * i;
    xo[i] = pce < 1792 ? (int)WS_X2B + pn * (int)X2B_BLK + pce * 16 : (int)WS_END;
  }
  STAMP(0, 27);
  poll_issue<4>(rs, xo, xin);
  STAMP(0, 28);
  poll_finish<4>(rs, xo, xin);
  STAMP(0, 26);
  bar_lds();
#pragma unroll
  for (int i = 0; i < 4; ++i) {
    const int pce = tid + 512 * i;
    if (i < 3 || pce < 1792) {
      const int dpi = pce >> 3, tq = pce & 7;
      LAS unsigned char* dst = L + G_X + tq * 4096 + (((dpi >> 2) ^ tq) * 16) + (dpi & 3) * 4;
#pragma unroll
      for (int k = 0; k < 4; ++k) *(LAS unsigned*)(dst + k * 1024) = xin[i][k];
    }
  }
}
__device__ __forceinline__ void ln_phase_a(LAS unsigned char* L, int j, int w, int r16, int q) {
  const int c16 = r16 < 14 ? r16 : 0;
  const float lmsk = r16 < 14 ? 1.0f : 0.0f;
  const int row = 4 * w + q;
  unsigned x[16];
#pragma unroll
  for (int jq = 0; jq < 4; ++jq) {
    const u32x4 t = *(const LAS u32x4*)(L + G_X + row * 1024 + (((((jq + j) & 3) * 14 + c16) ^ w) * 16));
    x[4 * jq] = t.x; x[4 * jq + 1] = t.y; x[4 * jq + 2] = t.z; x[4 * jq + 3] = t.w;
  }
  float r, mr;
  ln_stats(x, lmsk, r, mr);
  f32x2 h[16];
#pragma unroll
  for (int jq = 0; jq < 4; ++jq) {
    const LAS unsigned char* mp = L + G_MOD + (((jq + j) & 3) * 112 + c16 * 8) * 4;
    const f32x4 sb0 = *(const LAS f32x4*)mp, sb1 = *(const LAS f32x4*)(mp + 16);
    const f32x4 c10 = *(const LAS f32x4*)(mp + 1792), c11 = *(const LAS f32x4*)(mp + 1792 + 16);
#pragma unroll
    for (int k = 0; k < 4; ++k) {
      const f32x2 xv2 = {bf_lo(x[4 * jq + k]), bf_hi(x[4 * jq + k])};
      const f32x2 c2 = k < 2 ? f32x2{c10[2 * k], c10[2 * k + 1]} : f32x2{c11[2 * k - 4], c11[2 * k - 3]};
      const f32x2 sb2 = k < 2 ? f32x2{sb0[2 * k], sb0[2 * k + 1]} : f32x2{sb1[2 * k - 4], sb1[2 * k - 3]};
      const f32x2 n = xv2 * r + mr;
      h[4 * jq + k] = n * c2 + sb2;
      x[4 * jq + k] = pk_bf16(h[4 * jq + k].x, h[4 * jq + k].y);
    }
  }
  *(LAS u32x4*)(L + G_HQ + row * 240 + (r16 < 14 ? r16 : 14) * 16) = u32x4{x[0], x[1], x[2], x[3]};
  ln_stats(x, lmsk, r, mr);
#pragma unroll
  for (int jq = 0; jq < 4; ++jq) {
    u32x4 o;
#pragma unroll
    for (int k = 0; k < 4; ++k) {
      const f32x2 n = h[4 * jq + k] * r + mr;
      o[k] = pk_bf16(n.x, n.y);
    }
    *(LAS u32x4*)(L + G_T0 + row * AS + (r16 < 14 ? (((jq + j) & 3) * 14 + r16) * 16 : 896)) = o;
  }
}
__device__ __forceinline__ void x1_offsets(int pn, int j, int w, int r16, int q, int (&gof)[3]) {
  const int c16 = r16 < 14 ? r16 : 0;
  const int go = (int)WS_X1 + pn * 4 * 7168 + ((c16 >> 1) * 64 + w * 8 + (c16 & 1) * 4 + q) * 16;
#pragma unroll
  for (int i = 0; i < 3; ++i) gof[i] = go + ((j + 1 + i) & 3) * 7168;
}
__device__ __forceinline__ void ln_phase_c(LAS unsigned char* L, const u32x4 (&xr)[3], int j, int w, int r16, int q) {
  const int c16 = r16 < 14 ? r16 : 0;
  const float lmsk = r16 < 14 ? 1.0f : 0.0f;
  const int row = 4 * w + q;
  const u32x4 own = *(const LAS u32x4*)(L + G_HQ + row * 240 + c16 * 16);
  unsigned x[16];
#pragma unroll
  for (int k = 0; k < 4; ++k) x[k] = own[k];
#pragma unroll
  for (int k = 4; k < 16; ++k) x[k] = xr[(k >> 2) - 1][k & 3];
  float r, mr;
  ln_stats(x, lmsk, r, mr);
#pragma unroll
  for (int jq = 0; jq < 4; ++jq) {
    u32x4 o;
#pragma unroll
    for (int k = 0; k < 4; ++k) {
      const f32x2 n = f32x2{bf_lo(x[4 * jq + k]), bf_hi(x[4 * jq + k])} * r + mr;
      o[k] = pk_bf16(n.x, n.y);
    }
    *(LAS u32x4*)(L + G_T0 + row * AS + (r16 < 14 ? (((jq + j) & 3) * 14 + r16) * 16 : 896)) = o;
  }
}
__device__ __forceinline__ u32x2 gelu4(f32x4 a) {
  const unsigned u = pk_bf16(a[0], a[1]), v = pk_bf16(a[2], a[3]);
  u32x2 o;
  o.x = pk_bf16(gelu_tanh(bf_lo(u)), gelu_tanh(bf_hi(u)));
  o.y = pk_bf16(gelu_tanh(bf_lo(v)), gelu_tanh(bf_hi(v)));
  return o;
}

#define ST16(off, v) __builtin_amdgcn_raw_buffer_store_b128(v, rs, (int)(off), 0, AUX_SC1)
typedef __attribute__((ext_vector_type(4))) short s16x4;
typedef unsigned long long u64;

struct PackJob { const GAS unsigned short* src; int ldn, mat, ks, cb; };
__device__ __forceinline__ PackJob pack_job(const GArgs& p, int g) {
  PackJob jb;
  const uintptr_t a1 = (uintptr_t)p.w1, a2 = (uintptr_t)p.w2, a3 = (uintptr_t)p.w3, a4 = (uintptr_t)p.w4;
  uintptr_t lo = g < 56 ? a1 : a2, hi = g < 336 ? a3 : a4;
  asm volatile("" : "+s"(lo), "+s"(hi));
  jb.src = (const GAS unsigned short*)(g < 112 ? lo : hi);
  jb.mat = (g >= 56) + (g >= 112) + (g >= 336);
  jb.ldn = jb.mat == 2 ? 1792 : 448;
  g -= g < 112 ? (g < 56 ? 0 : 56) : (g < 336 ? 112 : 336);
  const int sh = jb.mat == 2 ? 4 : 2;
  jb.ks = g >> sh;
  jb.cb = g & ((1 << sh) - 1);
  return jb;
}
__device__ __forceinline__ void pack_load(const PackJob& jb, int lane, u32x4 (&r)[8]) {
  const int jn = lane & 15, q = lane >> 4, jl = jn < 14 ? jn : 0;
#pragma unroll
  for (int i = 0; i < 8; ++i) r[i] = *(const GAS u32x4*)(jb.src + (size_t)(jb.ks * 32 + q * 8 + i) * jb.ldn + jb.cb * 112 + jl * 8);
}
__device__ __forceinline__ void pack_shuffle(int lane, u32x4 (&r)[8], LAS unsigned char* Lw) {
  const int jn = lane & 15, q = lane >> 4;
  u32x4 o[8];
  transpose8x8(r, o);
#pragma unroll
  for (int i = 0; i < 8; ++i) *(LAS u32x4*)(Lw + (jn >> 1) * 1024 + (q * 16 + (jn & 1) * 8 + (i ^ (jn >> 1))) * 16) = o[i];
  lds_fence();
#pragma unroll
  for (int m = 0; m < 7; ++m) r[m] = *(const LAS u32x4*)(Lw + m * 1024 + (lane ^ m) * 16);
  lds_fence();
}
__device__ __forceinline__ void pack_store(const PackJob& jb, int lane, const u32x4 (&r)[8], __amdgpu_buffer_rsrc_t rs) {
  const int j3 = jb.ks / 14, ksl = jb.ks - j3 * 14, sl = (jb.cb - j3 - 1) & 3;
  const int na = jb.mat == 2 ? jb.cb * 7 : (jb.mat == 3 ? j3 : jb.cb) * 28, nb = jb.mat == 2 ? 1 : 4;
  const int f3 = sl < 3 ? 84 + ksl * 3 + sl : 126 + ksl;
  const int fb = jb.mat == 2 ? 28 + jb.ks * 4 : (jb.mat == 3 ? f3 : jb.mat * 14 + jb.ks);
#pragma unroll
  for (int m = 0; m < 7; ++m) {
    const int nt = na + m * nb;
    ST16(WS_WP + ((nt >> 2) * NFRAG + fb + (nt & 3)) * 1024 + lane * 16, r[m]);
  }
}
__device__ __forceinline__ void mod_load(const GArgs& p, int u, int tid, u32x4 (&r)[7], float (&sc)[7]) {
  const int rg = tid >> 3, pc = tid & 7;
#pragma unroll
  for (int i = 0; i < 7; ++i) r[i] = *(const u32x4*)(p.wmod + (size_t)(rg + 64 * i) * 2688 + u * 64 + pc * 8);
#pragma unroll
  for (int i = 0; i < 7; ++i) sc[i] = bf2f(p.cvec[rg + 64 * i]);
}
__device__ __forceinline__ void mod_finish(int u, unsigned tag32, int tid, const u32x4 (&r)[7], const float (&cv)[7], LAS float* sm, __amdgpu_buffer_rsrc_t rs) {
  const int rg = tid >> 3, pc = tid & 7;
  float acc[8];
#pragma unroll
  for (int k = 0; k < 8; ++k) acc[k] = 0.f;
#pragma unroll
  for (int i = 0; i < 7; ++i) {
    const float sc = rbf(cv[i] * __builtin_amdgcn_rcpf(1.0f + __builtin_amdgcn_exp2f(-1.4426950408889634f * cv[i])));
#pragma unroll
    for (int k = 0; k < 4; ++k) { acc[2 * k] += sc * bf_lo(r[i][k]); acc[2 * k + 1] += sc * bf_hi(r[i][k]); }
  }
  *(LAS f32x4*)(sm + rg * 68 + pc * 8) = f32x4{acc[0], acc[1], acc[2], acc[3]};
  *(LAS f32x4*)(sm + rg * 68 + pc * 8 + 4) = f32x4{acc[4], acc[5], acc[6], acc[7]};
  bar_lds();
  {
    const int col = tid & 63, part = tid >> 6;
    float s = 0.f;
#pragma unroll
    for (int k = 0; k < 8; ++k) s += sm[(part * 8 + k) * 68 + col];
    sm[64 * 68 + part * 64 + col] = s;
  }
  bar_lds();
  if (tid < 32) {
    f32x2 s = {0.f, 0.f};
#pragma unroll
    for (int k = 0; k < 8; ++k) s += *(const LAS f32x2*)(sm + 64 * 68 + k * 64 + 2 * tid);
    unsigned v = pk_bf16(s.x, s.y);
    const int sec = u / 7;
    if (sec == 1 || sec == 4) v = pk_bf16(bf_lo(v) + 1.0f, bf_hi(v) + 1.0f);
    if (u < 21) __builtin_amdgcn_raw_buffer_store_b64(u32x2{v, tag32}, rs, (int)WS_MODG + (u * 32 + tid) * 8, 0, AUX_SC1);
    else __builtin_amdgcn_raw_buffer_store_b32(v, rs, (int)WS_MOD + (u * 64 + 2 * tid) * 2, 0, AUX_SC1);
  }
}
template <int YOUNGER>
__device__ __forceinline__ void arrive_flag(LAS unsigned char* L, int cnt_slot, unsigned char* flag, u64 tag, int lane) {
  asm volatile("s_waitcnt vmcnt(%0)" ::"i"(YOUNGER) : "memory");
  if (lane == 0) {
    const unsigned old = __hip_atomic_fetch_add((LAS unsigned*)(L + G_CNT) + cnt_slot, 1u, __ATOMIC_RELAXED, __HIP_MEMORY_SCOPE_WORKGROUP);
    if (old == 7u) __hip_atomic_store((GAS u64*)flag, tag, __ATOMIC_RELAXED, __HIP_MEMORY_SCOPE_AGENT);
  }
}
__device__ __forceinline__ u32x2 flag_load(__amdgpu_buffer_rsrc_t rs, int off) { return __builtin_amdgcn_raw_buffer_load_b64(rs, off, 0, AUX_SC1); }
__device__ __forceinline__ u32x2 flag_wait(__amdgpu_buffer_rsrc_t rs, int off, u32x2 v, unsigned lo, unsigned hi, unsigned lomask) {
  for (unsigned spins = 0; spins < SPIN_CAP; ++spins) {
    if (!__any((((v.x ^ lo) & lomask) | (v.y ^ hi)) != 0u)) break;
    __builtin_amdgcn_s_sleep(2);
    asm volatile("" ::: "memory");
    v = flag_load(rs, off);
  }
  __builtin_amdgcn_fence(__ATOMIC_ACQUIRE, "wavefront");
  return v;
}

__device__ __forceinline__ void mod_table(LAS unsigned char* L, __amdgpu_buffer_rsrc_t rs, int lane, u32x2 fc, unsigned tlo, unsigned thi) {
  flag_wait(rs, (int)WS_FLAGC + (lane & 31) * 8, fc, ~tlo, ~thi, ~0u);
  const int l56 = lane < 56 ? lane : 0;
  const u32x4 m0 = __builtin_amdgcn_raw_buffer_load_b128(rs, (int)WS_MOD + 3 * 896 + l56 * 16, 0, AUX_SC1);
  const u32x4 m1 = __builtin_amdgcn_raw_buffer_load_b128(rs, (int)WS_MOD + 4 * 896 + l56 * 16, 0, AUX_SC1);
  LAS unsigned char* mp = L + G_MOD + l56 * 32;
  *(LAS f32x4*)mp = f32x4{bf_lo(m0.x), bf_hi(m0.x), bf_lo(m0.y), bf_hi(m0.y)};
  *(LAS f32x4*)(mp + 16) = f32x4{bf_lo(m0.z), bf_hi(m0.z), bf_lo(m0.w), bf_hi(m0.w)};
  *(LAS f32x4*)(mp + 1792) = f32x4{bf_lo(m1.x), bf_hi(m1.x), bf_lo(m1.y), bf_hi(m1.y)};
  *(LAS f32x4*)(mp + 1792 + 16) = f32x4{bf_lo(m1.z), bf_hi(m1.z), bf_lo(m1.w), bf_hi(m1.w)};
}

template <bool CONV>
__device__ __forceinline__ void body(const GArgs& p, LAS unsigned char* L, uintptr_t code0) {
  const int tid = threadIdx.x, lane = tid & 63;
  const int w = __builtin_amdgcn_readfirstlane(tid >> 6);
  const int bid = blockIdx.x;
  const int j = bid & 3, pn = (bid >> 3) * 2 + ((bid >> 2) & 1);
  const int r16 = lane & 15, q = lane >> 4;
  const bool cw = w < 7;
  const int wc = cw ? w : 6;
  const int dp = (bid & 7) * 28 + (bid >> 3);
  const __amdgpu_buffer_rsrc_t rs = __builtin_amdgcn_make_buffer_rsrc((void*)p.ws, 0, (int)WS_END, 0x00020000);
  const __amdgpu_buffer_rsrc_t rst = __builtin_amdgcn_make_buffer_rsrc((void*)p.ws, 0, cw ? (int)WS_END : 0, 0x00020000);
  const __amdgpu_buffer_rsrc_t rw =
      __builtin_amdgcn_make_buffer_rsrc((void*)(p.ws + WS_WP + (size_t)(j * 7 + wc) * NFRAG * 1024), 0, cw ? NFRAG * 1024 : 0, 0x00020000);
  const int voff = lane * 16;
  const int pslot = (wc * 64 + lane) * 16;
  const u32x4 ONES = {~0u, ~0u, ~0u, ~0u};
  STAMP(0, 0);
  const int l56 = lane < 56 ? lane : 0;
  const u32x4 xrow = *(const u32x4*)(p.x + (size_t)(bid * 8 + w) * D_ + l56 * 8);
  const u32x2 ep = flag_load(rs, (int)WS_EPOCH);
  const int cl = (bid >> 3) * 16 + (lane & 15);
  const unsigned touch = *(const GAS unsigned*)(code0 + (size_t)(cl < CODE_LINES ? cl : 0) * 128);
  __builtin_amdgcn_raw_buffer_store_b128(ONES, rst, (int)WS_X2B + bid * 7168 + pslot, 0, AUX_SC1);
  float are, aim, b_re, b_im, cre, cim, ldt;
  u32x4 mr[7], pk[8];
  float mc[7];
  const bool packer = w < 2 || (w == 2 && bid < 112);
  const PackJob jb = pack_job(p, w == 2 ? 448 + (bid < 112 ? bid : 0) : (CONV ? bid * 2 + (w & 1) : 0));
  if (CONV) {
    const size_t o = (size_t)(2 * dp + (w >> 2)) * NS_ + (w & 3) * 64 + lane;
    are = bf2f(p.a_re[o]); aim = bf2f(p.a_im[o]); b_re = bf2f(p.b_re[o]); b_im = bf2f(p.b_im[o]);
    cre = bf2f(p.c_re[o]); cim = bf2f(p.c_im[o]); ldt = bf2f(p.log_dt[2 * dp + (w >> 2)]);
  } else {
    SB0();
    mod_load(p, bid - 224, tid, mr, mc);
    SB0();
  }
  if (tid < 4) *((LAS unsigned*)(L + G_CNT) + tid) = 0u;
  {
    const float msk = lane < 56 ? 1.0f : 0.0f;
    float v[8];
#pragma unroll
    for (int k = 0; k < 4; ++k) { v[2 * k] = bf_lo(xrow[k]); v[2 * k + 1] = bf_hi(xrow[k]); }
    const float mean = wave_sum((((v[0] + v[1]) + (v[2] + v[3])) + ((v[4] + v[5]) + (v[6] + v[7]))) * msk) * (1.0f / D_);
    float qq = 0.f;
#pragma unroll
    for (int k = 0; k < 8; ++k) { const float d = v[k] - mean; qq += d * d; }
    const float rsd = __builtin_amdgcn_rcpf(ln_sd(wave_sum(qq * msk) * (1.0f / D_)));
    const float mb = rbf(mean);
    unsigned n[4];
#pragma unroll
    for (int k = 0; k < 4; ++k) {
      const unsigned t = pk_bf16(v[2 * k] - mb, v[2 * k + 1] - mb);
      n[k] = pk_bf16(bf_lo(t) * rsd, bf_hi(t) * rsd);
    }
    if (lane < 56) {
      *(LAS u32x4*)(L + P0_LN + w * LNS + lane * 32) = u32x4{n[0], xrow.x, n[1], xrow.y};
      *(LAS u32x4*)(L + P0_LN + w * LNS + lane * 32 + 16) = u32x4{n[2], xrow.z, n[3], xrow.w};
    }
  }
  bar_lds();
  {
    const LAS unsigned char* a = L + P0_LN + ((lane >> 2) & 3) * LNS + wc * 256 + q * 32 + (lane & 3) * 8;
#pragma unroll
    for (int u = 0; u < 2; ++u) {
      const s16x4 lo = __builtin_amdgcn_ds_read_tr16_b64_v4i16((LAS s16x4*)(a + u * 128));
      const s16x4 hi = __builtin_amdgcn_ds_read_tr16_b64_v4i16((LAS s16x4*)(a + u * 128 + 4 * LNS));
      const u32x2 l2 = __builtin_bit_cast(u32x2, lo), h2 = __builtin_bit_cast(u32x2, hi);
      __builtin_amdgcn_raw_buffer_store_b128(u32x4{l2.x, l2.y, h2.x, h2.y}, rst, (int)WS_XN + bid * (int)XN_BLK + ((wc * 2 + u) * 64 + lane) * 16, 0, AUX_SC1);
    }
  }
  u64 tag;
  {
    const u64 e = ((u64)__builtin_amdgcn_readfirstlane(ep.y) << 32) | (unsigned)__builtin_amdgcn_readfirstlane(ep.x);
    u64 z = e + 0x9E3779B97F4A7C15ull;
    z = (z ^ (z >> 30)) * 0xBF58476D1CE4E5B9ull;
    z = (z ^ (z >> 27)) * 0x94D049BB133111EBull;
    tag = z ^ (z >> 31);
  }
  const unsigned tag32 = ((unsigned)__builtin_amdgcn_readfirstlane(ep.x) + 1u) * 0x9E3779B1u ^ 0x5bd1e995u;
  const unsigned tlo = (unsigned)tag, thi = (unsigned)(tag >> 32);
  asm volatile("" ::"v"(touch));
  STAMP(0, 29);
  const bool gl = CONV && lane >= 32 && lane < 35;
  const int fao = gl ? (int)WS_MODG + ((lane - 32) * 224 + dp) * 8 : (int)WS_FLAGA + (w * 32 + (lane & 31)) * 8;
  u32x4 ring[RING];
  if (CONV) {
    const float dt = rbf(expf(ldt));
    const float ea = (dt * are) * 1.4426950408889634f, eb = (dt * aim) * 0.15915494309189535f;
    const float bre = dt * b_re, bim = dt * b_im;
    f32x2 wc;
    wc.x = cre * bre - cim * bim;
    wc.y = -(cre * bim + cim * bre);
    asm volatile("" ::"v"(ea), "v"(eb), "v"(wc.x), "v"(wc.y));
    SB0();
    if (packer) pack_load(jb, lane, pk);
    SB0();
    const float mx = rows_max(row16_max(fmaxf(fabsf(wc.x), fabsf(wc.y))));
    int ex = mx > 0.f ? __builtin_amdgcn_frexp_expf(mx) : 0;
    ex = ex < -120 ? -120 : (ex > 120 ? 120 : ex);
    const float scale = __builtin_amdgcn_ldexpf(1.0f, ex), iscale = __builtin_amdgcn_ldexpf(1.0f, -ex);
    wc.x *= iscale; wc.y *= iscale;
    LAS unsigned char* B0 = L + P0_KG + w * (2 * KG_BUF);
    LAS unsigned char* B1 = B0 + KG_BUF;
    const int wo = lane * 4;
    const int ro = r16 * KG_ROW + q * 16;
    f32x2 z8c = cexp_tab(ea, eb, 8.0f);
    z8c.y = -z8c.y;
    const f32x2 z1 = cexp_tab(ea, eb, 1.0f), z128 = cexp_tab(ea, eb, 128.0f), z1024 = cexp_tab(ea, eb, 1024.0f);
    {
      const f32x2 z16c = cmul(z8c, z8c);
      f32x2 t0 = wc, t1 = cmul(wc, z8c);
#pragma unroll
      for (int c = 0; c < 16; c += 2) {
        *(LAS unsigned*)(B0 + c * KG_ROW + wo) = pk_f16(t0);
        *(LAS unsigned*)(B0 + (c + 1) * KG_ROW + wo) = pk_f16(t1);
        if (c < 14) { t0 = cmul(t0, z16c); t1 = cmul(t1, z16c); }
      }
    }
    f32x2 ze[8];
    ze[0] = f32x2{1.0f, 0.0f}; ze[1] = z1; ze[2] = cmul(z1, z1); ze[3] = cmul(ze[2], z1); ze[4] = cmul(ze[2], ze[2]);
    ze[5] = cmul(ze[4], z1); ze[6] = cmul(ze[4], ze[2]); ze[7] = cmul(ze[4], ze[3]);
#pragma unroll
    for (int e = 0; e < 8; ++e) {
      *(LAS unsigned*)(B1 + e * KG_ROW + wo) = pk_f16(ze[e]);
      *(LAS unsigned*)(B1 + (8 + e) * KG_ROW + wo) = pk_f16(e == 0 ? z128 : cmul(z128, ze[e]));
    }
    lds_fence();
    u32x4 tf[4];
#pragma unroll
    for (int ks = 0; ks < 4; ++ks) tf[ks] = *(const LAS u32x4*)(B0 + ro + ks * 64);
    u32x4 rf[4];
#pragma unroll
    for (int ks = 0; ks < 4; ++ks) rf[ks] = *(const LAS u32x4*)(B1 + ro + ks * 64);
    lds_fence();
    if (packer) asm volatile("s_waitcnt vmcnt(8)" ::: "memory");
    else asm volatile("s_waitcnt vmcnt(0)" ::: "memory");
    arrive_flag<63>(L, 0, p.ws + WS_FLAGA + bid * 8, tag, lane);
    STAMP(0, 30);

    const f32x2 p2 = cmul(z128, z128), p3 = cmul(p2, z128), p4 = cmul(p2, p2), p5 = cmul(p4, z128), p6 = cmul(p4, p2), p7 = cmul(p4, p3);
    {
#pragma unroll
      for (int e = 0; e < 4; ++e) {
        *(LAS unsigned*)(B0 + e * KG_ROW + wo) = pk_f16(e == 0 ? p2 : cmul(p2, ze[2 * e]));
        *(LAS unsigned*)(B0 + (4 + e) * KG_ROW + wo) = pk_f16(e == 0 ? p3 : cmul(p3, ze[2 * e]));
      }
      *(LAS unsigned*)(B0 + 8 * KG_ROW + wo) = pk_f16(p4);
      *(LAS unsigned*)(B0 + 9 * KG_ROW + wo) = pk_f16(cmul(p4, ze[4]));
      *(LAS unsigned*)(B0 + 10 * KG_ROW + wo) = pk_f16(p5);
      *(LAS unsigned*)(B0 + 11 * KG_ROW + wo) = pk_f16(cmul(p5, ze[4]));
      *(LAS unsigned*)(B0 + 12 * KG_ROW + wo) = pk_f16(p6);
      *(LAS unsigned*)(B0 + 13 * KG_ROW + wo) = pk_f16(cmul(p6, ze[4]));
      *(LAS unsigned*)(B0 + 14 * KG_ROW + wo) = pk_f16(p7);
      *(LAS unsigned*)(B0 + 15 * KG_ROW + wo) = pk_f16(cmul(p7, ze[4]));
    }
    f32x4 acc0 = {0.f, 0.f, 0.f, 0.f}, acc1 = acc0, acc2 = acc0;
#pragma unroll
    for (int ks = 0; ks < 4; ++ks) acc0 = mfma16h(rf[ks], tf[ks], acc0);
    {
      *(LAS unsigned*)(B1 + 0 * KG_ROW + wo) = pk_f16(z1024);
      *(LAS unsigned*)(B1 + 1 * KG_ROW + wo) = pk_f16(cmul(z1024, z128));
      *(LAS unsigned*)(B1 + 2 * KG_ROW + wo) = pk_f16(cmul(z1024, p2));
      *(LAS unsigned*)(B1 + 3 * KG_ROW + wo) = pk_f16(cmul(z1024, p3));
      *(LAS unsigned*)(B1 + 4 * KG_ROW + wo) = pk_f16(cmul(z1024, p4));
      *(LAS unsigned*)(B1 + 5 * KG_ROW + wo) = pk_f16(cmul(z1024, p5));
      *(LAS unsigned*)(B1 + 6 * KG_ROW + wo) = pk_f16(cmul(z1024, p6));
      *(LAS unsigned*)(B1 + 7 * KG_ROW + wo) = pk_f16(cmul(z1024, p7));
      *(LAS unsigned*)(B1 + 8 * KG_ROW + wo) = pk_f16(cmul(z1024, z1024));
    }
    lds_fence();
#pragma unroll
    for (int ks = 0; ks < 4; ++ks) acc1 = mfma16h(*(const LAS u32x4*)(B0 + ro + ks * 64), tf[ks], acc1);
#pragma unroll
    for (int ks = 0; ks < 4; ++ks) acc2 = mfma16h(*(const LAS u32x4*)(B1 + ro + ks * 64), tf[ks], acc2);
    lds_fence();
    const u32x2 fa = flag_load(rs, fao);
    {
      LAS float* red = (LAS float*)B0;
#pragma unroll
      for (int r = 0; r < 4; ++r) {
        const int row = 4 * q + r;
        red[(row >> 3) * 128 + 8 * r16 + (row & 7)] = acc0[r] * scale;
        const int i1 = row < 8 ? 256 + (row >> 2) * 64 + 4 * r16 + (row & 3) : 384 + ((row - 8) >> 1) * 32 + 2 * r16 + (row & 1);
        red[i1] = acc1[r] * scale;
        const int i2 = row < 9 ? 512 + row * 16 + r16 : 700;
        red[i2] = acc2[r] * scale;
      }
    }
    if (packer) pack_shuffle(lane, pk, L + P0_PK + w * 8192);
    STAMP(0, 21);
    bar_lds();
    const int kc = tid >> 8, kt = tid & 255;
    LAS float* kd = (LAS float*)(L + P0_KG + kc * (8 * KG_BUF) + KG_BUF);
#pragma unroll
    for (int i = 0; i < 3; ++i) {
      const int oo = kt + 256 * i;
      if (i < 2 || oo < 648) {
        const LAS float* s = (const LAS float*)(L + P0_KG + kc * (8 * KG_BUF)) + oo;
        kd[oo] = (s[0] + s[2 * KG_BUF / 4]) + (s[4 * KG_BUF / 4] + s[6 * KG_BUF / 4]);
      }
    }
    if (kt == 0) kd[648] = 0.f;
    const unsigned OFFS = 0x087EDA52u;
    {
      int off = CV_KCOPY + kt * 16;
      if (kt >= 160) { const int k = kt - 160, s = k >> 3, ch = (k & 7) == 7 ? 6 : (k & 7); off = (272 * (s & 7) + ((OFFS >> (4 * (s & 7))) & 15) + 257 + ch) * 16; }
      *(LAS u32x4*)(L + kc * CV_CH + off) = u32x4{0u, 0u, 0u, 0u};
    }
    const u32x2 fv = flag_wait(rs, fao, fa, tlo, gl ? tag32 : thi, gl ? 0u : ~0u);
    STAMP(0, 22);
    const int tb8 = tid >> 1, xc = tid & 1, t8 = tb8 * 8, dch = 2 * dp + xc;
    const int xno = (int)WS_XN + tb8 * (int)XN_BLK + dp * 64 + xc * 16;
    const u32x4 nv = __builtin_amdgcn_raw_buffer_load_b128(rs, xno, 0, AUX_SC1);
    const unsigned sa2 = __builtin_amdgcn_readlane(fv.x, 32), ca2 = __builtin_amdgcn_readlane(fv.x, 33), ga2 = __builtin_amdgcn_readlane(fv.x, 34);
    const float sa = xc ? bf_hi(sa2) : bf_lo(sa2), ca1 = xc ? bf_hi(ca2) : bf_lo(ca2), ga = xc ? bf_hi(ga2) : bf_lo(ga2);
    const float dsk = bf2f(p.dskip[dch]);
    bar_lds();
    LAS unsigned char* base = L + kc * CV_CH;
    u32x4 k0;
    {
      float v[8];
#pragma unroll
      for (int i = 0; i < 8; i += 2) {
        const int l0 = 2048 - (kt * 8 + i), l1 = l0 - 1;
        const unsigned bits = pk_bf16((float)l0, (float)l1);
        int i0 = l0 < 128 ? l0 : (int)(bits & 0xffffu) - 17024;
        const int i1 = l1 < 128 ? l1 : (int)(bits >> 16) - 17024;
        if (i == 0) i0 = kt == 0 ? 648 : i0;
        v[i] = kd[i0];
        v[i + 1] = kd[i1];
      }
      k0 = u32x4{pk_bf16(v[0], v[1]), pk_bf16(v[2], v[3]), pk_bf16(v[4], v[5]), pk_bf16(v[6], v[7])};
      *(LAS u32x4*)(base + (2 + kt) * 16) = k0;
      if (kt < 8) {
        const unsigned k00 = pk_bf16(kd[0], 0.f);
        *(LAS u32x4*)(base + (272 * kt + ((OFFS >> (4 * kt)) & 15) + 256) * 16) = u32x4{kt == 0 ? k00 : 0u, 0u, 0u, 0u};
      }
    }
    bar_lds();
    {
      const u32x4 k1 = *(const LAS u32x4*)(base + (2 + kt + 1) * 16);
      unsigned wv[8] = {k0.x, k0.y, k0.z, k0.w, k1.x, k1.y, k1.z, k1.w};
      unsigned vv[7];
#pragma unroll
      for (int i = 0; i < 7; ++i) vv[i] = __builtin_amdgcn_alignbit(wv[i + 1], wv[i], 16);
#pragma unroll
      for (int s = 1; s < 8; ++s) {
        const int k = s >> 1;
        u32x4 o = (s & 1) ? u32x4{vv[k], vv[k + 1], vv[k + 2], vv[k + 3]} : u32x4{wv[k], wv[k + 1], wv[k + 2], wv[k + 3]};
        *(LAS u32x4*)(base + (272 * s + ((OFFS >> (4 * s)) & 15) + kt) * 16) = o;
      }
    }
    u32x4 hv;
    {
#pragma unroll
      for (int k = 0; k < 4; ++k) {
        const unsigned t = pk_bf16(bf_lo(nv[k]) * ca1, bf_hi(nv[k]) * ca1);
        hv[k] = pk_bf16(bf_lo(t) + sa, bf_hi(t) + sa);
      }
      *(LAS u32x4*)(L + xc * CV_CH + CV_KCOPY + (16 + (t8 >> 6)) * CV_UROW + (t8 & 63) * 2) = hv;
    }
    STAMP(0, 23);
    bar_lds();
    if (bid == 0 && tid == 0) {
      const u64 e = ((u64)ep.y << 32) | ep.x;
      __hip_atomic_store((GAS u64*)(p.ws + WS_EPOCH), e + 1ull, __ATOMIC_RELAXED, __HIP_MEMORY_SCOPE_AGENT);
    }
    const u32x4 xv = __builtin_amdgcn_raw_buffer_load_b128(rs, xno + 32, 0, AUX_SC1);
    ST16(WS_G + (bid * 512 + tid) * 16, ONES);
    if (packer) pack_store(jb, lane, pk, rs);
    SB0();
  {
    const int ch = w >> 2, wq = w & 3;
    f32x4 acc[2][4];
#pragma unroll
    for (int n = 0; n < 2; ++n)
#pragma unroll
      for (int m = 0; m < 4; ++m) acc[n][m] = f32x4{0.f, 0.f, 0.f, 0.f};
    const int s = (8 - r16) & 7, u = (r16 + 7) >> 3;
    const LAS unsigned char* ka = L + ch * CV_CH + (272 * s + ((OFFS >> (4 * s)) & 15) + 256 + q - u - 2 * (16 * wq) - 2 * 79) * 16;
    const LAS unsigned char* ub = L + ch * CV_CH + CV_KCOPY + (16 + r16 - 4 * wq - 3) * CV_UROW + q * 16;
#define KFRAG(f) (*(const LAS u32x4*)(ka + (79 - (f)) * 32))
    u32x4 alo[6], ahi[6];
    alo[0] = KFRAG(-2); alo[1] = KFRAG(-1); ahi[0] = KFRAG(62); ahi[1] = KFRAG(63);
#pragma unroll
    for (int dl = 0; dl < 4; ++dl) {
#pragma unroll
      for (int i = 0; i < 4; ++i) { alo[2 + i] = KFRAG(4 * dl + i); ahi[2 + i] = KFRAG(4 * dl + i + 64); }
#pragma unroll
      for (int ks = 0; ks < 2; ++ks) {
        const u32x4 b0 = *(const LAS u32x4*)(ub + (3 - dl) * CV_UROW + ks * 64);
        const u32x4 b1 = *(const LAS u32x4*)(ub + (19 - dl) * CV_UROW + ks * 64);
#pragma unroll
        for (int m = 0; m < 4; ++m) {
          acc[0][m] = mfma16(alo[m - 2 * ks + 2], b0, acc[0][m]);
          acc[1][m] = mfma16(alo[m - 2 * ks + 2], b1, acc[1][m]);
          acc[1][m] = mfma16(ahi[m - 2 * ks + 2], b0, acc[1][m]);
        }
      }
      alo[0] = alo[4]; alo[1] = alo[5]; ahi[0] = ahi[4]; ahi[1] = ahi[5];
    }
#undef KFRAG

    LAS unsigned char* P = L + CV_P + w * (32 * CV_PROW);
#pragma unroll
    for (int n = 0; n < 2; ++n)
#pragma unroll
      for (int m = 0; m < 4; ++m) *(LAS f32x4*)(P + (16 * n + r16) * CV_PROW + (16 * m + 4 * q) * 4) = acc[n][m];
    if (w == 3) mod_table(L, rs, lane, flag_load(rs, (int)WS_FLAGC + (lane & 31) * 8), tlo, thi);
    asm volatile("s_waitcnt vmcnt(0)" ::: "memory");
  }
  bar_lds();
  STAMP(0, 24);
    {
      const LAS unsigned char* P = L + CV_P + xc * (4 * 32 * CV_PROW) + (t8 >> 6) * CV_PROW + (t8 & 63) * 4;
      f32x4 y0 = {0.f, 0.f, 0.f, 0.f}, y1 = y0;
#pragma unroll
      for (int k = 0; k < 4; ++k) {
        y0 += *(const LAS f32x4*)(P + k * (32 * CV_PROW));
        y1 += *(const LAS f32x4*)(P + k * (32 * CV_PROW) + 16);
      }
      unsigned e[4];
#pragma unroll
      for (int k = 0; k < 4; ++k) {
        const float ya = k < 2 ? y0[2 * k] : y1[2 * k - 4], yb = k < 2 ? y0[2 * k + 1] : y1[2 * k - 3];
        const unsigned yy = pk_bf16(ya, yb), hd = pk_bf16(bf_lo(hv[k]) * dsk, bf_hi(hv[k]) * dsk);
        unsigned t = pk_bf16(bf_lo(yy) + bf_lo(hd), bf_hi(yy) + bf_hi(hd));
        t = pk_bf16(ga * bf_lo(t), ga * bf_hi(t));
        e[k] = pk_bf16(bf_lo(xv[k]) + bf_lo(t), bf_hi(xv[k]) + bf_hi(t));
      }
      const unsigned sx = xc ? e[0] : e[2], sy = xc ? e[1] : e[3];
      const unsigned rx = __builtin_amdgcn_update_dpp(0, (int)sx, 0xB1, 0xf, 0xf, true), ry = __builtin_amdgcn_update_dpp(0, (int)sy, 0xB1, 0xf, 0xf, true);
      const unsigned c0x = xc ? rx : e[0], c0y = xc ? ry : e[1];
      const unsigned c1x = xc ? e[2] : rx, c1y = xc ? e[3] : ry;
      u32x4 out;
      out.x = sane(__builtin_amdgcn_perm(c1x, c0x, 0x05040100u));
      out.y = sane(__builtin_amdgcn_perm(c1x, c0x, 0x07060302u));
      out.z = sane(__builtin_amdgcn_perm(c1y, c0y, 0x05040100u));
      out.w = sane(__builtin_amdgcn_perm(c1y, c0y, 0x07060302u));
      ST16(WS_X2B + (tid >> 3) * (int)X2B_BLK + dp * 128 + (tid & 7) * 16, out);
    }
  } else {
    const int c = bid - 224;
    LAS float* sm = (LAS float*)(L + P0_KG);
    mod_finish(c, tag32, tid, mr, mc, sm, rs);
    if (w == 0) asm volatile("s_waitcnt vmcnt(1)" ::: "memory");
    else asm volatile("s_waitcnt vmcnt(0)" ::: "memory");
    arrive_flag<63>(L, 0, p.ws + WS_FLAGA + bid * 8, tag, lane);
    STAMP(0, 21);
    if (c < 10) {
      mod_load(p, 32 + c, tid, mr, mc);
      bar_lds();
      mod_finish(32 + c, tag32, tid, mr, mc, sm, rs);
    }
    arrive_flag<0>(L, 2, p.ws + WS_FLAGC + c * 8, ~tag, lane);
    STAMP(0, 30);
    flag_wait(rs, fao, flag_load(rs, fao), tlo, thi, ~0u);
    if (w == 3) mod_table(L, rs, lane, flag_load(rs, (int)WS_FLAGC + (lane & 31) * 8), tlo, thi);
    bar_lds();
  }
  STAMP(0, 25);
  if (!cw) {
    gather_x2(p, L, rs, pn, tid);
    STAMP(0, 1);
    bar_lds();
    ln_phase_a(L, j, w, r16, q);
    STAMP(0, 2);
    bar_lds();
    {
      const unsigned char* src = p.ws + WS_WP + (size_t)(j * 7) * NFRAG * 1024 + NRING * 1024 + voff;
#pragma unroll
      for (int ww = 0; ww < 7; ++ww)
#pragma unroll
        for (int i = 0; i < NLDS; ++i)
          __builtin_amdgcn_global_load_lds((const GAS unsigned*)(src + (size_t)ww * NFRAG * 1024 + i * 1024), (LAS unsigned*)(L + G_WL + (ww * NLDS + i) * 1024), 16, 0, AUX_SC1);
    }
    bar_lds();
    STAMP(0, 4);
    bar_lds();
    bar_lds();
    {
      int gof[3];
      u32x4 xr[3];
      x1_offsets(pn, j, w, r16, q, gof);
      poll_load<3>(rs, gof, xr);
      ln_phase_c(L, xr, j, w, r16, q);
    }
    asm volatile("s_waitcnt vmcnt(0)" ::: "memory");
    STAMP(0, 7);
    bar_lds();
    bar_lds();
    return;
  }
  SB0();
  gather_x2(p, L, rs, pn, tid);
  ST16(WS_X1 + (pn * 4 + j) * 7168 + pslot, ONES);
  SB0();
  ring_fill<0, 8>(ring, rw, voff);
  STAMP(0, 1);
  bar_lds();
  u32x2 h2r[2], x2r[2], x1r[2];
  const int ncol = j * 112 + w * 16 + q * 4;
  const u32x2 gbp = __builtin_amdgcn_raw_buffer_load_b64(rs, (int)WS_MOD + (5 * 448 + ncol) * 2, 0, AUX_SC1);
#pragma unroll
  for (int T = 0; T < 2; ++T) {
    const int row = 16 * T + r16;
    x2r[T] = *(const LAS u32x2*)(L + G_X + row * 1024 + (((ncol >> 3) ^ (row >> 2)) * 16) + (ncol & 4) * 2);
  }
  SB0();
  ring_fill<8, 6>(ring, rw, voff);
  ln_phase_a(L, j, w, r16, q);
  SB0();
  ring_fill<14, 8>(ring, rw, voff);
  STAMP(0, 2);
  bar_lds();
#pragma unroll
  for (int T = 0; T < 2; ++T) h2r[T] = *(const LAS u32x2*)(L + G_HQ + (16 * T + r16) * 240 + (w * 16 + q * 4) * 2);
  const LAS unsigned char* ap0 = L + G_T0 + r16 * AS + q * 16;
  const LAS unsigned char* ap1 = L + G_T1 + r16 * AS + q * 16;
  const int tok = (q & 1) * 16 + r16, octc = 2 * w + (q >> 1);
  {
    f32x4 acc[1][2];
    STAMP(0, 16);
    gemm14<1, 0, 22, 7, 2, 5>(ap0, ring, rw, voff, acc);
    STAMP(0, 17);
    const u32x4 own = make_piece(gelu4(acc[0][0]), gelu4(acc[0][1]));
    __builtin_amdgcn_raw_buffer_store_b128(own, rs, (int)WS_G + (pn * 4 + j) * 7168 + pslot, 0, AUX_SC1);
    SB0();
    STAMP(0, 18);
#pragma unroll
    for (int i = 0; i < 3; ++i) ST16(WS_P + ((pn * 4 + j) * 3 + i) * 7168 + pslot, ONES);
    SB0();
    ring_fill<29, 4>(ring, rw, voff);
    int goff[4], loff[4];
    bool wrap0;
#pragma unroll
    for (int i = 0; i < 4; ++i) {
      const int jq = (j + i) & 3;
      const int sh = (jq & 1) ? -1 - (jq >> 1) : 1 + (jq >> 1);
      const int it = tok + sh;
      const int sp = pn + (it >= 32 ? -1 : (it < 0 ? 1 : 0));
      if (i == 0) wrap0 = sp != pn;
      goff[i] = (sp < 0 || sp > 63 || (i == 0 && sp == pn)) ? (int)WS_END : (int)WS_G + (sp * 4 + jq) * 7168 + pslot;
      loff[i] = G_T1 + (it & 31) * AS + (jq * 14 + octc) * 16;
    }
    STAMP(0, 3);
    bar_lds();
    u32x4 v[4];
    poll_issue<4>(rs, goff, v);
    ring_fill<33, 13>(ring, rw, voff);
    poll_finish<4>(rs, goff, v);
    *(LAS u32x4*)(L + loff[0]) = wrap0 ? v[0] : own;
#pragma unroll
    for (int i = 1; i < 4; ++i) *(LAS u32x4*)(L + loff[i]) = v[i];
  }
  STAMP(0, 4);
  bar_lds();
  {
    f32x4 acc[1][2];
    gemm14<1, 14, 46, 7, 2, 5>(ap1, ring, rw, voff, acc);
#pragma unroll
    for (int T = 0; T < 2; ++T) {
      const unsigned a = pk_bf16(acc[0][T][0], acc[0][T][1]), b = pk_bf16(acc[0][T][2], acc[0][T][3]);
      x1r[T].x = pk_bf16(bf_lo(h2r[T].x) + bf_lo(a), bf_hi(h2r[T].x) + bf_hi(a));
      x1r[T].y = pk_bf16(bf_lo(h2r[T].y) + bf_lo(b), bf_hi(h2r[T].y) + bf_hi(b));
    }
    const u32x4 own = make_piece(x1r[0], x1r[1]);
    __builtin_amdgcn_raw_buffer_store_b128(own, rs, (int)WS_X1 + (pn * 4 + j) * 7168 + (w * 64 + (tok >> 2) * 8 + (q >> 1) * 4 + (tok & 3)) * 16, 0, AUX_SC1);
    *(LAS u32x4*)(L + G_HQ + tok * 240 + octc * 16) = own;
    SB0();
    STAMP(0, 19);
    ring_fill<53, 7>(ring, rw, voff);
    int gof[3];
    u32x4 xr[3];
    x1_offsets(pn, j, w, r16, q, gof);
    STAMP(0, 5);
    bar_lds();
    poll_issue<3>(rs, gof, xr);
    ring_fill<60, 8>(ring, rw, voff);
    poll_finish<3>(rs, gof, xr);
    STAMP(0, 6);
    ln_phase_c(L, xr, j, w, r16, q);
  }
  STAMP(0, 7);
  bar_lds();
  {
    f32x4 acc[4][2];
    STAMP(0, 12);
    gemm14<4, 28, RING + 28, 28, 2, 2>(ap0, ring, rw, voff, acc);
    STAMP(0, 13);
#pragma unroll
    for (int i = 0; i < 4; ++i)
#pragma unroll
      for (int T = 0; T < 2; ++T) {
        const int g = RING + 56 + (i * 2 + T) * 2;
        ring[g % RING] = WLOAD(g);
        ring[(g + 1) % RING] = WLOAD(g + 1);
        SB0();
        *(LAS u32x2*)(L + G_T1 + (16 * T + r16) * AS + (w * 64 + i * 16 + q * 4) * 2) = gelu4(acc[i][T]);
        SB0();
      }
  }
  STAMP(0, 8);
  bar_lds();
  {
    {
      f32x4 acc[3][2];
      STAMP(0, 14);
      gemm14<3, 84, RING + 72, NRING - RING - 72, 1, 3>(ap1, ring, rw, voff, acc);
      STAMP(0, 15);
#pragma unroll
      for (int i = 0; i < 3; ++i) {
        const int jd = (j + 1 + i) & 3;
        const u32x4 o = {sane(pk_bf16(acc[i][0][0], acc[i][0][1])), sane(pk_bf16(acc[i][0][2], acc[i][0][3])),
                         sane(pk_bf16(acc[i][1][0], acc[i][1][1])), sane(pk_bf16(acc[i][1][2], acc[i][1][3]))};
        __builtin_amdgcn_raw_buffer_store_b128(o, rs, (int)WS_P + ((pn * 4 + jd) * 3 + (2 - i)) * 7168 + pslot, 0, AUX_SC1);
      }
      SB0();
    }
    STAMP(0, 9);
    f32x4 acc0 = {0.f, 0.f, 0.f, 0.f}, acc1 = acc0;
    {
      const LAS unsigned char* wl = L + G_WL + w * (NLDS * 1024) + voff;
#pragma unroll
      for (int ks = 0; ks < 14; ++ks) {
        const int f = 126 + ks;
        const u32x4 a = f < NRING ? ring[f % RING] : *(const LAS u32x4*)(wl + (f - NRING) * 1024);
        acc0 = mfma16(a, *(const LAS u32x4*)(ap1 + ks * 64), acc0);
        acc1 = mfma16(a, *(const LAS u32x4*)(ap1 + 16 * AS + ks * 64), acc1);
      }
    }
    STAMP(0, 10);
    u32x4 v[3];
    const int go = (int)WS_P + (pn * 4 + j) * 3 * 7168 + pslot;
    const int gof[3] = {go, go + 7168, go + 2 * 7168};
    poll_load<3>(rs, gof, v);
    STAMP(0, 11);
#pragma unroll
    for (int T = 0; T < 2; ++T) {
      const f32x4 ac = T ? acc1 : acc0;
      float hh[4];
#pragma unroll
      for (int k = 0; k < 2; ++k) {
        hh[2 * k] = ac[2 * k] + ((bf_lo(v[0][2 * T + k]) + bf_lo(v[1][2 * T + k])) + bf_lo(v[2][2 * T + k]));
        hh[2 * k + 1] = ac[2 * k + 1] + ((bf_hi(v[0][2 * T + k]) + bf_hi(v[1][2 * T + k])) + bf_hi(v[2][2 * T + k]));
      }
      unsigned a = pk_bf16(hh[0], hh[1]), b = pk_bf16(hh[2], hh[3]);
      a = pk_bf16(bf_lo(x1r[T].x) + bf_lo(a), bf_hi(x1r[T].x) + bf_hi(a));
      b = pk_bf16(bf_lo(x1r[T].y) + bf_lo(b), bf_hi(x1r[T].y) + bf_hi(b));
      a = pk_bf16(bf_lo(gbp.x) * bf_lo(a), bf_hi(gbp.x) * bf_hi(a));
      b = pk_bf16(bf_lo(gbp.y) * bf_lo(b), bf_hi(gbp.y) * bf_hi(b));
      u32x2 o;
      o.x = pk_bf16(bf_lo(x2r[T].x) + bf_lo(a), bf_hi(x2r[T].x) + bf_hi(a));
      o.y = pk_bf16(bf_lo(x2r[T].y) + bf_lo(b), bf_hi(x2r[T].y) + bf_hi(b));
      *(u32x2*)(p.out + (size_t)(pn * 32 + 16 * T + r16) * D_ + ncol) = o;
    }
  }
  STAMP(0, 20);
}

__global__ __launch_bounds__(512, 2) void k_all(GArgs p) {
  extern __shared__ __attribute__((aligned(16))) unsigned char smem[];
  const uintptr_t code0 = (uintptr_t)__builtin_amdgcn_s_getpc() & ~(uintptr_t)127;
  if (blockIdx.x < 224) body<true>(p, (LAS unsigned char*)smem, code0);
  else body<false>(p, (LAS unsigned char*)smem, code0);
}

}

extern "C" void kernel_launch(void* const* d_in, const int* in_sizes, int n_in, void* d_out, int out_size, void* d_ws,
                              size_t ws_size, hipStream_t stream) {
  (void)in_sizes; (void)n_in; (void)out_size; (void)ws_size;
  GArgs a;
  a.x = (const unsigned short*)d_in[0];
  a.a_re = (const unsigned short*)d_in[1];
  a.a_im = (const unsigned short*)d_in[2];
  a.b_re = (const unsigned short*)d_in[3];
  a.b_im = (const unsigned short*)d_in[4];
  a.c_re = (const unsigned short*)d_in[5];
  a.c_im = (const unsigned short*)d_in[6];
  a.log_dt = (const unsigned short*)d_in[7];
  a.dskip = (const unsigned short*)d_in[8];
  a.w1 = (const unsigned short*)d_in[10];
  a.w2 = (const unsigned short*)d_in[11];
  a.w3 = (const unsigned short*)d_in[12];
  a.w4 = (const unsigned short*)d_in[13];
  a.cvec = (const unsigned short*)d_in[14];
  a.wmod = (const unsigned short*)d_in[15];
  a.ws = (unsigned char*)d_ws;
  a.out = (unsigned short*)d_out;
  (void)hipFuncSetAttribute((const void*)k_all, hipFuncAttributeMaxDynamicSharedMemorySize, G_LDS);
  k_all<<<256, 512, G_LDS, stream>>>(a);
}
```

```cpp
#include <hip/hip_runtime.h>
#include <hip/hip_bf16.h>
#include <stdint.h>
#include <type_traits>

namespace {

typedef __attribute__((ext_vector_type(8))) short bf16x8;
typedef __attribute__((ext_vector_type(8))) _Float16 f16x8;
typedef __attribute__((ext_vector_type(2))) _Float16 f16x2;
typedef __attribute__((ext_vector_type(4))) float f32x4;
typedef __attribute__((ext_vector_type(2))) float f32x2;
typedef __attribute__((ext_vector_type(4))) unsigned u32x4;
typedef __attribute__((ext_vector_type(2))) unsigned u32x2;
typedef __attribute__((ext_vector_type(2))) __bf16 bf16x2v;
#define GAS __attribute__((address_space(1)))
#define LAS __attribute__((address_space(3)))

constexpr int L_ = 2048, D_ = 448, NS_ = 256;

constexpr size_t WS_EPOCH = 0;
constexpr size_t WS_FLAGC = 4608;
constexpr size_t WS_MODG = 16384;
constexpr size_t WS_STG = 32768;
constexpr int NFRAG = 140;
constexpr size_t WS_XG = 65536;
constexpr size_t XG_BLK = 224 * 64;
constexpr size_t WS_WP = WS_XG + 256 * XG_BLK;
constexpr size_t WS_X2B = WS_WP + (size_t)28 * NFRAG * 1024;
constexpr size_t X2B_BLK = 224 * 128;
constexpr size_t WS_G = WS_X2B + 64 * X2B_BLK;
constexpr size_t WS_X1 = WS_G + (size_t)64 * 4 * 7168;
constexpr size_t WS_P = WS_X1 + (size_t)64 * 4 * 7168;
constexpr size_t WS_END = WS_P + (size_t)64 * 12 * 7168;

#define STAMP(base, k) do { } while (0)

__device__ __forceinline__ float bf_lo(unsigned u) { return __builtin_bit_cast(float, u << 16); }
__device__ __forceinline__ float bf_hi(unsigned u) { return __builtin_bit_cast(float, u & 0xffff0000u); }
__device__ __forceinline__ float bf2f(unsigned short h) { return __builtin_bit_cast(float, (unsigned)h << 16); }
__device__ __forceinline__ unsigned pk_bf16(float a, float b) {
  f32x2 v = {a, b};
  bf16x2v r = __builtin_convertvector(v, bf16x2v);
  return __builtin_bit_cast(unsigned, r);
}
__device__ __forceinline__ unsigned pk_f16(f32x2 v) {
  f16x2 r = __builtin_convertvector(v, f16x2);
  return __builtin_bit_cast(unsigned, r);
}
__device__ __forceinline__ float rbf(float a) { return bf_lo(pk_bf16(a, a)); }
__device__ __forceinline__ f32x4 mfma16(u32x4 a, u32x4 b, f32x4 c) {
  return __builtin_amdgcn_mfma_f32_16x16x32_bf16(__builtin_bit_cast(bf16x8, a), __builtin_bit_cast(bf16x8, b), c, 0, 0, 0);
}
__device__ __forceinline__ f32x4 mfma16h(u32x4 a, u32x4 b, f32x4 c) {
  return __builtin_amdgcn_mfma_f32_16x16x32_f16(__builtin_bit_cast(f16x8, a), __builtin_bit_cast(f16x8, b), c, 0, 0, 0);
}
template <int CTRL>
__device__ __forceinline__ float dpp_f(float v) {
  return __builtin_bit_cast(float, __builtin_amdgcn_update_dpp(0, __builtin_bit_cast(int, v), CTRL, 0xf, 0xf, true));
}
__device__ __forceinline__ float row16_sum(float v) {
  v += dpp_f<0xB1>(v);
  v += dpp_f<0x4E>(v);
  v += dpp_f<0x141>(v);
  v += dpp_f<0x140>(v);
  return v;
}
__device__ __forceinline__ float row16_max(float v) {
  v = fmaxf(v, dpp_f<0xB1>(v));
  v = fmaxf(v, dpp_f<0x4E>(v));
  v = fmaxf(v, dpp_f<0x141>(v));
  v = fmaxf(v, dpp_f<0x140>(v));
  return v;
}
__device__ __forceinline__ void swap16(float v, float& a, float& b) {
  const unsigned u = __builtin_bit_cast(unsigned, v), u2 = u;
  auto r = __builtin_amdgcn_permlane16_swap(u, u2, false, false);
  const unsigned r0 = r[0], r1 = r[1];
  a = __builtin_bit_cast(float, r0); b = __builtin_bit_cast(float, r1);
}
__device__ __forceinline__ void swap32(float v, float& a, float& b) {
  const unsigned u = __builtin_bit_cast(unsigned, v), u2 = u;
  auto r = __builtin_amdgcn_permlane32_swap(u, u2, false, false);
  const unsigned r0 = r[0], r1 = r[1];
  a = __builtin_bit_cast(float, r0); b = __builtin_bit_cast(float, r1);
}
__device__ __forceinline__ float rows_sum(float v) {
  float a, b;
  swap16(v, a, b); v = a + b;
  swap32(v, a, b);
  return a + b;
}
__device__ __forceinline__ float rows_max(float v) {
  float a, b;
  swap16(v, a, b); v = fmaxf(a, b);
  swap32(v, a, b);
  return fmaxf(a, b);
}
template <int CTRL, int RMASK>
__device__ __forceinline__ float dpp_z(float v) {
  return __builtin_bit_cast(float, __builtin_amdgcn_update_dpp(0, __builtin_bit_cast(int, v), CTRL, RMASK, 0xf, false));
}
__device__ __forceinline__ float wave_scan(float v) {
  v += dpp_z<0x111, 0xf>(v);
  v += dpp_z<0x112, 0xf>(v);
  v += dpp_z<0x114, 0xf>(v);
  v += dpp_z<0x118, 0xf>(v);
  v += dpp_z<0x142, 0xa>(v);
  v += dpp_z<0x143, 0xc>(v);
  return v;
}
__device__ __forceinline__ float wave_sum(float v) { return rows_sum(row16_sum(v)); }
__device__ __forceinline__ float gelu_tanh(float x) {
  const float c1 = -2.0f * 0.7978845608028654f * 1.4426950408889634f;
  const float c2 = c1 * 0.044715f;
  float t = x * (c1 + c2 * x * x);
  float e = __builtin_amdgcn_exp2f(t);
  return x * __builtin_amdgcn_rcpf(1.0f + e);
}
__device__ __forceinline__ float ln_sd(float var) { return rbf(__builtin_amdgcn_sqrtf(rbf(rbf(var) + 1e-5f))); }
__device__ __forceinline__ void lds_fence() { asm volatile("" ::: "memory"); }
__device__ __forceinline__ void bar_lds() {
  asm volatile("s_waitcnt lgkmcnt(0)" ::: "memory");
  __builtin_amdgcn_s_barrier();
  asm volatile("" ::: "memory");
}

__device__ __forceinline__ void transpose8x8(const u32x4 (&r)[8], u32x4 (&o)[8]) {
#pragma unroll
  for (int c2 = 0; c2 < 4; ++c2) {
#pragma unroll
    for (int rp = 0; rp < 4; ++rp) {
      unsigned a = r[2 * rp][c2], b = r[2 * rp + 1][c2];
      o[2 * c2][rp] = __builtin_amdgcn_perm(b, a, 0x05040100u);
      o[2 * c2 + 1][rp] = __builtin_amdgcn_perm(b, a, 0x07060302u);
    }
  }
}
__device__ __forceinline__ f32x2 cmul(f32x2 a, f32x2 b) {
  f32x2 r, t;
  asm("v_pk_mul_f32 %0, %1, %2 op_sel_hi:[0,1]" : "=v"(t) : "v"(a), "v"(b));
  asm("v_pk_fma_f32 %0, %1, %2, %3 op_sel:[1,1,0] op_sel_hi:[1,0,1] neg_lo:[1,0,0]" : "=v"(r) : "v"(a), "v"(b), "v"(t));
  return r;
}
__device__ __forceinline__ f32x2 cexp_tab(float a, float b, float l) {
  const float e = __builtin_amdgcn_exp2f(a * l), ph = __builtin_amdgcn_fractf(b * l);
  f32x2 r;
  r.x = e * __builtin_amdgcn_cosf(ph);
  r.y = e * __builtin_amdgcn_sinf(ph);
  return r;
}

constexpr int CV_KCOPY = 8 * 272 * 16;
constexpr int CV_UROW = 160;
constexpr int CV_U = 48 * CV_UROW;
constexpr int CV_CH = CV_KCOPY + CV_U;
constexpr int CV_LDS = 2 * CV_CH;
constexpr int CV_PROW = 272;
static_assert(4 * 32 * CV_PROW <= CV_KCOPY, "partials alias the kernel copies");

struct GArgs {
  const unsigned short* x;
  const unsigned short* a_re;
  const unsigned short* a_im;
  const unsigned short* b_re;
  const unsigned short* b_im;
  const unsigned short* c_re;
  const unsigned short* c_im;
  const unsigned short* log_dt;
  const unsigned short* dskip;
  const unsigned short* w1;
  const unsigned short* w2;
  const unsigned short* w3;
  const unsigned short* w4;
  const unsigned short* cvec;
  const unsigned short* wmod;
  unsigned char* ws;
  unsigned short* out;
};
constexpr int AS = 928;
constexpr int G_T0 = 0;
constexpr int G_T1 = G_T0 + 32 * AS;
constexpr int G_X = G_T1;
constexpr int G_HQ = G_X + 32768;
constexpr int G_WL = G_HQ + 32 * 240;
constexpr int NLDS = 12;
constexpr int NRING = NFRAG - NLDS;
constexpr int G_MOD = G_WL + 7 * NLDS * 1024;
constexpr int G_CNT = G_MOD + 2 * 448 * 4;
constexpr int P0_MS = G_CNT + 16;
constexpr int G_LDS = P0_MS + 256;
constexpr int KG_ROW = 288;
constexpr int KG_BUF = 16 * KG_ROW;
constexpr int P0_KG = CV_LDS;
constexpr int P0_LN = 0;
constexpr int LNS = 928;
constexpr int P0_PK = 16384;
static_assert(P0_KG + 16 * KG_BUF <= G_CNT, "tables");
static_assert(G_MOD >= CV_LDS && G_LDS <= 163840, "LDS map");
constexpr int RING = 40;
constexpr int RINGB = 16;
constexpr int RING0 = RINGB + 6;
constexpr int CV_P = CV_LDS;
static_assert(CV_P + 8 * 32 * CV_PROW <= G_MOD, "partials");
constexpr unsigned SPIN_CAP = 1u << 14;
constexpr int AUX_SC1 = 16;
constexpr int CODE_LINES = 480;
#define PRETOUCH 1
#define X2_DELAY 16
#define POLL_SLEEP 6

__device__ __forceinline__ unsigned sane(unsigned v) { return v < 0xFFFFFFFEu ? v : 0xFFFFFFFEu; }
__device__ __forceinline__ unsigned umax4(const u32x4& v) { return max(max(v.x, v.y), max(v.z, v.w)); }
__device__ __forceinline__ bool bad4(const u32x4& v) { return umax4(v) == ~0u; }
__device__ __forceinline__ u32x4 make_piece(u32x2 a, u32x2 b) {
  auto rx = __builtin_amdgcn_permlane16_swap(a.x, b.x, false, false);
  auto ry = __builtin_amdgcn_permlane16_swap(a.y, b.y, false, false);
  return u32x4{sane(rx[0]), sane(ry[0]), sane(rx[1]), sane(ry[1])};
}
template <int N>
__device__ __forceinline__ void poll_issue(__amdgpu_buffer_rsrc_t r, const int (&off)[N], u32x4 (&v)[N]) {
#pragma unroll
  for (int i = 0; i < N; ++i) v[i] = __builtin_amdgcn_raw_buffer_load_b128(r, off[i], 0, AUX_SC1);
  __builtin_amdgcn_sched_barrier(0);
}
template <int N>
__device__ __forceinline__ void poll_finish(__amdgpu_buffer_rsrc_t r, const int (&off)[N], u32x4 (&v)[N]) {
  unsigned m = 0u;
#pragma unroll
  for (int i = 0; i < N; ++i) m = max(m, umax4(v[i]));
  if (__builtin_expect(__any(m == ~0u), 0)) {
    for (unsigned spins = 0; spins < SPIN_CAP; ++spins) {
      __builtin_amdgcn_s_sleep(POLL_SLEEP);
      asm volatile("" ::: "memory");
#pragma unroll
      for (int i = 0; i < N; ++i)
        if (bad4(v[i])) v[i] = __builtin_amdgcn_raw_buffer_load_b128(r, off[i], 0, AUX_SC1);
      m = 0u;
#pragma unroll
      for (int i = 0; i < N; ++i) m = max(m, umax4(v[i]));
      if (!__any(m == ~0u)) break;
    }
  }
}
template <int N>
__device__ __forceinline__ void poll_load(__amdgpu_buffer_rsrc_t r, const int (&off)[N], u32x4 (&v)[N]) {
  poll_issue<N>(r, off, v);
  poll_finish<N>(r, off, v);
}
__device__ __forceinline__ float dot2bf(unsigned a, unsigned b, float c) {
  return __builtin_amdgcn_fdot2_f32_bf16(__builtin_bit_cast(bf16x2v, a), __builtin_bit_cast(bf16x2v, b), c, false);
}
__device__ __forceinline__ void ln_stats(const unsigned (&x)[16], float lmsk, float& r, float& mr) {
  float s0 = 0.f, s1 = 0.f, q0 = 0.f, q1 = 0.f;
#pragma unroll
  for (int k = 0; k < 16; k += 2) {
    s0 = dot2bf(x[k], 0x3f803f80u, s0); q0 = dot2bf(x[k], x[k], q0);
    s1 = dot2bf(x[k + 1], 0x3f803f80u, s1); q1 = dot2bf(x[k + 1], x[k + 1], q1);
  }
  const float mean = row16_sum((s0 + s1) * lmsk) * (1.0f / D_);
  const float msq = row16_sum((q0 + q1) * lmsk) * (1.0f / D_);
  const float var = fmaxf(msq - mean * mean, 0.f);
  r = __builtin_amdgcn_rcpf(ln_sd(var));
  mr = -rbf(mean) * r;
}

#define SB0() __builtin_amdgcn_sched_barrier(0)
#define WLOAD(f) __builtin_amdgcn_raw_buffer_load_b128(rw, voff + ((f) & 3) * 1024, ((f) >> 2) * 4096, AUX_SC1)
template <int NT, int F0, int LSTART, int LNUM, int LEVERY, int PD>
__device__ __forceinline__ void gemm14(const LAS unsigned char* ap, u32x4 (&ring)[RING], __amdgpu_buffer_rsrc_t rw, int voff, f32x4 (&acc)[NT][2]) {
  u32x4 bq[PD][2];
#pragma unroll
  for (int i = 0; i < PD; ++i) { bq[i][0] = *(const LAS u32x4*)(ap + i * 64); bq[i][1] = *(const LAS u32x4*)(ap + 16 * AS + i * 64); }
#pragma unroll
  for (int i = 0; i < NT; ++i) { acc[i][0] = f32x4{0.f, 0.f, 0.f, 0.f}; acc[i][1] = acc[i][0]; }
  SB0();
#pragma unroll
  for (int ks = 0; ks < 14; ++ks) {
#pragma unroll
    for (int i = 0; i < NT; ++i) {
      const int st = ks * NT + i, f = F0 + st;
      acc[i][0] = mfma16(ring[f % RING], bq[ks % PD][0], acc[i][0]);
      acc[i][1] = mfma16(ring[f % RING], bq[ks % PD][1], acc[i][1]);
      if (st % LEVERY == 0 && st / LEVERY < LNUM) {
        const int g = LSTART + st / LEVERY;
        ring[g % RING] = WLOAD(g);
      }
      if (i == NT - 1 && ks + PD < 14) {
        bq[ks % PD][0] = *(const LAS u32x4*)(ap + (ks + PD) * 64);
        bq[ks % PD][1] = *(const LAS u32x4*)(ap + 16 * AS + (ks + PD) * 64);
      }
      SB0();
    }
  }
}
template <int F0, int N>
__device__ __forceinline__ void ring_fill(u32x4 (&ring)[RING], __amdgpu_buffer_rsrc_t rw, int voff) {
#pragma unroll
  for (int f = F0; f < F0 + N; ++f) { ring[f % RING] = WLOAD(f); SB0(); }
}

template <int DELAY>
__device__ __forceinline__ void gather_x2(const GArgs& p, LAS unsigned char* L, __amdgpu_buffer_rsrc_t rs, int pn, int tid) {
  const int lane = tid & 63, w = tid >> 6; (void)lane; (void)w; (void)p;
  u32x4 xin[4];
  int xo[4];
#pragma unroll
  for (int i = 0; i < 4; ++i) {
    const int pce = tid + 512 * i;
    xo[i] = pce < 1792 ? (int)WS_X2B + pn * (int)X2B_BLK + pce * 16 : (int)WS_END;
  }
  if (DELAY) __builtin_amdgcn_s_sleep(DELAY);
  STAMP(0, 27);
  poll_issue<4>(rs, xo, xin);
  poll_finish<4>(rs, xo, xin);
  STAMP(0, 26);
  bar_lds();
#pragma unroll
  for (int i = 0; i < 4; ++i) {
    const int pce = tid + 512 * i;
    if (i < 3 || pce < 1792) {
      const int dpi = pce >> 3, tq = pce & 7;
      LAS unsigned char* dst = L + G_X + tq * 4096 + (((dpi >> 2) ^ tq) * 16) + (dpi & 3) * 4;
#pragma unroll
      for (int k = 0; k < 4; ++k) *(LAS unsigned*)(dst + k * 1024) = xin[i][k];
    }
  }
}
template <class H>
__device__ __forceinline__ void ln_phase_a(LAS unsigned char* L, int j, int w, int r16, int q, H hook) {
  const int c16 = r16 < 14 ? r16 : 0;
  const float lmsk = r16 < 14 ? 1.0f : 0.0f;
  const int row = 4 * w + q;
  unsigned x[16];
#pragma unroll
  for (int jq = 0; jq < 4; ++jq) {
    const u32x4 t = *(const LAS u32x4*)(L + G_X + row * 1024 + (((((jq + j) & 3) * 14 + c16) ^ w) * 16));
    x[4 * jq] = t.x; x[4 * jq + 1] = t.y; x[4 * jq + 2] = t.z; x[4 * jq + 3] = t.w;
  }
  float r, mr;
  ln_stats(x, lmsk, r, mr);
  hook(std::integral_constant<int, 0>{});
  f32x2 h[16];
#pragma unroll
  for (int jq = 0; jq < 4; ++jq) {
    const LAS unsigned char* mp = L + G_MOD + (((jq + j) & 3) * 112 + c16 * 8) * 4;
    const f32x4 sb0 = *(const LAS f32x4*)mp, sb1 = *(const LAS f32x4*)(mp + 16);
    const f32x4 c10 = *(const LAS f32x4*)(mp + 1792), c11 = *(const LAS f32x4*)(mp + 1792 + 16);
#pragma unroll
    for (int k = 0; k < 4; ++k) {
      const f32x2 xv2 = {bf_lo(x[4 * jq + k]), bf_hi(x[4 * jq + k])};
      const f32x2 c2 = k < 2 ? f32x2{c10[2 * k], c10[2 * k + 1]} : f32x2{c11[2 * k - 4], c11[2 * k - 3]};
      const f32x2 sb2 = k < 2 ? f32x2{sb0[2 * k], sb0[2 * k + 1]} : f32x2{sb1[2 * k - 4], sb1[2 * k - 3]};
      const f32x2 n = xv2 * r + mr;
      h[4 * jq + k] = n * c2 + sb2;
      x[4 * jq + k] = pk_bf16(h[4 * jq + k].x, h[4 * jq + k].y);
    }
  }
  *(LAS u32x4*)(L + G_HQ + row * 240 + (r16 < 14 ? r16 : 14) * 16) = u32x4{x[0], x[1], x[2], x[3]};
  hook(std::integral_constant<int, 1>{});
  ln_stats(x, lmsk, r, mr);
  hook(std::integral_constant<int, 2>{});
#pragma unroll
  for (int jq = 0; jq < 4; ++jq) {
    u32x4 o;
#pragma unroll
    for (int k = 0; k < 4; ++k) {
      const f32x2 n = h[4 * jq + k] * r + mr;
      o[k] = pk_bf16(n.x, n.y);
    }
    *(LAS u32x4*)(L + G_T0 + row * AS + (r16 < 14 ? (((jq + j) & 3) * 14 + r16) * 16 : 896)) = o;
  }
}
__device__ __forceinline__ void x1_offsets(int pn, int j, int w, int r16, int q, int (&gof)[3]) {
  const int c16 = r16 < 14 ? r16 : 0;
  const int go = (int)WS_X1 + pn * 4 * 7168 + ((c16 >> 1) * 64 + w * 8 + (c16 & 1) * 4 + q) * 16;
#pragma unroll
  for (int i = 0; i < 3; ++i) gof[i] = go + ((j + 1 + i) & 3) * 7168;
}
__device__ __forceinline__ void ln_phase_c(LAS unsigned char* L, const u32x4 (&xr)[3], int j, int w, int r16, int q) {
  const int c16 = r16 < 14 ? r16 : 0;
  const float lmsk = r16 < 14 ? 1.0f : 0.0f;
  const int row = 4 * w + q;
  const u32x4 own = *(const LAS u32x4*)(L + G_HQ + row * 240 + c16 * 16);
  unsigned x[16];
#pragma unroll
  for (int k = 0; k < 4; ++k) x[k] = own[k];
#pragma unroll
  for (int k = 4; k < 16; ++k) x[k] = xr[(k >> 2) - 1][k & 3];
  float r, mr;
  ln_stats(x, lmsk, r, mr);
#pragma unroll
  for (int jq = 0; jq < 4; ++jq) {
    u32x4 o;
#pragma unroll
    for (int k = 0; k < 4; ++k) {
      const f32x2 n = f32x2{bf_lo(x[4 * jq + k]), bf_hi(x[4 * jq + k])} * r + mr;
      o[k] = pk_bf16(n.x, n.y);
    }
    *(LAS u32x4*)(L + G_T0 + row * AS + (r16 < 14 ? (((jq + j) & 3) * 14 + r16) * 16 : 896)) = o;
  }
}
__device__ __forceinline__ u32x2 gelu4(f32x4 a) {
  const unsigned u = pk_bf16(a[0], a[1]), v = pk_bf16(a[2], a[3]);
  u32x2 o;
  o.x = pk_bf16(gelu_tanh(bf_lo(u)), gelu_tanh(bf_hi(u)));
  o.y = pk_bf16(gelu_tanh(bf_lo(v)), gelu_tanh(bf_hi(v)));
  return o;
}

#define ST16(off, v) __builtin_amdgcn_raw_buffer_store_b128(v, rs, (int)(off), 0, AUX_SC1)
typedef __attribute__((ext_vector_type(4))) short s16x4;
typedef unsigned long long u64;

struct PackJob { const GAS unsigned short* src; int ldn, mat, ks, cb, na, nb, fb; };
__device__ __forceinline__ PackJob pack_job(const GArgs& p, int g) {
  PackJob jb;
  const uintptr_t a1 = (uintptr_t)p.w1, a2 = (uintptr_t)p.w2, a3 = (uintptr_t)p.w3, a4 = (uintptr_t)p.w4;
  uintptr_t lo = g < 56 ? a1 : a2, hi = g < 336 ? a3 : a4;
  asm volatile("" : "+s"(lo), "+s"(hi));
  jb.src = (const GAS unsigned short*)(g < 112 ? lo : hi);
  jb.mat = (g >= 56) + (g >= 112) + (g >= 336);
  jb.ldn = jb.mat == 2 ? 1792 : 448;
  g -= g < 112 ? (g < 56 ? 0 : 56) : (g < 336 ? 112 : 336);
  const int sh = jb.mat == 2 ? 4 : 2;
  jb.ks = g >> sh;
  jb.cb = g & ((1 << sh) - 1);
  const int j3 = (jb.ks * 4682) >> 16, ksl = jb.ks - j3 * 14, sl = (jb.cb - j3 - 1) & 3;
  const int is2 = jb.mat == 2, is3 = jb.mat == 3;
  jb.na = is2 * jb.cb * 7 + (1 - is2) * (is3 * j3 + (1 - is3) * jb.cb) * 28;
  jb.nb = 4 - 3 * is2;
  const int own = sl == 3;
  const int f3 = (1 - own) * (84 + ksl * 3 + sl) + own * (126 + ksl);
  jb.fb = is2 * (28 + jb.ks * 4) + is3 * f3 + (1 - is2 - is3) * (jb.mat * 14 + jb.ks);
  jb.na = __builtin_amdgcn_readfirstlane(jb.na); jb.nb = __builtin_amdgcn_readfirstlane(jb.nb); jb.fb = __builtin_amdgcn_readfirstlane(jb.fb);
  return jb;
}
__device__ __forceinline__ void pack_load(const PackJob& jb, int lane, u32x4 (&r)[8]) {
  const int jn = lane & 15, q = lane >> 4, jl = jn < 14 ? jn : 0;
#pragma unroll
  for (int i = 0; i < 8; ++i) r[i] = *(const GAS u32x4*)(jb.src + (size_t)(jb.ks * 32 + q * 8 + i) * jb.ldn + jb.cb * 112 + jl * 8);
}
__device__ __forceinline__ void pack_shuffle(int lane, u32x4 (&r)[8], LAS unsigned char* Lw) {
  const int jn = lane & 15, q = lane >> 4;
  u32x4 o[8];
  transpose8x8(r, o);
#pragma unroll
  for (int i = 0; i < 8; ++i) *(LAS u32x4*)(Lw + (jn >> 1) * 1024 + (q * 16 + (jn & 1) * 8 + (i ^ (jn >> 1))) * 16) = o[i];
  lds_fence();
#pragma unroll
  for (int m = 0; m < 7; ++m) r[m] = *(const LAS u32x4*)(Lw + m * 1024 + (lane ^ m) * 16);
  lds_fence();
}
__device__ __forceinline__ void pack_store1(const PackJob& jb, int lane, const u32x4 (&r)[8], __amdgpu_buffer_rsrc_t rs, int m) {
  const int nt = jb.na + m * jb.nb;
  __builtin_amdgcn_raw_buffer_store_b128(r[m], rs, lane * 16, (int)WS_WP + ((nt >> 2) * NFRAG + jb.fb + (nt & 3)) * 1024, AUX_SC1);
}
__device__ __forceinline__ void mod_load(const GArgs& p, int u, int tid, u32x4 (&r)[7], float (&sc)[7]) {
  const int rg = tid >> 3, pc = tid & 7;
#pragma unroll
  for (int i = 0; i < 7; ++i) r[i] = *(const u32x4*)(p.wmod + (size_t)(rg + 64 * i) * 2688 + u * 64 + pc * 8);
#pragma unroll
  for (int i = 0; i < 7; ++i) sc[i] = bf2f(p.cvec[rg + 64 * i]);
}
__device__ __forceinline__ void mod_finish(int u, unsigned tag32, int tid, const u32x4 (&r)[7], const float (&cv)[7], LAS float* sm, __amdgpu_buffer_rsrc_t rs) {
  const int rg = tid >> 3, pc = tid & 7;
  float acc[8];
#pragma unroll
  for (int k = 0; k < 8; ++k) acc[k] = 0.f;
#pragma unroll
  for (int i = 0; i < 7; ++i) {
    const float sc = rbf(cv[i] * __builtin_amdgcn_rcpf(1.0f + __builtin_amdgcn_exp2f(-1.4426950408889634f * cv[i])));
#pragma unroll
    for (int k = 0; k < 4; ++k) { acc[2 * k] += sc * bf_lo(r[i][k]); acc[2 * k + 1] += sc * bf_hi(r[i][k]); }
  }
  *(LAS f32x4*)(sm + rg * 68 + pc * 8) = f32x4{acc[0], acc[1], acc[2], acc[3]};
  *(LAS f32x4*)(sm + rg * 68 + pc * 8 + 4) = f32x4{acc[4], acc[5], acc[6], acc[7]};
  bar_lds();
  {
    const int col = tid & 63, part = tid >> 6;
    float s = 0.f;
#pragma unroll
    for (int k = 0; k < 8; ++k) s += sm[(part * 8 + k) * 68 + col];
    sm[64 * 68 + part * 64 + col] = s;
  }
  bar_lds();
  if (tid < 32) {
    f32x2 s = {0.f, 0.f};
#pragma unroll
    for (int k = 0; k < 8; ++k) s += *(const LAS f32x2*)(sm + 64 * 68 + k * 64 + 2 * tid);
    unsigned v = pk_bf16(s.x, s.y);
    const int sec = u / 7;
    if (sec == 1 || sec == 4) v = pk_bf16(bf_lo(v) + 1.0f, bf_hi(v) + 1.0f);
    __builtin_amdgcn_raw_buffer_store_b64(u32x2{v, tag32}, rs, (int)WS_MODG + (u * 32 + tid) * 8, 0, AUX_SC1);
  }
}
template <int YOUNGER>
__device__ __forceinline__ void arrive_flag(LAS unsigned char* L, int cnt_slot, unsigned char* flag, u64 tag, int lane) {
  asm volatile("s_waitcnt vmcnt(%0)" ::"i"(YOUNGER) : "memory");
  if (lane == 0) {
    const unsigned old = __hip_atomic_fetch_add((LAS unsigned*)(L + G_CNT) + cnt_slot, 1u, __ATOMIC_RELAXED, __HIP_MEMORY_SCOPE_WORKGROUP);
    if (old == 7u) __hip_atomic_store((GAS u64*)flag, tag, __ATOMIC_RELAXED, __HIP_MEMORY_SCOPE_AGENT);
  }
}
__device__ __forceinline__ u32x2 flag_load(__amdgpu_buffer_rsrc_t rs, int off) { return __builtin_amdgcn_raw_buffer_load_b64(rs, off, 0, AUX_SC1); }
__device__ __forceinline__ u32x2 flag_wait(__amdgpu_buffer_rsrc_t rs, int off, u32x2 v, unsigned lo, unsigned hi, unsigned lomask) {
  if (__builtin_expect(__any((((v.x ^ lo) & lomask) | (v.y ^ hi)) != 0u), 0)) {
    for (unsigned spins = 0; spins < SPIN_CAP; ++spins) {
      __builtin_amdgcn_s_sleep(2);
      asm volatile("" ::: "memory");
      v = flag_load(rs, off);
      if (!__any((((v.x ^ lo) & lomask) | (v.y ^ hi)) != 0u)) break;
    }
  }
  __builtin_amdgcn_fence(__ATOMIC_ACQUIRE, "wavefront");
  return v;
}
__device__ __forceinline__ void stat_wait(__amdgpu_buffer_rsrc_t rs, int off, u32x4& a, u32x4& b, unsigned tag32) {
  if (__builtin_expect(__any((((a.y ^ tag32) | (a.w ^ tag32)) | ((b.y ^ tag32) | (b.w ^ tag32))) != 0u), 0)) {
    for (unsigned spins = 0; spins < SPIN_CAP; ++spins) {
      __builtin_amdgcn_s_sleep(2);
      asm volatile("" ::: "memory");
      a = __builtin_amdgcn_raw_buffer_load_b128(rs, off, 0, AUX_SC1);
      b = __builtin_amdgcn_raw_buffer_load_b128(rs, off + 16, 0, AUX_SC1);
      if (!__any((((a.y ^ tag32) | (a.w ^ tag32)) | ((b.y ^ tag32) | (b.w ^ tag32))) != 0u)) break;
    }
  }
}
#define FILL_P(i) do { SB0(); __builtin_amdgcn_raw_buffer_store_b128(ONES, rst, (int)WS_P + ((pn * 4 + j) * 3 + (i)) * 7168 + pslot, 0, AUX_SC1); SB0(); } while (0)
#define FILL_X1() do { SB0(); __builtin_amdgcn_raw_buffer_store_b128(ONES, rst, (int)WS_X1 + (pn * 4 + j) * 7168 + pslot, 0, AUX_SC1); SB0(); } while (0)
template <bool CONV>
__device__ __forceinline__ void body(const GArgs& p, LAS unsigned char* L, uintptr_t code0) {
  const int tid = threadIdx.x, lane = tid & 63;
  const int w = __builtin_amdgcn_readfirstlane(tid >> 6);
  const int bid = blockIdx.x;
  const int j = bid & 3, pn = (bid >> 3) * 2 + ((bid >> 2) & 1);
  const int r16 = lane & 15, q = lane >> 4;
  const bool cw = w < 7;
  const int wc = cw ? w : 6;
  const int dp = (bid & 7) * 28 + (bid >> 3);
  const __amdgpu_buffer_rsrc_t rs = __builtin_amdgcn_make_buffer_rsrc((void*)p.ws, 0, (int)WS_END, 0x00020000);
  const __amdgpu_buffer_rsrc_t rst = __builtin_amdgcn_make_buffer_rsrc((void*)p.ws, 0, cw ? (int)WS_END : 0, 0x00020000);
  const __amdgpu_buffer_rsrc_t rw =
      __builtin_amdgcn_make_buffer_rsrc((void*)(p.ws + WS_WP + (size_t)(j * 7 + wc) * NFRAG * 1024), 0, cw ? NFRAG * 1024 : 0, 0x00020000);
  const int voff = lane * 16;
  const int pslot = (wc * 64 + lane) * 16;
  const u32x4 ONES = {~0u, ~0u, ~0u, ~0u};
  STAMP(0, 0);
  const int l56 = lane < 56 ? lane : 0;
  const u32x4 xrow = *(const u32x4*)(p.x + (size_t)(bid * 8 + w) * D_ + l56 * 8);
  const u32x2 ep = flag_load(rs, (int)WS_EPOCH);
  const int cl = (bid >> 3) * 16 + (lane & 15);
  const unsigned touch = *(const GAS unsigned*)(code0 + (size_t)(cl < CODE_LINES ? cl : 0) * 128);
  float are, aim, b_re, b_im, cre, cim, ldt;
  u32x4 mr[7], pk[8];
  float mc[7];
  const bool packer = CONV ? w < 2 : (w < 4 && bid < 252);
  const PackJob jb = pack_job(p, CONV ? bid * 2 + (w & 1) : (packer ? 448 + (bid - 224) * 4 + w : 0));
  if (CONV) {
    const size_t o = (size_t)(2 * dp + (w >> 2)) * NS_ + (w & 3) * 64 + lane;
    SB0();
    are = bf2f(p.a_re[o]); aim = bf2f(p.a_im[o]); b_re = bf2f(p.b_re[o]); b_im = bf2f(p.b_im[o]);
    cre = bf2f(p.c_re[o]); cim = bf2f(p.c_im[o]); ldt = bf2f(p.log_dt[2 * dp + (w >> 2)]);
    SB0();
  }
  __builtin_amdgcn_s_barrier();
  __builtin_amdgcn_raw_buffer_store_b128(ONES, rst, (int)WS_X2B + bid * 7168 + pslot, 0, AUX_SC1);
  __builtin_amdgcn_raw_buffer_store_b128(ONES, rst, (int)WS_G + bid * 7168 + pslot, 0, AUX_SC1);
  SB0();
  if (tid < 4) *((LAS unsigned*)(L + G_CNT) + tid) = 0u;
  u64 tag;
  {
    const u64 e = ((u64)__builtin_amdgcn_readfirstlane(ep.y) << 32) | (unsigned)__builtin_amdgcn_readfirstlane(ep.x);
    u64 z = e + 0x9E3779B97F4A7C15ull;
    z = (z ^ (z >> 30)) * 0xBF58476D1CE4E5B9ull;
    z = (z ^ (z >> 27)) * 0x94D049BB133111EBull;
    tag = z ^ (z >> 31);
  }
  const unsigned tag32 = ((unsigned)__builtin_amdgcn_readfirstlane(ep.x) + 1u) * 0x9E3779B1u ^ 0x5bd1e995u;
  const unsigned tlo = (unsigned)tag, thi = (unsigned)(tag >> 32);
  unsigned stv;
#define PUT_STAT() do { if (lane == 0) __builtin_amdgcn_raw_buffer_store_b64(u32x2{stv, tag32}, rs, (int)WS_STG + (bid * 8 + w) * 8, 0, AUX_SC1); } while (0)
  {
    const float msk = lane < 56 ? 1.0f : 0.0f;
    float v[8];
#pragma unroll
    for (int k = 0; k < 4; ++k) { v[2 * k] = bf_lo(xrow[k]); v[2 * k + 1] = bf_hi(xrow[k]); }
    const float mean = wave_sum((((v[0] + v[1]) + (v[2] + v[3])) + ((v[4] + v[5]) + (v[6] + v[7]))) * msk) * (1.0f / D_);
    float qq = 0.f;
#pragma unroll
    for (int k = 0; k < 8; ++k) { const float d = v[k] - mean; qq += d * d; }
    const float sd = ln_sd(wave_sum(qq * msk) * (1.0f / D_));
    stv = pk_bf16(mean, sd);
    if (lane < 56) *(LAS u32x4*)(L + P0_LN + w * LNS + lane * 16) = xrow;
    bar_lds();
#pragma unroll
    for (int u = 0; u < 2; ++u) {
      const int pce = (wc * 2 + u) * 64 + lane;
      const LAS unsigned* a = (const LAS unsigned*)(L + P0_LN + (pce & 3) * (2 * LNS) + (pce >> 2) * 4);
      __builtin_amdgcn_raw_buffer_store_b128(u32x4{a[0], tag32, a[LNS / 4], tag32}, rst, (int)WS_XG + bid * (int)XG_BLK + pce * 16, 0, AUX_SC1);
    }
  }
  asm volatile("" ::"v"(touch));
  STAMP(0, 29);
  const int sto = (int)WS_STG + tid * 32;
  u32x4 ring[RING];
  if (CONV) {
    const float dt = rbf(expf(ldt));
    const float ea = (dt * are) * 1.4426950408889634f, eb = (dt * aim) * 0.15915494309189535f;
    const float bre = dt * b_re, bim = dt * b_im;
    f32x2 wc;
    wc.x = cre * bre - cim * bim;
    wc.y = -(cre * bim + cim * bre);
    asm volatile("" ::"v"(ea), "v"(eb), "v"(wc.x), "v"(wc.y));
    const float mx = rows_max(row16_max(fmaxf(fabsf(wc.x), fabsf(wc.y))));
    int ex = mx > 0.f ? __builtin_amdgcn_frexp_expf(mx) : 0;
    ex = ex < -120 ? -120 : (ex > 120 ? 120 : ex);
    const float scale = __builtin_amdgcn_ldexpf(1.0f, ex), iscale = __builtin_amdgcn_ldexpf(1.0f, -ex);
    wc.x *= iscale; wc.y *= iscale;
    LAS unsigned char* B0 = L + P0_KG + w * (2 * KG_BUF);
    LAS unsigned char* B1 = B0 + KG_BUF;
    const int wo = lane * 4;
    const int ro = r16 * KG_ROW + q * 16;
    f32x2 z8c = cexp_tab(ea, eb, 8.0f);
    z8c.y = -z8c.y;
    const f32x2 z1 = cexp_tab(ea, eb, 1.0f), z128 = cexp_tab(ea, eb, 128.0f), z1024 = cexp_tab(ea, eb, 1024.0f);
    {
      const f32x2 z16c = cmul(z8c, z8c);
      f32x2 t0 = wc, t1 = cmul(wc, z8c);
#pragma unroll
      for (int c = 0; c < 16; c += 2) {
        *(LAS unsigned*)(B0 + c * KG_ROW + wo) = pk_f16(t0);
        *(LAS unsigned*)(B0 + (c + 1) * KG_ROW + wo) = pk_f16(t1);
        if (c < 14) { t0 = cmul(t0, z16c); t1 = cmul(t1, z16c); }
      }
    }
    SB0();
    asm volatile("s_waitcnt vmcnt(2)" ::: "memory");
    PUT_STAT();
    SB0();
    f32x2 ze[8];
    ze[0] = f32x2{1.0f, 0.0f}; ze[1] = z1; ze[2] = cmul(z1, z1); ze[3] = cmul(ze[2], z1); ze[4] = cmul(ze[2], ze[2]);
    ze[5] = cmul(ze[4], z1); ze[6] = cmul(ze[4], ze[2]); ze[7] = cmul(ze[4], ze[3]);
#pragma unroll
    for (int e = 0; e < 8; ++e) {
      *(LAS unsigned*)(B1 + e * KG_ROW + wo) = pk_f16(ze[e]);
      *(LAS unsigned*)(B1 + (8 + e) * KG_ROW + wo) = pk_f16(e == 0 ? z128 : cmul(z128, ze[e]));
    }
    lds_fence();
    u32x4 tf[4];
#pragma unroll
    for (int ks = 0; ks < 4; ++ks) tf[ks] = *(const LAS u32x4*)(B0 + ro + ks * 64);
    u32x4 rf[4];
#pragma unroll
    for (int ks = 0; ks < 4; ++ks) rf[ks] = *(const LAS u32x4*)(B1 + ro + ks * 64);
    lds_fence();
    SB0();
    if (packer) pack_load(jb, lane, pk);
    SB0();
    STAMP(0, 30);
    const f32x2 p2 = cmul(z128, z128), p3 = cmul(p2, z128), p4 = cmul(p2, p2), p5 = cmul(p4, z128), p6 = cmul(p4, p2), p7 = cmul(p4, p3);
    {
#pragma unroll
      for (int e = 0; e < 4; ++e) {
        *(LAS unsigned*)(B0 + e * KG_ROW + wo) = pk_f16(e == 0 ? p2 : cmul(p2, ze[2 * e]));
        *(LAS unsigned*)(B0 + (4 + e) * KG_ROW + wo) = pk_f16(e == 0 ? p3 : cmul(p3, ze[2 * e]));
      }
      *(LAS unsigned*)(B0 + 8 * KG_ROW + wo) = pk_f16(p4);
      *(LAS unsigned*)(B0 + 9 * KG_ROW + wo) = pk_f16(cmul(p4, ze[4]));
      *(LAS unsigned*)(B0 + 10 * KG_ROW + wo) = pk_f16(p5);
      *(LAS unsigned*)(B0 + 11 * KG_ROW + wo) = pk_f16(cmul(p5, ze[4]));
      *(LAS unsigned*)(B0 + 12 * KG_ROW + wo) = pk_f16(p6);
      *(LAS unsigned*)(B0 + 13 * KG_ROW + wo) = pk_f16(cmul(p6, ze[4]));
      *(LAS unsigned*)(B0 + 14 * KG_ROW + wo) = pk_f16(p7);
      *(LAS unsigned*)(B0 + 15 * KG_ROW + wo) = pk_f16(cmul(p7, ze[4]));
    }
    f32x4 acc0 = {0.f, 0.f, 0.f, 0.f}, acc1 = acc0, acc2 = acc0;
#pragma unroll
    for (int ks = 0; ks < 4; ++ks) acc0 = mfma16h(rf[ks], tf[ks], acc0);
    {
      *(LAS unsigned*)(B1 + 0 * KG_ROW + wo) = pk_f16(z1024);
      *(LAS unsigned*)(B1 + 1 * KG_ROW + wo) = pk_f16(cmul(z1024, z128));
      *(LAS unsigned*)(B1 + 2 * KG_ROW + wo) = pk_f16(cmul(z1024, p2));
      *(LAS unsigned*)(B1 + 3 * KG_ROW + wo) = pk_f16(cmul(z1024, p3));
      *(LAS unsigned*)(B1 + 4 * KG_ROW + wo) = pk_f16(cmul(z1024, p4));
      *(LAS unsigned*)(B1 + 5 * KG_ROW + wo) = pk_f16(cmul(z1024, p5));
      *(LAS unsigned*)(B1 + 6 * KG_ROW + wo) = pk_f16(cmul(z1024, p6));
      *(LAS unsigned*)(B1 + 7 * KG_ROW + wo) = pk_f16(cmul(z1024, p7));
      *(LAS unsigned*)(B1 + 8 * KG_ROW + wo) = pk_f16(cmul(z1024, z1024));
    }
    lds_fence();
#pragma unroll
    for (int ks = 0; ks < 4; ++ks) acc1 = mfma16h(*(const LAS u32x4*)(B0 + ro + ks * 64), tf[ks], acc1);
#pragma unroll
    for (int ks = 0; ks < 4; ++ks) acc2 = mfma16h(*(const LAS u32x4*)(B1 + ro + ks * 64), tf[ks], acc2);
    lds_fence();
    {
      LAS float* red = (LAS float*)B0;
#pragma unroll
      for (int r = 0; r < 4; ++r) {
        const int row = 4 * q + r;
        red[(row >> 3) * 128 + 8 * r16 + (row & 7)] = acc0[r] * scale;
        const int i1 = row < 8 ? 256 + (row >> 2) * 64 + 4 * r16 + (row & 3) : 384 + ((row - 8) >> 1) * 32 + 2 * r16 + (row & 1);
        red[i1] = acc1[r] * scale;
        const int i2 = row < 9 ? 512 + row * 16 + r16 : 700;
        red[i2] = acc2[r] * scale;
      }
    }
    STAMP(0, 21);
    bar_lds();
    const int mgo = (int)WS_MODG + ((lane < 3 ? lane : 0) * 224 + dp) * 8;
    SB0();
    u32x4 sg0 = __builtin_amdgcn_raw_buffer_load_b128(rs, sto, 0, AUX_SC1), sg1 = __builtin_amdgcn_raw_buffer_load_b128(rs, sto + 16, 0, AUX_SC1);
    const int xgo = (int)WS_XG + (tid >> 1) * (int)XG_BLK + dp * 64 + (tid & 1) * 32;
    u32x4 xg0 = __builtin_amdgcn_raw_buffer_load_b128(rs, xgo, 0, AUX_SC1), xg1 = __builtin_amdgcn_raw_buffer_load_b128(rs, xgo + 16, 0, AUX_SC1);
    const unsigned dsk2 = *(const unsigned*)(p.dskip + 2 * dp);
    SB0();
    const int kc = tid >> 8, kt = tid & 255;
    LAS float* kd = (LAS float*)(L + P0_KG + kc * (8 * KG_BUF) + KG_BUF);
#pragma unroll
    for (int i = 0; i < 3; ++i) {
      const int oo = kt + 256 * i;
      if (i < 2 || oo < 648) {
        const LAS float* s = (const LAS float*)(L + P0_KG + kc * (8 * KG_BUF)) + oo;
        kd[oo] = (s[0] + s[2 * KG_BUF / 4]) + (s[4 * KG_BUF / 4] + s[6 * KG_BUF / 4]);
      }
    }
    if (kt == 0) kd[648] = 0.f;
    const unsigned OFFS = 0x087EDA52u;
    {
      int off = CV_KCOPY + kt * 16;
      if (kt >= 160) { const int k = kt - 160, s = k >> 3, ch = (k & 7) == 7 ? 6 : (k & 7); off = (272 * (s & 7) + ((OFFS >> (4 * (s & 7))) & 15) + 257 + ch) * 16; }
      *(LAS u32x4*)(L + kc * CV_CH + off) = u32x4{0u, 0u, 0u, 0u};
    }
    bar_lds();
    LAS unsigned char* base = L + kc * CV_CH;
    u32x4 k0;
    {
      float v[8];
#pragma unroll
      for (int i = 0; i < 8; i += 2) {
        const int l0 = 2048 - (kt * 8 + i), l1 = l0 - 1;
        const unsigned bits = pk_bf16((float)l0, (float)l1);
        int i0 = l0 < 128 ? l0 : (int)(bits & 0xffffu) - 17024;
        const int i1 = l1 < 128 ? l1 : (int)(bits >> 16) - 17024;
        if (i == 0) i0 = kt == 0 ? 648 : i0;
        v[i] = kd[i0];
        v[i + 1] = kd[i1];
      }
      k0 = u32x4{pk_bf16(v[0], v[1]), pk_bf16(v[2], v[3]), pk_bf16(v[4], v[5]), pk_bf16(v[6], v[7])};
      *(LAS u32x4*)(base + (2 + kt) * 16) = k0;
      if (kt < 8) {
        const unsigned k00 = pk_bf16(kd[0], 0.f);
        *(LAS u32x4*)(base + (272 * kt + ((OFFS >> (4 * kt)) & 15) + 256) * 16) = u32x4{kt == 0 ? k00 : 0u, 0u, 0u, 0u};
      }
    }
    bar_lds();
    {
      const u32x4 k1 = *(const LAS u32x4*)(base + (2 + kt + 1) * 16);
      unsigned wv[8] = {k0.x, k0.y, k0.z, k0.w, k1.x, k1.y, k1.z, k1.w};
      unsigned vv[7];
#pragma unroll
      for (int i = 0; i < 7; ++i) vv[i] = __builtin_amdgcn_alignbit(wv[i + 1], wv[i], 16);
#pragma unroll
      for (int s = 1; s < 8; ++s) {
        const int k = s >> 1;
        u32x4 o = (s & 1) ? u32x4{vv[k], vv[k + 1], vv[k + 2], vv[k + 3]} : u32x4{wv[k], wv[k + 1], wv[k + 2], wv[k + 3]};
        *(LAS u32x4*)(base + (272 * s + ((OFFS >> (4 * s)) & 15) + kt) * 16) = o;
      }
    }
    float cum0[4], cum1[4];
    {
      const LAS float* kd0 = (const LAS float*)(L + P0_KG + KG_BUF);
      const LAS float* kd1 = (const LAS float*)(L + P0_KG + 8 * KG_BUF + KG_BUF);
#pragma unroll
      for (int i = 0; i < 4; i += 2) {
        const int l0 = 4 * tid + i;
        const unsigned bits = pk_bf16((float)l0, (float)(l0 + 1));
        const int i0 = l0 < 128 ? l0 : (int)(bits & 0xffffu) - 17024, i1 = l0 < 128 ? l0 + 1 : (int)(bits >> 16) - 17024;
        cum0[i] = kd0[i0]; cum0[i + 1] = kd0[i1]; cum1[i] = kd1[i0]; cum1[i + 1] = kd1[i1];
      }
#pragma unroll
      for (int i = 1; i < 4; ++i) { cum0[i] += cum0[i - 1]; cum1[i] += cum1[i - 1]; }
      const float inc0 = wave_scan(cum0[3]), inc1 = wave_scan(cum1[3]);
      const float e0 = inc0 - cum0[3], e1 = inc1 - cum1[3];
#pragma unroll
      for (int i = 0; i < 4; ++i) { cum0[i] += e0; cum1[i] += e1; }
      if (lane == 63) *(LAS f32x2*)(L + P0_MS + w * 8) = f32x2{inc0, inc1};
    }
#define GRAN_BAD() __any(((((sg0.y ^ tag32) | (sg0.w ^ tag32)) | ((sg1.y ^ tag32) | (sg1.w ^ tag32))) | (((xg0.y ^ tag32) | (xg0.w ^ tag32)) | ((xg1.y ^ tag32) | (xg1.w ^ tag32)))) != 0u)
    if (__builtin_expect(GRAN_BAD(), 0)) {
      for (unsigned spins = 0; spins < SPIN_CAP; ++spins) {
        __builtin_amdgcn_s_sleep(2);
        asm volatile("" ::: "memory");
        sg0 = __builtin_amdgcn_raw_buffer_load_b128(rs, sto, 0, AUX_SC1); sg1 = __builtin_amdgcn_raw_buffer_load_b128(rs, sto + 16, 0, AUX_SC1);
        xg0 = __builtin_amdgcn_raw_buffer_load_b128(rs, xgo, 0, AUX_SC1); xg1 = __builtin_amdgcn_raw_buffer_load_b128(rs, xgo + 16, 0, AUX_SC1);
        if (!GRAN_BAD()) break;
      }
    }
#undef GRAN_BAD
    const unsigned xq[4] = {xg0.x, xg0.z, xg1.x, xg1.z};
    STAMP(0, 22);
    unsigned hq[4];
    {
      const unsigned sv[4] = {sg0.x, sg0.z, sg1.x, sg1.z};
#pragma unroll
      for (int k = 0; k < 4; ++k) {
        const float m = bf_lo(sv[k]), r = __builtin_amdgcn_rcpf(bf_hi(sv[k]));
        unsigned t = pk_bf16(bf_lo(xq[k]) - m, bf_hi(xq[k]) - m);
        hq[k] = pk_bf16(bf_lo(t) * r, bf_hi(t) * r);
      }
      LAS unsigned char* U = L + CV_KCOPY + (16 + (tid >> 4)) * CV_UROW + (tid & 15) * 8;
      *(LAS u32x2*)U = u32x2{__builtin_amdgcn_perm(hq[1], hq[0], 0x05040100u), __builtin_amdgcn_perm(hq[3], hq[2], 0x05040100u)};
      *(LAS u32x2*)(U + CV_CH) = u32x2{__builtin_amdgcn_perm(hq[1], hq[0], 0x07060302u), __builtin_amdgcn_perm(hq[3], hq[2], 0x07060302u)};
    }
    if (packer) pack_shuffle(lane, pk, L + P0_KG + (w + 1) * (2 * KG_BUF));
    STAMP(0, 23);
    bar_lds();
    if (bid == 0 && tid == 0) {
      const u64 e = ((u64)ep.y << 32) | ep.x;
      __hip_atomic_store((GAS u64*)(p.ws + WS_EPOCH), e + 1ull, __ATOMIC_RELAXED, __HIP_MEMORY_SCOPE_AGENT);
    }
    u32x2 fv = flag_load(rs, mgo);
    const __amdgpu_buffer_rsrc_t rpk = __builtin_amdgcn_make_buffer_rsrc((void*)p.ws, 0, packer ? (int)WS_END : 0, 0x00020000);
    SB0();
  {
    const int ch = w >> 2, wq = w & 3;
    f32x4 acc[2][4];
#pragma unroll
    for (int n = 0; n < 2; ++n)
#pragma unroll
      for (int m = 0; m < 4; ++m) acc[n][m] = f32x4{0.f, 0.f, 0.f, 0.f};
    const int s = (8 - r16) & 7, u = (r16 + 7) >> 3;
    const LAS unsigned char* ka = L + ch * CV_CH + (272 * s + ((OFFS >> (4 * s)) & 15) + 256 + q - u - 2 * (16 * wq) - 2 * 79) * 16;
    const LAS unsigned char* ub = L + ch * CV_CH + CV_KCOPY + (16 + r16 - 4 * wq - 3) * CV_UROW + q * 16;
#define KFRAG(f) (*(const LAS u32x4*)(ka + (79 - (f)) * 32))
    u32x4 alo[18], ahi[18], bf[4][4];
#define CSTAGE(dl)                                                                                      \
  {                                                                                                     \
    _Pragma("unroll") for (int ks = 0; ks < 2; ++ks) {                                                  \
      bf[dl][2 * ks] = *(const LAS u32x4*)(ub + (3 - (dl)) * CV_UROW + ks * 64);                        \
      bf[dl][2 * ks + 1] = *(const LAS u32x4*)(ub + (19 - (dl)) * CV_UROW + ks * 64);                   \
    }                                                                                                   \
    _Pragma("unroll") for (int i = 0; i < 4; ++i) {                                                     \
      alo[2 + 4 * (dl) + i] = KFRAG(4 * (dl) + i);                                                      \
      ahi[2 + 4 * (dl) + i] = KFRAG(4 * (dl) + i + 64);                                                 \
    }                                                                                                   \
  }
    CSTAGE(0)
    alo[0] = KFRAG(-2); alo[1] = KFRAG(-1); ahi[0] = KFRAG(62); ahi[1] = KFRAG(63);
    SB0();
#pragma unroll
    for (int m = 0; m < 7; ++m) pack_store1(jb, lane, pk, rpk, m);
    FILL_X1(); FILL_P(0); FILL_P(1); FILL_P(2);
    SB0();
#pragma unroll
    for (int dl = 0; dl < 4; ++dl) {
      if (dl < 3) CSTAGE(dl + 1)
#pragma unroll
      for (int ks = 0; ks < 2; ++ks) {
#pragma unroll
        for (int m = 0; m < 4; ++m) {
          acc[0][m] = mfma16(alo[4 * dl + m - 2 * ks + 2], bf[dl][2 * ks], acc[0][m]);
          acc[1][m] = mfma16(alo[4 * dl + m - 2 * ks + 2], bf[dl][2 * ks + 1], acc[1][m]);
          acc[1][m] = mfma16(ahi[4 * dl + m - 2 * ks + 2], bf[dl][2 * ks], acc[1][m]);
        }
      }
      if (dl < 3) {
#pragma unroll
        for (int i = 0; i < 12; ++i) {
          __builtin_amdgcn_sched_group_barrier(0x100, 1, 0);
          __builtin_amdgcn_sched_group_barrier(0x8, 2, 0);
        }
      }
      SB0();
    }
#undef CSTAGE
    STAMP(0, 28);
#undef KFRAG

    LAS unsigned char* P = L + CV_P + w * (32 * CV_PROW);
#pragma unroll
    for (int n = 0; n < 2; ++n)
#pragma unroll
      for (int m = 0; m < 4; ++m) *(LAS f32x4*)(P + (16 * n + r16) * CV_PROW + (16 * m + 4 * q) * 4) = acc[n][m];
    }
    fv = flag_wait(rs, mgo, fv, 0u, tag32, 0u);
    const unsigned sa2 = __builtin_amdgcn_readlane(fv.x, 0), ca2 = __builtin_amdgcn_readlane(fv.x, 1), ga2 = __builtin_amdgcn_readlane(fv.x, 2);
    unsigned hd[4];
    {
      const f32x2 t2 = *(const LAS f32x2*)(L + P0_MS + (lane & 7) * 8);
      const float mk = (lane & 15) < w ? 1.0f : 0.0f;
      const float b0 = row16_sum((lane & 8) ? 0.f : t2.x * mk), b1 = row16_sum((lane & 8) ? 0.f : t2.y * mk);
#pragma unroll
      for (int k = 0; k < 4; ++k) {
        unsigned h = pk_bf16(bf_lo(hq[k]) * bf_lo(ca2), bf_hi(hq[k]) * bf_hi(ca2));
        h = pk_bf16(bf_lo(h) + bf_lo(sa2), bf_hi(h) + bf_hi(sa2));
        hd[k] = pk_bf16(bf_lo(h) * bf_lo(dsk2), bf_hi(h) * bf_hi(dsk2));
        cum0[k] = (cum0[k] + b0) * bf_lo(sa2);
        cum1[k] = (cum1[k] + b1) * bf_hi(sa2);
      }
    }
    asm volatile("s_waitcnt vmcnt(4)" ::: "memory");
    bar_lds();
    STAMP(0, 24);
    {
      const LAS unsigned char* P = L + CV_P + (tid >> 4) * CV_PROW + (tid & 15) * 16;
      f32x4 pa[8];
#pragma unroll
      for (int k = 0; k < 8; ++k) pa[k] = *(const LAS f32x4*)(P + k * (32 * CV_PROW));
      const f32x4 y0 = (pa[0] + pa[1]) + (pa[2] + pa[3]), y1 = (pa[4] + pa[5]) + (pa[6] + pa[7]);
      u32x4 out;
#pragma unroll
      for (int k = 0; k < 4; ++k) {
        const unsigned yy = pk_bf16(y0[k] * bf_lo(ca2) + cum0[k], y1[k] * bf_hi(ca2) + cum1[k]);
        unsigned t = pk_bf16(bf_lo(yy) + bf_lo(hd[k]), bf_hi(yy) + bf_hi(hd[k]));
        t = pk_bf16(bf_lo(ga2) * bf_lo(t), bf_hi(ga2) * bf_hi(t));
        out[k] = sane(pk_bf16(bf_lo(xq[k]) + bf_lo(t), bf_hi(xq[k]) + bf_hi(t)));
      }
      ST16(WS_X2B + (tid >> 3) * (int)X2B_BLK + dp * 128 + (tid & 7) * 16, out);
    }
  } else {
    const int c = bid - 224;
    LAS float* sm = (LAS float*)(L + P0_KG);
    SB0();
    mod_load(p, c, tid, mr, mc);
    SB0();
    asm volatile("s_waitcnt vmcnt(16)" ::: "memory");
    PUT_STAT();
    mod_finish(c, tag32, tid, mr, mc, sm, rs);
    FILL_X1(); FILL_P(0); FILL_P(1); FILL_P(2);
    STAMP(0, 21);
    if (c < 10) {
      mod_load(p, 32 + c, tid, mr, mc);
      bar_lds();
      mod_finish(32 + c, tag32, tid, mr, mc, sm, rs);
    }
    if (packer) {
      pack_load(jb, lane, pk);
      pack_shuffle(lane, pk, L + P0_PK + w * 8192);
#pragma unroll
      for (int m = 0; m < 7; ++m) pack_store1(jb, lane, pk, rs, m);
    }
    arrive_flag<0>(L, 2, p.ws + WS_FLAGC + c * 8, ~tag, lane);
    STAMP(0, 30);
    {
      u32x4 sg0 = __builtin_amdgcn_raw_buffer_load_b128(rs, sto, 0, AUX_SC1), sg1 = __builtin_amdgcn_raw_buffer_load_b128(rs, sto + 16, 0, AUX_SC1);
      stat_wait(rs, sto, sg0, sg1, tag32);
    }
    bar_lds();
  }
  STAMP(0, 25);
  if (!cw) {
    {
      const int mo = (int)WS_MODG + 672 * 8 + l56 * 64;
      u32x4 g0 = __builtin_amdgcn_raw_buffer_load_b128(rs, mo, 0, AUX_SC1), g1 = __builtin_amdgcn_raw_buffer_load_b128(rs, mo + 16, 0, AUX_SC1);
      u32x4 g2 = __builtin_amdgcn_raw_buffer_load_b128(rs, mo + 32, 0, AUX_SC1), g3 = __builtin_amdgcn_raw_buffer_load_b128(rs, mo + 48, 0, AUX_SC1);
      stat_wait(rs, mo, g0, g1, tag32);
      stat_wait(rs, mo + 32, g2, g3, tag32);
      LAS unsigned char* mp = L + G_MOD + l56 * 64;
      *(LAS f32x4*)mp = f32x4{bf_lo(g0.x), bf_hi(g0.x), bf_lo(g0.z), bf_hi(g0.z)};
      *(LAS f32x4*)(mp + 16) = f32x4{bf_lo(g1.x), bf_hi(g1.x), bf_lo(g1.z), bf_hi(g1.z)};
      *(LAS f32x4*)(mp + 32) = f32x4{bf_lo(g2.x), bf_hi(g2.x), bf_lo(g2.z), bf_hi(g2.z)};
      *(LAS f32x4*)(mp + 48) = f32x4{bf_lo(g3.x), bf_hi(g3.x), bf_lo(g3.z), bf_hi(g3.z)};
    }
    gather_x2<0>(p, L, rs, pn, tid);
    STAMP(0, 1);
    bar_lds();
    ln_phase_a(L, j, w, r16, q, [](auto) {});
    STAMP(0, 2);
    bar_lds();
    {
      const int fo = (int)WS_FLAGC + (lane & 31) * 8;
      flag_wait(rs, fo, flag_load(rs, fo), ~tlo, ~thi, ~0u);
      const unsigned char* src = p.ws + WS_WP + (size_t)(j * 7) * NFRAG * 1024 + NRING * 1024 + voff;
#pragma unroll
      for (int ww = 0; ww < 7; ++ww)
#pragma unroll
        for (int i = 0; i < NLDS; ++i)
          __builtin_amdgcn_global_load_lds((const GAS unsigned*)(src + (size_t)ww * NFRAG * 1024 + i * 1024), (LAS unsigned*)(L + G_WL + (ww * NLDS + i) * 1024), 16, 0, AUX_SC1);
    }
    bar_lds();
    STAMP(0, 4);
    bar_lds();
    bar_lds();
    {
      int gof[3];
      u32x4 xr[3];
      x1_offsets(pn, j, w, r16, q, gof);
      poll_load<3>(rs, gof, xr);
      ln_phase_c(L, xr, j, w, r16, q);
    }
    asm volatile("s_waitcnt vmcnt(0)" ::: "memory");
    STAMP(0, 7);
    bar_lds();
    bar_lds();
    return;
  }
  SB0();
  gather_x2<X2_DELAY>(p, L, rs, pn, tid);
  SB0();
  ring_fill<0, 8>(ring, rw, voff);
  STAMP(0, 1);
  bar_lds();
  u32x2 h2r[2], x2r[2], x1r[2];
  const int ncol = j * 112 + w * 16 + q * 4;
  const int gbo = (int)WS_MODG + (5 * 224 + (ncol >> 1)) * 8;
  u32x4 gbg = __builtin_amdgcn_raw_buffer_load_b128(rs, gbo, 0, AUX_SC1);
#pragma unroll
  for (int T = 0; T < 2; ++T) {
    const int row = 16 * T + r16;
    x2r[T] = *(const LAS u32x2*)(L + G_X + row * 1024 + (((ncol >> 3) ^ (row >> 2)) * 16) + (ncol & 4) * 2);
  }
  SB0();
  ring_fill<8, 5>(ring, rw, voff);
  ln_phase_a(L, j, w, r16, q, [&](auto k) { SB0(); ring_fill<13 + 5 * decltype(k)::value, 5>(ring, rw, voff); });
  SB0();
  ring_fill<28, 4>(ring, rw, voff);
  STAMP(0, 2);
  bar_lds();
  u32x2 gbp;
  {
    u32x4 dup = gbg;
    if (__builtin_expect(__any(((gbg.y ^ tag32) | (gbg.w ^ tag32)) != 0u), 0)) {
      for (unsigned spins = 0; spins < SPIN_CAP; ++spins) {
        asm volatile("" ::: "memory");
        dup = __builtin_amdgcn_raw_buffer_load_b128(rs, gbo, 0, AUX_SC1);
        if (!__any(((dup.y ^ tag32) | (dup.w ^ tag32)) != 0u)) break;
      }
    }
    gbp = u32x2{dup.x, dup.z};
  }
#pragma unroll
  for (int T = 0; T < 2; ++T) h2r[T] = *(const LAS u32x2*)(L + G_HQ + (16 * T + r16) * 240 + (w * 16 + q * 4) * 2);
  const LAS unsigned char* ap0 = L + G_T0 + r16 * AS + q * 16;
  const LAS unsigned char* ap1 = L + G_T1 + r16 * AS + q * 16;
  const int tok = (q & 1) * 16 + r16, octc = 2 * w + (q >> 1);
  {
    f32x4 acc[1][2];
    STAMP(0, 16);
    gemm14<1, 0, 32, 7, 2, 5>(ap0, ring, rw, voff, acc);
    STAMP(0, 17);
    const u32x4 own = make_piece(gelu4(acc[0][0]), gelu4(acc[0][1]));
    __builtin_amdgcn_raw_buffer_store_b128(own, rs, (int)WS_G + (pn * 4 + j) * 7168 + pslot, 0, AUX_SC1);
    SB0();
    STAMP(0, 18);
    ring_fill<39, 4>(ring, rw, voff);
    int goff[4], loff[4];
    bool wrap0;
#pragma unroll
    for (int i = 0; i < 4; ++i) {
      const int jq = (j + i) & 3;
      const int sh = (jq & 1) ? -1 - (jq >> 1) : 1 + (jq >> 1);
      const int it = tok + sh;
      const int sp = pn + (it >= 32 ? -1 : (it < 0 ? 1 : 0));
      if (i == 0) wrap0 = sp != pn;
      goff[i] = (sp < 0 || sp > 63 || (i == 0 && sp == pn)) ? (int)WS_END : (int)WS_G + (sp * 4 + jq) * 7168 + pslot;
      loff[i] = G_T1 + (it & 31) * AS + (jq * 14 + octc) * 16;
    }
    STAMP(0, 3);
    bar_lds();
    u32x4 v[4];
    poll_issue<4>(rs, goff, v);
    __builtin_amdgcn_s_sleep(3);
    ring_fill<43, 5>(ring, rw, voff);
    poll_finish<4>(rs, goff, v);
    *(LAS u32x4*)(L + loff[0]) = wrap0 ? v[0] : own;
#pragma unroll
    for (int i = 1; i < 4; ++i) *(LAS u32x4*)(L + loff[i]) = v[i];
  }
  STAMP(0, 4);
  bar_lds();
  {
    f32x4 acc[1][2];
    gemm14<1, 14, 48, 7, 2, 5>(ap1, ring, rw, voff, acc);
#pragma unroll
    for (int T = 0; T < 2; ++T) {
      const unsigned a = pk_bf16(acc[0][T][0], acc[0][T][1]), b = pk_bf16(acc[0][T][2], acc[0][T][3]);
      x1r[T].x = pk_bf16(bf_lo(h2r[T].x) + bf_lo(a), bf_hi(h2r[T].x) + bf_hi(a));
      x1r[T].y = pk_bf16(bf_lo(h2r[T].y) + bf_lo(b), bf_hi(h2r[T].y) + bf_hi(b));
    }
    const u32x4 own = make_piece(x1r[0], x1r[1]);
    __builtin_amdgcn_raw_buffer_store_b128(own, rs, (int)WS_X1 + (pn * 4 + j) * 7168 + (w * 64 + (tok >> 2) * 8 + (q >> 1) * 4 + (tok & 3)) * 16, 0, AUX_SC1);
    *(LAS u32x4*)(L + G_HQ + tok * 240 + octc * 16) = own;
    SB0();
    STAMP(0, 19);
    ring_fill<55, 4>(ring, rw, voff);
    int gof[3];
    u32x4 xr[3];
    x1_offsets(pn, j, w, r16, q, gof);
    STAMP(0, 5);
    bar_lds();
    poll_issue<3>(rs, gof, xr);
    __builtin_amdgcn_s_sleep(3);
    ring_fill<59, 5>(ring, rw, voff);
    poll_finish<3>(rs, gof, xr);
    STAMP(0, 6);
    ring_fill<64, 4>(ring, rw, voff);
    ln_phase_c(L, xr, j, w, r16, q);
  }
  STAMP(0, 7);
  bar_lds();
  {
    f32x4 acc[4][2];
    STAMP(0, 12);
    gemm14<4, 28, RING + 28, 28, 2, 2>(ap0, ring, rw, voff, acc);
    STAMP(0, 13);
#pragma unroll
    for (int i = 0; i < 4; ++i)
#pragma unroll
      for (int T = 0; T < 2; ++T) {
        const int g = RING + 56 + (i * 2 + T) * 2;
        ring[g % RING] = WLOAD(g);
        ring[(g + 1) % RING] = WLOAD(g + 1);
        SB0();
        *(LAS u32x2*)(L + G_T1 + (16 * T + r16) * AS + (w * 64 + i * 16 + q * 4) * 2) = gelu4(acc[i][T]);
        SB0();
      }
  }
  STAMP(0, 8);
  bar_lds();
  {
    {
      f32x4 acc[3][2];
      STAMP(0, 14);
      gemm14<3, 84, RING + 72, NRING - RING - 72, 1, 3>(ap1, ring, rw, voff, acc);
      STAMP(0, 15);
#pragma unroll
      for (int i = 0; i < 3; ++i) {
        const int jd = (j + 1 + i) & 3;
        const u32x4 o = {sane(pk_bf16(acc[i][0][0], acc[i][0][1])), sane(pk_bf16(acc[i][0][2], acc[i][0][3])),
                         sane(pk_bf16(acc[i][1][0], acc[i][1][1])), sane(pk_bf16(acc[i][1][2], acc[i][1][3]))};
        __builtin_amdgcn_raw_buffer_store_b128(o, rs, (int)WS_P + ((pn * 4 + jd) * 3 + (2 - i)) * 7168 + pslot, 0, AUX_SC1);
      }
      SB0();
    }
    STAMP(0, 9);
    f32x4 acc0 = {0.f, 0.f, 0.f, 0.f}, acc1 = acc0;
    {
      const LAS unsigned char* wl = L + G_WL + w * (NLDS * 1024) + voff;
#pragma unroll
      for (int ks = 0; ks < 14; ++ks) {
        const int f = 126 + ks;
        const u32x4 a = f < NRING ? ring[f % RING] : *(const LAS u32x4*)(wl + (f - NRING) * 1024);
        acc0 = mfma16(a, *(const LAS u32x4*)(ap1 + ks * 64), acc0);
        acc1 = mfma16(a, *(const LAS u32x4*)(ap1 + 16 * AS + ks * 64), acc1);
      }
    }
    STAMP(0, 10);
    u32x4 v[3];
    const int go = (int)WS_P + (pn * 4 + j) * 3 * 7168 + pslot;
    const int gof[3] = {go, go + 7168, go + 2 * 7168};
    poll_load<3>(rs, gof, v);
    STAMP(0, 11);
#pragma unroll
    for (int T = 0; T < 2; ++T) {
      const f32x4 ac = T ? acc1 : acc0;
      float hh[4];
#pragma unroll
      for (int k = 0; k < 2; ++k) {
        hh[2 * k] = ac[2 * k] + ((bf_lo(v[0][2 * T + k]) + bf_lo(v[1][2 * T + k])) + bf_lo(v[2][2 * T + k]));
        hh[2 * k + 1] = ac[2 * k + 1] + ((bf_hi(v[0][2 * T + k]) + bf_hi(v[1][2 * T + k])) + bf_hi(v[2][2 * T + k]));
      }
      unsigned a = pk_bf16(hh[0], hh[1]), b = pk_bf16(hh[2], hh[3]);
      a = pk_bf16(bf_lo(x1r[T].x) + bf_lo(a), bf_hi(x1r[T].x) + bf_hi(a));
      b = pk_bf16(bf_lo(x1r[T].y) + bf_lo(b), bf_hi(x1r[T].y) + bf_hi(b));
      a = pk_bf16(bf_lo(gbp.x) * bf_lo(a), bf_hi(gbp.x) * bf_hi(a));
      b = pk_bf16(bf_lo(gbp.y) * bf_lo(b), bf_hi(gbp.y) * bf_hi(b));
      u32x2 o;
      o.x = pk_bf16(bf_lo(x2r[T].x) + bf_lo(a), bf_hi(x2r[T].x) + bf_hi(a));
      o.y = pk_bf16(bf_lo(x2r[T].y) + bf_lo(b), bf_hi(x2r[T].y) + bf_hi(b));
      *(u32x2*)(p.out + (size_t)(pn * 32 + 16 * T + r16) * D_ + ncol) = o;
    }
  }
  STAMP(0, 20);
}

__global__ __launch_bounds__(512, 2) void k_all(GArgs p) {
  extern __shared__ __attribute__((aligned(16))) unsigned char smem[];
  const uintptr_t code0 = (uintptr_t)__builtin_amdgcn_s_getpc() & ~(uintptr_t)127;
  if (blockIdx.x < 224) body<true>(p, (LAS unsigned char*)smem, code0);
  else body<false>(p, (LAS unsigned char*)smem, code0);
}

}

extern "C" void kernel_launch(void* const* d_in, const int* in_sizes, int n_in, void* d_out, int out_size, void* d_ws,
                              size_t ws_size, hipStream_t stream) {
  (void)in_sizes; (void)n_in; (void)out_size; (void)ws_size;
  GArgs a;
  a.x = (const unsigned short*)d_in[0];
  a.a_re = (const unsigned short*)d_in[1];
  a.a_im = (const unsigned short*)d_in[2];
  a.b_re = (const unsigned short*)d_in[3];
  a.b_im = (const unsigned short*)d_in[4];
  a.c_re = (const unsigned short*)d_in[5];
  a.c_im = (const unsigned short*)d_in[6];
  a.log_dt = (const unsigned short*)d_in[7];
  a.dskip = (const unsigned short*)d_in[8];
  a.w1 = (const unsigned short*)d_in[10];
  a.w2 = (const unsigned short*)d_in[11];
  a.w3 = (const unsigned short*)d_in[12];
  a.w4 = (const unsigned short*)d_in[13];
  a.cvec = (const unsigned short*)d_in[14];
  a.wmod = (const unsigned short*)d_in[15];
  a.ws = (unsigned char*)d_ws;
  a.out = (unsigned short*)d_out;
  (void)hipFuncSetAttribute((const void*)k_all, hipFuncAttributeMaxDynamicSharedMemorySize, G_LDS);
  k_all<<<256, 512, G_LDS, stream>>>(a);
}
```

```cpp
#include <hip/hip_runtime.h>
#include <hip/hip_bf16.h>
#include <stdint.h>
#include <type_traits>
#include <utility>

namespace {

typedef __attribute__((ext_vector_type(8))) short bf16x8;
typedef __attribute__((ext_vector_type(8))) _Float16 f16x8;
typedef __attribute__((ext_vector_type(2))) _Float16 f16x2;
typedef __attribute__((ext_vector_type(4))) float f32x4;
typedef __attribute__((ext_vector_type(2))) float f32x2;
typedef __attribute__((ext_vector_type(4))) unsigned u32x4;
typedef __attribute__((ext_vector_type(2))) unsigned u32x2;
typedef __attribute__((ext_vector_type(2))) __bf16 bf16x2v;
#define GAS __attribute__((address_space(1)))
#define LAS __attribute__((address_space(3)))

constexpr int L_ = 2048, D_ = 448, NS_ = 256;

constexpr size_t WS_EPOCH = 0;
constexpr size_t WS_FLAGC = 4608;
constexpr size_t WS_MODG = 16384;
constexpr size_t WS_STG = 32768;
constexpr int NFRAG = 140;
constexpr size_t WS_XG = 65536;
constexpr size_t XG_BLK = 224 * 64;
constexpr size_t WS_WP = WS_XG + 256 * XG_BLK;
constexpr size_t WS_X2B = WS_WP + (size_t)28 * NFRAG * 1024;
constexpr size_t X2B_BLK = 224 * 128;
constexpr size_t WS_G = WS_X2B + 64 * X2B_BLK;
constexpr size_t WS_X1 = WS_G + (size_t)64 * 4 * 7168;
constexpr size_t WS_P = WS_X1 + (size_t)64 * 4 * 7168;
constexpr size_t WS_END = WS_P + (size_t)64 * 12 * 7168;

#define STAMP(base, k) do { } while (0)

__device__ __forceinline__ float bf_lo(unsigned u) { return __builtin_bit_cast(float, u << 16); }
__device__ __forceinline__ float bf_hi(unsigned u) { return __builtin_bit_cast(float, u & 0xffff0000u); }
__device__ __forceinline__ float bf2f(unsigned short h) { return __builtin_bit_cast(float, (unsigned)h << 16); }
__device__ __forceinline__ unsigned pk_bf16(float a, float b) {
  f32x2 v = {a, b};
  bf16x2v r = __builtin_convertvector(v, bf16x2v);
  return __builtin_bit_cast(unsigned, r);
}
__device__ __forceinline__ unsigned pk_f16(f32x2 v) {
  f16x2 r = __builtin_convertvector(v, f16x2);
  return __builtin_bit_cast(unsigned, r);
}
__device__ __forceinline__ float rbf(float a) { return bf_lo(pk_bf16(a, a)); }
__device__ __forceinline__ f32x4 mfma16(u32x4 a, u32x4 b, f32x4 c) {
  return __builtin_amdgcn_mfma_f32_16x16x32_bf16(__builtin_bit_cast(bf16x8, a), __builtin_bit_cast(bf16x8, b), c, 0, 0, 0);
}
__device__ __forceinline__ f32x4 mfma16h(u32x4 a, u32x4 b, f32x4 c) {
  return __builtin_amdgcn_mfma_f32_16x16x32_f16(__builtin_bit_cast(f16x8, a), __builtin_bit_cast(f16x8, b), c, 0, 0, 0);
}
template <int CTRL>
__device__ __forceinline__ float dpp_f(float v) {
  return __builtin_bit_cast(float, __builtin_amdgcn_update_dpp(0, __builtin_bit_cast(int, v), CTRL, 0xf, 0xf, true));
}
__device__ __forceinline__ float row16_sum(float v) {
  v += dpp_f<0xB1>(v);
  v += dpp_f<0x4E>(v);
  v += dpp_f<0x141>(v);
  v += dpp_f<0x140>(v);
  return v;
}
__device__ __forceinline__ float row16_max(float v) {
  v = fmaxf(v, dpp_f<0xB1>(v));
  v = fmaxf(v, dpp_f<0x4E>(v));
  v = fmaxf(v, dpp_f<0x141>(v));
  v = fmaxf(v, dpp_f<0x140>(v));
  return v;
}
__device__ __forceinline__ void swap16(float v, float& a, float& b) {
  const unsigned u = __builtin_bit_cast(unsigned, v), u2 = u;
  auto r = __builtin_amdgcn_permlane16_swap(u, u2, false, false);
  const unsigned r0 = r[0], r1 = r[1];
  a = __builtin_bit_cast(float, r0); b = __builtin_bit_cast(float, r1);
}
__device__ __forceinline__ void swap32(float v, float& a, float& b) {
  const unsigned u = __builtin_bit_cast(unsigned, v), u2 = u;
  auto r = __builtin_amdgcn_permlane32_swap(u, u2, false, false);
  const unsigned r0 = r[0], r1 = r[1];
  a = __builtin_bit_cast(float, r0); b = __builtin_bit_cast(float, r1);
}
__device__ __forceinline__ float rows_sum(float v) {
  float a, b;
  swap16(v, a, b); v = a + b;
  swap32(v, a, b);
  return a + b;
}
__device__ __forceinline__ float rows_max(float v) {
  float a, b;
  swap16(v, a, b); v = fmaxf(a, b);
  swap32(v, a, b);
  return fmaxf(a, b);
}
template <int CTRL, int RMASK>
__device__ __forceinline__ float dpp_z(float v) {
  return __builtin_bit_cast(float, __builtin_amdgcn_update_dpp(0, __builtin_bit_cast(int, v), CTRL, RMASK, 0xf, false));
}
__device__ __forceinline__ float wave_scan(float v) {
  v += dpp_z<0x111, 0xf>(v);
  v += dpp_z<0x112, 0xf>(v);
  v += dpp_z<0x114, 0xf>(v);
  v += dpp_z<0x118, 0xf>(v);
  v += dpp_z<0x142, 0xa>(v);
  v += dpp_z<0x143, 0xc>(v);
  return v;
}
__device__ __forceinline__ float wave_sum(float v) { return rows_sum(row16_sum(v)); }
__device__ __forceinline__ float gelu_tanh(float x) {
  const float c1 = -2.0f * 0.7978845608028654f * 1.4426950408889634f;
  const float c2 = c1 * 0.044715f;
  float t = x * (c1 + c2 * x * x);
  float e = __builtin_amdgcn_exp2f(t);
  return x * __builtin_amdgcn_rcpf(1.0f + e);
}
__device__ __forceinline__ float ln_sd(float var) { return rbf(__builtin_amdgcn_sqrtf(rbf(rbf(var) + 1e-5f))); }
__device__ __forceinline__ void lds_fence() { asm volatile("" ::: "memory"); }
__device__ __forceinline__ void bar_lds() {
  asm volatile("s_waitcnt lgkmcnt(0)" ::: "memory");
  __builtin_amdgcn_s_barrier();
  asm volatile("" ::: "memory");
}

__device__ __forceinline__ void transpose8x8(const u32x4 (&r)[8], u32x4 (&o)[8]) {
#pragma unroll
  for (int c2 = 0; c2 < 4; ++c2) {
#pragma unroll
    for (int rp = 0; rp < 4; ++rp) {
      unsigned a = r[2 * rp][c2], b = r[2 * rp + 1][c2];
      o[2 * c2][rp] = __builtin_amdgcn_perm(b, a, 0x05040100u);
      o[2 * c2 + 1][rp] = __builtin_amdgcn_perm(b, a, 0x07060302u);
    }
  }
}
__device__ __forceinline__ f32x2 cmul(f32x2 a, f32x2 b) {
  f32x2 r, t;
  asm("v_pk_mul_f32 %0, %1, %2 op_sel_hi:[0,1]" : "=v"(t) : "v"(a), "v"(b));
  asm("v_pk_fma_f32 %0, %1, %2, %3 op_sel:[1,1,0] op_sel_hi:[1,0,1] neg_lo:[1,0,0]" : "=v"(r) : "v"(a), "v"(b), "v"(t));
  return r;
}
__device__ __forceinline__ f32x2 cexp_tab(float a, float b, float l) {
  const float e = __builtin_amdgcn_exp2f(a * l), ph = __builtin_amdgcn_fractf(b * l);
  f32x2 r;
  r.x = e * __builtin_amdgcn_cosf(ph);
  r.y = e * __builtin_amdgcn_sinf(ph);
  return r;
}

constexpr int CV_KCOPY = 8 * 272 * 16;
constexpr int CV_UROW = 160;
constexpr int CV_U = 48 * CV_UROW;
constexpr int CV_CH = CV_KCOPY + CV_U;
constexpr int CV_LDS = 2 * CV_CH;
constexpr int CV_PROW = 272;
static_assert(4 * 32 * CV_PROW <= CV_KCOPY, "partials alias the kernel copies");

struct GArgs {
  const unsigned short* x;
  const unsigned short* a_re;
  const unsigned short* a_im;
  const unsigned short* b_re;
  const unsigned short* b_im;
  const unsigned short* c_re;
  const unsigned short* c_im;
  const unsigned short* log_dt;
  const unsigned short* dskip;
  const unsigned short* w1;
  const unsigned short* w2;
  const unsigned short* w3;
  const unsigned short* w4;
  const unsigned short* cvec;
  const unsigned short* wmod;
  unsigned char* ws;
  unsigned short* out;
};
constexpr int AS = 928;
constexpr int G_T0 = 0;
constexpr int G_T1 = G_T0 + 32 * AS;
constexpr int G_X = G_T1;
constexpr int G_HQ = G_X + 32768;
constexpr int G_WL = G_HQ + 32 * 240;
constexpr int NLDS = 12;
constexpr int NRING = NFRAG - NLDS;
constexpr int G_MOD = G_WL + 7 * NLDS * 1024;
constexpr int G_CNT = G_MOD + 2 * 448 * 4;
constexpr int P0_MS = G_CNT + 16;
constexpr int G_LDS = P0_MS + 256;
constexpr int KG_ROW = 288;
constexpr int KG_BUF = 16 * KG_ROW;
constexpr int P0_KG = CV_LDS;
constexpr int P0_LN = 0;
constexpr int LNS = 928;
constexpr int P0_PK = 16384;
static_assert(P0_KG + 16 * KG_BUF <= G_CNT, "tables");
static_assert(G_MOD >= CV_LDS && G_LDS <= 163840, "LDS map");
constexpr int RING = 44;
constexpr int RINGB = 16;
constexpr int RING0 = RINGB + 6;
constexpr int CV_P = CV_LDS;
static_assert(CV_P + 8 * 32 * CV_PROW <= G_MOD, "partials");
constexpr unsigned SPIN_CAP = 1u << 14;
constexpr int AUX_SC1 = 16;
constexpr int CODE_LINES = 320;
#define PRETOUCH 1
#define X2_DELAY 16
#define POLL_SLEEP 6

__device__ __forceinline__ unsigned sane(unsigned v) { return v < 0xFFFFFFFEu ? v : 0xFFFFFFFEu; }
__device__ __forceinline__ unsigned umax4(const u32x4& v) { return max(max(v.x, v.y), max(v.z, v.w)); }
__device__ __forceinline__ bool bad4(const u32x4& v) { return umax4(v) == ~0u; }
__device__ __forceinline__ u32x4 make_piece(u32x2 a, u32x2 b) {
  auto rx = __builtin_amdgcn_permlane16_swap(a.x, b.x, false, false);
  auto ry = __builtin_amdgcn_permlane16_swap(a.y, b.y, false, false);
  return u32x4{sane(rx[0]), sane(ry[0]), sane(rx[1]), sane(ry[1])};
}
template <int N>
__device__ __forceinline__ void poll_issue(__amdgpu_buffer_rsrc_t r, const int (&off)[N], u32x4 (&v)[N]) {
#pragma unroll
  for (int i = 0; i < N; ++i) v[i] = __builtin_amdgcn_raw_buffer_load_b128(r, off[i], 0, AUX_SC1);
  __builtin_amdgcn_sched_barrier(0);
}
template <int N>
__device__ __forceinline__ void poll_finish(__amdgpu_buffer_rsrc_t r, const int (&off)[N], u32x4 (&v)[N]) {
  unsigned m = 0u;
#pragma unroll
  for (int i = 0; i < N; ++i) m = max(m, umax4(v[i]));
  if (__builtin_expect(__any(m == ~0u), 0)) {
    for (unsigned spins = 0; spins < SPIN_CAP; ++spins) {
      __builtin_amdgcn_s_sleep(POLL_SLEEP);
      asm volatile("" ::: "memory");
#pragma unroll
      for (int i = 0; i < N; ++i)
        if (bad4(v[i])) v[i] = __builtin_amdgcn_raw_buffer_load_b128(r, off[i], 0, AUX_SC1);
      m = 0u;
#pragma unroll
      for (int i = 0; i < N; ++i) m = max(m, umax4(v[i]));
      if (!__any(m == ~0u)) break;
    }
  }
}
template <int N>
__device__ __forceinline__ void poll_load(__amdgpu_buffer_rsrc_t r, const int (&off)[N], u32x4 (&v)[N]) {
  poll_issue<N>(r, off, v);
  poll_finish<N>(r, off, v);
}
__device__ __forceinline__ float dot2bf(unsigned a, unsigned b, float c) {
  return __builtin_amdgcn_fdot2_f32_bf16(__builtin_bit_cast(bf16x2v, a), __builtin_bit_cast(bf16x2v, b), c, false);
}
__device__ __forceinline__ void ln_stats(const unsigned (&x)[16], float lmsk, float& r, float& mr) {
  float s0 = 0.f, s1 = 0.f, q0 = 0.f, q1 = 0.f;
#pragma unroll
  for (int k = 0; k < 16; k += 2) {
    s0 = dot2bf(x[k], 0x3f803f80u, s0); q0 = dot2bf(x[k], x[k], q0);
    s1 = dot2bf(x[k + 1], 0x3f803f80u, s1); q1 = dot2bf(x[k + 1], x[k + 1], q1);
  }
  const float mean = row16_sum((s0 + s1) * lmsk) * (1.0f / D_);
  const float msq = row16_sum((q0 + q1) * lmsk) * (1.0f / D_);
  const float var = fmaxf(msq - mean * mean, 0.f);
  r = __builtin_amdgcn_rcpf(ln_sd(var));
  mr = -rbf(mean) * r;
}

template <class F, int... I>
__device__ __forceinline__ void static_for_impl(F f, std::integer_sequence<int, I...>) { (f(std::integral_constant<int, I>{}), ...); }
template <int N, class F>
__device__ __forceinline__ void static_for(F f) { static_for_impl(f, std::make_integer_sequence<int, N>{}); }
#define SB0() __builtin_amdgcn_sched_barrier(0)
#define WLOAD(f) __builtin_amdgcn_raw_buffer_load_b128(rw, voff + ((f) & 3) * 1024, ((f) >> 2) * 4096, AUX_SC1)
template <int NT, int F0, int LSTART, int LNUM, int LEVERY, int PD>
__device__ __forceinline__ void gemm14(const LAS unsigned char* ap, u32x4 (&ring)[RING], __amdgpu_buffer_rsrc_t rw, int voff, f32x4 (&acc)[NT][2]) {
  u32x4 bq[PD][2];
#pragma unroll
  for (int i = 0; i < PD; ++i) { bq[i][0] = *(const LAS u32x4*)(ap + i * 64); bq[i][1] = *(const LAS u32x4*)(ap + 16 * AS + i * 64); }
#pragma unroll
  for (int i = 0; i < NT; ++i) { acc[i][0] = f32x4{0.f, 0.f, 0.f, 0.f}; acc[i][1] = acc[i][0]; }
  SB0();
#pragma unroll
  for (int ks = 0; ks < 14; ++ks) {
#pragma unroll
    for (int i = 0; i < NT; ++i) {
      const int st = ks * NT + i, f = F0 + st;
      acc[i][0] = mfma16(ring[f % RING], bq[ks % PD][0], acc[i][0]);
      acc[i][1] = mfma16(ring[f % RING], bq[ks % PD][1], acc[i][1]);
      if (st % LEVERY == 0 && st / LEVERY < LNUM) {
        const int g = LSTART + st / LEVERY;
        ring[g % RING] = WLOAD(g);
      }
      if (i == NT - 1 && ks + PD < 14) {
        bq[ks % PD][0] = *(const LAS u32x4*)(ap + (ks + PD) * 64);
        bq[ks % PD][1] = *(const LAS u32x4*)(ap + 16 * AS + (ks + PD) * 64);
      }
      SB0();
    }
  }
}
template <int NT, int F0, int KS0, int KSTEP, int NKS, int LSTART, int LNUM, int PD, bool INIT, class S>
__device__ __forceinline__ void gemm_p(const LAS unsigned char* ap, u32x4 (&ring)[RING], __amdgpu_buffer_rsrc_t rw, int voff, f32x4 (&acc)[NT][2], S side) {
  u32x4 bq[PD][2];
#pragma unroll
  for (int i = 0; i < PD; ++i) {
    bq[i][0] = *(const LAS u32x4*)(ap + (KS0 + i * KSTEP) * 64);
    bq[i][1] = *(const LAS u32x4*)(ap + 16 * AS + (KS0 + i * KSTEP) * 64);
  }
  if (INIT) {
#pragma unroll
    for (int i = 0; i < NT; ++i) { acc[i][0] = f32x4{0.f, 0.f, 0.f, 0.f}; acc[i][1] = acc[i][0]; }
  }
  SB0();
  static_for<NKS * NT>([&](auto stc) {
    constexpr int st = decltype(stc)::value, k = st / NT, i = st % NT, f = F0 + st;
    acc[i][0] = mfma16(ring[f % RING], bq[k % PD][0], acc[i][0]);
    acc[i][1] = mfma16(ring[f % RING], bq[k % PD][1], acc[i][1]);
    constexpr int l0 = st * LNUM / (NKS * NT), l1 = (st + 1) * LNUM / (NKS * NT);
    if constexpr (l1 > l0) {
      static_assert(LSTART + l0 - RING <= f, "slot still in use");
      ring[(LSTART + l0) % RING] = WLOAD(LSTART + l0);
    }
    if constexpr (i == NT - 1 && k + PD < NKS) {
      bq[k % PD][0] = *(const LAS u32x4*)(ap + (KS0 + (k + PD) * KSTEP) * 64);
      bq[k % PD][1] = *(const LAS u32x4*)(ap + 16 * AS + (KS0 + (k + PD) * KSTEP) * 64);
    }
    side(stc);
    SB0();
  });
}
template <int F0, int N>
__device__ __forceinline__ void ring_fill(u32x4 (&ring)[RING], __amdgpu_buffer_rsrc_t rw, int voff) {
#pragma unroll
  for (int f = F0; f < F0 + N; ++f) { ring[f % RING] = WLOAD(f); SB0(); }
}

template <int DELAY>
__device__ __forceinline__ void gather_x2(const GArgs& p, LAS unsigned char* L, __amdgpu_buffer_rsrc_t rs, int pn, int tid) {
  const int lane = tid & 63, w = tid >> 6; (void)lane; (void)w; (void)p;
  u32x4 xin[4];
  int xo[4];
#pragma unroll
  for (int i = 0; i < 4; ++i) {
    const int pce = tid + 512 * i;
    xo[i] = pce < 1792 ? (int)WS_X2B + pn * (int)X2B_BLK + pce * 16 : (int)WS_END;
  }
  if (DELAY) __builtin_amdgcn_s_sleep(DELAY);
  STAMP(0, 27);
  poll_issue<4>(rs, xo, xin);
  poll_finish<4>(rs, xo, xin);
  STAMP(0, 26);
#pragma unroll
  for (int i = 0; i < 4; ++i) {
    const int pce = tid + 512 * i;
    if (i < 3 || pce < 1792) {
      const int dpi = pce >> 3, tq = pce & 7;
      LAS unsigned char* dst = L + G_X + tq * 4096 + (((dpi >> 2) ^ tq) * 16) + (dpi & 3) * 4;
#pragma unroll
      for (int k = 0; k < 4; ++k) *(LAS unsigned*)(dst + k * 1024) = xin[i][k];
    }
  }
}
template <class H>
__device__ __forceinline__ void ln_phase_a(LAS unsigned char* L, int j, int w, int r16, int q, H hook) {
  const int c16 = r16 < 14 ? r16 : 0;
  const float lmsk = r16 < 14 ? 1.0f : 0.0f;
  const int row = 4 * w + q;
  unsigned x[16];
#pragma unroll
  for (int jq = 0; jq < 4; ++jq) {
    const u32x4 t = *(const LAS u32x4*)(L + G_X + row * 1024 + (((((jq + j) & 3) * 14 + c16) ^ w) * 16));
    x[4 * jq] = t.x; x[4 * jq + 1] = t.y; x[4 * jq + 2] = t.z; x[4 * jq + 3] = t.w;
  }
  float r, mr;
  ln_stats(x, lmsk, r, mr);
  hook(std::integral_constant<int, 0>{});
  f32x2 h[16];
#pragma unroll
  for (int jq = 0; jq < 4; ++jq) {
    const LAS unsigned char* mp = L + G_MOD + (((jq + j) & 3) * 112 + c16 * 8) * 4;
    const f32x4 sb0 = *(const LAS f32x4*)mp, sb1 = *(const LAS f32x4*)(mp + 16);
    const f32x4 c10 = *(const LAS f32x4*)(mp + 1792), c11 = *(const LAS f32x4*)(mp + 1792 + 16);
#pragma unroll
    for (int k = 0; k < 4; ++k) {
      const f32x2 xv2 = {bf_lo(x[4 * jq + k]), bf_hi(x[4 * jq + k])};
      const f32x2 c2 = k < 2 ? f32x2{c10[2 * k], c10[2 * k + 1]} : f32x2{c11[2 * k - 4], c11[2 * k - 3]};
      const f32x2 sb2 = k < 2 ? f32x2{sb0[2 * k], sb0[2 * k + 1]} : f32x2{sb1[2 * k - 4], sb1[2 * k - 3]};
      const f32x2 n = xv2 * r + mr;
      h[4 * jq + k] = n * c2 + sb2;
      x[4 * jq + k] = pk_bf16(h[4 * jq + k].x, h[4 * jq + k].y);
    }
  }
  *(LAS u32x4*)(L + G_HQ + row * 240 + (r16 < 14 ? r16 : 14) * 16) = u32x4{x[0], x[1], x[2], x[3]};
  hook(std::integral_constant<int, 1>{});
  ln_stats(x, lmsk, r, mr);
  hook(std::integral_constant<int, 2>{});
#pragma unroll
  for (int jq = 0; jq < 4; ++jq) {
    u32x4 o;
#pragma unroll
    for (int k = 0; k < 4; ++k) {
      const f32x2 n = h[4 * jq + k] * r + mr;
      o[k] = pk_bf16(n.x, n.y);
    }
    *(LAS u32x4*)(L + G_T0 + row * AS + (r16 < 14 ? (((jq + j) & 3) * 14 + r16) * 16 : 896)) = o;
  }
}
__device__ __forceinline__ void x1_offsets(int pn, int j, int w, int r16, int q, int (&gof)[3]) {
  const int c16 = r16 < 14 ? r16 : 0;
  const int go = (int)WS_X1 + pn * 4 * 7168 + ((c16 >> 1) * 64 + w * 8 + (c16 & 1) * 4 + q) * 16;
#pragma unroll
  for (int i = 0; i < 3; ++i) gof[i] = go + ((j + 1 + i) & 3) * 7168;
}
__device__ __forceinline__ void ln_phase_c(LAS unsigned char* L, const u32x4 (&xr)[3], int j, int w, int r16, int q) {
  const int c16 = r16 < 14 ? r16 : 0;
  const float lmsk = r16 < 14 ? 1.0f : 0.0f;
  const int row = 4 * w + q;
  const u32x4 own = *(const LAS u32x4*)(L + G_HQ + row * 240 + c16 * 16);
  unsigned x[16];
#pragma unroll
  for (int k = 0; k < 4; ++k) x[k] = own[k];
#pragma unroll
  for (int k = 4; k < 16; ++k) x[k] = xr[(k >> 2) - 1][k & 3];
  float r, mr;
  ln_stats(x, lmsk, r, mr);
#pragma unroll
  for (int jq = 0; jq < 4; ++jq) {
    u32x4 o;
#pragma unroll
    for (int k = 0; k < 4; ++k) {
      const f32x2 n = f32x2{bf_lo(x[4 * jq + k]), bf_hi(x[4 * jq + k])} * r + mr;
      o[k] = pk_bf16(n.x, n.y);
    }
    *(LAS u32x4*)(L + G_T0 + row * AS + (r16 < 14 ? (((jq + j) & 3) * 14 + r16) * 16 : 896)) = o;
  }
}
__device__ __forceinline__ unsigned gelu2(float a, float b) {
  const unsigned u = pk_bf16(a, b);
  return pk_bf16(gelu_tanh(bf_lo(u)), gelu_tanh(bf_hi(u)));
}
__device__ __forceinline__ u32x2 gelu4(f32x4 a) {
  const unsigned u = pk_bf16(a[0], a[1]), v = pk_bf16(a[2], a[3]);
  u32x2 o;
  o.x = pk_bf16(gelu_tanh(bf_lo(u)), gelu_tanh(bf_hi(u)));
  o.y = pk_bf16(gelu_tanh(bf_lo(v)), gelu_tanh(bf_hi(v)));
  return o;
}

#define ST16(off, v) __builtin_amdgcn_raw_buffer_store_b128(v, rs, (int)(off), 0, AUX_SC1)
typedef __attribute__((ext_vector_type(4))) short s16x4;
typedef unsigned long long u64;

struct PackJob { const GAS unsigned short* src; int ldn, mat, ks, cb, na, nb, fb; };
__device__ __forceinline__ PackJob pack_job(const GArgs& p, int g) {
  PackJob jb;
  const uintptr_t a1 = (uintptr_t)p.w1, a2 = (uintptr_t)p.w2, a3 = (uintptr_t)p.w3, a4 = (uintptr_t)p.w4;
  uintptr_t lo = g < 56 ? a1 : a2, hi = g < 336 ? a3 : a4;
  asm volatile("" : "+s"(lo), "+s"(hi));
  jb.src = (const GAS unsigned short*)(g < 112 ? lo : hi);
  jb.mat = (g >= 56) + (g >= 112) + (g >= 336);
  jb.ldn = jb.mat == 2 ? 1792 : 448;
  g -= g < 112 ? (g < 56 ? 0 : 56) : (g < 336 ? 112 : 336);
  const int sh = jb.mat == 2 ? 4 : 2;
  jb.ks = g >> sh;
  jb.cb = g & ((1 << sh) - 1);
  const int j3 = (jb.ks * 4682) >> 16, ksl = jb.ks - j3 * 14, sl = (jb.cb - j3 - 1) & 3;
  const int is2 = jb.mat == 2, is3 = jb.mat == 3;
  jb.na = is2 * jb.cb * 7 + (1 - is2) * (is3 * j3 + (1 - is3) * jb.cb) * 28;
  jb.nb = 4 - 3 * is2;
  const int own = sl == 3;
  const int f3 = (1 - own) * (84 + (ksl & 1) * 21 + (ksl >> 1) * 3 + sl) + own * (126 + ksl);
  jb.fb = is2 * (28 + jb.ks * 2) + is3 * f3 + (1 - is2 - is3) * (jb.mat * 14 + jb.ks);
  jb.na = __builtin_amdgcn_readfirstlane(jb.na); jb.nb = __builtin_amdgcn_readfirstlane(jb.nb); jb.fb = __builtin_amdgcn_readfirstlane(jb.fb);
  return jb;
}
__device__ __forceinline__ void pack_load(const PackJob& jb, int lane, u32x4 (&r)[8]) {
  const int jn = lane & 15, q = lane >> 4, jl = jn < 14 ? jn : 0;
#pragma unroll
  for (int i = 0; i < 8; ++i) r[i] = *(const GAS u32x4*)(jb.src + (size_t)(jb.ks * 32 + q * 8 + i) * jb.ldn + jb.cb * 112 + jl * 8);
}
__device__ __forceinline__ void pack_shuffle(int lane, u32x4 (&r)[8], LAS unsigned char* Lw) {
  const int jn = lane & 15, q = lane >> 4;
  u32x4 o[8];
  transpose8x8(r, o);
#pragma unroll
  for (int i = 0; i < 8; ++i) *(LAS u32x4*)(Lw + (jn >> 1) * 1024 + (q * 16 + (jn & 1) * 8 + (i ^ (jn >> 1))) * 16) = o[i];
  lds_fence();
#pragma unroll
  for (int m = 0; m < 7; ++m) r[m] = *(const LAS u32x4*)(Lw + m * 1024 + (lane ^ m) * 16);
  lds_fence();
}
__device__ __forceinline__ void pack_store1(const PackJob& jb, int lane, const u32x4 (&r)[8], __amdgpu_buffer_rsrc_t rs, int m) {
  const int nt = jb.na + m * jb.nb;
  __builtin_amdgcn_raw_buffer_store_b128(r[m], rs, lane * 16, (int)WS_WP + ((nt >> 2) * NFRAG + jb.fb + ((nt >> 1) & 1) * 28 + (nt & 1)) * 1024, AUX_SC1);
}
__device__ __forceinline__ void mod_load(const GArgs& p, int u, int tid, u32x4 (&r)[7], float (&sc)[7]) {
  const int rg = tid >> 3, pc = tid & 7;
#pragma unroll
  for (int i = 0; i < 7; ++i) r[i] = *(const u32x4*)(p.wmod + (size_t)(rg + 64 * i) * 2688 + u * 64 + pc * 8);
#pragma unroll
  for (int i = 0; i < 7; ++i) sc[i] = bf2f(p.cvec[rg + 64 * i]);
}
__device__ __forceinline__ void mod_finish(int u, unsigned tag32, int tid, const u32x4 (&r)[7], const float (&cv)[7], LAS float* sm, __amdgpu_buffer_rsrc_t rs) {
  const int rg = tid >> 3, pc = tid & 7;
  float acc[8];
#pragma unroll
  for (int k = 0; k < 8; ++k) acc[k] = 0.f;
#pragma unroll
  for (int i = 0; i < 7; ++i) {
    const float sc = rbf(cv[i] * __builtin_amdgcn_rcpf(1.0f + __builtin_amdgcn_exp2f(-1.4426950408889634f * cv[i])));
#pragma unroll
    for (int k = 0; k < 4; ++k) { acc[2 * k] += sc * bf_lo(r[i][k]); acc[2 * k + 1] += sc * bf_hi(r[i][k]); }
  }
  *(LAS f32x4*)(sm + rg * 68 + pc * 8) = f32x4{acc[0], acc[1], acc[2], acc[3]};
  *(LAS f32x4*)(sm + rg * 68 + pc * 8 + 4) = f32x4{acc[4], acc[5], acc[6], acc[7]};
  bar_lds();
  {
    const int col = tid & 63, part = tid >> 6;
    float s = 0.f;
#pragma unroll
    for (int k = 0; k < 8; ++k) s += sm[(part * 8 + k) * 68 + col];
    sm[64 * 68 + part * 64 + col] = s;
  }
  bar_lds();
  if (tid < 32) {
    f32x2 s = {0.f, 0.f};
#pragma unroll
    for (int k = 0; k < 8; ++k) s += *(const LAS f32x2*)(sm + 64 * 68 + k * 64 + 2 * tid);
    unsigned v = pk_bf16(s.x, s.y);
    const int sec = u / 7;
    if (sec == 1 || sec == 4) v = pk_bf16(bf_lo(v) + 1.0f, bf_hi(v) + 1.0f);
    __builtin_amdgcn_raw_buffer_store_b64(u32x2{v, tag32}, rs, (int)WS_MODG + (u * 32 + tid) * 8, 0, AUX_SC1);
  }
}
template <int YOUNGER>
__device__ __forceinline__ void arrive_flag(LAS unsigned char* L, int cnt_slot, unsigned char* flag, u64 tag, int lane) {
  asm volatile("s_waitcnt vmcnt(%0)" ::"i"(YOUNGER) : "memory");
  if (lane == 0) {
    const unsigned old = __hip_atomic_fetch_add((LAS unsigned*)(L + G_CNT) + cnt_slot, 1u, __ATOMIC_RELAXED, __HIP_MEMORY_SCOPE_WORKGROUP);
    if (old == 7u) __hip_atomic_store((GAS u64*)flag, tag, __ATOMIC_RELAXED, __HIP_MEMORY_SCOPE_AGENT);
  }
}
__device__ __forceinline__ u32x2 flag_load(__amdgpu_buffer_rsrc_t rs, int off) { return __builtin_amdgcn_raw_buffer_load_b64(rs, off, 0, AUX_SC1); }
__device__ __forceinline__ u32x2 flag_wait(__amdgpu_buffer_rsrc_t rs, int off, u32x2 v, unsigned lo, unsigned hi, unsigned lomask) {
  if (__builtin_expect(__any((((v.x ^ lo) & lomask) | (v.y ^ hi)) != 0u), 0)) {
    for (unsigned spins = 0; spins < SPIN_CAP; ++spins) {
      __builtin_amdgcn_s_sleep(2);
      asm volatile("" ::: "memory");
      v = flag_load(rs, off);
      if (!__any((((v.x ^ lo) & lomask) | (v.y ^ hi)) != 0u)) break;
    }
  }
  __builtin_amdgcn_fence(__ATOMIC_ACQUIRE, "wavefront");
  return v;
}
__device__ __forceinline__ void stat_wait(__amdgpu_buffer_rsrc_t rs, int off, u32x4& a, u32x4& b, unsigned tag32) {
  if (__builtin_expect(__any((((a.y ^ tag32) | (a.w ^ tag32)) | ((b.y ^ tag32) | (b.w ^ tag32))) != 0u), 0)) {
    for (unsigned spins = 0; spins < SPIN_CAP; ++spins) {
      __builtin_amdgcn_s_sleep(2);
      asm volatile("" ::: "memory");
      a = __builtin_amdgcn_raw_buffer_load_b128(rs, off, 0, AUX_SC1);
      b = __builtin_amdgcn_raw_buffer_load_b128(rs, off + 16, 0, AUX_SC1);
      if (!__any((((a.y ^ tag32) | (a.w ^ tag32)) | ((b.y ^ tag32) | (b.w ^ tag32))) != 0u)) break;
    }
  }
}
#define FILL_P(i) do { SB0(); __builtin_amdgcn_raw_buffer_store_b128(ONES, rst, (int)WS_P + ((pn * 4 + j) * 3 + (i)) * 7168 + pslot, 0, AUX_SC1); SB0(); } while (0)
#define FILL_X1() do { SB0(); __builtin_amdgcn_raw_buffer_store_b128(ONES, rst, (int)WS_X1 + (pn * 4 + j) * 7168 + pslot, 0, AUX_SC1); SB0(); } while (0)
__device__ __forceinline__ void body(const GArgs& p, LAS unsigned char* L, uintptr_t code0) {
  const bool CONV = blockIdx.x < 224;
  const int tid = threadIdx.x, lane = tid & 63;
  const int w = __builtin_amdgcn_readfirstlane(tid >> 6);
  const int bid = blockIdx.x;
  const int j = bid & 3, pn = (bid >> 3) * 2 + ((bid >> 2) & 1);
  const int r16 = lane & 15, q = lane >> 4;
  const bool cw = w < 7;
  const int wc = cw ? w : 6;
  const int dp = (bid & 7) * 28 + (CONV ? bid >> 3 : 0);
  const __amdgpu_buffer_rsrc_t rs = __builtin_amdgcn_make_buffer_rsrc((void*)p.ws, 0, (int)WS_END, 0x00020000);
  const __amdgpu_buffer_rsrc_t rst = __builtin_amdgcn_make_buffer_rsrc((void*)p.ws, 0, cw ? (int)WS_END : 0, 0x00020000);
  const __amdgpu_buffer_rsrc_t rw =
      __builtin_amdgcn_make_buffer_rsrc((void*)(p.ws + WS_WP + (size_t)(j * 7 + wc) * NFRAG * 1024), 0, cw ? NFRAG * 1024 : 0, 0x00020000);
  const int voff = lane * 16;
  const int pslot = (wc * 64 + lane) * 16;
  const u32x4 ONES = {~0u, ~0u, ~0u, ~0u};
  STAMP(0, 0);
  const int l56 = lane < 56 ? lane : 0;
  const u32x4 xrow = *(const u32x4*)(p.x + (size_t)(bid * 8 + w) * D_ + l56 * 8);
  const u32x2 ep = flag_load(rs, (int)WS_EPOCH);
  const int cl = (bid >> 3) * 16 + (lane & 15);
  const unsigned touch = *(const GAS unsigned*)(code0 + (size_t)(cl < CODE_LINES ? cl : 0) * 128);
  float are, aim, b_re, b_im, cre, cim, ldt;
  u32x4 mr[7], pk[8];
  float mc[7];
  const bool packer = CONV ? w < 2 : (w < 4 && bid < 252);
  const PackJob jb = pack_job(p, CONV ? bid * 2 + (w & 1) : (packer ? 448 + (bid - 224) * 4 + w : 0));
  {
    const size_t o = (size_t)(2 * dp + (w >> 2)) * NS_ + (w & 3) * 64 + lane;
    SB0();
    are = bf2f(p.a_re[o]); aim = bf2f(p.a_im[o]); b_re = bf2f(p.b_re[o]); b_im = bf2f(p.b_im[o]);
    cre = bf2f(p.c_re[o]); cim = bf2f(p.c_im[o]); ldt = bf2f(p.log_dt[2 * dp + (w >> 2)]);
    SB0();
  }
  __builtin_amdgcn_s_barrier();
  __builtin_amdgcn_raw_buffer_store_b128(ONES, rst, (int)WS_X2B + bid * 7168 + pslot, 0, AUX_SC1);
  __builtin_amdgcn_raw_buffer_store_b128(ONES, rst, (int)WS_G + bid * 7168 + pslot, 0, AUX_SC1);
  SB0();
  if (tid < 4) *((LAS unsigned*)(L + G_CNT) + tid) = 0u;
  u64 tag;
  {
    const u64 e = ((u64)__builtin_amdgcn_readfirstlane(ep.y) << 32) | (unsigned)__builtin_amdgcn_readfirstlane(ep.x);
    u64 z = e + 0x9E3779B97F4A7C15ull;
    z = (z ^ (z >> 30)) * 0xBF58476D1CE4E5B9ull;
    z = (z ^ (z >> 27)) * 0x94D049BB133111EBull;
    tag = z ^ (z >> 31);
  }
  const unsigned tag32 = ((unsigned)__builtin_amdgcn_readfirstlane(ep.x) + 1u) * 0x9E3779B1u ^ 0x5bd1e995u;
  const unsigned tlo = (unsigned)tag, thi = (unsigned)(tag >> 32);
  unsigned stv;
#define PUT_STAT() do { if (lane == 0) __builtin_amdgcn_raw_buffer_store_b64(u32x2{stv, tag32}, rs, (int)WS_STG + (bid * 8 + w) * 8, 0, AUX_SC1); } while (0)
  {
    const float msk = lane < 56 ? 1.0f : 0.0f;
    float v[8];
#pragma unroll
    for (int k = 0; k < 4; ++k) { v[2 * k] = bf_lo(xrow[k]); v[2 * k + 1] = bf_hi(xrow[k]); }
    const float mean = wave_sum((((v[0] + v[1]) + (v[2] + v[3])) + ((v[4] + v[5]) + (v[6] + v[7]))) * msk) * (1.0f / D_);
    float qq = 0.f;
#pragma unroll
    for (int k = 0; k < 8; ++k) { const float d = v[k] - mean; qq += d * d; }
    const float sd = ln_sd(wave_sum(qq * msk) * (1.0f / D_));
    stv = pk_bf16(mean, sd);
    if (lane < 56) *(LAS u32x4*)(L + P0_LN + w * LNS + lane * 16) = xrow;
    bar_lds();
#pragma unroll
    for (int u = 0; u < 2; ++u) {
      const int pce = (wc * 2 + u) * 64 + lane;
      const LAS unsigned* a = (const LAS unsigned*)(L + P0_LN + (pce & 3) * (2 * LNS) + (pce >> 2) * 4);
      __builtin_amdgcn_raw_buffer_store_b128(u32x4{a[0], tag32, a[LNS / 4], tag32}, rst, (int)WS_XG + bid * (int)XG_BLK + pce * 16, 0, AUX_SC1);
    }
  }
  asm volatile("" ::"v"(touch));
  STAMP(0, 29);
  const int sto = (int)WS_STG + tid * 32;
  u32x4 ring[RING];
  if (CONV) {
    const float dt = rbf(expf(ldt));
    const float ea = (dt * are) * 1.4426950408889634f, eb = (dt * aim) * 0.15915494309189535f;
    const float bre = dt * b_re, bim = dt * b_im;
    f32x2 wc;
    wc.x = cre * bre - cim * bim;
    wc.y = -(cre * bim + cim * bre);
    asm volatile("" ::"v"(ea), "v"(eb), "v"(wc.x), "v"(wc.y));
    const float mx = rows_max(row16_max(fmaxf(fabsf(wc.x), fabsf(wc.y))));
    int ex = mx > 0.f ? __builtin_amdgcn_frexp_expf(mx) : 0;
    ex = ex < -120 ? -120 : (ex > 120 ? 120 : ex);
    const float scale = __builtin_amdgcn_ldexpf(1.0f, ex), iscale = __builtin_amdgcn_ldexpf(1.0f, -ex);
    wc.x *= iscale; wc.y *= iscale;
    LAS unsigned char* B0 = L + P0_KG + w * (2 * KG_BUF);
    LAS unsigned char* B1 = B0 + KG_BUF;
    const int wo = lane * 4;
    const int ro = r16 * KG_ROW + q * 16;
    f32x2 z8c = cexp_tab(ea, eb, 8.0f);
    z8c.y = -z8c.y;
    const f32x2 z1 = cexp_tab(ea, eb, 1.0f), z128 = cexp_tab(ea, eb, 128.0f), z1024 = cexp_tab(ea, eb, 1024.0f);
    {
      const f32x2 z16c = cmul(z8c, z8c);
      f32x2 t0 = wc, t1 = cmul(wc, z8c);
#pragma unroll
      for (int c = 0; c < 16; c += 2) {
        *(LAS unsigned*)(B0 + c * KG_ROW + wo) = pk_f16(t0);
        *(LAS unsigned*)(B0 + (c + 1) * KG_ROW + wo) = pk_f16(t1);
        if (c < 14) { t0 = cmul(t0, z16c); t1 = cmul(t1, z16c); }
      }
    }
    SB0();
    asm volatile("s_waitcnt vmcnt(2)" ::: "memory");
    PUT_STAT();
    SB0();
    f32x2 ze[8];
    ze[0] = f32x2{1.0f, 0.0f}; ze[1] = z1; ze[2] = cmul(z1, z1); ze[3] = cmul(ze[2], z1); ze[4] = cmul(ze[2], ze[2]);
    ze[5] = cmul(ze[4], z1); ze[6] = cmul(ze[4], ze[2]); ze[7] = cmul(ze[4], ze[3]);
#pragma unroll
    for (int e = 0; e < 8; ++e) {
      *(LAS unsigned*)(B1 + e * KG_ROW + wo) = pk_f16(ze[e]);
      *(LAS unsigned*)(B1 + (8 + e) * KG_ROW + wo) = pk_f16(e == 0 ? z128 : cmul(z128, ze[e]));
    }
    lds_fence();
    u32x4 tf[4];
#pragma unroll
    for (int ks = 0; ks < 4; ++ks) tf[ks] = *(const LAS u32x4*)(B0 + ro + ks * 64);
    u32x4 rf[4];
#pragma unroll
    for (int ks = 0; ks < 4; ++ks) rf[ks] = *(const LAS u32x4*)(B1 + ro + ks * 64);
    lds_fence();
    SB0();
    if (packer) pack_load(jb, lane, pk);
    SB0();
    STAMP(0, 30);
    const f32x2 p2 = cmul(z128, z128), p3 = cmul(p2, z128), p4 = cmul(p2, p2), p5 = cmul(p4, z128), p6 = cmul(p4, p2), p7 = cmul(p4, p3);
    {
#pragma unroll
      for (int e = 0; e < 4; ++e) {
        *(LAS unsigned*)(B0 + e * KG_ROW + wo) = pk_f16(e == 0 ? p2 : cmul(p2, ze[2 * e]));
        *(LAS unsigned*)(B0 + (4 + e) * KG_ROW + wo) = pk_f16(e == 0 ? p3 : cmul(p3, ze[2 * e]));
      }
      *(LAS unsigned*)(B0 + 8 * KG_ROW + wo) = pk_f16(p4);
      *(LAS unsigned*)(B0 + 9 * KG_ROW + wo) = pk_f16(cmul(p4, ze[4]));
      *(LAS unsigned*)(B0 + 10 * KG_ROW + wo) = pk_f16(p5);
      *(LAS unsigned*)(B0 + 11 * KG_ROW + wo) = pk_f16(cmul(p5, ze[4]));
      *(LAS unsigned*)(B0 + 12 * KG_ROW + wo) = pk_f16(p6);
      *(LAS unsigned*)(B0 + 13 * KG_ROW + wo) = pk_f16(cmul(p6, ze[4]));
      *(LAS unsigned*)(B0 + 14 * KG_ROW + wo) = pk_f16(p7);
      *(LAS unsigned*)(B0 + 15 * KG_ROW + wo) = pk_f16(cmul(p7, ze[4]));
    }
    f32x4 acc0 = {0.f, 0.f, 0.f, 0.f}, acc1 = acc0, acc2 = acc0;
#pragma unroll
    for (int ks = 0; ks < 4; ++ks) acc0 = mfma16h(rf[ks], tf[ks], acc0);
    {
      *(LAS unsigned*)(B1 + 0 * KG_ROW + wo) = pk_f16(z1024);
      *(LAS unsigned*)(B1 + 1 * KG_ROW + wo) = pk_f16(cmul(z1024, z128));
      *(LAS unsigned*)(B1 + 2 * KG_ROW + wo) = pk_f16(cmul(z1024, p2));
      *(LAS unsigned*)(B1 + 3 * KG_ROW + wo) = pk_f16(cmul(z1024, p3));
      *(LAS unsigned*)(B1 + 4 * KG_ROW + wo) = pk_f16(cmul(z1024, p4));
      *(LAS unsigned*)(B1 + 5 * KG_ROW + wo) = pk_f16(cmul(z1024, p5));
      *(LAS unsigned*)(B1 + 6 * KG_ROW + wo) = pk_f16(cmul(z1024, p6));
      *(LAS unsigned*)(B1 + 7 * KG_ROW + wo) = pk_f16(cmul(z1024, p7));
      *(LAS unsigned*)(B1 + 8 * KG_ROW + wo) = pk_f16(cmul(z1024, z1024));
    }
    lds_fence();
#pragma unroll
    for (int ks = 0; ks < 4; ++ks) acc1 = mfma16h(*(const LAS u32x4*)(B0 + ro + ks * 64), tf[ks], acc1);
#pragma unroll
    for (int ks = 0; ks < 4; ++ks) acc2 = mfma16h(*(const LAS u32x4*)(B1 + ro + ks * 64), tf[ks], acc2);
    lds_fence();
    {
      LAS float* red = (LAS float*)B0;
#pragma unroll
      for (int r = 0; r < 4; ++r) {
        const int row = 4 * q + r;
        red[(row >> 3) * 128 + 8 * r16 + (row & 7)] = acc0[r] * scale;
        const int i1 = row < 8 ? 256 + (row >> 2) * 64 + 4 * r16 + (row & 3) : 384 + ((row - 8) >> 1) * 32 + 2 * r16 + (row & 1);
        red[i1] = acc1[r] * scale;
        const int i2 = row < 9 ? 512 + row * 16 + r16 : 700;
        red[i2] = acc2[r] * scale;
      }
    }
    STAMP(0, 21);
    bar_lds();
    const int mgo = (int)WS_MODG + ((lane < 3 ? lane : 0) * 224 + dp) * 8;
    SB0();
    u32x4 sg0 = __builtin_amdgcn_raw_buffer_load_b128(rs, sto, 0, AUX_SC1), sg1 = __builtin_amdgcn_raw_buffer_load_b128(rs, sto + 16, 0, AUX_SC1);
    const int xgo = (int)WS_XG + (tid >> 1) * (int)XG_BLK + dp * 64 + (tid & 1) * 32;
    u32x4 xg0 = __builtin_amdgcn_raw_buffer_load_b128(rs, xgo, 0, AUX_SC1), xg1 = __builtin_amdgcn_raw_buffer_load_b128(rs, xgo + 16, 0, AUX_SC1);
    const unsigned dsk2 = *(const unsigned*)(p.dskip + 2 * dp);
    SB0();
    const int kc = tid >> 8, kt = tid & 255;
    LAS float* kd = (LAS float*)(L + P0_KG + kc * (8 * KG_BUF) + KG_BUF);
#pragma unroll
    for (int i = 0; i < 3; ++i) {
      const int oo = kt + 256 * i;
      if (i < 2 || oo < 648) {
        const LAS float* s = (const LAS float*)(L + P0_KG + kc * (8 * KG_BUF)) + oo;
        kd[oo] = (s[0] + s[2 * KG_BUF / 4]) + (s[4 * KG_BUF / 4] + s[6 * KG_BUF / 4]);
      }
    }
    if (kt == 0) kd[648] = 0.f;
    const unsigned OFFS = 0x087EDA52u;
    {
      int off = CV_KCOPY + kt * 16;
      if (kt >= 160) { const int k = kt - 160, s = k >> 3, ch = (k & 7) == 7 ? 6 : (k & 7); off = (272 * (s & 7) + ((OFFS >> (4 * (s & 7))) & 15) + 257 + ch) * 16; }
      *(LAS u32x4*)(L + kc * CV_CH + off) = u32x4{0u, 0u, 0u, 0u};
    }
    bar_lds();
    LAS unsigned char* base = L + kc * CV_CH;
    u32x4 k0;
    {
      float v[8];
#pragma unroll
      for (int i = 0; i < 8; i += 2) {
        const int l0 = 2048 - (kt * 8 + i), l1 = l0 - 1;
        const unsigned bits = pk_bf16((float)l0, (float)l1);
        int i0 = l0 < 128 ? l0 : (int)(bits & 0xffffu) - 17024;
        const int i1 = l1 < 128 ? l1 : (int)(bits >> 16) - 17024;
        if (i == 0) i0 = kt == 0 ? 648 : i0;
        v[i] = kd[i0];
        v[i + 1] = kd[i1];
      }
      k0 = u32x4{pk_bf16(v[0], v[1]), pk_bf16(v[2], v[3]), pk_bf16(v[4], v[5]), pk_bf16(v[6], v[7])};
      *(LAS u32x4*)(base + (2 + kt) * 16) = k0;
      if (kt < 8) {
        const unsigned k00 = pk_bf16(kd[0], 0.f);
        *(LAS u32x4*)(base + (272 * kt + ((OFFS >> (4 * kt)) & 15) + 256) * 16) = u32x4{kt == 0 ? k00 : 0u, 0u, 0u, 0u};
      }
    }
    bar_lds();
    {
      const u32x4 k1 = *(const LAS u32x4*)(base + (2 + kt + 1) * 16);
      unsigned wv[8] = {k0.x, k0.y, k0.z, k0.w, k1.x, k1.y, k1.z, k1.w};
      unsigned vv[7];
#pragma unroll
      for (int i = 0; i < 7; ++i) vv[i] = __builtin_amdgcn_alignbit(wv[i + 1], wv[i], 16);
#pragma unroll
      for (int s = 1; s < 8; ++s) {
        const int k = s >> 1;
        u32x4 o = (s & 1) ? u32x4{vv[k], vv[k + 1], vv[k + 2], vv[k + 3]} : u32x4{wv[k], wv[k + 1], wv[k + 2], wv[k + 3]};
        *(LAS u32x4*)(base + (272 * s + ((OFFS >> (4 * s)) & 15) + kt) * 16) = o;
      }
    }
    float cum0[4], cum1[4];
    {
      const LAS float* kd0 = (const LAS float*)(L + P0_KG + KG_BUF);
      const LAS float* kd1 = (const LAS float*)(L + P0_KG + 8 * KG_BUF + KG_BUF);
#pragma unroll
      for (int i = 0; i < 4; i += 2) {
        const int l0 = 4 * tid + i;
        const unsigned bits = pk_bf16((float)l0, (float)(l0 + 1));
        const int i0 = l0 < 128 ? l0 : (int)(bits & 0xffffu) - 17024, i1 = l0 < 128 ? l0 + 1 : (int)(bits >> 16) - 17024;
        cum0[i] = kd0[i0]; cum0[i + 1] = kd0[i1]; cum1[i] = kd1[i0]; cum1[i + 1] = kd1[i1];
      }
#pragma unroll
      for (int i = 1; i < 4; ++i) { cum0[i] += cum0[i - 1]; cum1[i] += cum1[i - 1]; }
      const float inc0 = wave_scan(cum0[3]), inc1 = wave_scan(cum1[3]);
      const float e0 = inc0 - cum0[3], e1 = inc1 - cum1[3];
#pragma unroll
      for (int i = 0; i < 4; ++i) { cum0[i] += e0; cum1[i] += e1; }
      if (lane == 63) *(LAS f32x2*)(L + P0_MS + w * 8) = f32x2{inc0, inc1};
    }
#define GRAN_BAD() __any(((((sg0.y ^ tag32) | (sg0.w ^ tag32)) | ((sg1.y ^ tag32) | (sg1.w ^ tag32))) | (((xg0.y ^ tag32) | (xg0.w ^ tag32)) | ((xg1.y ^ tag32) | (xg1.w ^ tag32)))) != 0u)
    if (__builtin_expect(GRAN_BAD(), 0)) {
      for (unsigned spins = 0; spins < SPIN_CAP; ++spins) {
        __builtin_amdgcn_s_sleep(2);
        asm volatile("" ::: "memory");
        sg0 = __builtin_amdgcn_raw_buffer_load_b128(rs, sto, 0, AUX_SC1); sg1 = __builtin_amdgcn_raw_buffer_load_b128(rs, sto + 16, 0, AUX_SC1);
        xg0 = __builtin_amdgcn_raw_buffer_load_b128(rs, xgo, 0, AUX_SC1); xg1 = __builtin_amdgcn_raw_buffer_load_b128(rs, xgo + 16, 0, AUX_SC1);
        if (!GRAN_BAD()) break;
      }
    }
#undef GRAN_BAD
    const unsigned xq[4] = {xg0.x, xg0.z, xg1.x, xg1.z};
    STAMP(0, 22);
    unsigned hq[4];
    {
      const unsigned sv[4] = {sg0.x, sg0.z, sg1.x, sg1.z};
#pragma unroll
      for (int k = 0; k < 4; ++k) {
        const float m = bf_lo(sv[k]), r = __builtin_amdgcn_rcpf(bf_hi(sv[k]));
        unsigned t = pk_bf16(bf_lo(xq[k]) - m, bf_hi(xq[k]) - m);
        hq[k] = pk_bf16(bf_lo(t) * r, bf_hi(t) * r);
      }
      LAS unsigned char* U = L + CV_KCOPY + (16 + (tid >> 4)) * CV_UROW + (tid & 15) * 8;
      *(LAS u32x2*)U = u32x2{__builtin_amdgcn_perm(hq[1], hq[0], 0x05040100u), __builtin_amdgcn_perm(hq[3], hq[2], 0x05040100u)};
      *(LAS u32x2*)(U + CV_CH) = u32x2{__builtin_amdgcn_perm(hq[1], hq[0], 0x07060302u), __builtin_amdgcn_perm(hq[3], hq[2], 0x07060302u)};
    }
    if (packer) pack_shuffle(lane, pk, L + P0_KG + (w + 1) * (2 * KG_BUF));
    STAMP(0, 23);
    bar_lds();
    if (bid == 0 && tid == 0) {
      const u64 e = ((u64)ep.y << 32) | ep.x;
      __hip_atomic_store((GAS u64*)(p.ws + WS_EPOCH), e + 1ull, __ATOMIC_RELAXED, __HIP_MEMORY_SCOPE_AGENT);
    }
    u32x2 fv = flag_load(rs, mgo);
    const __amdgpu_buffer_rsrc_t rpk = __builtin_amdgcn_make_buffer_rsrc((void*)p.ws, 0, packer ? (int)WS_END : 0, 0x00020000);
    SB0();
  {
    const int ch = w >> 2, wq = w & 3;
    f32x4 acc[2][4];
#pragma unroll
    for (int n = 0; n < 2; ++n)
#pragma unroll
      for (int m = 0; m < 4; ++m) acc[n][m] = f32x4{0.f, 0.f, 0.f, 0.f};
    const int s = (8 - r16) & 7, u = (r16 + 7) >> 3;
    const LAS unsigned char* ka = L + ch * CV_CH + (272 * s + ((OFFS >> (4 * s)) & 15) + 256 + q - u - 2 * (16 * wq) - 2 * 79) * 16;
    const LAS unsigned char* ub = L + ch * CV_CH + CV_KCOPY + (16 + r16 - 4 * wq - 3) * CV_UROW + q * 16;
#define KFRAG(f) (*(const LAS u32x4*)(ka + (79 - (f)) * 32))
    u32x4 alo[18], ahi[18], bf[4][4];
#define CSTAGE(dl)                                                                                      \
  {                                                                                                     \
    _Pragma("unroll") for (int ks = 0; ks < 2; ++ks) {                                                  \
      bf[dl][2 * ks] = *(const LAS u32x4*)(ub + (3 - (dl)) * CV_UROW + ks * 64);                        \
      bf[dl][2 * ks + 1] = *(const LAS u32x4*)(ub + (19 - (dl)) * CV_UROW + ks * 64);                   \
    }                                                                                                   \
    _Pragma("unroll") for (int i = 0; i < 4; ++i) {                                                     \
      alo[2 + 4 * (dl) + i] = KFRAG(4 * (dl) + i);                                                      \
      ahi[2 + 4 * (dl) + i] = KFRAG(4 * (dl) + i + 64);                                                 \
    }                                                                                                   \
  }
    CSTAGE(0)
    alo[0] = KFRAG(-2); alo[1] = KFRAG(-1); ahi[0] = KFRAG(62); ahi[1] = KFRAG(63);
    SB0();
#pragma unroll
    for (int m = 0; m < 7; ++m) pack_store1(jb, lane, pk, rpk, m);
    FILL_X1(); FILL_P(0); FILL_P(1); FILL_P(2);
    SB0();
#pragma unroll
    for (int dl = 0; dl < 4; ++dl) {
      if (dl < 3) CSTAGE(dl + 1)
#pragma unroll
      for (int ks = 0; ks < 2; ++ks) {
#pragma unroll
        for (int m = 0; m < 4; ++m) {
          acc[0][m] = mfma16(alo[4 * dl + m - 2 * ks + 2], bf[dl][2 * ks], acc[0][m]);
          acc[1][m] = mfma16(alo[4 * dl + m - 2 * ks + 2], bf[dl][2 * ks + 1], acc[1][m]);
          acc[1][m] = mfma16(ahi[4 * dl + m - 2 * ks + 2], bf[dl][2 * ks], acc[1][m]);
        }
      }
      if (dl < 3) {
#pragma unroll
        for (int i = 0; i < 12; ++i) {
          __builtin_amdgcn_sched_group_barrier(0x100, 1, 0);
          __builtin_amdgcn_sched_group_barrier(0x8, 2, 0);
        }
      }
      SB0();
    }
#undef CSTAGE
    STAMP(0, 28);
#undef KFRAG

    LAS unsigned char* P = L + CV_P + w * (32 * CV_PROW);
#pragma unroll
    for (int n = 0; n < 2; ++n)
#pragma unroll
      for (int m = 0; m < 4; ++m) *(LAS f32x4*)(P + (16 * n + r16) * CV_PROW + (16 * m + 4 * q) * 4) = acc[n][m];
    }
    fv = flag_wait(rs, mgo, fv, 0u, tag32, 0u);
    const unsigned sa2 = __builtin_amdgcn_readlane(fv.x, 0), ca2 = __builtin_amdgcn_readlane(fv.x, 1), ga2 = __builtin_amdgcn_readlane(fv.x, 2);
    unsigned hd[4];
    {
      const f32x2 t2 = *(const LAS f32x2*)(L + P0_MS + (lane & 7) * 8);
      const float mk = (lane & 15) < w ? 1.0f : 0.0f;
      const float b0 = row16_sum((lane & 8) ? 0.f : t2.x * mk), b1 = row16_sum((lane & 8) ? 0.f : t2.y * mk);
#pragma unroll
      for (int k = 0; k < 4; ++k) {
        unsigned h = pk_bf16(bf_lo(hq[k]) * bf_lo(ca2), bf_hi(hq[k]) * bf_hi(ca2));
        h = pk_bf16(bf_lo(h) + bf_lo(sa2), bf_hi(h) + bf_hi(sa2));
        hd[k] = pk_bf16(bf_lo(h) * bf_lo(dsk2), bf_hi(h) * bf_hi(dsk2));
        cum0[k] = (cum0[k] + b0) * bf_lo(sa2);
        cum1[k] = (cum1[k] + b1) * bf_hi(sa2);
      }
    }
    asm volatile("s_waitcnt vmcnt(4)" ::: "memory");
    bar_lds();
    STAMP(0, 24);
    {
      const LAS unsigned char* P = L + CV_P + (tid >> 4) * CV_PROW + (tid & 15) * 16;
      f32x4 pa[8];
#pragma unroll
      for (int k = 0; k < 8; ++k) pa[k] = *(const LAS f32x4*)(P + k * (32 * CV_PROW));
      const f32x4 y0 = (pa[0] + pa[1]) + (pa[2] + pa[3]), y1 = (pa[4] + pa[5]) + (pa[6] + pa[7]);
      u32x4 out;
#pragma unroll
      for (int k = 0; k < 4; ++k) {
        const unsigned yy = pk_bf16(y0[k] * bf_lo(ca2) + cum0[k], y1[k] * bf_hi(ca2) + cum1[k]);
        unsigned t = pk_bf16(bf_lo(yy) + bf_lo(hd[k]), bf_hi(yy) + bf_hi(hd[k]));
        t = pk_bf16(bf_lo(ga2) * bf_lo(t), bf_hi(ga2) * bf_hi(t));
        out[k] = sane(pk_bf16(bf_lo(xq[k]) + bf_lo(t), bf_hi(xq[k]) + bf_hi(t)));
      }
      ST16(WS_X2B + (tid >> 3) * (int)X2B_BLK + dp * 128 + (tid & 7) * 16, out);
    }
  } else {
    const int c = bid - 224;
    LAS float* sm = (LAS float*)(L + P0_KG);
    SB0();
    mod_load(p, c, tid, mr, mc);
    SB0();
    asm volatile("s_waitcnt vmcnt(16)" ::: "memory");
    PUT_STAT();
    mod_finish(c, tag32, tid, mr, mc, sm, rs);
    FILL_X1(); FILL_P(0); FILL_P(1); FILL_P(2);
    STAMP(0, 21);
    if (c < 10) {
      mod_load(p, 32 + c, tid, mr, mc);
      bar_lds();
      mod_finish(32 + c, tag32, tid, mr, mc, sm, rs);
    }
    if (packer) {
      pack_load(jb, lane, pk);
      pack_shuffle(lane, pk, L + P0_PK + w * 8192);
#pragma unroll
      for (int m = 0; m < 7; ++m) pack_store1(jb, lane, pk, rs, m);
    }
    arrive_flag<0>(L, 2, p.ws + WS_FLAGC + c * 8, ~tag, lane);
    STAMP(0, 30);
    {
      u32x4 sg0 = __builtin_amdgcn_raw_buffer_load_b128(rs, sto, 0, AUX_SC1), sg1 = __builtin_amdgcn_raw_buffer_load_b128(rs, sto + 16, 0, AUX_SC1);
      stat_wait(rs, sto, sg0, sg1, tag32);
    }
    bar_lds();
  }
  STAMP(0, 25);
  if (!cw) {
    {
      const int mo = (int)WS_MODG + 672 * 8 + l56 * 64;
      u32x4 g0 = __builtin_amdgcn_raw_buffer_load_b128(rs, mo, 0, AUX_SC1), g1 = __builtin_amdgcn_raw_buffer_load_b128(rs, mo + 16, 0, AUX_SC1);
      u32x4 g2 = __builtin_amdgcn_raw_buffer_load_b128(rs, mo + 32, 0, AUX_SC1), g3 = __builtin_amdgcn_raw_buffer_load_b128(rs, mo + 48, 0, AUX_SC1);
      stat_wait(rs, mo, g0, g1, tag32);
      stat_wait(rs, mo + 32, g2, g3, tag32);
      LAS unsigned char* mp = L + G_MOD + l56 * 64;
      *(LAS f32x4*)mp = f32x4{bf_lo(g0.x), bf_hi(g0.x), bf_lo(g0.z), bf_hi(g0.z)};
      *(LAS f32x4*)(mp + 16) = f32x4{bf_lo(g1.x), bf_hi(g1.x), bf_lo(g1.z), bf_hi(g1.z)};
      *(LAS f32x4*)(mp + 32) = f32x4{bf_lo(g2.x), bf_hi(g2.x), bf_lo(g2.z), bf_hi(g2.z)};
      *(LAS f32x4*)(mp + 48) = f32x4{bf_lo(g3.x), bf_hi(g3.x), bf_lo(g3.z), bf_hi(g3.z)};
    }
    gather_x2<0>(p, L, rs, pn, tid);
    STAMP(0, 1);
    bar_lds();
    ln_phase_a(L, j, w, r16, q, [](auto) {});
    STAMP(0, 2);
    bar_lds();
    {
      const int fo = (int)WS_FLAGC + (lane & 31) * 8;
      flag_wait(rs, fo, flag_load(rs, fo), ~tlo, ~thi, ~0u);
      const unsigned char* src = p.ws + WS_WP + (size_t)(j * 7) * NFRAG * 1024 + NRING * 1024 + voff;
#pragma unroll
      for (int ww = 0; ww < 7; ++ww)
#pragma unroll
        for (int i = 0; i < NLDS; ++i)
          __builtin_amdgcn_global_load_lds((const GAS unsigned*)(src + (size_t)ww * NFRAG * 1024 + i * 1024), (LAS unsigned*)(L + G_WL + (ww * NLDS + i) * 1024), 16, 0, AUX_SC1);
    }
    bar_lds();
    STAMP(0, 4);
    bar_lds();
    bar_lds();
    {
      int gof[3];
      u32x4 xr[3];
      x1_offsets(pn, j, w, r16, q, gof);
      poll_load<3>(rs, gof, xr);
      ln_phase_c(L, xr, j, w, r16, q);
    }
    asm volatile("s_waitcnt vmcnt(0)" ::: "memory");
    STAMP(0, 7);
    bar_lds();
    return;
  }
  SB0();
  gather_x2<X2_DELAY>(p, L, rs, pn, tid);
  SB0();
  ring_fill<0, 8>(ring, rw, voff);
  STAMP(0, 1);
  bar_lds();
  u32x2 h2r[2], x2r[2], x1r[2];
  const int ncol = j * 112 + w * 16 + q * 4;
  const int gbo = (int)WS_MODG + (5 * 224 + (ncol >> 1)) * 8;
  u32x4 gbg = __builtin_amdgcn_raw_buffer_load_b128(rs, gbo, 0, AUX_SC1);
#pragma unroll
  for (int T = 0; T < 2; ++T) {
    const int row = 16 * T + r16;
    x2r[T] = *(const LAS u32x2*)(L + G_X + row * 1024 + (((ncol >> 3) ^ (row >> 2)) * 16) + (ncol & 4) * 2);
  }
  SB0();
  ring_fill<8, 5>(ring, rw, voff);
  ln_phase_a(L, j, w, r16, q, [&](auto k) { SB0(); ring_fill<13 + 5 * decltype(k)::value, 5>(ring, rw, voff); });
  SB0();
  ring_fill<28, 4>(ring, rw, voff);
  STAMP(0, 2);
  bar_lds();
  u32x2 gbp;
  {
    u32x4 dup = gbg;
    if (__builtin_expect(__any(((gbg.y ^ tag32) | (gbg.w ^ tag32)) != 0u), 0)) {
      for (unsigned spins = 0; spins < SPIN_CAP; ++spins) {
        asm volatile("" ::: "memory");
        dup = __builtin_amdgcn_raw_buffer_load_b128(rs, gbo, 0, AUX_SC1);
        if (!__any(((dup.y ^ tag32) | (dup.w ^ tag32)) != 0u)) break;
      }
    }
    gbp = u32x2{dup.x, dup.z};
  }
#pragma unroll
  for (int T = 0; T < 2; ++T) h2r[T] = *(const LAS u32x2*)(L + G_HQ + (16 * T + r16) * 240 + (w * 16 + q * 4) * 2);
  const LAS unsigned char* ap0 = L + G_T0 + r16 * AS + q * 16;
  const LAS unsigned char* ap1 = L + G_T1 + r16 * AS + q * 16;
  const int tok = (q & 1) * 16 + r16, octc = 2 * w + (q >> 1);
  {
    f32x4 acc[1][2];
    STAMP(0, 16);
    gemm14<1, 0, 32, 7, 2, 5>(ap0, ring, rw, voff, acc);
    STAMP(0, 17);
    const u32x4 own = make_piece(gelu4(acc[0][0]), gelu4(acc[0][1]));
    __builtin_amdgcn_raw_buffer_store_b128(own, rs, (int)WS_G + (pn * 4 + j) * 7168 + pslot, 0, AUX_SC1);
    SB0();
    STAMP(0, 18);
    ring_fill<39, 4>(ring, rw, voff);
    int goff[4], loff[4];
    bool wrap0;
#pragma unroll
    for (int i = 0; i < 4; ++i) {
      const int jq = (j + i) & 3;
      const int sh = (jq & 1) ? -1 - (jq >> 1) : 1 + (jq >> 1);
      const int it = tok + sh;
      const int sp = pn + (it >= 32 ? -1 : (it < 0 ? 1 : 0));
      if (i == 0) wrap0 = sp != pn;
      goff[i] = (sp < 0 || sp > 63 || (i == 0 && sp == pn)) ? (int)WS_END : (int)WS_G + (sp * 4 + jq) * 7168 + pslot;
      loff[i] = G_T1 + (it & 31) * AS + (jq * 14 + octc) * 16;
    }
    STAMP(0, 3);
    bar_lds();
    u32x4 v[4];
    poll_issue<4>(rs, goff, v);
    __builtin_amdgcn_s_sleep(3);
    ring_fill<43, 5>(ring, rw, voff);
    poll_finish<4>(rs, goff, v);
    *(LAS u32x4*)(L + loff[0]) = wrap0 ? v[0] : own;
#pragma unroll
    for (int i = 1; i < 4; ++i) *(LAS u32x4*)(L + loff[i]) = v[i];
  }
  STAMP(0, 4);
  bar_lds();
  {
    f32x4 acc[1][2];
    gemm14<1, 14, 48, 7, 2, 5>(ap1, ring, rw, voff, acc);
#pragma unroll
    for (int T = 0; T < 2; ++T) {
      const unsigned a = pk_bf16(acc[0][T][0], acc[0][T][1]), b = pk_bf16(acc[0][T][2], acc[0][T][3]);
      x1r[T].x = pk_bf16(bf_lo(h2r[T].x) + bf_lo(a), bf_hi(h2r[T].x) + bf_hi(a));
      x1r[T].y = pk_bf16(bf_lo(h2r[T].y) + bf_lo(b), bf_hi(h2r[T].y) + bf_hi(b));
    }
    const u32x4 own = make_piece(x1r[0], x1r[1]);
    __builtin_amdgcn_raw_buffer_store_b128(own, rs, (int)WS_X1 + (pn * 4 + j) * 7168 + (w * 64 + (tok >> 2) * 8 + (q >> 1) * 4 + (tok & 3)) * 16, 0, AUX_SC1);
    *(LAS u32x4*)(L + G_HQ + tok * 240 + octc * 16) = own;
    SB0();
    STAMP(0, 19);
    ring_fill<55, 4>(ring, rw, voff);
    int gof[3];
    u32x4 xr[3];
    x1_offsets(pn, j, w, r16, q, gof);
    STAMP(0, 5);
    bar_lds();
    poll_issue<3>(rs, gof, xr);
    __builtin_amdgcn_s_sleep(3);
    ring_fill<59, 5>(ring, rw, voff);
    poll_finish<3>(rs, gof, xr);
    STAMP(0, 6);
    ring_fill<64, 4>(ring, rw, voff);
    ln_phase_c(L, xr, j, w, r16, q);
    SB0();
    ring_fill<68, 4>(ring, rw, voff);
  }
  STAMP(0, 7);
  bar_lds();
  {
    {
      f32x4 ca[2][2], cb2[2][2], acc[3][2];
      LAS unsigned char* hp = L + G_T1 + r16 * AS + (w * 64 + q * 4) * 2;
      unsigned glo;
      STAMP(0, 12);
      gemm_p<2, 28, 0, 1, 14, 72, 19, 3, true>(ap0, ring, rw, voff, ca, [](auto) {});
      gemm_p<2, 56, 0, 1, 14, 91, 19, 3, true>(ap0, ring, rw, voff, cb2, [&](auto stc) {
        constexpr int st = decltype(stc)::value;
        if constexpr (st % 3 == 1 && st / 3 < 8) {
          constexpr int c = st / 3, i = c >> 2, T = (c >> 1) & 1;
          if constexpr ((c & 1) == 0) glo = gelu2(ca[i][T][0], ca[i][T][1]);
          else *(LAS u32x2*)(hp + T * 16 * AS + i * 32) = u32x2{glo, gelu2(ca[i][T][2], ca[i][T][3])};
        }
      });
      STAMP(0, 13);
      bar_lds();
      gemm_p<3, 84, 0, 2, 7, 110, 18, 3, true>(ap1, ring, rw, voff, acc, [&](auto stc) {
        constexpr int st = decltype(stc)::value;
        if constexpr (st % 2 == 1 && st / 2 < 8) {
          constexpr int c = st / 2, i = c >> 2, T = (c >> 1) & 1;
          if constexpr ((c & 1) == 0) glo = gelu2(cb2[i][T][0], cb2[i][T][1]);
          else *(LAS u32x2*)(hp + T * 16 * AS + (2 + i) * 32) = u32x2{glo, gelu2(cb2[i][T][2], cb2[i][T][3])};
        }
      });
      STAMP(0, 8);
      bar_lds();
      STAMP(0, 14);
      gemm_p<3, 105, 1, 2, 7, 0, 0, 3, false>(ap1, ring, rw, voff, acc, [](auto) {});
      STAMP(0, 15);
#pragma unroll
      for (int i = 0; i < 3; ++i) {
        const int jd = (j + 1 + i) & 3;
        const u32x4 o = {sane(pk_bf16(acc[i][0][0], acc[i][0][1])), sane(pk_bf16(acc[i][0][2], acc[i][0][3])),
                         sane(pk_bf16(acc[i][1][0], acc[i][1][1])), sane(pk_bf16(acc[i][1][2], acc[i][1][3]))};
        __builtin_amdgcn_raw_buffer_store_b128(o, rs, (int)WS_P + ((pn * 4 + jd) * 3 + (2 - i)) * 7168 + pslot, 0, AUX_SC1);
      }
      SB0();
    }
    STAMP(0, 9);
    f32x4 acc0 = {0.f, 0.f, 0.f, 0.f}, acc1 = acc0;
    {
      const LAS unsigned char* wl = L + G_WL + w * (NLDS * 1024) + voff;
#pragma unroll
      for (int ks = 0; ks < 14; ++ks) {
        const int f = 126 + ks;
        const u32x4 a = f < NRING ? ring[f % RING] : *(const LAS u32x4*)(wl + (f - NRING) * 1024);
        acc0 = mfma16(a, *(const LAS u32x4*)(ap1 + ks * 64), acc0);
        acc1 = mfma16(a, *(const LAS u32x4*)(ap1 + 16 * AS + ks * 64), acc1);
      }
    }
    STAMP(0, 10);
    u32x4 v[3];
    const int go = (int)WS_P + (pn * 4 + j) * 3 * 7168 + pslot;
    const int gof[3] = {go, go + 7168, go + 2 * 7168};
    poll_load<3>(rs, gof, v);
    STAMP(0, 11);
#pragma unroll
    for (int T = 0; T < 2; ++T) {
      const f32x4 ac = T ? acc1 : acc0;
      float hh[4];
#pragma unroll
      for (int k = 0; k < 2; ++k) {
        hh[2 * k] = ac[2 * k] + ((bf_lo(v[0][2 * T + k]) + bf_lo(v[1][2 * T + k])) + bf_lo(v[2][2 * T + k]));
        hh[2 * k + 1] = ac[2 * k + 1] + ((bf_hi(v[0][2 * T + k]) + bf_hi(v[1][2 * T + k])) + bf_hi(v[2][2 * T + k]));
      }
      unsigned a = pk_bf16(hh[0], hh[1]), b = pk_bf16(hh[2], hh[3]);
      a = pk_bf16(bf_lo(x1r[T].x) + bf_lo(a), bf_hi(x1r[T].x) + bf_hi(a));
      b = pk_bf16(bf_lo(x1r[T].y) + bf_lo(b), bf_hi(x1r[T].y) + bf_hi(b));
      a = pk_bf16(bf_lo(gbp.x) * bf_lo(a), bf_hi(gbp.x) * bf_hi(a));
      b = pk_bf16(bf_lo(gbp.y) * bf_lo(b), bf_hi(gbp.y) * bf_hi(b));
      u32x2 o;
      o.x = pk_bf16(bf_lo(x2r[T].x) + bf_lo(a), bf_hi(x2r[T].x) + bf_hi(a));
      o.y = pk_bf16(bf_lo(x2r[T].y) + bf_lo(b), bf_hi(x2r[T].y) + bf_hi(b));
      *(u32x2*)(p.out + (size_t)(pn * 32 + 16 * T + r16) * D_ + ncol) = o;
    }
  }
  STAMP(0, 20);
}

__global__ __launch_bounds__(512, 2) void k_all(GArgs p) {
  extern __shared__ __attribute__((aligned(16))) unsigned char smem[];
  const uintptr_t code0 = (uintptr_t)__builtin_amdgcn_s_getpc() & ~(uintptr_t)127;
  body(p, (LAS unsigned char*)smem, code0);
}

}

extern "C" void kernel_launch(void* const* d_in, const int* in_sizes, int n_in, void* d_out, int out_size, void* d_ws,
                              size_t ws_size, hipStream_t stream) {
  (void)in_sizes; (void)n_in; (void)out_size; (void)ws_size;
  GArgs a;
  a.x = (const unsigned short*)d_in[0];
  a.a_re = (const unsigned short*)d_in[1];
  a.a_im = (const unsigned short*)d_in[2];
  a.b_re = (const unsigned short*)d_in[3];
  a.b_im = (const unsigned short*)d_in[4];
  a.c_re = (const unsigned short*)d_in[5];
  a.c_im = (const unsigned short*)d_in[6];
  a.log_dt = (const unsigned short*)d_in[7];
  a.dskip = (const unsigned short*)d_in[8];
  a.w1 = (const unsigned short*)d_in[10];
  a.w2 = (const unsigned short*)d_in[11];
  a.w3 = (const unsigned short*)d_in[12];
  a.w4 = (const unsigned short*)d_in[13];
  a.cvec = (const unsigned short*)d_in[14];
  a.wmod = (const unsigned short*)d_in[15];
  a.ws = (unsigned char*)d_ws;
  a.out = (unsigned short*)d_out;
  (void)hipFuncSetAttribute((const void*)k_all, hipFuncAttributeMaxDynamicSharedMemorySize, G_LDS);
  k_all<<<256, 512, G_LDS, stream>>>(a);
}
```

```cpp
#include <hip/hip_runtime.h>
#include <hip/hip_bf16.h>
#include <stdint.h>
#include <type_traits>
#include <utility>

namespace {

typedef __attribute__((ext_vector_type(8))) short bf16x8;
typedef __attribute__((ext_vector_type(8))) _Float16 f16x8;
typedef __attribute__((ext_vector_type(2))) _Float16 f16x2;
typedef __attribute__((ext_vector_type(4))) float f32x4;
typedef __attribute__((ext_vector_type(2))) float f32x2;
typedef __attribute__((ext_vector_type(4))) unsigned u32x4;
typedef __attribute__((ext_vector_type(2))) unsigned u32x2;
typedef __attribute__((ext_vector_type(2))) __bf16 bf16x2v;
#define GAS __attribute__((address_space(1)))
#define LAS __attribute__((address_space(3)))

constexpr int L_ = 2048, D_ = 448, NS_ = 256;

constexpr size_t WS_EPOCH = 0;
constexpr size_t WS_FLAGC = 4608;
constexpr size_t WS_MODG = 16384;
constexpr size_t WS_STG = 32768;
constexpr int NFRAG = 140;
constexpr size_t WS_XG = 65536;
constexpr size_t XG_BLK = 224 * 64;
constexpr size_t WS_WP = WS_XG + 256 * XG_BLK;
constexpr size_t WS_X2B = WS_WP + (size_t)28 * NFRAG * 1024;
constexpr size_t X2B_BLK = 224 * 128;
constexpr size_t WS_G = WS_X2B + 64 * X2B_BLK;
constexpr size_t WS_X1 = WS_G + (size_t)64 * 4 * 7168;
constexpr size_t WS_P = WS_X1 + (size_t)64 * 4 * 7168;
constexpr size_t WS_END = WS_P + (size_t)64 * 12 * 7168;

#define STAMP(base, k) do { } while (0)

__device__ __forceinline__ float bf_lo(unsigned u) { return __builtin_bit_cast(float, u << 16); }
__device__ __forceinline__ float bf_hi(unsigned u) { return __builtin_bit_cast(float, u & 0xffff0000u); }
__device__ __forceinline__ float bf2f(unsigned short h) { return __builtin_bit_cast(float, (unsigned)h << 16); }
__device__ __forceinline__ unsigned pk_bf16(float a, float b) {
  f32x2 v = {a, b};
  bf16x2v r = __builtin_convertvector(v, bf16x2v);
  return __builtin_bit_cast(unsigned, r);
}
__device__ __forceinline__ unsigned pk_f16(f32x2 v) {
  f16x2 r = __builtin_convertvector(v, f16x2);
  return __builtin_bit_cast(unsigned, r);
}
__device__ __forceinline__ float rbf(float a) { return bf_lo(pk_bf16(a, a)); }
__device__ __forceinline__ f32x4 mfma16(u32x4 a, u32x4 b, f32x4 c) {
  return __builtin_amdgcn_mfma_f32_16x16x32_bf16(__builtin_bit_cast(bf16x8, a), __builtin_bit_cast(bf16x8, b), c, 0, 0, 0);
}
__device__ __forceinline__ f32x4 mfma16h(u32x4 a, u32x4 b, f32x4 c) {
  return __builtin_amdgcn_mfma_f32_16x16x32_f16(__builtin_bit_cast(f16x8, a), __builtin_bit_cast(f16x8, b), c, 0, 0, 0);
}
template <int CTRL>
__device__ __forceinline__ float dpp_f(float v) {
  return __builtin_bit_cast(float, __builtin_amdgcn_update_dpp(0, __builtin_bit_cast(int, v), CTRL, 0xf, 0xf, true));
}
__device__ __forceinline__ float row16_sum(float v) {
  v += dpp_f<0xB1>(v);
  v += dpp_f<0x4E>(v);
  v += dpp_f<0x141>(v);
  v += dpp_f<0x140>(v);
  return v;
}
__device__ __forceinline__ float row16_max(float v) {
  v = fmaxf(v, dpp_f<0xB1>(v));
  v = fmaxf(v, dpp_f<0x4E>(v));
  v = fmaxf(v, dpp_f<0x141>(v));
  v = fmaxf(v, dpp_f<0x140>(v));
  return v;
}
__device__ __forceinline__ void swap16(float v, float& a, float& b) {
  const unsigned u = __builtin_bit_cast(unsigned, v), u2 = u;
  auto r = __builtin_amdgcn_permlane16_swap(u, u2, false, false);
  const unsigned r0 = r[0], r1 = r[1];
  a = __builtin_bit_cast(float, r0); b = __builtin_bit_cast(float, r1);
}
__device__ __forceinline__ void swap32(float v, float& a, float& b) {
  const unsigned u = __builtin_bit_cast(unsigned, v), u2 = u;
  auto r = __builtin_amdgcn_permlane32_swap(u, u2, false, false);
  const unsigned r0 = r[0], r1 = r[1];
  a = __builtin_bit_cast(float, r0); b = __builtin_bit_cast(float, r1);
}
__device__ __forceinline__ float rows_sum(float v) {
  float a, b;
  swap16(v, a, b); v = a + b;
  swap32(v, a, b);
  return a + b;
}
__device__ __forceinline__ float rows_max(float v) {
  float a, b;
  swap16(v, a, b); v = fmaxf(a, b);
  swap32(v, a, b);
  return fmaxf(a, b);
}
template <int CTRL, int RMASK>
__device__ __forceinline__ float dpp_z(float v) {
  return __builtin_bit_cast(float, __builtin_amdgcn_update_dpp(0, __builtin_bit_cast(int, v), CTRL, RMASK, 0xf, false));
}
__device__ __forceinline__ float wave_scan(float v) {
  v += dpp_z<0x111, 0xf>(v);
  v += dpp_z<0x112, 0xf>(v);
  v += dpp_z<0x114, 0xf>(v);
  v += dpp_z<0x118, 0xf>(v);
  v += dpp_z<0x142, 0xa>(v);
  v += dpp_z<0x143, 0xc>(v);
  return v;
}
__device__ __forceinline__ float wave_sum(float v) { return rows_sum(row16_sum(v)); }
__device__ __forceinline__ float gelu_tanh(float x) {
  const float c1 = -2.0f * 0.7978845608028654f * 1.4426950408889634f;
  const float c2 = c1 * 0.044715f;
  float t = x * (c1 + c2 * x * x);
  float e = __builtin_amdgcn_exp2f(t);
  return x * __builtin_amdgcn_rcpf(1.0f + e);
}
__device__ __forceinline__ float ln_sd(float var) { return rbf(__builtin_amdgcn_sqrtf(rbf(rbf(var) + 1e-5f))); }
__device__ __forceinline__ void lds_fence() { asm volatile("" ::: "memory"); }
__device__ __forceinline__ void bar_lds() {
  asm volatile("s_waitcnt lgkmcnt(0)" ::: "memory");
  __builtin_amdgcn_s_barrier();
  asm volatile("" ::: "memory");
}

__device__ __forceinline__ void transpose8x8(const u32x4 (&r)[8], u32x4 (&o)[8]) {
#pragma unroll
  for (int c2 = 0; c2 < 4; ++c2) {
#pragma unroll
    for (int rp = 0; rp < 4; ++rp) {
      unsigned a = r[2 * rp][c2], b = r[2 * rp + 1][c2];
      o[2 * c2][rp] = __builtin_amdgcn_perm(b, a, 0x05040100u);
      o[2 * c2 + 1][rp] = __builtin_amdgcn_perm(b, a, 0x07060302u);
    }
  }
}
__device__ __forceinline__ f32x2 cmul(f32x2 a, f32x2 b) {
  f32x2 r, t;
  asm("v_pk_mul_f32 %0, %1, %2 op_sel_hi:[0,1]" : "=v"(t) : "v"(a), "v"(b));
  asm("v_pk_fma_f32 %0, %1, %2, %3 op_sel:[1,1,0] op_sel_hi:[1,0,1] neg_lo:[1,0,0]" : "=v"(r) : "v"(a), "v"(b), "v"(t));
  return r;
}
__device__ __forceinline__ f32x2 cexp_tab(float a, float b, float l) {
  const float e = __builtin_amdgcn_exp2f(a * l), ph = __builtin_amdgcn_fractf(b * l);
  f32x2 r;
  r.x = e * __builtin_amdgcn_cosf(ph);
  r.y = e * __builtin_amdgcn_sinf(ph);
  return r;
}

constexpr int CV_KCOPY = 8 * 272 * 16;
constexpr int CV_UROW = 160;
constexpr int CV_U = 48 * CV_UROW;
constexpr int CV_CH = CV_KCOPY + CV_U;
constexpr int CV_LDS = 2 * CV_CH;
constexpr int CV_PROW = 272;
static_assert(4 * 32 * CV_PROW <= CV_KCOPY, "partials alias the kernel copies");

struct GArgs {
  const unsigned short* x;
  const unsigned short* a_re;
  const unsigned short* a_im;
  const unsigned short* b_re;
  const unsigned short* b_im;
  const unsigned short* c_re;
  const unsigned short* c_im;
  const unsigned short* log_dt;
  const unsigned short* dskip;
  const unsigned short* w1;
  const unsigned short* w2;
  const unsigned short* w3;
  const unsigned short* w4;
  const unsigned short* cvec;
  const unsigned short* wmod;
  unsigned char* ws;
  unsigned short* out;
};
constexpr int AS = 928;
constexpr int G_T0 = 0;
constexpr int G_T1 = G_T0 + 32 * AS;
constexpr int G_X = G_T1;
constexpr int G_HQ = G_X + 32768;
constexpr int G_WL = G_HQ + 32 * 240;
constexpr int NLDS = 12;
constexpr int NRING = NFRAG - NLDS;
constexpr int G_MOD = G_WL + 7 * NLDS * 1024;
constexpr int G_CNT = G_MOD + 2 * 448 * 4;
constexpr int P0_MS = G_CNT + 16;
constexpr int G_LDS = P0_MS + 256;
constexpr int KG_ROW = 288;
constexpr int KG_BUF = 16 * KG_ROW;
constexpr int P0_KG = CV_LDS;
constexpr int P0_LN = 0;
constexpr int LNS = 928;
constexpr int P0_PK = 16384;
static_assert(P0_KG + 16 * KG_BUF <= G_CNT, "tables");
static_assert(G_MOD >= CV_LDS && G_LDS <= 163840, "LDS map");
constexpr int RING = 44;
constexpr int RINGB = 16;
constexpr int RING0 = RINGB + 6;
constexpr int CV_P = CV_LDS;
static_assert(CV_P + 8 * 32 * CV_PROW <= G_MOD, "partials");
constexpr unsigned SPIN_CAP = 1u << 14;
constexpr int AUX_SC1 = 16;
constexpr int CODE_LINES = 320;
#define PRETOUCH 1
#define X2_DELAY 20
#define POLL_SLEEP 1

__device__ __forceinline__ unsigned sane(unsigned v) { return v < 0xFFFFFFFEu ? v : 0xFFFFFFFEu; }
__device__ __forceinline__ unsigned umax4(const u32x4& v) { return max(max(v.x, v.y), max(v.z, v.w)); }
__device__ __forceinline__ bool bad4(const u32x4& v) { return umax4(v) == ~0u; }
__device__ __forceinline__ u32x4 make_piece(u32x2 a, u32x2 b) {
  auto rx = __builtin_amdgcn_permlane16_swap(a.x, b.x, false, false);
  auto ry = __builtin_amdgcn_permlane16_swap(a.y, b.y, false, false);
  return u32x4{sane(rx[0]), sane(ry[0]), sane(rx[1]), sane(ry[1])};
}
template <int N>
__device__ __forceinline__ void poll_issue(__amdgpu_buffer_rsrc_t r, const int (&off)[N], u32x4 (&v)[N]) {
#pragma unroll
  for (int i = 0; i < N; ++i) v[i] = __builtin_amdgcn_raw_buffer_load_b128(r, off[i], 0, AUX_SC1);
  __builtin_amdgcn_sched_barrier(0);
}
template <int N>
__device__ __forceinline__ void poll_finish(__amdgpu_buffer_rsrc_t r, const int (&off)[N], u32x4 (&v)[N]) {
  unsigned m = 0u;
#pragma unroll
  for (int i = 0; i < N; ++i) m = max(m, umax4(v[i]));
  if (__builtin_expect(__any(m == ~0u), 0)) {
    for (unsigned spins = 0; spins < SPIN_CAP; ++spins) {
      __builtin_amdgcn_s_sleep(POLL_SLEEP);
      asm volatile("" ::: "memory");
#pragma unroll
      for (int i = 0; i < N; ++i)
        if (bad4(v[i])) v[i] = __builtin_amdgcn_raw_buffer_load_b128(r, off[i], 0, AUX_SC1);
      m = 0u;
#pragma unroll
      for (int i = 0; i < N; ++i) m = max(m, umax4(v[i]));
      if (!__any(m == ~0u)) break;
    }
  }
}
template <int N>
__device__ __forceinline__ void poll_load(__amdgpu_buffer_rsrc_t r, const int (&off)[N], u32x4 (&v)[N]) {
  poll_issue<N>(r, off, v);
  poll_finish<N>(r, off, v);
}
__device__ __forceinline__ float dot2bf(unsigned a, unsigned b, float c) {
  return __builtin_amdgcn_fdot2_f32_bf16(__builtin_bit_cast(bf16x2v, a), __builtin_bit_cast(bf16x2v, b), c, false);
}
__device__ __forceinline__ void ln_stats(const unsigned (&x)[16], float lmsk, float& r, float& mr) {
  float s0 = 0.f, s1 = 0.f, q0 = 0.f, q1 = 0.f;
#pragma unroll
  for (int k = 0; k < 16; k += 2) {
    s0 = dot2bf(x[k], 0x3f803f80u, s0); q0 = dot2bf(x[k], x[k], q0);
    s1 = dot2bf(x[k + 1], 0x3f803f80u, s1); q1 = dot2bf(x[k + 1], x[k + 1], q1);
  }
  const float mean = row16_sum((s0 + s1) * lmsk) * (1.0f / D_);
  const float msq = row16_sum((q0 + q1) * lmsk) * (1.0f / D_);
  const float var = fmaxf(msq - mean * mean, 0.f);
  r = __builtin_amdgcn_rcpf(ln_sd(var));
  mr = -rbf(mean) * r;
}

template <class F, int... I>
__device__ __forceinline__ void static_for_impl(F f, std::integer_sequence<int, I...>) { (f(std::integral_constant<int, I>{}), ...); }
template <int N, class F>
__device__ __forceinline__ void static_for(F f) { static_for_impl(f, std::make_integer_sequence<int, N>{}); }
#define SB0() __builtin_amdgcn_sched_barrier(0)
#define WLOAD(f) __builtin_amdgcn_raw_buffer_load_b128(rw, voff + ((f) & 3) * 1024, ((f) >> 2) * 4096, AUX_SC1)
template <int NT, int F0, int LSTART, int LNUM, int LEVERY, int PD>
__device__ __forceinline__ void gemm14(const LAS unsigned char* ap, u32x4 (&ring)[RING], __amdgpu_buffer_rsrc_t rw, int voff, f32x4 (&acc)[NT][2]) {
  u32x4 bq[PD][2];
#pragma unroll
  for (int i = 0; i < PD; ++i) { bq[i][0] = *(const LAS u32x4*)(ap + i * 64); bq[i][1] = *(const LAS u32x4*)(ap + 16 * AS + i * 64); }
#pragma unroll
  for (int i = 0; i < NT; ++i) { acc[i][0] = f32x4{0.f, 0.f, 0.f, 0.f}; acc[i][1] = acc[i][0]; }
  SB0();
#pragma unroll
  for (int ks = 0; ks < 14; ++ks) {
#pragma unroll
    for (int i = 0; i < NT; ++i) {
      const int st = ks * NT + i, f = F0 + st;
      acc[i][0] = mfma16(ring[f % RING], bq[ks % PD][0], acc[i][0]);
      acc[i][1] = mfma16(ring[f % RING], bq[ks % PD][1], acc[i][1]);
      if (st % LEVERY == 0 && st / LEVERY < LNUM) {
        const int g = LSTART + st / LEVERY;
        ring[g % RING] = WLOAD(g);
      }
      if (i == NT - 1 && ks + PD < 14) {
        bq[ks % PD][0] = *(const LAS u32x4*)(ap + (ks + PD) * 64);
        bq[ks % PD][1] = *(const LAS u32x4*)(ap + 16 * AS + (ks + PD) * 64);
      }
      SB0();
    }
  }
}
template <int NT, int F0, int KS0, int KSTEP, int NKS, int LSTART, int LNUM, int PD, bool INIT, class S>
__device__ __forceinline__ void gemm_p(const LAS unsigned char* ap, u32x4 (&ring)[RING], __amdgpu_buffer_rsrc_t rw, int voff, f32x4 (&acc)[NT][2], S side) {
  u32x4 bq[PD][2];
#pragma unroll
  for (int i = 0; i < PD; ++i) {
    bq[i][0] = *(const LAS u32x4*)(ap + (KS0 + i * KSTEP) * 64);
    bq[i][1] = *(const LAS u32x4*)(ap + 16 * AS + (KS0 + i * KSTEP) * 64);
  }
  if (INIT) {
#pragma unroll
    for (int i = 0; i < NT; ++i) { acc[i][0] = f32x4{0.f, 0.f, 0.f, 0.f}; acc[i][1] = acc[i][0]; }
  }
  SB0();
  static_for<NKS * NT>([&](auto stc) {
    constexpr int st = decltype(stc)::value, k = st / NT, i = st % NT, f = F0 + st;
    acc[i][0] = mfma16(ring[f % RING], bq[k % PD][0], acc[i][0]);
    acc[i][1] = mfma16(ring[f % RING], bq[k % PD][1], acc[i][1]);
    constexpr int l0 = st * LNUM / (NKS * NT), l1 = (st + 1) * LNUM / (NKS * NT);
    if constexpr (l1 > l0) {
      static_assert(LSTART + l0 - RING <= f, "slot still in use");
      ring[(LSTART + l0) % RING] = WLOAD(LSTART + l0);
    }
    if constexpr (i == NT - 1 && k + PD < NKS) {
      bq[k % PD][0] = *(const LAS u32x4*)(ap + (KS0 + (k + PD) * KSTEP) * 64);
      bq[k % PD][1] = *(const LAS u32x4*)(ap + 16 * AS + (KS0 + (k + PD) * KSTEP) * 64);
    }
    side(stc);
    SB0();
  });
}
template <int F0, int N>
__device__ __forceinline__ void ring_fill(u32x4 (&ring)[RING], __amdgpu_buffer_rsrc_t rw, int voff) {
#pragma unroll
  for (int f = F0; f < F0 + N; ++f) { ring[f % RING] = WLOAD(f); SB0(); }
}

template <int DELAY>
__device__ __forceinline__ void gather_x2(const GArgs& p, LAS unsigned char* L, __amdgpu_buffer_rsrc_t rs, int pn, int tid) {
  const int lane = tid & 63, w = tid >> 6; (void)lane; (void)w; (void)p;
  u32x4 xin[4];
  int xo[4];
#pragma unroll
  for (int i = 0; i < 4; ++i) {
    const int pce = tid + 512 * i;
    xo[i] = pce < 1792 ? (int)WS_X2B + pn * (int)X2B_BLK + pce * 16 : (int)WS_END;
  }
  if (DELAY) __builtin_amdgcn_s_sleep(DELAY);
  STAMP(0, 27);
  poll_issue<4>(rs, xo, xin);
  poll_finish<4>(rs, xo, xin);
  STAMP(0, 26);
#pragma unroll
  for (int i = 0; i < 4; ++i) {
    const int pce = tid + 512 * i;
    if (i < 3 || pce < 1792) {
      const int dpi = pce >> 3, tq = pce & 7;
      LAS unsigned char* dst = L + G_X + tq * 4096 + (((dpi >> 2) ^ tq) * 16) + (dpi & 3) * 4;
#pragma unroll
      for (int k = 0; k < 4; ++k) *(LAS unsigned*)(dst + k * 1024) = xin[i][k];
    }
  }
}
template <class H>
__device__ __forceinline__ void ln_phase_a(LAS unsigned char* L, int j, int w, int r16, int q, H hook) {
  const int c16 = r16 < 14 ? r16 : 0;
  const float lmsk = r16 < 14 ? 1.0f : 0.0f;
  const int row = 4 * w + q;
  unsigned x[16];
#pragma unroll
  for (int jq = 0; jq < 4; ++jq) {
    const u32x4 t = *(const LAS u32x4*)(L + G_X + row * 1024 + (((((jq + j) & 3) * 14 + c16) ^ w) * 16));
    x[4 * jq] = t.x; x[4 * jq + 1] = t.y; x[4 * jq + 2] = t.z; x[4 * jq + 3] = t.w;
  }
  float r, mr;
  ln_stats(x, lmsk, r, mr);
  hook(std::integral_constant<int, 0>{});
  f32x2 h[16];
#pragma unroll
  for (int jq = 0; jq < 4; ++jq) {
    const LAS unsigned char* mp = L + G_MOD + (((jq + j) & 3) * 112 + c16 * 8) * 4;
    const f32x4 sb0 = *(const LAS f32x4*)mp, sb1 = *(const LAS f32x4*)(mp + 16);
    const f32x4 c10 = *(const LAS f32x4*)(mp + 1792), c11 = *(const LAS f32x4*)(mp + 1792 + 16);
#pragma unroll
    for (int k = 0; k < 4; ++k) {
      const f32x2 xv2 = {bf_lo(x[4 * jq + k]), bf_hi(x[4 * jq + k])};
      const f32x2 c2 = k < 2 ? f32x2{c10[2 * k], c10[2 * k + 1]} : f32x2{c11[2 * k - 4], c11[2 * k - 3]};
      const f32x2 sb2 = k < 2 ? f32x2{sb0[2 * k], sb0[2 * k + 1]} : f32x2{sb1[2 * k - 4], sb1[2 * k - 3]};
      const f32x2 n = xv2 * r + mr;
      h[4 * jq + k] = n * c2 + sb2;
      x[4 * jq + k] = pk_bf16(h[4 * jq + k].x, h[4 * jq + k].y);
    }
  }
  *(LAS u32x4*)(L + G_HQ + row * 240 + (r16 < 14 ? r16 : 14) * 16) = u32x4{x[0], x[1], x[2], x[3]};
  hook(std::integral_constant<int, 1>{});
  ln_stats(x, lmsk, r, mr);
  hook(std::integral_constant<int, 2>{});
#pragma unroll
  for (int jq = 0; jq < 4; ++jq) {
    u32x4 o;
#pragma unroll
    for (int k = 0; k < 4; ++k) {
      const f32x2 n = h[4 * jq + k] * r + mr;
      o[k] = pk_bf16(n.x, n.y);
    }
    *(LAS u32x4*)(L + G_T0 + row * AS + (r16 < 14 ? (((jq + j) & 3) * 14 + r16) * 16 : 896)) = o;
  }
}
__device__ __forceinline__ void x1_offsets(int pn, int j, int w, int r16, int q, int (&gof)[3]) {
  const int c16 = r16 < 14 ? r16 : 0;
  const int go = (int)WS_X1 + pn * 4 * 7168 + ((c16 >> 1) * 64 + w * 8 + (c16 & 1) * 4 + q) * 16;
#pragma unroll
  for (int i = 0; i < 3; ++i) gof[i] = go + ((j + 1 + i) & 3) * 7168;
}
__device__ __forceinline__ void ln_phase_c(LAS unsigned char* L, const u32x4 (&xr)[3], int j, int w, int r16, int q) {
  const int c16 = r16 < 14 ? r16 : 0;
  const float lmsk = r16 < 14 ? 1.0f : 0.0f;
  const int row = 4 * w + q;
  const u32x4 own = *(const LAS u32x4*)(L + G_HQ + row * 240 + c16 * 16);
  unsigned x[16];
#pragma unroll
  for (int k = 0; k < 4; ++k) x[k] = own[k];
#pragma unroll
  for (int k = 4; k < 16; ++k) x[k] = xr[(k >> 2) - 1][k & 3];
  float r, mr;
  ln_stats(x, lmsk, r, mr);
#pragma unroll
  for (int jq = 0; jq < 4; ++jq) {
    u32x4 o;
#pragma unroll
    for (int k = 0; k < 4; ++k) {
      const f32x2 n = f32x2{bf_lo(x[4 * jq + k]), bf_hi(x[4 * jq + k])} * r + mr;
      o[k] = pk_bf16(n.x, n.y);
    }
    *(LAS u32x4*)(L + G_T0 + row * AS + (r16 < 14 ? (((jq + j) & 3) * 14 + r16) * 16 : 896)) = o;
  }
}
__device__ __forceinline__ unsigned gelu2(float a, float b) {
  return pk_bf16(gelu_tanh(a), gelu_tanh(b));
}
__device__ __forceinline__ u32x2 gelu4(f32x4 a) { return u32x2{gelu2(a[0], a[1]), gelu2(a[2], a[3])}; }

#define ST16(off, v) __builtin_amdgcn_raw_buffer_store_b128(v, rs, (int)(off), 0, AUX_SC1)
typedef __attribute__((ext_vector_type(4))) short s16x4;
typedef unsigned long long u64;

struct PackJob { const GAS unsigned short* src; int ldn, mat, ks, cb, na, nb, fb; };
__device__ __forceinline__ PackJob pack_job(const GArgs& p, int g) {
  PackJob jb;
  const uintptr_t a1 = (uintptr_t)p.w1, a2 = (uintptr_t)p.w2, a3 = (uintptr_t)p.w3, a4 = (uintptr_t)p.w4;
  uintptr_t lo = g < 56 ? a1 : a2, hi = g < 336 ? a3 : a4;
  asm volatile("" : "+s"(lo), "+s"(hi));
  jb.src = (const GAS unsigned short*)(g < 112 ? lo : hi);
  jb.mat = (g >= 56) + (g >= 112) + (g >= 336);
  jb.ldn = jb.mat == 2 ? 1792 : 448;
  g -= g < 112 ? (g < 56 ? 0 : 56) : (g < 336 ? 112 : 336);
  const int sh = jb.mat == 2 ? 4 : 2;
  jb.ks = g >> sh;
  jb.cb = g & ((1 << sh) - 1);
  const int j3 = (jb.ks * 4682) >> 16, ksl = jb.ks - j3 * 14, sl = (jb.cb - j3 - 1) & 3;
  const int is2 = jb.mat == 2, is3 = jb.mat == 3;
  jb.na = is2 * jb.cb * 7 + (1 - is2) * (is3 * j3 + (1 - is3) * jb.cb) * 28;
  jb.nb = 4 - 3 * is2;
  const int own = sl == 3;
  const int f3 = (1 - own) * (84 + (ksl & 1) * 21 + (ksl >> 1) * 3 + sl) + own * (126 + ksl);
  jb.fb = is2 * (28 + jb.ks * 2) + is3 * f3 + (1 - is2 - is3) * (jb.mat * 14 + jb.ks);
  jb.na = __builtin_amdgcn_readfirstlane(jb.na); jb.nb = __builtin_amdgcn_readfirstlane(jb.nb); jb.fb = __builtin_amdgcn_readfirstlane(jb.fb);
  return jb;
}
__device__ __forceinline__ void pack_load(const PackJob& jb, int lane, u32x4 (&r)[8]) {
  const int jn = lane & 15, q = lane >> 4, jl = jn < 14 ? jn : 0;
#pragma unroll
  for (int i = 0; i < 8; ++i) r[i] = *(const GAS u32x4*)(jb.src + (size_t)(jb.ks * 32 + q * 8 + i) * jb.ldn + jb.cb * 112 + jl * 8);
}
__device__ __forceinline__ void pack_shuffle(int lane, u32x4 (&r)[8], LAS unsigned char* Lw) {
  const int jn = lane & 15, q = lane >> 4;
  u32x4 o[8];
  transpose8x8(r, o);
#pragma unroll
  for (int i = 0; i < 8; ++i) *(LAS u32x4*)(Lw + (jn >> 1) * 1024 + (q * 16 + (jn & 1) * 8 + (i ^ (jn >> 1))) * 16) = o[i];
  lds_fence();
#pragma unroll
  for (int m = 0; m < 7; ++m) r[m] = *(const LAS u32x4*)(Lw + m * 1024 + (lane ^ m) * 16);
  lds_fence();
}
__device__ __forceinline__ void pack_store1(const PackJob& jb, int lane, const u32x4 (&r)[8], __amdgpu_buffer_rsrc_t rs, int m) {
  const int nt = jb.na + m * jb.nb;
  __builtin_amdgcn_raw_buffer_store_b128(r[m], rs, lane * 16, (int)WS_WP + ((nt >> 2) * NFRAG + jb.fb + ((nt >> 1) & 1) * 28 + (nt & 1)) * 1024, AUX_SC1);
}
__device__ __forceinline__ void mod_load(const GArgs& p, int u, int tid, u32x4 (&r)[7], float (&sc)[7]) {
  const int rg = tid >> 3, pc = tid & 7;
#pragma unroll
  for (int i = 0; i < 7; ++i) r[i] = *(const u32x4*)(p.wmod + (size_t)(rg + 64 * i) * 2688 + u * 64 + pc * 8);
#pragma unroll
  for (int i = 0; i < 7; ++i) sc[i] = bf2f(p.cvec[rg + 64 * i]);
}
__device__ __forceinline__ void mod_finish(int u, unsigned tag32, int tid, const u32x4 (&r)[7], const float (&cv)[7], LAS float* sm, __amdgpu_buffer_rsrc_t rs) {
  const int rg = tid >> 3, pc = tid & 7;
  float acc[8];
#pragma unroll
  for (int k = 0; k < 8; ++k) acc[k] = 0.f;
#pragma unroll
  for (int i = 0; i < 7; ++i) {
    const float sc = rbf(cv[i] * __builtin_amdgcn_rcpf(1.0f + __builtin_amdgcn_exp2f(-1.4426950408889634f * cv[i])));
#pragma unroll
    for (int k = 0; k < 4; ++k) { acc[2 * k] += sc * bf_lo(r[i][k]); acc[2 * k + 1] += sc * bf_hi(r[i][k]); }
  }
  *(LAS f32x4*)(sm + rg * 68 + pc * 8) = f32x4{acc[0], acc[1], acc[2], acc[3]};
  *(LAS f32x4*)(sm + rg * 68 + pc * 8 + 4) = f32x4{acc[4], acc[5], acc[6], acc[7]};
  bar_lds();
  {
    const int col = tid & 63, part = tid >> 6;
    float s = 0.f;
#pragma unroll
    for (int k = 0; k < 8; ++k) s += sm[(part * 8 + k) * 68 + col];
    sm[64 * 68 + part * 64 + col] = s;
  }
  bar_lds();
  if (tid < 32) {
    f32x2 s = {0.f, 0.f};
#pragma unroll
    for (int k = 0; k < 8; ++k) s += *(const LAS f32x2*)(sm + 64 * 68 + k * 64 + 2 * tid);
    unsigned v = pk_bf16(s.x, s.y);
    const int sec = u / 7;
    if (sec == 1 || sec == 4) v = pk_bf16(bf_lo(v) + 1.0f, bf_hi(v) + 1.0f);
    __builtin_amdgcn_raw_buffer_store_b64(u32x2{v, tag32}, rs, (int)WS_MODG + (u * 32 + tid) * 8, 0, AUX_SC1);
  }
}
template <int YOUNGER>
__device__ __forceinline__ void arrive_flag(LAS unsigned char* L, int cnt_slot, unsigned char* flag, u64 tag, int lane) {
  asm volatile("s_waitcnt vmcnt(%0)" ::"i"(YOUNGER) : "memory");
  if (lane == 0) {
    const unsigned old = __hip_atomic_fetch_add((LAS unsigned*)(L + G_CNT) + cnt_slot, 1u, __ATOMIC_RELAXED, __HIP_MEMORY_SCOPE_WORKGROUP);
    if (old == 7u) __hip_atomic_store((GAS u64*)flag, tag, __ATOMIC_RELAXED, __HIP_MEMORY_SCOPE_AGENT);
  }
}
__device__ __forceinline__ u32x2 flag_load(__amdgpu_buffer_rsrc_t rs, int off) { return __builtin_amdgcn_raw_buffer_load_b64(rs, off, 0, AUX_SC1); }
__device__ __forceinline__ u32x2 flag_wait(__amdgpu_buffer_rsrc_t rs, int off, u32x2 v, unsigned lo, unsigned hi, unsigned lomask) {
  if (__builtin_expect(__any((((v.x ^ lo) & lomask) | (v.y ^ hi)) != 0u), 0)) {
    for (unsigned spins = 0; spins < SPIN_CAP; ++spins) {
      __builtin_amdgcn_s_sleep(2);
      asm volatile("" ::: "memory");
      v = flag_load(rs, off);
      if (!__any((((v.x ^ lo) & lomask) | (v.y ^ hi)) != 0u)) break;
    }
  }
  __builtin_amdgcn_fence(__ATOMIC_ACQUIRE, "wavefront");
  return v;
}
__device__ __forceinline__ void stat_wait(__amdgpu_buffer_rsrc_t rs, int off, u32x4& a, u32x4& b, unsigned tag32) {
  if (__builtin_expect(__any((((a.y ^ tag32) | (a.w ^ tag32)) | ((b.y ^ tag32) | (b.w ^ tag32))) != 0u), 0)) {
    for (unsigned spins = 0; spins < SPIN_CAP; ++spins) {
      __builtin_amdgcn_s_sleep(2);
      asm volatile("" ::: "memory");
      a = __builtin_amdgcn_raw_buffer_load_b128(rs, off, 0, AUX_SC1);
      b = __builtin_amdgcn_raw_buffer_load_b128(rs, off + 16, 0, AUX_SC1);
      if (!__any((((a.y ^ tag32) | (a.w ^ tag32)) | ((b.y ^ tag32) | (b.w ^ tag32))) != 0u)) break;
    }
  }
}
#define FILL_P(i) do { SB0(); __builtin_amdgcn_raw_buffer_store_b128(ONES, rst, (int)WS_P + ((pn * 4 + j) * 3 + (i)) * 7168 + pslot, 0, AUX_SC1); SB0(); } while (0)
#define FILL_X1() do { SB0(); __builtin_amdgcn_raw_buffer_store_b128(ONES, rst, (int)WS_X1 + (pn * 4 + j) * 7168 + pslot, 0, AUX_SC1); SB0(); } while (0)
__device__ __forceinline__ void body(const GArgs& p, LAS unsigned char* L, uintptr_t code0) {
  const bool CONV = blockIdx.x < 224;
  const int tid = threadIdx.x, lane = tid & 63;
  const int w = __builtin_amdgcn_readfirstlane(tid >> 6);
  const int bid = blockIdx.x;
  const int j = bid & 3, pn = (bid >> 3) * 2 + ((bid >> 2) & 1);
  const int r16 = lane & 15, q = lane >> 4;
  const bool cw = w < 7;
  const int wc = cw ? w : 6;
  const int dp = (bid & 7) * 28 + (CONV ? bid >> 3 : 0);
  const __amdgpu_buffer_rsrc_t rs = __builtin_amdgcn_make_buffer_rsrc((void*)p.ws, 0, (int)WS_END, 0x00020000);
  const __amdgpu_buffer_rsrc_t rst = __builtin_amdgcn_make_buffer_rsrc((void*)p.ws, 0, cw ? (int)WS_END : 0, 0x00020000);
  const __amdgpu_buffer_rsrc_t rw =
      __builtin_amdgcn_make_buffer_rsrc((void*)(p.ws + WS_WP + (size_t)(j * 7 + wc) * NFRAG * 1024), 0, cw ? NFRAG * 1024 : 0, 0x00020000);
  const int voff = lane * 16;
  const int pslot = (wc * 64 + lane) * 16;
  const u32x4 ONES = {~0u, ~0u, ~0u, ~0u};
  STAMP(0, 0);
  const int l56 = lane < 56 ? lane : 0;
  const u32x4 xrow = *(const u32x4*)(p.x + (size_t)(bid * 8 + w) * D_ + l56 * 8);
  const u32x2 ep = flag_load(rs, (int)WS_EPOCH);
  const int cl = (bid >> 3) * 16 + (lane & 15);
  const unsigned touch = *(const GAS unsigned*)(code0 + (size_t)(cl < CODE_LINES ? cl : 0) * 128);
  float are, aim, b_re, b_im, cre, cim, ldt;
  u32x4 mr[7], pk[8];
  float mc[7];
  {
    const size_t o = (size_t)(2 * dp + (w >> 2)) * NS_ + (w & 3) * 64 + lane;
    SB0();
    are = bf2f(p.a_re[o]); aim = bf2f(p.a_im[o]); b_re = bf2f(p.b_re[o]); b_im = bf2f(p.b_im[o]);
    cre = bf2f(p.c_re[o]); cim = bf2f(p.c_im[o]); ldt = bf2f(p.log_dt[2 * dp + (w >> 2)]);
    SB0();
  }
  __builtin_amdgcn_s_barrier();
  __builtin_amdgcn_raw_buffer_store_b128(ONES, rst, (int)WS_X2B + bid * 7168 + pslot, 0, AUX_SC1);
  __builtin_amdgcn_raw_buffer_store_b128(ONES, rst, (int)WS_G + bid * 7168 + pslot, 0, AUX_SC1);
  SB0();
  const int pg = (bid - 224) * 6 + w;
  const bool packer = CONV ? w < 2 : (w < 6 && pg < 112);
  const PackJob jb = pack_job(p, CONV ? bid * 2 + (w & 1) : (packer ? 448 + pg : 0));
  if (tid < 4) *((LAS unsigned*)(L + G_CNT) + tid) = 0u;
  u64 tag;
  {
    const u64 e = ((u64)__builtin_amdgcn_readfirstlane(ep.y) << 32) | (unsigned)__builtin_amdgcn_readfirstlane(ep.x);
    u64 z = e + 0x9E3779B97F4A7C15ull;
    z = (z ^ (z >> 30)) * 0xBF58476D1CE4E5B9ull;
    z = (z ^ (z >> 27)) * 0x94D049BB133111EBull;
    tag = z ^ (z >> 31);
  }
  const unsigned tag32 = ((unsigned)__builtin_amdgcn_readfirstlane(ep.x) + 1u) * 0x9E3779B1u ^ 0x5bd1e995u;
  const unsigned tlo = (unsigned)tag, thi = (unsigned)(tag >> 32);
  unsigned stv;
#define PUT_STAT() do { if (lane == 0) __builtin_amdgcn_raw_buffer_store_b64(u32x2{stv, tag32}, rs, (int)WS_STG + (bid * 8 + w) * 8, 0, AUX_SC1); } while (0)
  {
    const float msk = lane < 56 ? 1.0f : 0.0f;
    float v[8];
#pragma unroll
    for (int k = 0; k < 4; ++k) { v[2 * k] = bf_lo(xrow[k]); v[2 * k + 1] = bf_hi(xrow[k]); }
    const float mean = wave_sum((((v[0] + v[1]) + (v[2] + v[3])) + ((v[4] + v[5]) + (v[6] + v[7]))) * msk) * (1.0f / D_);
    float qq = 0.f;
#pragma unroll
    for (int k = 0; k < 8; ++k) { const float d = v[k] - mean; qq += d * d; }
    const float sd = ln_sd(wave_sum(qq * msk) * (1.0f / D_));
    stv = pk_bf16(mean, sd);
    if (lane < 56) *(LAS u32x4*)(L + P0_LN + w * LNS + lane * 16) = xrow;
    bar_lds();
#pragma unroll
    for (int u = 0; u < 2; ++u) {
      const int pce = (wc * 2 + u) * 64 + lane;
      const LAS unsigned* a = (const LAS unsigned*)(L + P0_LN + (pce & 3) * (2 * LNS) + (pce >> 2) * 4);
      __builtin_amdgcn_raw_buffer_store_b128(u32x4{a[0], tag32, a[LNS / 4], tag32}, rst, (int)WS_XG + bid * (int)XG_BLK + pce * 16, 0, AUX_SC1);
    }
  }
  asm volatile("" ::"v"(touch));
  STAMP(0, 29);
  const int sto = (int)WS_STG + tid * 32;
  u32x4 ring[RING];
  if (__builtin_expect(CONV, 1)) {
    const float dt = rbf(expf(ldt));
    const float ea = (dt * are) * 1.4426950408889634f, eb = (dt * aim) * 0.15915494309189535f;
    const float bre = dt * b_re, bim = dt * b_im;
    f32x2 wc;
    wc.x = cre * bre - cim * bim;
    wc.y = -(cre * bim + cim * bre);
    asm volatile("" ::"v"(ea), "v"(eb), "v"(wc.x), "v"(wc.y));
    const float mx = rows_max(row16_max(fmaxf(fabsf(wc.x), fabsf(wc.y))));
    int ex = mx > 0.f ? __builtin_amdgcn_frexp_expf(mx) : 0;
    ex = ex < -120 ? -120 : (ex > 120 ? 120 : ex);
    const float scale = __builtin_amdgcn_ldexpf(1.0f, ex), iscale = __builtin_amdgcn_ldexpf(1.0f, -ex);
    wc.x *= iscale; wc.y *= iscale;
    LAS unsigned char* B0 = L + P0_KG + w * (2 * KG_BUF);
    LAS unsigned char* B1 = B0 + KG_BUF;
    const int wo = lane * 4;
    const int ro = r16 * KG_ROW + q * 16;
    f32x2 z8c = cexp_tab(ea, eb, 8.0f);
    z8c.y = -z8c.y;
    const f32x2 z1 = cexp_tab(ea, eb, 1.0f), z128 = cexp_tab(ea, eb, 128.0f), z1024 = cexp_tab(ea, eb, 1024.0f);
    {
      const f32x2 z16c = cmul(z8c, z8c);
      f32x2 t0 = wc, t1 = cmul(wc, z8c);
#pragma unroll
      for (int c = 0; c < 16; c += 2) {
        *(LAS unsigned*)(B0 + c * KG_ROW + wo) = pk_f16(t0);
        *(LAS unsigned*)(B0 + (c + 1) * KG_ROW + wo) = pk_f16(t1);
        if (c < 14) { t0 = cmul(t0, z16c); t1 = cmul(t1, z16c); }
      }
    }
    SB0();
    asm volatile("s_waitcnt vmcnt(2)" ::: "memory");
    PUT_STAT();
    SB0();
    f32x2 ze[8];
    ze[0] = f32x2{1.0f, 0.0f}; ze[1] = z1; ze[2] = cmul(z1, z1); ze[3] = cmul(ze[2], z1); ze[4] = cmul(ze[2], ze[2]);
    ze[5] = cmul(ze[4], z1); ze[6] = cmul(ze[4], ze[2]); ze[7] = cmul(ze[4], ze[3]);
#pragma unroll
    for (int e = 0; e < 8; ++e) {
      *(LAS unsigned*)(B1 + e * KG_ROW + wo) = pk_f16(ze[e]);
      *(LAS unsigned*)(B1 + (8 + e) * KG_ROW + wo) = pk_f16(e == 0 ? z128 : cmul(z128, ze[e]));
    }
    lds_fence();
    u32x4 tf[4];
#pragma unroll
    for (int ks = 0; ks < 4; ++ks) tf[ks] = *(const LAS u32x4*)(B0 + ro + ks * 64);
    u32x4 rf[4];
#pragma unroll
    for (int ks = 0; ks < 4; ++ks) rf[ks] = *(const LAS u32x4*)(B1 + ro + ks * 64);
    lds_fence();
    SB0();
    if (packer) pack_load(jb, lane, pk);
    SB0();
    STAMP(0, 30);
    const f32x2 p2 = cmul(z128, z128), p3 = cmul(p2, z128), p4 = cmul(p2, p2), p5 = cmul(p4, z128), p6 = cmul(p4, p2), p7 = cmul(p4, p3);
    {
#pragma unroll
      for (int e = 0; e < 4; ++e) {
        *(LAS unsigned*)(B0 + e * KG_ROW + wo) = pk_f16(e == 0 ? p2 : cmul(p2, ze[2 * e]));
        *(LAS unsigned*)(B0 + (4 + e) * KG_ROW + wo) = pk_f16(e == 0 ? p3 : cmul(p3, ze[2 * e]));
      }
      *(LAS unsigned*)(B0 + 8 * KG_ROW + wo) = pk_f16(p4);
      *(LAS unsigned*)(B0 + 9 * KG_ROW + wo) = pk_f16(cmul(p4, ze[4]));
      *(LAS unsigned*)(B0 + 10 * KG_ROW + wo) = pk_f16(p5);
      *(LAS unsigned*)(B0 + 11 * KG_ROW + wo) = pk_f16(cmul(p5, ze[4]));
      *(LAS unsigned*)(B0 + 12 * KG_ROW + wo) = pk_f16(p6);
      *(LAS unsigned*)(B0 + 13 * KG_ROW + wo) = pk_f16(cmul(p6, ze[4]));
      *(LAS unsigned*)(B0 + 14 * KG_ROW + wo) = pk_f16(p7);
      *(LAS unsigned*)(B0 + 15 * KG_ROW + wo) = pk_f16(cmul(p7, ze[4]));
    }
    f32x4 acc0 = {0.f, 0.f, 0.f, 0.f}, acc1 = acc0, acc2 = acc0;
#pragma unroll
    for (int ks = 0; ks < 4; ++ks) acc0 = mfma16h(rf[ks], tf[ks], acc0);
    {
      *(LAS unsigned*)(B1 + 0 * KG_ROW + wo) = pk_f16(z1024);
      *(LAS unsigned*)(B1 + 1 * KG_ROW + wo) = pk_f16(cmul(z1024, z128));
      *(LAS unsigned*)(B1 + 2 * KG_ROW + wo) = pk_f16(cmul(z1024, p2));
      *(LAS unsigned*)(B1 + 3 * KG_ROW + wo) = pk_f16(cmul(z1024, p3));
      *(LAS unsigned*)(B1 + 4 * KG_ROW + wo) = pk_f16(cmul(z1024, p4));
      *(LAS unsigned*)(B1 + 5 * KG_ROW + wo) = pk_f16(cmul(z1024, p5));
      *(LAS unsigned*)(B1 + 6 * KG_ROW + wo) = pk_f16(cmul(z1024, p6));
      *(LAS unsigned*)(B1 + 7 * KG_ROW + wo) = pk_f16(cmul(z1024, p7));
      *(LAS unsigned*)(B1 + 8 * KG_ROW + wo) = pk_f16(cmul(z1024, z1024));
    }
    lds_fence();
#pragma unroll
    for (int ks = 0; ks < 4; ++ks) acc1 = mfma16h(*(const LAS u32x4*)(B0 + ro + ks * 64), tf[ks], acc1);
#pragma unroll
    for (int ks = 0; ks < 4; ++ks) acc2 = mfma16h(*(const LAS u32x4*)(B1 + ro + ks * 64), tf[ks], acc2);
    lds_fence();
    {
      LAS float* red = (LAS float*)B0;
      acc0 *= scale; acc1 *= scale; acc2 *= scale;
      *(LAS f32x4*)(red + (q >> 1) * 128 + 8 * r16 + (q & 1) * 4) = acc0;
      const int i1 = q < 2 ? 256 + q * 64 + 4 * r16 : 384 + (q - 2) * 64 + 2 * r16, d1 = q < 2 ? 2 : 32;
      *(LAS f32x2*)(red + i1) = f32x2{acc1[0], acc1[1]};
      *(LAS f32x2*)(red + i1 + d1) = f32x2{acc1[2], acc1[3]};
      LAS float* r2 = red + 512 + q * 64 + r16;
      r2[0] = acc2[0]; r2[16] = acc2[1]; r2[32] = acc2[2]; r2[48] = acc2[3];
    }
    STAMP(0, 21);
    bar_lds();
    const int mgo = (int)WS_MODG + ((lane < 3 ? lane : 0) * 224 + dp) * 8;
    SB0();
    u32x4 sg0 = __builtin_amdgcn_raw_buffer_load_b128(rs, sto, 0, AUX_SC1), sg1 = __builtin_amdgcn_raw_buffer_load_b128(rs, sto + 16, 0, AUX_SC1);
    const int xgo = (int)WS_XG + (tid >> 1) * (int)XG_BLK + dp * 64 + (tid & 1) * 32;
    u32x4 xg0 = __builtin_amdgcn_raw_buffer_load_b128(rs, xgo, 0, AUX_SC1), xg1 = __builtin_amdgcn_raw_buffer_load_b128(rs, xgo + 16, 0, AUX_SC1);
    const unsigned dsk2 = *(const unsigned*)(p.dskip + 2 * dp);
    SB0();
    const int kc = tid >> 8, kt = tid & 255;
    LAS float* kd = (LAS float*)(L + P0_KG + kc * (8 * KG_BUF) + KG_BUF);
#pragma unroll
    for (int i = 0; i < 3; ++i) {
      const int oo = kt + 256 * i;
      if (i < 2 || oo < 648) {
        const LAS float* s = (const LAS float*)(L + P0_KG + kc * (8 * KG_BUF)) + oo;
        kd[oo] = (s[0] + s[2 * KG_BUF / 4]) + (s[4 * KG_BUF / 4] + s[6 * KG_BUF / 4]);
      }
    }
    if (kt == 0) kd[648] = 0.f;
    const unsigned OFFS = 0x087EDA52u;
    {
      int off = CV_KCOPY + kt * 16;
      if (kt >= 160) { const int k = kt - 160, s = k >> 3, ch = (k & 7) == 7 ? 6 : (k & 7); off = (272 * (s & 7) + ((OFFS >> (4 * (s & 7))) & 15) + 257 + ch) * 16; }
      *(LAS u32x4*)(L + kc * CV_CH + off) = u32x4{0u, 0u, 0u, 0u};
    }
    bar_lds();
    LAS unsigned char* base = L + kc * CV_CH;
    u32x4 k0;
    {
      float v[8];
#pragma unroll
      for (int i = 0; i < 8; i += 2) {
        const int l0 = 2048 - (kt * 8 + i), l1 = l0 - 1;
        const unsigned bits = pk_bf16((float)l0, (float)l1);
        int i0 = l0 < 128 ? l0 : (int)(bits & 0xffffu) - 17024;
        const int i1 = l1 < 128 ? l1 : (int)(bits >> 16) - 17024;
        if (i == 0) i0 = kt == 0 ? 648 : i0;
        v[i] = kd[i0];
        v[i + 1] = kd[i1];
      }
      k0 = u32x4{pk_bf16(v[0], v[1]), pk_bf16(v[2], v[3]), pk_bf16(v[4], v[5]), pk_bf16(v[6], v[7])};
      *(LAS u32x4*)(base + (2 + kt) * 16) = k0;
      if (kt < 8) {
        const unsigned k00 = pk_bf16(kd[0], 0.f);
        *(LAS u32x4*)(base + (272 * kt + ((OFFS >> (4 * kt)) & 15) + 256) * 16) = u32x4{kt == 0 ? k00 : 0u, 0u, 0u, 0u};
      }
    }
    bar_lds();
    {
      const u32x4 k1 = *(const LAS u32x4*)(base + (2 + kt + 1) * 16);
      unsigned wv[8] = {k0.x, k0.y, k0.z, k0.w, k1.x, k1.y, k1.z, k1.w};
      unsigned vv[7];
#pragma unroll
      for (int i = 0; i < 7; ++i) vv[i] = __builtin_amdgcn_alignbit(wv[i + 1], wv[i], 16);
#pragma unroll
      for (int s = 1; s < 8; ++s) {
        const int k = s >> 1;
        u32x4 o = (s & 1) ? u32x4{vv[k], vv[k + 1], vv[k + 2], vv[k + 3]} : u32x4{wv[k], wv[k + 1], wv[k + 2], wv[k + 3]};
        *(LAS u32x4*)(base + (272 * s + ((OFFS >> (4 * s)) & 15) + kt) * 16) = o;
      }
    }
    float cum0[4], cum1[4];
    {
      const LAS float* kd0 = (const LAS float*)(L + P0_KG + KG_BUF);
      const LAS float* kd1 = (const LAS float*)(L + P0_KG + 8 * KG_BUF + KG_BUF);
#pragma unroll
      for (int i = 0; i < 4; i += 2) {
        const int l0 = 4 * tid + i;
        const unsigned bits = pk_bf16((float)l0, (float)(l0 + 1));
        const int i0 = l0 < 128 ? l0 : (int)(bits & 0xffffu) - 17024, i1 = l0 < 128 ? l0 + 1 : (int)(bits >> 16) - 17024;
        cum0[i] = kd0[i0]; cum0[i + 1] = kd0[i1]; cum1[i] = kd1[i0]; cum1[i + 1] = kd1[i1];
      }
#pragma unroll
      for (int i = 1; i < 4; ++i) { cum0[i] += cum0[i - 1]; cum1[i] += cum1[i - 1]; }
      const float inc0 = wave_scan(cum0[3]), inc1 = wave_scan(cum1[3]);
      const float e0 = inc0 - cum0[3], e1 = inc1 - cum1[3];
#pragma unroll
      for (int i = 0; i < 4; ++i) { cum0[i] += e0; cum1[i] += e1; }
      if (lane == 63) *(LAS f32x2*)(L + P0_MS + w * 8) = f32x2{inc0, inc1};
    }
#define GRAN_BAD() __any(((((sg0.y ^ tag32) | (sg0.w ^ tag32)) | ((sg1.y ^ tag32) | (sg1.w ^ tag32))) | (((xg0.y ^ tag32) | (xg0.w ^ tag32)) | ((xg1.y ^ tag32) | (xg1.w ^ tag32)))) != 0u)
    if (__builtin_expect(GRAN_BAD(), 0)) {
      for (unsigned spins = 0; spins < SPIN_CAP; ++spins) {
        __builtin_amdgcn_s_sleep(2);
        asm volatile("" ::: "memory");
        sg0 = __builtin_amdgcn_raw_buffer_load_b128(rs, sto, 0, AUX_SC1); sg1 = __builtin_amdgcn_raw_buffer_load_b128(rs, sto + 16, 0, AUX_SC1);
        xg0 = __builtin_amdgcn_raw_buffer_load_b128(rs, xgo, 0, AUX_SC1); xg1 = __builtin_amdgcn_raw_buffer_load_b128(rs, xgo + 16, 0, AUX_SC1);
        if (!GRAN_BAD()) break;
      }
    }
#undef GRAN_BAD
    const unsigned xq[4] = {xg0.x, xg0.z, xg1.x, xg1.z};
    STAMP(0, 22);
    unsigned hq[4];
    {
      const unsigned sv[4] = {sg0.x, sg0.z, sg1.x, sg1.z};
#pragma unroll
      for (int k = 0; k < 4; ++k) {
        const float m = bf_lo(sv[k]), r = __builtin_amdgcn_rcpf(bf_hi(sv[k]));
        unsigned t = pk_bf16(bf_lo(xq[k]) - m, bf_hi(xq[k]) - m);
        hq[k] = pk_bf16(bf_lo(t) * r, bf_hi(t) * r);
      }
      LAS unsigned char* U = L + CV_KCOPY + (16 + (tid >> 4)) * CV_UROW + (tid & 15) * 8;
      *(LAS u32x2*)U = u32x2{__builtin_amdgcn_perm(hq[1], hq[0], 0x05040100u), __builtin_amdgcn_perm(hq[3], hq[2], 0x05040100u)};
      *(LAS u32x2*)(U + CV_CH) = u32x2{__builtin_amdgcn_perm(hq[1], hq[0], 0x07060302u), __builtin_amdgcn_perm(hq[3], hq[2], 0x07060302u)};
    }
    if (packer) pack_shuffle(lane, pk, L + P0_KG + (w + 1) * (2 * KG_BUF));
    STAMP(0, 23);
    bar_lds();
    if (bid == 0 && tid == 0) {
      const u64 e = ((u64)ep.y << 32) | ep.x;
      __hip_atomic_store((GAS u64*)(p.ws + WS_EPOCH), e + 1ull, __ATOMIC_RELAXED, __HIP_MEMORY_SCOPE_AGENT);
    }
    u32x2 fv = flag_load(rs, mgo);
    const __amdgpu_buffer_rsrc_t rpk = __builtin_amdgcn_make_buffer_rsrc((void*)p.ws, 0, packer ? (int)WS_END : 0, 0x00020000);
    SB0();
  {
    const int ch = w >> 2, wq = w & 3;
    f32x4 acc[2][4];
#pragma unroll
    for (int n = 0; n < 2; ++n)
#pragma unroll
      for (int m = 0; m < 4; ++m) acc[n][m] = f32x4{0.f, 0.f, 0.f, 0.f};
    const int s = (8 - r16) & 7, u = (r16 + 7) >> 3;
    const LAS unsigned char* ka = L + ch * CV_CH + (272 * s + ((OFFS >> (4 * s)) & 15) + 256 + q - u - 2 * (16 * wq) - 2 * 79) * 16;
    const LAS unsigned char* ub = L + ch * CV_CH + CV_KCOPY + (16 + r16 - 4 * wq - 3) * CV_UROW + q * 16;
#define KFRAG(f) (*(const LAS u32x4*)(ka + (79 - (f)) * 32))
    u32x4 alo[18], ahi[18], bf[4][4];
#define CSTAGE(dl)                                                                                      \
  {                                                                                                     \
    _Pragma("unroll") for (int ks = 0; ks < 2; ++ks) {                                                  \
      bf[dl][2 * ks] = *(const LAS u32x4*)(ub + (3 - (dl)) * CV_UROW + ks * 64);                        \
      bf[dl][2 * ks + 1] = *(const LAS u32x4*)(ub + (19 - (dl)) * CV_UROW + ks * 64);                   \
    }                                                                                                   \
    _Pragma("unroll") for (int i = 0; i < 4; ++i) {                                                     \
      alo[2 + 4 * (dl) + i] = KFRAG(4 * (dl) + i);                                                      \
      ahi[2 + 4 * (dl) + i] = KFRAG(4 * (dl) + i + 64);                                                 \
    }                                                                                                   \
  }
    CSTAGE(0)
    alo[0] = KFRAG(-2); alo[1] = KFRAG(-1); ahi[0] = KFRAG(62); ahi[1] = KFRAG(63);
    SB0();
#pragma unroll
    for (int m = 0; m < 7; ++m) pack_store1(jb, lane, pk, rpk, m);
    FILL_X1(); FILL_P(0); FILL_P(1); FILL_P(2);
    SB0();
#pragma unroll
    for (int dl = 0; dl < 4; ++dl) {
      if (dl < 3) CSTAGE(dl + 1)
#pragma unroll
      for (int ks = 0; ks < 2; ++ks) {
#pragma unroll
        for (int m = 0; m < 4; ++m) {
          acc[0][m] = mfma16(alo[4 * dl + m - 2 * ks + 2], bf[dl][2 * ks], acc[0][m]);
          acc[1][m] = mfma16(alo[4 * dl + m - 2 * ks + 2], bf[dl][2 * ks + 1], acc[1][m]);
          acc[1][m] = mfma16(ahi[4 * dl + m - 2 * ks + 2], bf[dl][2 * ks], acc[1][m]);
        }
      }
      if (dl < 3) {
#pragma unroll
        for (int i = 0; i < 12; ++i) {
          __builtin_amdgcn_sched_group_barrier(0x100, 1, 0);
          __builtin_amdgcn_sched_group_barrier(0x8, 2, 0);
        }
      }
      SB0();
    }
#undef CSTAGE
    STAMP(0, 28);
#undef KFRAG

    LAS unsigned char* P = L + CV_P + w * (32 * CV_PROW);
#pragma unroll
    for (int n = 0; n < 2; ++n)
#pragma unroll
      for (int m = 0; m < 4; ++m) *(LAS f32x4*)(P + (16 * n + r16) * CV_PROW + (16 * m + 4 * q) * 4) = acc[n][m];
    }
    fv = flag_wait(rs, mgo, fv, 0u, tag32, 0u);
    const unsigned sa2 = __builtin_amdgcn_readlane(fv.x, 0), ca2 = __builtin_amdgcn_readlane(fv.x, 1), ga2 = __builtin_amdgcn_readlane(fv.x, 2);
    unsigned hd[4];
    {
      const f32x2 t2 = *(const LAS f32x2*)(L + P0_MS + (lane & 7) * 8);
      const float mk = (lane & 15) < w ? 1.0f : 0.0f;
      const float b0 = row16_sum((lane & 8) ? 0.f : t2.x * mk), b1 = row16_sum((lane & 8) ? 0.f : t2.y * mk);
#pragma unroll
      for (int k = 0; k < 4; ++k) {
        unsigned h = pk_bf16(bf_lo(hq[k]) * bf_lo(ca2), bf_hi(hq[k]) * bf_hi(ca2));
        h = pk_bf16(bf_lo(h) + bf_lo(sa2), bf_hi(h) + bf_hi(sa2));
        hd[k] = pk_bf16(bf_lo(h) * bf_lo(dsk2), bf_hi(h) * bf_hi(dsk2));
        cum0[k] = (cum0[k] + b0) * bf_lo(sa2);
        cum1[k] = (cum1[k] + b1) * bf_hi(sa2);
      }
    }
    asm volatile("s_waitcnt vmcnt(4)" ::: "memory");
    bar_lds();
    STAMP(0, 24);
    {
      const LAS unsigned char* P = L + CV_P + (tid >> 4) * CV_PROW + (tid & 15) * 16;
      f32x4 pa[8];
#pragma unroll
      for (int k = 0; k < 8; ++k) pa[k] = *(const LAS f32x4*)(P + k * (32 * CV_PROW));
      const f32x4 y0 = (pa[0] + pa[1]) + (pa[2] + pa[3]), y1 = (pa[4] + pa[5]) + (pa[6] + pa[7]);
      u32x4 out;
#pragma unroll
      for (int k = 0; k < 4; ++k) {
        const unsigned yy = pk_bf16(y0[k] * bf_lo(ca2) + cum0[k], y1[k] * bf_hi(ca2) + cum1[k]);
        unsigned t = pk_bf16(bf_lo(yy) + bf_lo(hd[k]), bf_hi(yy) + bf_hi(hd[k]));
        t = pk_bf16(bf_lo(ga2) * bf_lo(t), bf_hi(ga2) * bf_hi(t));
        out[k] = sane(pk_bf16(bf_lo(xq[k]) + bf_lo(t), bf_hi(xq[k]) + bf_hi(t)));
      }
      ST16(WS_X2B + (tid >> 3) * (int)X2B_BLK + dp * 128 + (tid & 7) * 16, out);
    }
  } else {
    const int c = bid - 224;
    LAS float* sm = (LAS float*)(L + P0_KG);
    SB0();
    mod_load(p, c, tid, mr, mc);
    SB0();
    asm volatile("s_waitcnt vmcnt(16)" ::: "memory");
    PUT_STAT();
    mod_finish(c, tag32, tid, mr, mc, sm, rs);
    STAMP(0, 21);
    if (c >= 22) {
      mod_load(p, 10 + c, tid, mr, mc);
      bar_lds();
      mod_finish(10 + c, tag32, tid, mr, mc, sm, rs);
    }
    if (packer) {
      pack_load(jb, lane, pk);
      pack_shuffle(lane, pk, L + P0_PK + w * 8192);
#pragma unroll
      for (int m = 0; m < 7; ++m) pack_store1(jb, lane, pk, rs, m);
    }
    arrive_flag<0>(L, 2, p.ws + WS_FLAGC + c * 8, ~tag, lane);
    STAMP(0, 30);
    FILL_X1(); FILL_P(0); FILL_P(1); FILL_P(2);
    {
      u32x4 sg0 = __builtin_amdgcn_raw_buffer_load_b128(rs, sto, 0, AUX_SC1), sg1 = __builtin_amdgcn_raw_buffer_load_b128(rs, sto + 16, 0, AUX_SC1);
      stat_wait(rs, sto, sg0, sg1, tag32);
    }
    bar_lds();
  }
  STAMP(0, 25);
  if (__builtin_expect(!cw, 0)) {
    {
      const int mo = (int)WS_MODG + 672 * 8 + l56 * 64;
      u32x4 g0 = __builtin_amdgcn_raw_buffer_load_b128(rs, mo, 0, AUX_SC1), g1 = __builtin_amdgcn_raw_buffer_load_b128(rs, mo + 16, 0, AUX_SC1);
      u32x4 g2 = __builtin_amdgcn_raw_buffer_load_b128(rs, mo + 32, 0, AUX_SC1), g3 = __builtin_amdgcn_raw_buffer_load_b128(rs, mo + 48, 0, AUX_SC1);
      stat_wait(rs, mo, g0, g1, tag32);
      stat_wait(rs, mo + 32, g2, g3, tag32);
      LAS unsigned char* mp = L + G_MOD + l56 * 64;
      *(LAS f32x4*)mp = f32x4{bf_lo(g0.x), bf_hi(g0.x), bf_lo(g0.z), bf_hi(g0.z)};
      *(LAS f32x4*)(mp + 16) = f32x4{bf_lo(g1.x), bf_hi(g1.x), bf_lo(g1.z), bf_hi(g1.z)};
      *(LAS f32x4*)(mp + 32) = f32x4{bf_lo(g2.x), bf_hi(g2.x), bf_lo(g2.z), bf_hi(g2.z)};
      *(LAS f32x4*)(mp + 48) = f32x4{bf_lo(g3.x), bf_hi(g3.x), bf_lo(g3.z), bf_hi(g3.z)};
    }
    gather_x2<0>(p, L, rs, pn, tid);
    STAMP(0, 1);
    bar_lds();
    const int fo = (int)WS_FLAGC + (lane & 31) * 8;
    const u32x2 fc = flag_load(rs, fo);
    ln_phase_a(L, j, w, r16, q, [](auto) {});
    STAMP(0, 2);
    bar_lds();
    flag_wait(rs, fo, fc, ~tlo, ~thi, ~0u);
    const unsigned char* src = p.ws + WS_WP + (size_t)(j * 7) * NFRAG * 1024 + NRING * 1024 + voff;
#define DMA_HALF(h)                                                                                                         \
  _Pragma("unroll") for (int ww = 0; ww < 7; ++ww) _Pragma("unroll") for (int i = (h) * (NLDS / 2); i < ((h) + 1) * (NLDS / 2); ++i) \
      __builtin_amdgcn_global_load_lds((const GAS unsigned*)(src + (size_t)ww * NFRAG * 1024 + i * 1024),                     \
                                       (LAS unsigned*)(L + G_WL + (ww * NLDS + i) * 1024), 16, 0, AUX_SC1);
    DMA_HALF(0)
    bar_lds();
    STAMP(0, 4);
    bar_lds();
    DMA_HALF(1)
#undef DMA_HALF
    bar_lds();
    {
      int gof[3];
      u32x4 xr[3];
      x1_offsets(pn, j, w, r16, q, gof);
      poll_load<3>(rs, gof, xr);
      ln_phase_c(L, xr, j, w, r16, q);
    }
    asm volatile("s_waitcnt vmcnt(0)" ::: "memory");
    STAMP(0, 7);
    bar_lds();
    return;
  }
  SB0();
  gather_x2<X2_DELAY>(p, L, rs, pn, tid);
  STAMP(0, 1);
  bar_lds();
  u32x2 h2r[2], x2r[2], x1r[2];
  const int ncol = j * 112 + w * 16 + q * 4;
  const int gbo = (int)WS_MODG + (5 * 224 + (ncol >> 1)) * 8;
  u32x4 gbg = __builtin_amdgcn_raw_buffer_load_b128(rs, gbo, 0, AUX_SC1);
#pragma unroll
  for (int T = 0; T < 2; ++T) {
    const int row = 16 * T + r16;
    x2r[T] = *(const LAS u32x2*)(L + G_X + row * 1024 + (((ncol >> 3) ^ (row >> 2)) * 16) + (ncol & 4) * 2);
  }
  SB0();
  ring_fill<0, 8>(ring, rw, voff);
  ln_phase_a(L, j, w, r16, q, [&](auto k) { SB0(); ring_fill<8 + 6 * decltype(k)::value, 6>(ring, rw, voff); });
  SB0();
  ring_fill<26, 6>(ring, rw, voff);
  STAMP(0, 2);
  bar_lds();
  u32x2 gbp;
  {
    u32x4 dup = gbg;
    if (__builtin_expect(__any(((gbg.y ^ tag32) | (gbg.w ^ tag32)) != 0u), 0)) {
      for (unsigned spins = 0; spins < SPIN_CAP; ++spins) {
        asm volatile("" ::: "memory");
        dup = __builtin_amdgcn_raw_buffer_load_b128(rs, gbo, 0, AUX_SC1);
        if (!__any(((dup.y ^ tag32) | (dup.w ^ tag32)) != 0u)) break;
      }
    }
    gbp = u32x2{dup.x, dup.z};
  }
#pragma unroll
  for (int T = 0; T < 2; ++T) h2r[T] = *(const LAS u32x2*)(L + G_HQ + (16 * T + r16) * 240 + (w * 16 + q * 4) * 2);
  const LAS unsigned char* ap0 = L + G_T0 + r16 * AS + q * 16;
  const LAS unsigned char* ap1 = L + G_T1 + r16 * AS + q * 16;
  const int tok = (q & 1) * 16 + r16, octc = 2 * w + (q >> 1);
  {
    f32x4 acc[1][2];
    STAMP(0, 16);
    gemm14<1, 0, 32, 7, 2, 5>(ap0, ring, rw, voff, acc);
    STAMP(0, 17);
    const u32x4 own = make_piece(gelu4(acc[0][0]), gelu4(acc[0][1]));
    __builtin_amdgcn_raw_buffer_store_b128(own, rs, (int)WS_G + (pn * 4 + j) * 7168 + pslot, 0, AUX_SC1);
    SB0();
    STAMP(0, 18);
    ring_fill<39, 4>(ring, rw, voff);
    int goff[4], loff[4];
    bool wrap0;
#pragma unroll
    for (int i = 0; i < 4; ++i) {
      const int jq = (j + i) & 3;
      const int sh = (jq & 1) ? -1 - (jq >> 1) : 1 + (jq >> 1);
      const int it = tok + sh;
      const int sp = pn + (it >= 32 ? -1 : (it < 0 ? 1 : 0));
      if (i == 0) wrap0 = sp != pn;
      goff[i] = (sp < 0 || sp > 63 || (i == 0 && sp == pn)) ? (int)WS_END : (int)WS_G + (sp * 4 + jq) * 7168 + pslot;
      loff[i] = G_T1 + (it & 31) * AS + (jq * 14 + octc) * 16;
    }
    STAMP(0, 3);
    bar_lds();
    u32x4 v[4];
    poll_issue<4>(rs, goff, v);
    __builtin_amdgcn_s_sleep(3);
    ring_fill<43, 5>(ring, rw, voff);
    poll_finish<4>(rs, goff, v);
    *(LAS u32x4*)(L + loff[0]) = wrap0 ? v[0] : own;
#pragma unroll
    for (int i = 1; i < 4; ++i) *(LAS u32x4*)(L + loff[i]) = v[i];
  }
  STAMP(0, 4);
  bar_lds();
  {
    f32x4 acc[1][2];
    gemm14<1, 14, 48, 7, 2, 5>(ap1, ring, rw, voff, acc);
#pragma unroll
    for (int T = 0; T < 2; ++T) {
      const unsigned a = pk_bf16(acc[0][T][0], acc[0][T][1]), b = pk_bf16(acc[0][T][2], acc[0][T][3]);
      x1r[T].x = pk_bf16(bf_lo(h2r[T].x) + bf_lo(a), bf_hi(h2r[T].x) + bf_hi(a));
      x1r[T].y = pk_bf16(bf_lo(h2r[T].y) + bf_lo(b), bf_hi(h2r[T].y) + bf_hi(b));
    }
    const u32x4 own = make_piece(x1r[0], x1r[1]);
    __builtin_amdgcn_raw_buffer_store_b128(own, rs, (int)WS_X1 + (pn * 4 + j) * 7168 + (w * 64 + (tok >> 2) * 8 + (q >> 1) * 4 + (tok & 3)) * 16, 0, AUX_SC1);
    *(LAS u32x4*)(L + G_HQ + tok * 240 + octc * 16) = own;
    SB0();
    STAMP(0, 19);
    ring_fill<55, 4>(ring, rw, voff);
    int gof[3];
    u32x4 xr[3];
    x1_offsets(pn, j, w, r16, q, gof);
    STAMP(0, 5);
    bar_lds();
    poll_issue<3>(rs, gof, xr);
    __builtin_amdgcn_s_sleep(3);
    ring_fill<59, 5>(ring, rw, voff);
    poll_finish<3>(rs, gof, xr);
    STAMP(0, 6);
    ring_fill<64, 4>(ring, rw, voff);
    ln_phase_c(L, xr, j, w, r16, q);
    SB0();
    ring_fill<68, 4>(ring, rw, voff);
  }
  STAMP(0, 7);
  bar_lds();
  {
    {
      f32x4 ca[2][2], cb2[2][2], acc[3][2];
      LAS unsigned char* hp = L + G_T1 + r16 * AS + (w * 64 + q * 4) * 2;
      unsigned glo;
      STAMP(0, 12);
      gemm_p<2, 28, 0, 1, 14, 72, 19, 3, true>(ap0, ring, rw, voff, ca, [](auto) {});
      gemm_p<2, 56, 0, 1, 14, 91, 19, 3, true>(ap0, ring, rw, voff, cb2, [&](auto stc) {
        constexpr int st = decltype(stc)::value;
        if constexpr (st % 3 == 1 && st / 3 < 8) {
          constexpr int c = st / 3, i = c >> 2, T = (c >> 1) & 1;
          if constexpr ((c & 1) == 0) glo = gelu2(ca[i][T][0], ca[i][T][1]);
          else *(LAS u32x2*)(hp + T * 16 * AS + i * 32) = u32x2{glo, gelu2(ca[i][T][2], ca[i][T][3])};
        }
      });
      STAMP(0, 13);
      bar_lds();
      gemm_p<3, 84, 0, 2, 7, 110, 18, 3, true>(ap1, ring, rw, voff, acc, [&](auto stc) {
        constexpr int st = decltype(stc)::value;
        if constexpr (st % 2 == 1 && st / 2 < 8) {
          constexpr int c = st / 2, i = c >> 2, T = (c >> 1) & 1;
          if constexpr ((c & 1) == 0) glo = gelu2(cb2[i][T][0], cb2[i][T][1]);
          else *(LAS u32x2*)(hp + T * 16 * AS + (2 + i) * 32) = u32x2{glo, gelu2(cb2[i][T][2], cb2[i][T][3])};
        }
      });
      STAMP(0, 8);
      bar_lds();
      STAMP(0, 14);
      gemm_p<3, 105, 1, 2, 7, 0, 0, 3, false>(ap1, ring, rw, voff, acc, [](auto) {});
      STAMP(0, 15);
#pragma unroll
      for (int i = 0; i < 3; ++i) {
        const int jd = (j + 1 + i) & 3;
        const u32x4 o = {sane(pk_bf16(acc[i][0][0], acc[i][0][1])), sane(pk_bf16(acc[i][0][2], acc[i][0][3])),
                         sane(pk_bf16(acc[i][1][0], acc[i][1][1])), sane(pk_bf16(acc[i][1][2], acc[i][1][3]))};
        __builtin_amdgcn_raw_buffer_store_b128(o, rs, (int)WS_P + ((pn * 4 + jd) * 3 + (2 - i)) * 7168 + pslot, 0, AUX_SC1);
      }
      SB0();
    }
    STAMP(0, 9);
    f32x4 acc0 = {0.f, 0.f, 0.f, 0.f}, acc1 = acc0;
    u32x4 v[3];
    const int go = (int)WS_P + (pn * 4 + j) * 3 * 7168 + pslot;
    const int gof[3] = {go, go + 7168, go + 2 * 7168};
    {
      const LAS unsigned char* wl = L + G_WL + w * (NLDS * 1024) + voff;
      u32x4 wa[14], b0[14], b1[14];
#pragma unroll
      for (int ks = 0; ks < 14; ++ks) {
        const int f = 126 + ks;
        wa[ks] = f < NRING ? ring[f % RING] : *(const LAS u32x4*)(wl + (f - NRING) * 1024);
        b0[ks] = *(const LAS u32x4*)(ap1 + ks * 64);
        b1[ks] = *(const LAS u32x4*)(ap1 + 16 * AS + ks * 64);
      }
      SB0();
#pragma unroll
      for (int ks = 0; ks < 14; ++ks) {
        acc0 = mfma16(wa[ks], b0[ks], acc0);
        acc1 = mfma16(wa[ks], b1[ks], acc1);
      }
      SB0();
    }
    STAMP(0, 10);
    poll_load<3>(rs, gof, v);
    STAMP(0, 11);
#pragma unroll
    for (int T = 0; T < 2; ++T) {
      const f32x4 ac = T ? acc1 : acc0;
      float hh[4];
#pragma unroll
      for (int k = 0; k < 2; ++k) {
        hh[2 * k] = ac[2 * k] + ((bf_lo(v[0][2 * T + k]) + bf_lo(v[1][2 * T + k])) + bf_lo(v[2][2 * T + k]));
        hh[2 * k + 1] = ac[2 * k + 1] + ((bf_hi(v[0][2 * T + k]) + bf_hi(v[1][2 * T + k])) + bf_hi(v[2][2 * T + k]));
      }
      unsigned a = pk_bf16(hh[0], hh[1]), b = pk_bf16(hh[2], hh[3]);
      a = pk_bf16(bf_lo(x1r[T].x) + bf_lo(a), bf_hi(x1r[T].x) + bf_hi(a));
      b = pk_bf16(bf_lo(x1r[T].y) + bf_lo(b), bf_hi(x1r[T].y) + bf_hi(b));
      a = pk_bf16(bf_lo(gbp.x) * bf_lo(a), bf_hi(gbp.x) * bf_hi(a));
      b = pk_bf16(bf_lo(gbp.y) * bf_lo(b), bf_hi(gbp.y) * bf_hi(b));
      u32x2 o;
      o.x = pk_bf16(bf_lo(x2r[T].x) + bf_lo(a), bf_hi(x2r[T].x) + bf_hi(a));
      o.y = pk_bf16(bf_lo(x2r[T].y) + bf_lo(b), bf_hi(x2r[T].y) + bf_hi(b));
      *(u32x2*)(p.out + (size_t)(pn * 32 + 16 * T + r16) * D_ + ncol) = o;
    }
  }
  STAMP(0, 20);
}

__global__ __launch_bounds__(512, 2) void k_all(GArgs p) {
  extern __shared__ __attribute__((aligned(16))) unsigned char smem[];
  const uintptr_t code0 = (uintptr_t)__builtin_amdgcn_s_getpc() & ~(uintptr_t)127;
  body(p, (LAS unsigned char*)smem, code0);
}

}

extern "C" void kernel_launch(void* const* d_in, const int* in_sizes, int n_in, void* d_out, int out_size, void* d_ws,
                              size_t ws_size, hipStream_t stream) {
  (void)in_sizes; (void)n_in; (void)out_size; (void)ws_size;
  GArgs a;
  a.x = (const unsigned short*)d_in[0];
  a.a_re = (const unsigned short*)d_in[1];
  a.a_im = (const unsigned short*)d_in[2];
  a.b_re = (const unsigned short*)d_in[3];
  a.b_im = (const unsigned short*)d_in[4];
  a.c_re = (const unsigned short*)d_in[5];
  a.c_im = (const unsigned short*)d_in[6];
  a.log_dt = (const unsigned short*)d_in[7];
  a.dskip = (const unsigned short*)d_in[8];
  a.w1 = (const unsigned short*)d_in[10];
  a.w2 = (const unsigned short*)d_in[11];
  a.w3 = (const unsigned short*)d_in[12];
  a.w4 = (const unsigned short*)d_in[13];
  a.cvec = (const unsigned short*)d_in[14];
  a.wmod = (const unsigned short*)d_in[15];
  a.ws = (unsigned char*)d_ws;
  a.out = (unsigned short*)d_out;
  (void)hipFuncSetAttribute((const void*)k_all, hipFuncAttributeMaxDynamicSharedMemorySize, G_LDS);
  k_all<<<256, 512, G_LDS, stream>>>(a);
}
```

```cpp
#include <hip/hip_runtime.h>
#include <hip/hip_bf16.h>
#include <stdint.h>
#include <type_traits>
#include <utility>

namespace {

typedef __attribute__((ext_vector_type(8))) short bf16x8;
typedef __attribute__((ext_vector_type(8))) _Float16 f16x8;
typedef __attribute__((ext_vector_type(2))) _Float16 f16x2;
typedef __attribute__((ext_vector_type(4))) float f32x4;
typedef __attribute__((ext_vector_type(2))) float f32x2;
typedef __attribute__((ext_vector_type(4))) unsigned u32x4;
typedef __attribute__((ext_vector_type(2))) unsigned u32x2;
typedef __attribute__((ext_vector_type(2))) __bf16 bf16x2v;
#define GAS __attribute__((address_space(1)))
#define LAS __attribute__((address_space(3)))

constexpr int L_ = 2048, D_ = 448, NS_ = 256;

constexpr size_t WS_EPOCH = 0;
constexpr size_t WS_FLAGC = 4608;
constexpr size_t WS_MODG = 16384;
constexpr size_t WS_STG = 32768;
constexpr int NFRAG = 140;
constexpr size_t WS_XG = 65536;
constexpr size_t XG_BLK = 224 * 64;
constexpr size_t WS_WP = WS_XG + 256 * XG_BLK;
constexpr size_t WS_X2B = WS_WP + (size_t)28 * NFRAG * 1024;
constexpr size_t X2B_BLK = 224 * 128;
constexpr size_t WS_G = WS_X2B + 64 * X2B_BLK;
constexpr size_t WS_X1 = WS_G + (size_t)64 * 4 * 7168;
constexpr size_t WS_P = WS_X1 + (size_t)64 * 4 * 7168;
constexpr size_t WS_END = WS_P + (size_t)64 * 12 * 7168;

#define STAMP(base, k) do { } while (0)

__device__ __forceinline__ float bf_lo(unsigned u) { return __builtin_bit_cast(float, u << 16); }
__device__ __forceinline__ float bf_hi(unsigned u) { return __builtin_bit_cast(float, u & 0xffff0000u); }
__device__ __forceinline__ float bf2f(unsigned short h) { return __builtin_bit_cast(float, (unsigned)h << 16); }
__device__ __forceinline__ unsigned pk_bf16(float a, float b) {
  f32x2 v = {a, b};
  bf16x2v r = __builtin_convertvector(v, bf16x2v);
  return __builtin_bit_cast(unsigned, r);
}
__device__ __forceinline__ unsigned pk_f16(f32x2 v) {
  f16x2 r = __builtin_convertvector(v, f16x2);
  return __builtin_bit_cast(unsigned, r);
}
__device__ __forceinline__ float rbf(float a) { return bf_lo(pk_bf16(a, a)); }
__device__ __forceinline__ f32x4 mfma16(u32x4 a, u32x4 b, f32x4 c) {
  return __builtin_amdgcn_mfma_f32_16x16x32_bf16(__builtin_bit_cast(bf16x8, a), __builtin_bit_cast(bf16x8, b), c, 0, 0, 0);
}
__device__ __forceinline__ f32x4 mfma16h(u32x4 a, u32x4 b, f32x4 c) {
  return __builtin_amdgcn_mfma_f32_16x16x32_f16(__builtin_bit_cast(f16x8, a), __builtin_bit_cast(f16x8, b), c, 0, 0, 0);
}
template <int CTRL>
__device__ __forceinline__ float dpp_f(float v) {
  return __builtin_bit_cast(float, __builtin_amdgcn_update_dpp(0, __builtin_bit_cast(int, v), CTRL, 0xf, 0xf, true));
}
__device__ __forceinline__ float row16_sum(float v) {
  v += dpp_f<0xB1>(v);
  v += dpp_f<0x4E>(v);
  v += dpp_f<0x141>(v);
  v += dpp_f<0x140>(v);
  return v;
}
__device__ __forceinline__ float row16_max(float v) {
  v = fmaxf(v, dpp_f<0xB1>(v));
  v = fmaxf(v, dpp_f<0x4E>(v));
  v = fmaxf(v, dpp_f<0x141>(v));
  v = fmaxf(v, dpp_f<0x140>(v));
  return v;
}
__device__ __forceinline__ void swap16(float v, float& a, float& b) {
  const unsigned u = __builtin_bit_cast(unsigned, v), u2 = u;
  auto r = __builtin_amdgcn_permlane16_swap(u, u2, false, false);
  const unsigned r0 = r[0], r1 = r[1];
  a = __builtin_bit_cast(float, r0); b = __builtin_bit_cast(float, r1);
}
__device__ __forceinline__ void swap32(float v, float& a, float& b) {
  const unsigned u = __builtin_bit_cast(unsigned, v), u2 = u;
  auto r = __builtin_amdgcn_permlane32_swap(u, u2, false, false);
  const unsigned r0 = r[0], r1 = r[1];
  a = __builtin_bit_cast(float, r0); b = __builtin_bit_cast(float, r1);
}
__device__ __forceinline__ float rows_sum(float v) {
  float a, b;
  swap16(v, a, b); v = a + b;
  swap32(v, a, b);
  return a + b;
}
__device__ __forceinline__ float rows_max(float v) {
  float a, b;
  swap16(v, a, b); v = fmaxf(a, b);
  swap32(v, a, b);
  return fmaxf(a, b);
}
template <int CTRL, int RMASK>
__device__ __forceinline__ float dpp_z(float v) {
  return __builtin_bit_cast(float, __builtin_amdgcn_update_dpp(0, __builtin_bit_cast(int, v), CTRL, RMASK, 0xf, false));
}
__device__ __forceinline__ float wave_scan(float v) {
  v += dpp_z<0x111, 0xf>(v);
  v += dpp_z<0x112, 0xf>(v);
  v += dpp_z<0x114, 0xf>(v);
  v += dpp_z<0x118, 0xf>(v);
  v += dpp_z<0x142, 0xa>(v);
  v += dpp_z<0x143, 0xc>(v);
  return v;
}
__device__ __forceinline__ float wave_sum(float v) { return rows_sum(row16_sum(v)); }
__device__ __forceinline__ float gelu_tanh(float x) {
  const float c1 = -2.0f * 0.7978845608028654f * 1.4426950408889634f;
  const float c2 = c1 * 0.044715f;
  float t = x * (c1 + c2 * x * x);
  float e = __builtin_amdgcn_exp2f(t);
  return x * __builtin_amdgcn_rcpf(1.0f + e);
}
__device__ __forceinline__ float ln_sd(float var) { return rbf(__builtin_amdgcn_sqrtf(rbf(rbf(var) + 1e-5f))); }
__device__ __forceinline__ void lds_fence() { asm volatile("" ::: "memory"); }
__device__ __forceinline__ void bar_lds() {
  asm volatile("s_waitcnt lgkmcnt(0)" ::: "memory");
  __builtin_amdgcn_s_barrier();
  asm volatile("" ::: "memory");
}

__device__ __forceinline__ void transpose8x8(const u32x4 (&r)[8], u32x4 (&o)[8]) {
#pragma unroll
  for (int c2 = 0; c2 < 4; ++c2) {
#pragma unroll
    for (int rp = 0; rp < 4; ++rp) {
      unsigned a = r[2 * rp][c2], b = r[2 * rp + 1][c2];
      o[2 * c2][rp] = __builtin_amdgcn_perm(b, a, 0x05040100u);
      o[2 * c2 + 1][rp] = __builtin_amdgcn_perm(b, a, 0x07060302u);
    }
  }
}
__device__ __forceinline__ f32x2 cmul(f32x2 a, f32x2 b) {
  f32x2 r;
  asm("v_pk_mul_f32 %0, %1, %2 op_sel_hi:[0,1]\n\tv_pk_fma_f32 %0, %1, %2, %0 op_sel:[1,1,0] op_sel_hi:[1,0,1] neg_lo:[1,0,0]"
      : "=&v"(r) : "v"(a), "v"(b));
  return r;
}
__device__ __forceinline__ f32x2 cexp_tab(float a, float b, float l) {
  const float e = __builtin_amdgcn_exp2f(a * l), ph = __builtin_amdgcn_fractf(b * l);
  f32x2 r;
  r.x = e * __builtin_amdgcn_cosf(ph);
  r.y = e * __builtin_amdgcn_sinf(ph);
  return r;
}

constexpr int CV_KCOPY = 8 * 272 * 16;
constexpr int CV_UROW = 160;
constexpr int CV_U = 48 * CV_UROW;
constexpr int CV_CH = CV_KCOPY + CV_U;
constexpr int CV_LDS = 2 * CV_CH;
constexpr int CV_PROW = 272;
static_assert(4 * 32 * CV_PROW <= CV_KCOPY, "partials alias the kernel copies");

struct GArgs {
  const unsigned short* x;
  const unsigned short* a_re;
  const unsigned short* a_im;
  const unsigned short* b_re;
  const unsigned short* b_im;
  const unsigned short* c_re;
  const unsigned short* c_im;
  const unsigned short* log_dt;
  const unsigned short* dskip;
  const unsigned short* w1;
  const unsigned short* w2;
  const unsigned short* w3;
  const unsigned short* w4;
  const unsigned short* cvec;
  const unsigned short* wmod;
  unsigned char* ws;
  unsigned short* out;
};
constexpr int AS = 928;
constexpr int G_T0 = 0;
constexpr int G_T1 = G_T0 + 32 * AS;
constexpr int G_X = G_T1;
constexpr int G_HQ = G_X + 32768;
constexpr int G_WL = G_HQ + 32 * 240;
constexpr int NLDS = 12;
constexpr int NRING = NFRAG - NLDS;
constexpr int G_MOD = G_WL + 7 * NLDS * 1024;
constexpr int G_CNT = G_MOD + 2 * 448 * 4;
constexpr int P0_MS = G_CNT + 16;
constexpr int G_LDS = P0_MS + 256;
constexpr int KG_ROW = 288;
constexpr int KG_BUF = 16 * KG_ROW;
constexpr int P0_KG = CV_LDS;
constexpr int P0_LN = 0;
constexpr int LNS = 928;
constexpr int P0_PK = 16384;
static_assert(P0_KG + 16 * KG_BUF <= G_CNT, "tables");
static_assert(G_MOD >= CV_LDS && G_LDS <= 163840, "LDS map");
constexpr int RING = 44;
constexpr int CV_P = CV_LDS;
static_assert(CV_P + 8 * 32 * CV_PROW <= G_MOD, "partials");
constexpr unsigned SPIN_CAP = 1u << 17;
constexpr int AUX_SC1 = 16;
constexpr int CODE_LINES = 320;
#define PRETOUCH 1
#define X2_DELAY 20
#define POLL_SLEEP 1

__device__ __forceinline__ unsigned sane(unsigned v) { return v < 0xFFFFFFFEu ? v : 0xFFFFFFFEu; }
__device__ __forceinline__ unsigned umax4(const u32x4& v) { return max(max(v.x, v.y), max(v.z, v.w)); }
__device__ __forceinline__ bool bad4(const u32x4& v) { return umax4(v) == ~0u; }
__device__ __forceinline__ u32x4 make_piece(u32x2 a, u32x2 b) {
  auto rx = __builtin_amdgcn_permlane16_swap(a.x, b.x, false, false);
  auto ry = __builtin_amdgcn_permlane16_swap(a.y, b.y, false, false);
  return u32x4{sane(rx[0]), sane(ry[0]), sane(rx[1]), sane(ry[1])};
}
template <int N>
__device__ __forceinline__ void poll_issue(__amdgpu_buffer_rsrc_t r, const int (&off)[N], u32x4 (&v)[N]) {
#pragma unroll
  for (int i = 0; i < N; ++i) v[i] = __builtin_amdgcn_raw_buffer_load_b128(r, off[i], 0, AUX_SC1);
  __builtin_amdgcn_sched_barrier(0);
}
template <int N>
__device__ __forceinline__ void poll_finish(__amdgpu_buffer_rsrc_t r, const int (&off)[N], u32x4 (&v)[N]) {
  unsigned m = 0u;
#pragma unroll
  for (int i = 0; i < N; ++i) m = max(m, umax4(v[i]));
  if (__builtin_expect(__any(m == ~0u), 0)) {
    for (unsigned spins = 0; spins < SPIN_CAP; ++spins) {
      __builtin_amdgcn_s_sleep(POLL_SLEEP);
      asm volatile("" ::: "memory");
#pragma unroll
      for (int i = 0; i < N; ++i)
        if (bad4(v[i])) v[i] = __builtin_amdgcn_raw_buffer_load_b128(r, off[i], 0, AUX_SC1);
      m = 0u;
#pragma unroll
      for (int i = 0; i < N; ++i) m = max(m, umax4(v[i]));
      if (!__any(m == ~0u)) break;
    }
  }
}
template <int N>
__device__ __forceinline__ void poll_load(__amdgpu_buffer_rsrc_t r, const int (&off)[N], u32x4 (&v)[N]) {
  poll_issue<N>(r, off, v);
  poll_finish<N>(r, off, v);
}
__device__ __forceinline__ float dot2bf(unsigned a, unsigned b, float c) {
  return __builtin_amdgcn_fdot2_f32_bf16(__builtin_bit_cast(bf16x2v, a), __builtin_bit_cast(bf16x2v, b), c, false);
}
__device__ __forceinline__ void ln_stats(const unsigned (&x)[16], float lmsk, float& r, float& mr) {
  float s0 = 0.f, s1 = 0.f, q0 = 0.f, q1 = 0.f;
#pragma unroll
  for (int k = 0; k < 16; k += 2) {
    s0 = dot2bf(x[k], 0x3f803f80u, s0); q0 = dot2bf(x[k], x[k], q0);
    s1 = dot2bf(x[k + 1], 0x3f803f80u, s1); q1 = dot2bf(x[k + 1], x[k + 1], q1);
  }
  const float mean = row16_sum((s0 + s1) * lmsk) * (1.0f / D_);
  const float msq = row16_sum((q0 + q1) * lmsk) * (1.0f / D_);
  const float var = fmaxf(msq - mean * mean, 0.f);
  r = __builtin_amdgcn_rcpf(ln_sd(var));
  mr = -rbf(mean) * r;
}

template <class F, int... I>
__device__ __forceinline__ void static_for_impl(F f, std::integer_sequence<int, I...>) { (f(std::integral_constant<int, I>{}), ...); }
template <int N, class F>
__device__ __forceinline__ void static_for(F f) { static_for_impl(f, std::make_integer_sequence<int, N>{}); }
#define SB0() __builtin_amdgcn_sched_barrier(0)
#define WLOAD(f) __builtin_amdgcn_raw_buffer_load_b128(rw, voff + ((f) & 3) * 1024, ((f) >> 2) * 4096, AUX_SC1)
template <int NT, int F0, int LSTART, int LNUM, int LEVERY, int PD>
__device__ __forceinline__ void gemm14(const LAS unsigned char* ap, u32x4 (&ring)[RING], __amdgpu_buffer_rsrc_t rw, int voff, f32x4 (&acc)[NT][2]) {
  u32x4 bq[PD][2];
#pragma unroll
  for (int i = 0; i < PD; ++i) { bq[i][0] = *(const LAS u32x4*)(ap + i * 64); bq[i][1] = *(const LAS u32x4*)(ap + 16 * AS + i * 64); }
#pragma unroll
  for (int i = 0; i < NT; ++i) { acc[i][0] = f32x4{0.f, 0.f, 0.f, 0.f}; acc[i][1] = acc[i][0]; }
  SB0();
#pragma unroll
  for (int ks = 0; ks < 14; ++ks) {
#pragma unroll
    for (int i = 0; i < NT; ++i) {
      const int st = ks * NT + i, f = F0 + st;
      acc[i][0] = mfma16(ring[f % RING], bq[ks % PD][0], acc[i][0]);
      acc[i][1] = mfma16(ring[f % RING], bq[ks % PD][1], acc[i][1]);
      if (st % LEVERY == 0 && st / LEVERY < LNUM) {
        const int g = LSTART + st / LEVERY;
        ring[g % RING] = WLOAD(g);
      }
      if (i == NT - 1 && ks + PD < 14) {
        bq[ks % PD][0] = *(const LAS u32x4*)(ap + (ks + PD) * 64);
        bq[ks % PD][1] = *(const LAS u32x4*)(ap + 16 * AS + (ks + PD) * 64);
      }
      SB0();
    }
  }
}
template <int NT, int F0, int KS0, int KSTEP, int NKS, int LSTART, int LNUM, int PD, bool INIT, class S>
__device__ __forceinline__ void gemm_p(const LAS unsigned char* ap, u32x4 (&ring)[RING], __amdgpu_buffer_rsrc_t rw, int voff, f32x4 (&acc)[NT][2], S side) {
  u32x4 bq[PD][2];
#pragma unroll
  for (int i = 0; i < PD; ++i) {
    bq[i][0] = *(const LAS u32x4*)(ap + (KS0 + i * KSTEP) * 64);
    bq[i][1] = *(const LAS u32x4*)(ap + 16 * AS + (KS0 + i * KSTEP) * 64);
  }
  if (INIT) {
#pragma unroll
    for (int i = 0; i < NT; ++i) { acc[i][0] = f32x4{0.f, 0.f, 0.f, 0.f}; acc[i][1] = acc[i][0]; }
  }
  SB0();
  static_for<NKS * NT>([&](auto stc) {
    constexpr int st = decltype(stc)::value, k = st / NT, i = st % NT, f = F0 + st;
    acc[i][0] = mfma16(ring[f % RING], bq[k % PD][0], acc[i][0]);
    acc[i][1] = mfma16(ring[f % RING], bq[k % PD][1], acc[i][1]);
    constexpr int l0 = st * LNUM / (NKS * NT), l1 = (st + 1) * LNUM / (NKS * NT);
    if constexpr (l1 > l0) {
      static_assert(LSTART + l0 - RING <= f, "slot still in use");
      ring[(LSTART + l0) % RING] = WLOAD(LSTART + l0);
    }
    if constexpr (i == NT - 1 && k + PD < NKS) {
      bq[k % PD][0] = *(const LAS u32x4*)(ap + (KS0 + (k + PD) * KSTEP) * 64);
      bq[k % PD][1] = *(const LAS u32x4*)(ap + 16 * AS + (KS0 + (k + PD) * KSTEP) * 64);
    }
    side(stc);
    SB0();
  });
}
template <int F0, int N>
__device__ __forceinline__ void ring_fill(u32x4 (&ring)[RING], __amdgpu_buffer_rsrc_t rw, int voff) {
#pragma unroll
  for (int f = F0; f < F0 + N; ++f) { ring[f % RING] = WLOAD(f); SB0(); }
}

template <int DELAY>
__device__ __forceinline__ void gather_x2(const GArgs& p, LAS unsigned char* L, __amdgpu_buffer_rsrc_t rs, int pn, int tid) {
  const int lane = tid & 63, w = tid >> 6; (void)lane; (void)w; (void)p;
  u32x4 xin[4];
  int xo[4];
#pragma unroll
  for (int i = 0; i < 4; ++i) {
    const int pce = tid + 512 * i;
    xo[i] = pce < 1792 ? (int)WS_X2B + pn * (int)X2B_BLK + pce * 16 : (int)WS_END;
  }
  if (DELAY) __builtin_amdgcn_s_sleep(DELAY);
  poll_issue<4>(rs, xo, xin);
  poll_finish<4>(rs, xo, xin);
  STAMP(0, 26);
#pragma unroll
  for (int i = 0; i < 4; ++i) {
    const int pce = tid + 512 * i;
    if (i < 3 || pce < 1792) {
      const int dpi = pce >> 3, tq = pce & 7;
      LAS unsigned char* dst = L + G_X + tq * 4096 + (((dpi >> 2) ^ tq) * 16) + (dpi & 3) * 4;
#pragma unroll
      for (int k = 0; k < 4; ++k) *(LAS unsigned*)(dst + k * 1024) = xin[i][k];
    }
  }
}
template <class H>
__device__ __forceinline__ void ln_phase_a(LAS unsigned char* L, int j, int w, int r16, int q, H hook) {
  const int c16 = r16 < 14 ? r16 : 0;
  const float lmsk = r16 < 14 ? 1.0f : 0.0f;
  const int row = 4 * w + q;
  unsigned x[16];
#pragma unroll
  for (int jq = 0; jq < 4; ++jq) {
    const u32x4 t = *(const LAS u32x4*)(L + G_X + row * 1024 + (((((jq + j) & 3) * 14 + c16) ^ w) * 16));
    x[4 * jq] = t.x; x[4 * jq + 1] = t.y; x[4 * jq + 2] = t.z; x[4 * jq + 3] = t.w;
  }
  float r, mr;
  ln_stats(x, lmsk, r, mr);
  hook(std::integral_constant<int, 0>{});
  f32x2 h[16];
#pragma unroll
  for (int jq = 0; jq < 4; ++jq) {
    const LAS unsigned char* mp = L + G_MOD + (((jq + j) & 3) * 112 + c16 * 8) * 4;
    const f32x4 sb0 = *(const LAS f32x4*)mp, sb1 = *(const LAS f32x4*)(mp + 16);
    const f32x4 c10 = *(const LAS f32x4*)(mp + 1792), c11 = *(const LAS f32x4*)(mp + 1792 + 16);
#pragma unroll
    for (int k = 0; k < 4; ++k) {
      const f32x2 xv2 = {bf_lo(x[4 * jq + k]), bf_hi(x[4 * jq + k])};
      const f32x2 c2 = k < 2 ? f32x2{c10[2 * k], c10[2 * k + 1]} : f32x2{c11[2 * k - 4], c11[2 * k - 3]};
      const f32x2 sb2 = k < 2 ? f32x2{sb0[2 * k], sb0[2 * k + 1]} : f32x2{sb1[2 * k - 4], sb1[2 * k - 3]};
      const f32x2 n = xv2 * r + mr;
      h[4 * jq + k] = n * c2 + sb2;
      x[4 * jq + k] = pk_bf16(h[4 * jq + k].x, h[4 * jq + k].y);
    }
  }
  *(LAS u32x4*)(L + G_HQ + row * 240 + (r16 < 14 ? r16 : 14) * 16) = u32x4{x[0], x[1], x[2], x[3]};
  hook(std::integral_constant<int, 1>{});
  ln_stats(x, lmsk, r, mr);
  hook(std::integral_constant<int, 2>{});
#pragma unroll
  for (int jq = 0; jq < 4; ++jq) {
    u32x4 o;
#pragma unroll
    for (int k = 0; k < 4; ++k) {
      const f32x2 n = h[4 * jq + k] * r + mr;
      o[k] = pk_bf16(n.x, n.y);
    }
    *(LAS u32x4*)(L + G_T0 + row * AS + (r16 < 14 ? (((jq + j) & 3) * 14 + r16) * 16 : 896)) = o;
  }
}
__device__ __forceinline__ void x1_offsets(int pn, int j, int w, int r16, int q, int (&gof)[3]) {
  const int c16 = r16 < 14 ? r16 : 0;
  const int go = (int)WS_X1 + pn * 4 * 7168 + ((c16 >> 1) * 64 + w * 8 + (c16 & 1) * 4 + q) * 16;
#pragma unroll
  for (int i = 0; i < 3; ++i) gof[i] = go + ((j + 1 + i) & 3) * 7168;
}
__device__ __forceinline__ void ln_phase_c(LAS unsigned char* L, const u32x4 (&xr)[3], int j, int w, int r16, int q) {
  const int c16 = r16 < 14 ? r16 : 0;
  const float lmsk = r16 < 14 ? 1.0f : 0.0f;
  const int row = 4 * w + q;
  const u32x4 own = *(const LAS u32x4*)(L + G_HQ + row * 240 + c16 * 16);
  unsigned x[16];
#pragma unroll
  for (int k = 0; k < 4; ++k) x[k] = own[k];
#pragma unroll
  for (int k = 4; k < 16; ++k) x[k] = xr[(k >> 2) - 1][k & 3];
  float r, mr;
  ln_stats(x, lmsk, r, mr);
#pragma unroll
  for (int jq = 0; jq < 4; ++jq) {
    u32x4 o;
#pragma unroll
    for (int k = 0; k < 4; ++k) {
      const f32x2 n = f32x2{bf_lo(x[4 * jq + k]), bf_hi(x[4 * jq + k])} * r + mr;
      o[k] = pk_bf16(n.x, n.y);
    }
    *(LAS u32x4*)(L + G_T0 + row * AS + (r16 < 14 ? (((jq + j) & 3) * 14 + r16) * 16 : 896)) = o;
  }
}
__device__ __forceinline__ unsigned gelu2(float a, float b) {
  return pk_bf16(gelu_tanh(a), gelu_tanh(b));
}
__device__ __forceinline__ u32x2 gelu4(f32x4 a) { return u32x2{gelu2(a[0], a[1]), gelu2(a[2], a[3])}; }

#define ST16(off, v) __builtin_amdgcn_raw_buffer_store_b128(v, rs, (int)(off), 0, AUX_SC1)
typedef unsigned long long u64;

struct PackJob { const GAS unsigned short* src; int ldn, mat, ks, cb, na, nb, fb; };
__device__ __forceinline__ PackJob pack_job(const GArgs& p, int g) {
  PackJob jb;
  const uintptr_t a1 = (uintptr_t)p.w1, a2 = (uintptr_t)p.w2, a3 = (uintptr_t)p.w3, a4 = (uintptr_t)p.w4;
  uintptr_t lo = g < 56 ? a1 : a2, hi = g < 336 ? a3 : a4;
  asm volatile("" : "+s"(lo), "+s"(hi));
  jb.src = (const GAS unsigned short*)(g < 112 ? lo : hi);
  jb.mat = (g >= 56) + (g >= 112) + (g >= 336);
  jb.ldn = jb.mat == 2 ? 1792 : 448;
  g -= g < 112 ? (g < 56 ? 0 : 56) : (g < 336 ? 112 : 336);
  const int sh = jb.mat == 2 ? 4 : 2;
  jb.ks = g >> sh;
  jb.cb = g & ((1 << sh) - 1);
  const int j3 = (jb.ks * 4682) >> 16, ksl = jb.ks - j3 * 14, sl = (jb.cb - j3 - 1) & 3;
  const int is2 = jb.mat == 2, is3 = jb.mat == 3;
  jb.na = is2 * jb.cb * 7 + (1 - is2) * (is3 * j3 + (1 - is3) * jb.cb) * 28;
  jb.nb = 4 - 3 * is2;
  const int own = sl == 3;
  const int f3 = (1 - own) * (84 + (ksl & 1) * 21 + (ksl >> 1) * 3 + sl) + own * (126 + ksl);
  jb.fb = is2 * (28 + jb.ks * 2) + is3 * f3 + (1 - is2 - is3) * (jb.mat * 14 + jb.ks);
  jb.na = __builtin_amdgcn_readfirstlane(jb.na); jb.nb = __builtin_amdgcn_readfirstlane(jb.nb); jb.fb = __builtin_amdgcn_readfirstlane(jb.fb);
  return jb;
}
__device__ __forceinline__ void pack_load(const PackJob& jb, int lane, u32x4 (&r)[8]) {
  const int jn = lane & 15, q = lane >> 4, jl = jn < 14 ? jn : 0;
#pragma unroll
  for (int i = 0; i < 8; ++i) r[i] = *(const GAS u32x4*)(jb.src + (size_t)(jb.ks * 32 + q * 8 + i) * jb.ldn + jb.cb * 112 + jl * 8);
}
__device__ __forceinline__ void pack_shuffle(int lane, u32x4 (&r)[8], LAS unsigned char* Lw) {
  const int jn = lane & 15, q = lane >> 4;
  u32x4 o[8];
  transpose8x8(r, o);
#pragma unroll
  for (int i = 0; i < 8; ++i) *(LAS u32x4*)(Lw + (jn >> 1) * 1024 + (q * 16 + (jn & 1) * 8 + (i ^ (jn >> 1))) * 16) = o[i];
  lds_fence();
#pragma unroll
  for (int m = 0; m < 7; ++m) r[m] = *(const LAS u32x4*)(Lw + m * 1024 + (lane ^ m) * 16);
  lds_fence();
}
__device__ __forceinline__ void pack_store1(const PackJob& jb, int lane, const u32x4 (&r)[8], __amdgpu_buffer_rsrc_t rs, int m) {
  const int nt = jb.na + m * jb.nb;
  __builtin_amdgcn_raw_buffer_store_b128(r[m], rs, lane * 16, (int)WS_WP + ((nt >> 2) * NFRAG + jb.fb + ((nt >> 1) & 1) * 28 + (nt & 1)) * 1024, AUX_SC1);
}
__device__ __forceinline__ void mod_load(const GArgs& p, int u, int tid, u32x4 (&r)[7], float (&sc)[7]) {
  const int rg = tid >> 3, pc = tid & 7;
#pragma unroll
  for (int i = 0; i < 7; ++i) r[i] = *(const u32x4*)(p.wmod + (size_t)(rg + 64 * i) * 2688 + u * 64 + pc * 8);
#pragma unroll
  for (int i = 0; i < 7; ++i) sc[i] = bf2f(p.cvec[rg + 64 * i]);
}
__device__ __forceinline__ void mod_finish(int u, unsigned tag32, int tid, const u32x4 (&r)[7], const float (&cv)[7], LAS float* sm, __amdgpu_buffer_rsrc_t rs) {
  const int rg = tid >> 3, pc = tid & 7;
  float acc[8];
#pragma unroll
  for (int k = 0; k < 8; ++k) acc[k] = 0.f;
#pragma unroll
  for (int i = 0; i < 7; ++i) {
    const float sc = rbf(cv[i] * __builtin_amdgcn_rcpf(1.0f + __builtin_amdgcn_exp2f(-1.4426950408889634f * cv[i])));
#pragma unroll
    for (int k = 0; k < 4; ++k) { acc[2 * k] += sc * bf_lo(r[i][k]); acc[2 * k + 1] += sc * bf_hi(r[i][k]); }
  }
  *(LAS f32x4*)(sm + rg * 68 + pc * 8) = f32x4{acc[0], acc[1], acc[2], acc[3]};
  *(LAS f32x4*)(sm + rg * 68 + pc * 8 + 4) = f32x4{acc[4], acc[5], acc[6], acc[7]};
  bar_lds();
  {
    const int col = tid & 63, part = tid >> 6;
    float s = 0.f;
#pragma unroll
    for (int k = 0; k < 8; ++k) s += sm[(part * 8 + k) * 68 + col];
    sm[64 * 68 + part * 64 + col] = s;
  }
  bar_lds();
  if (tid < 32) {
    f32x2 s = {0.f, 0.f};
#pragma unroll
    for (int k = 0; k < 8; ++k) s += *(const LAS f32x2*)(sm + 64 * 68 + k * 64 + 2 * tid);
    unsigned v = pk_bf16(s.x, s.y);
    const int sec = u / 7;
    if (sec == 1 || sec == 4) v = pk_bf16(bf_lo(v) + 1.0f, bf_hi(v) + 1.0f);
    __builtin_amdgcn_raw_buffer_store_b64(u32x2{v, tag32}, rs, (int)WS_MODG + (u * 32 + tid) * 8, 0, AUX_SC1);
  }
}
template <int YOUNGER>
__device__ __forceinline__ void arrive_flag(LAS unsigned char* L, int cnt_slot, unsigned char* flag, u64 tag, int lane) {
  asm volatile("s_waitcnt vmcnt(%0)" ::"i"(YOUNGER) : "memory");
  if (lane == 0) {
    const unsigned old = __hip_atomic_fetch_add((LAS unsigned*)(L + G_CNT) + cnt_slot, 1u, __ATOMIC_RELAXED, __HIP_MEMORY_SCOPE_WORKGROUP);
    if (old == 7u) __hip_atomic_store((GAS u64*)flag, tag, __ATOMIC_RELAXED, __HIP_MEMORY_SCOPE_AGENT);
  }
}
__device__ __forceinline__ u32x2 flag_load(__amdgpu_buffer_rsrc_t rs, int off) { return __builtin_amdgcn_raw_buffer_load_b64(rs, off, 0, AUX_SC1); }
__device__ __forceinline__ u32x2 flag_wait(__amdgpu_buffer_rsrc_t rs, int off, u32x2 v, unsigned lo, unsigned hi, unsigned lomask) {
  if (__builtin_expect(__any((((v.x ^ lo) & lomask) | (v.y ^ hi)) != 0u), 0)) {
    for (unsigned spins = 0; spins < SPIN_CAP; ++spins) {
      __builtin_amdgcn_s_sleep(2);
      asm volatile("" ::: "memory");
      v = flag_load(rs, off);
      if (!__any((((v.x ^ lo) & lomask) | (v.y ^ hi)) != 0u)) break;
    }
  }
  __builtin_amdgcn_fence(__ATOMIC_ACQUIRE, "wavefront");
  return v;
}
__device__ __forceinline__ void stat_wait(__amdgpu_buffer_rsrc_t rs, int off, u32x4& a, u32x4& b, unsigned tag32) {
  if (__builtin_expect(__any((((a.y ^ tag32) | (a.w ^ tag32)) | ((b.y ^ tag32) | (b.w ^ tag32))) != 0u), 0)) {
    for (unsigned spins = 0; spins < SPIN_CAP; ++spins) {
      __builtin_amdgcn_s_sleep(2);
      asm volatile("" ::: "memory");
      a = __builtin_amdgcn_raw_buffer_load_b128(rs, off, 0, AUX_SC1);
      b = __builtin_amdgcn_raw_buffer_load_b128(rs, off + 16, 0, AUX_SC1);
      if (!__any((((a.y ^ tag32) | (a.w ^ tag32)) | ((b.y ^ tag32) | (b.w ^ tag32))) != 0u)) break;
    }
  }
}
#define FILL_P(i) do { SB0(); __builtin_amdgcn_raw_buffer_store_b128(ONES, rst, (int)WS_P + ((pn * 4 + j) * 3 + (i)) * 7168 + pslot, 0, AUX_SC1); SB0(); } while (0)
#define FILL_X1() do { SB0(); __builtin_amdgcn_raw_buffer_store_b128(ONES, rst, (int)WS_X1 + (pn * 4 + j) * 7168 + pslot, 0, AUX_SC1); SB0(); } while (0)
__device__ __forceinline__ void body(const GArgs& p, LAS unsigned char* L, uintptr_t code0) {
  const bool CONV = blockIdx.x < 224;
  const int tid = threadIdx.x, lane = tid & 63;
  const int w = __builtin_amdgcn_readfirstlane(tid >> 6);
  const int bid = blockIdx.x;
  const int j = bid & 3, pn = (bid >> 3) * 2 + ((bid >> 2) & 1);
  const int r16 = lane & 15, q = lane >> 4;
  const bool cw = w < 7;
  const int wc = cw ? w : 6;
  const int dp = (bid & 7) * 28 + (CONV ? bid >> 3 : 0);
  const __amdgpu_buffer_rsrc_t rs = __builtin_amdgcn_make_buffer_rsrc((void*)p.ws, 0, (int)WS_END, 0x00020000);
  const __amdgpu_buffer_rsrc_t rst = __builtin_amdgcn_make_buffer_rsrc((void*)p.ws, 0, cw ? (int)WS_END : 0, 0x00020000);
  const __amdgpu_buffer_rsrc_t rw =
      __builtin_amdgcn_make_buffer_rsrc((void*)(p.ws + WS_WP + (size_t)(j * 7 + wc) * NFRAG * 1024), 0, cw ? NFRAG * 1024 : 0, 0x00020000);
  const int voff = lane * 16;
  const int pslot = (wc * 64 + lane) * 16;
  const u32x4 ONES = {~0u, ~0u, ~0u, ~0u};
  STAMP(0, 0);
  const int l56 = lane < 56 ? lane : 0;
  const u32x4 xrow = *(const u32x4*)(p.x + (size_t)(bid * 8 + w) * D_ + l56 * 8);
  const u32x2 ep = flag_load(rs, (int)WS_EPOCH);
  const int cl = ((bid >> 3) & 3) * 80 + w * 10 + (lane < 10 ? lane : 0);
  const unsigned touch = *(const GAS unsigned*)(code0 + (size_t)(cl < CODE_LINES ? cl : 0) * 128);
  float are, aim, b_re, b_im, cre, cim, ldt;
  u32x4 mr[7], pk[8];
  float mc[7];
  {
    const size_t o = (size_t)(2 * dp + (w >> 2)) * NS_ + (w & 3) * 64 + lane;
    SB0();
    are = bf2f(p.a_re[o]); aim = bf2f(p.a_im[o]); b_re = bf2f(p.b_re[o]); b_im = bf2f(p.b_im[o]);
    cre = bf2f(p.c_re[o]); cim = bf2f(p.c_im[o]); ldt = bf2f(p.log_dt[2 * dp + (w >> 2)]);
    SB0();
  }
  __builtin_amdgcn_s_barrier();
  __builtin_amdgcn_raw_buffer_store_b128(ONES, rst, (int)WS_X2B + bid * 7168 + pslot, 0, AUX_SC1);
  __builtin_amdgcn_raw_buffer_store_b128(ONES, rst, (int)WS_G + bid * 7168 + pslot, 0, AUX_SC1);
  SB0();
  const int pg = (bid - 224) * 6 + w;
  const bool packer = CONV ? w < 2 : (w < 6 && pg < 112);
  const PackJob jb = pack_job(p, CONV ? bid * 2 + (w & 1) : (packer ? 448 + pg : 0));
  if (tid < 4) *((LAS unsigned*)(L + G_CNT) + tid) = 0u;
  u64 tag;
  {
    const u64 e = ((u64)__builtin_amdgcn_readfirstlane(ep.y) << 32) | (unsigned)__builtin_amdgcn_readfirstlane(ep.x);
    u64 z = e + 0x9E3779B97F4A7C15ull;
    z = (z ^ (z >> 30)) * 0xBF58476D1CE4E5B9ull;
    z = (z ^ (z >> 27)) * 0x94D049BB133111EBull;
    tag = z ^ (z >> 31);
  }
  const unsigned tag32 = ((unsigned)__builtin_amdgcn_readfirstlane(ep.x) + 1u) * 0x9E3779B1u ^ 0x5bd1e995u;
  const unsigned tlo = (unsigned)tag, thi = (unsigned)(tag >> 32);
  unsigned stv;
#define PUT_STAT() do { if (lane == 0) __builtin_amdgcn_raw_buffer_store_b64(u32x2{stv, tag32}, rs, (int)WS_STG + (bid * 8 + w) * 8, 0, AUX_SC1); } while (0)
  {
    const float msk = lane < 56 ? 1.0f : 0.0f;
    float v[8];
#pragma unroll
    for (int k = 0; k < 4; ++k) { v[2 * k] = bf_lo(xrow[k]); v[2 * k + 1] = bf_hi(xrow[k]); }
    const float mean = wave_sum((((v[0] + v[1]) + (v[2] + v[3])) + ((v[4] + v[5]) + (v[6] + v[7]))) * msk) * (1.0f / D_);
    float qq = 0.f;
#pragma unroll
    for (int k = 0; k < 8; ++k) { const float d = v[k] - mean; qq += d * d; }
    const float sd = ln_sd(wave_sum(qq * msk) * (1.0f / D_));
    stv = pk_bf16(mean, sd);
    if (lane < 56) *(LAS u32x4*)(L + P0_LN + w * LNS + lane * 16) = xrow;
    bar_lds();
#pragma unroll
    for (int u = 0; u < 2; ++u) {
      const int pce = (wc * 2 + u) * 64 + lane;
      const LAS unsigned* a = (const LAS unsigned*)(L + P0_LN + (pce & 3) * (2 * LNS) + (pce >> 2) * 4);
      __builtin_amdgcn_raw_buffer_store_b128(u32x4{a[0], tag32, a[LNS / 4], tag32}, rst, (int)WS_XG + bid * (int)XG_BLK + pce * 16, 0, AUX_SC1);
    }
  }
  asm volatile("" ::"v"(touch));
  STAMP(0, 29);
  const int sto = (int)WS_STG + tid * 32;
  u32x4 ring[RING];
  if (__builtin_expect(CONV, 1)) {
    const float dt = rbf(expf(ldt));
    const float ea = (dt * are) * 1.4426950408889634f, eb = (dt * aim) * 0.15915494309189535f;
    const float bre = dt * b_re, bim = dt * b_im;
    f32x2 wc;
    wc.x = cre * bre - cim * bim;
    wc.y = -(cre * bim + cim * bre);
    asm volatile("" ::"v"(ea), "v"(eb), "v"(wc.x), "v"(wc.y));
    const float mx = rows_max(row16_max(fmaxf(fabsf(wc.x), fabsf(wc.y))));
    int ex = mx > 0.f ? __builtin_amdgcn_frexp_expf(mx) : 0;
    ex = ex < -120 ? -120 : (ex > 120 ? 120 : ex);
    const float scale = __builtin_amdgcn_ldexpf(1.0f, ex), iscale = __builtin_amdgcn_ldexpf(1.0f, -ex);
    wc.x *= iscale; wc.y *= iscale;
    LAS unsigned char* B0 = L + P0_KG + w * (2 * KG_BUF);
    LAS unsigned char* B1 = B0 + KG_BUF;
    const int wo = lane * 4;
    const int ro = r16 * KG_ROW + q * 16;
    f32x2 z8c = cexp_tab(ea, eb, 8.0f);
    z8c.y = -z8c.y;
    const f32x2 z1 = cexp_tab(ea, eb, 1.0f), z128 = cexp_tab(ea, eb, 128.0f), z1024 = cexp_tab(ea, eb, 1024.0f);
    {
      const f32x2 z16c = cmul(z8c, z8c);
      f32x2 t0 = wc, t1 = cmul(wc, z8c);
#pragma unroll
      for (int c = 0; c < 16; c += 2) {
        *(LAS unsigned*)(B0 + c * KG_ROW + wo) = pk_f16(t0);
        *(LAS unsigned*)(B0 + (c + 1) * KG_ROW + wo) = pk_f16(t1);
        if (c < 14) { t0 = cmul(t0, z16c); t1 = cmul(t1, z16c); }
      }
    }
    SB0();
    asm volatile("s_waitcnt vmcnt(2)" ::: "memory");
    PUT_STAT();
    SB0();
    f32x2 ze[8];
    ze[0] = f32x2{1.0f, 0.0f}; ze[1] = z1; ze[2] = cmul(z1, z1); ze[3] = cmul(ze[2], z1); ze[4] = cmul(ze[2], ze[2]);
    ze[5] = cmul(ze[4], z1); ze[6] = cmul(ze[4], ze[2]); ze[7] = cmul(ze[4], ze[3]);
#pragma unroll
    for (int e = 0; e < 8; ++e) {
      *(LAS unsigned*)(B1 + e * KG_ROW + wo) = pk_f16(ze[e]);
      *(LAS unsigned*)(B1 + (8 + e) * KG_ROW + wo) = pk_f16(e == 0 ? z128 : cmul(z128, ze[e]));
    }
    lds_fence();
    u32x4 tf[4];
#pragma unroll
    for (int ks = 0; ks < 4; ++ks) tf[ks] = *(const LAS u32x4*)(B0 + ro + ks * 64);
    u32x4 rf[4];
#pragma unroll
    for (int ks = 0; ks < 4; ++ks) rf[ks] = *(const LAS u32x4*)(B1 + ro + ks * 64);
    lds_fence();
    SB0();
    if (packer) pack_load(jb, lane, pk);
    SB0();
    STAMP(0, 30);
    const f32x2 p2 = cmul(z128, z128), p3 = cmul(p2, z128), p4 = cmul(p2, p2), p5 = cmul(p4, z128), p6 = cmul(p4, p2), p7 = cmul(p4, p3);
    {
#pragma unroll
      for (int e = 0; e < 4; ++e) {
        *(LAS unsigned*)(B0 + e * KG_ROW + wo) = pk_f16(e == 0 ? p2 : cmul(p2, ze[2 * e]));
        *(LAS unsigned*)(B0 + (4 + e) * KG_ROW + wo) = pk_f16(e == 0 ? p3 : cmul(p3, ze[2 * e]));
      }
      *(LAS unsigned*)(B0 + 8 * KG_ROW + wo) = pk_f16(p4);
      *(LAS unsigned*)(B0 + 9 * KG_ROW + wo) = pk_f16(cmul(p4, ze[4]));
      *(LAS unsigned*)(B0 + 10 * KG_ROW + wo) = pk_f16(p5);
      *(LAS unsigned*)(B0 + 11 * KG_ROW + wo) = pk_f16(cmul(p5, ze[4]));
      *(LAS unsigned*)(B0 + 12 * KG_ROW + wo) = pk_f16(p6);
      *(LAS unsigned*)(B0 + 13 * KG_ROW + wo) = pk_f16(cmul(p6, ze[4]));
      *(LAS unsigned*)(B0 + 14 * KG_ROW + wo) = pk_f16(p7);
      *(LAS unsigned*)(B0 + 15 * KG_ROW + wo) = pk_f16(cmul(p7, ze[4]));
    }
    f32x4 acc0 = {0.f, 0.f, 0.f, 0.f}, acc1 = acc0, acc2 = acc0;
#pragma unroll
    for (int ks = 0; ks < 4; ++ks) acc0 = mfma16h(rf[ks], tf[ks], acc0);
    {
      *(LAS unsigned*)(B1 + 0 * KG_ROW + wo) = pk_f16(z1024);
      *(LAS unsigned*)(B1 + 1 * KG_ROW + wo) = pk_f16(cmul(z1024, z128));
      *(LAS unsigned*)(B1 + 2 * KG_ROW + wo) = pk_f16(cmul(z1024, p2));
      *(LAS unsigned*)(B1 + 3 * KG_ROW + wo) = pk_f16(cmul(z1024, p3));
      *(LAS unsigned*)(B1 + 4 * KG_ROW + wo) = pk_f16(cmul(z1024, p4));
      *(LAS unsigned*)(B1 + 5 * KG_ROW + wo) = pk_f16(cmul(z1024, p5));
      *(LAS unsigned*)(B1 + 6 * KG_ROW + wo) = pk_f16(cmul(z1024, p6));
      *(LAS unsigned*)(B1 + 7 * KG_ROW + wo) = pk_f16(cmul(z1024, p7));
      *(LAS unsigned*)(B1 + 8 * KG_ROW + wo) = pk_f16(cmul(z1024, z1024));
    }
    lds_fence();
#pragma unroll
    for (int ks = 0; ks < 4; ++ks) acc1 = mfma16h(*(const LAS u32x4*)(B0 + ro + ks * 64), tf[ks], acc1);
#pragma unroll
    for (int ks = 0; ks < 4; ++ks) acc2 = mfma16h(*(const LAS u32x4*)(B1 + ro + ks * 64), tf[ks], acc2);
    lds_fence();
    {
      LAS float* red = (LAS float*)B0;
      acc0 *= scale; acc1 *= scale; acc2 *= scale;
      *(LAS f32x4*)(red + (q >> 1) * 128 + 8 * r16 + (q & 1) * 4) = acc0;
      const int i1 = q < 2 ? 256 + q * 64 + 4 * r16 : 384 + (q - 2) * 64 + 2 * r16, d1 = q < 2 ? 2 : 32;
      *(LAS f32x2*)(red + i1) = f32x2{acc1[0], acc1[1]};
      *(LAS f32x2*)(red + i1 + d1) = f32x2{acc1[2], acc1[3]};
      LAS float* r2 = red + 512 + q * 64 + r16;
      r2[0] = acc2[0]; r2[16] = acc2[1]; r2[32] = acc2[2]; r2[48] = acc2[3];
    }
    STAMP(0, 21);
    bar_lds();
    const int mgo = (int)WS_MODG + ((lane < 3 ? lane : 0) * 224 + dp) * 8;
    SB0();
    u32x4 sg0 = __builtin_amdgcn_raw_buffer_load_b128(rs, sto, 0, AUX_SC1), sg1 = __builtin_amdgcn_raw_buffer_load_b128(rs, sto + 16, 0, AUX_SC1);
    const int xgo = (int)WS_XG + (tid >> 1) * (int)XG_BLK + dp * 64 + (tid & 1) * 32;
    u32x4 xg0 = __builtin_amdgcn_raw_buffer_load_b128(rs, xgo, 0, AUX_SC1), xg1 = __builtin_amdgcn_raw_buffer_load_b128(rs, xgo + 16, 0, AUX_SC1);
    const unsigned dsk2 = *(const unsigned*)(p.dskip + 2 * dp);
    SB0();
    const int kc = tid >> 8, kt = tid & 255;
    LAS float* kd = (LAS float*)(L + P0_KG + kc * (8 * KG_BUF) + KG_BUF);
#pragma unroll
    for (int i = 0; i < 3; ++i) {
      const int oo = kt + 256 * i;
      if (i < 2 || oo < 648) {
        const LAS float* s = (const LAS float*)(L + P0_KG + kc * (8 * KG_BUF)) + oo;
        kd[oo] = (s[0] + s[2 * KG_BUF / 4]) + (s[4 * KG_BUF / 4] + s[6 * KG_BUF / 4]);
      }
    }
    if (kt == 0) kd[648] = 0.f;
    const unsigned OFFS = 0x087EDA52u;
    {
      int off = CV_KCOPY + kt * 16;
      if (kt >= 160) { const int k = kt - 160, s = k >> 3, ch = (k & 7) == 7 ? 6 : (k & 7); off = (272 * (s & 7) + ((OFFS >> (4 * (s & 7))) & 15) + 257 + ch) * 16; }
      *(LAS u32x4*)(L + kc * CV_CH + off) = u32x4{0u, 0u, 0u, 0u};
    }
    bar_lds();
    LAS unsigned char* base = L + kc * CV_CH;
    u32x4 k0;
    {
      float v[8];
#pragma unroll
      for (int i = 0; i < 8; i += 2) {
        const int l0 = 2048 - (kt * 8 + i), l1 = l0 - 1;
        const unsigned bits = pk_bf16((float)l0, (float)l1);
        int i0 = l0 < 128 ? l0 : (int)(bits & 0xffffu) - 17024;
        const int i1 = l1 < 128 ? l1 : (int)(bits >> 16) - 17024;
        if (i == 0) i0 = kt == 0 ? 648 : i0;
        v[i] = kd[i0];
        v[i + 1] = kd[i1];
      }
      k0 = u32x4{pk_bf16(v[0], v[1]), pk_bf16(v[2], v[3]), pk_bf16(v[4], v[5]), pk_bf16(v[6], v[7])};
      *(LAS u32x4*)(base + (2 + kt) * 16) = k0;
      if (kt < 8) {
        const unsigned k00 = pk_bf16(kd[0], 0.f);
        *(LAS u32x4*)(base + (272 * kt + ((OFFS >> (4 * kt)) & 15) + 256) * 16) = u32x4{kt == 0 ? k00 : 0u, 0u, 0u, 0u};
      }
    }
    bar_lds();
    {
      const u32x4 k1 = *(const LAS u32x4*)(base + (2 + kt + 1) * 16);
      unsigned wv[8] = {k0.x, k0.y, k0.z, k0.w, k1.x, k1.y, k1.z, k1.w};
      unsigned vv[7];
#pragma unroll
      for (int i = 0; i < 7; ++i) vv[i] = __builtin_amdgcn_alignbit(wv[i + 1], wv[i], 16);
#pragma unroll
      for (int s = 1; s < 8; ++s) {
        const int k = s >> 1;
        u32x4 o = (s & 1) ? u32x4{vv[k], vv[k + 1], vv[k + 2], vv[k + 3]} : u32x4{wv[k], wv[k + 1], wv[k + 2], wv[k + 3]};
        *(LAS u32x4*)(base + (272 * s + ((OFFS >> (4 * s)) & 15) + kt) * 16) = o;
      }
    }
    float cum0[4], cum1[4];
    {
      const LAS float* kd0 = (const LAS float*)(L + P0_KG + KG_BUF);
      const LAS float* kd1 = (const LAS float*)(L + P0_KG + 8 * KG_BUF + KG_BUF);
#pragma unroll
      for (int i = 0; i < 4; i += 2) {
        const int l0 = 4 * tid + i;
        const unsigned bits = pk_bf16((float)l0, (float)(l0 + 1));
        const int i0 = l0 < 128 ? l0 : (int)(bits & 0xffffu) - 17024, i1 = l0 < 128 ? l0 + 1 : (int)(bits >> 16) - 17024;
        cum0[i] = kd0[i0]; cum0[i + 1] = kd0[i1]; cum1[i] = kd1[i0]; cum1[i + 1] = kd1[i1];
      }
#pragma unroll
      for (int i = 1; i < 4; ++i) { cum0[i] += cum0[i - 1]; cum1[i] += cum1[i - 1]; }
      const float inc0 = wave_scan(cum0[3]), inc1 = wave_scan(cum1[3]);
      const float e0 = inc0 - cum0[3], e1 = inc1 - cum1[3];
#pragma unroll
      for (int i = 0; i < 4; ++i) { cum0[i] += e0; cum1[i] += e1; }
      if (lane == 63) *(LAS f32x2*)(L + P0_MS + w * 8) = f32x2{inc0, inc1};
    }
#define GRAN_BAD() __any(((((sg0.y ^ tag32) | (sg0.w ^ tag32)) | ((sg1.y ^ tag32) | (sg1.w ^ tag32))) | (((xg0.y ^ tag32) | (xg0.w ^ tag32)) | ((xg1.y ^ tag32) | (xg1.w ^ tag32)))) != 0u)
    if (__builtin_expect(GRAN_BAD(), 0)) {
      for (unsigned spins = 0; spins < SPIN_CAP; ++spins) {
        __builtin_amdgcn_s_sleep(2);
        asm volatile("" ::: "memory");
        sg0 = __builtin_amdgcn_raw_buffer_load_b128(rs, sto, 0, AUX_SC1); sg1 = __builtin_amdgcn_raw_buffer_load_b128(rs, sto + 16, 0, AUX_SC1);
        xg0 = __builtin_amdgcn_raw_buffer_load_b128(rs, xgo, 0, AUX_SC1); xg1 = __builtin_amdgcn_raw_buffer_load_b128(rs, xgo + 16, 0, AUX_SC1);
        if (!GRAN_BAD()) break;
      }
    }
#undef GRAN_BAD
    const unsigned xq[4] = {xg0.x, xg0.z, xg1.x, xg1.z};
    STAMP(0, 22);
    unsigned hq[4];
    {
      const unsigned sv[4] = {sg0.x, sg0.z, sg1.x, sg1.z};
#pragma unroll
      for (int k = 0; k < 4; ++k) {
        const float m = bf_lo(sv[k]), r = __builtin_amdgcn_rcpf(bf_hi(sv[k]));
        unsigned t = pk_bf16(bf_lo(xq[k]) - m, bf_hi(xq[k]) - m);
        hq[k] = pk_bf16(bf_lo(t) * r, bf_hi(t) * r);
      }
      LAS unsigned char* U = L + CV_KCOPY + (16 + (tid >> 4)) * CV_UROW + (tid & 15) * 8;
      *(LAS u32x2*)U = u32x2{__builtin_amdgcn_perm(hq[1], hq[0], 0x05040100u), __builtin_amdgcn_perm(hq[3], hq[2], 0x05040100u)};
      *(LAS u32x2*)(U + CV_CH) = u32x2{__builtin_amdgcn_perm(hq[1], hq[0], 0x07060302u), __builtin_amdgcn_perm(hq[3], hq[2], 0x07060302u)};
    }
    if (packer) pack_shuffle(lane, pk, L + P0_KG + (w + 1) * (2 * KG_BUF));
    STAMP(0, 23);
    bar_lds();
    if (bid == 0 && tid == 0) {
      const u64 e = ((u64)ep.y << 32) | ep.x;
      __hip_atomic_store((GAS u64*)(p.ws + WS_EPOCH), e + 1ull, __ATOMIC_RELAXED, __HIP_MEMORY_SCOPE_AGENT);
    }
    u32x2 fv = flag_load(rs, mgo);
    const __amdgpu_buffer_rsrc_t rpk = __builtin_amdgcn_make_buffer_rsrc((void*)p.ws, 0, packer ? (int)WS_END : 0, 0x00020000);
    SB0();
  {
    const int ch = w >> 2, wq = w & 3;
    f32x4 acc[2][4];
#pragma unroll
    for (int n = 0; n < 2; ++n)
#pragma unroll
      for (int m = 0; m < 4; ++m) acc[n][m] = f32x4{0.f, 0.f, 0.f, 0.f};
    const int s = (8 - r16) & 7, u = (r16 + 7) >> 3;
    const LAS unsigned char* ka = L + ch * CV_CH + (272 * s + ((OFFS >> (4 * s)) & 15) + 256 + q - u - 2 * (16 * wq) - 2 * 79) * 16;
    const LAS unsigned char* ub = L + ch * CV_CH + CV_KCOPY + (16 + r16 - 4 * wq - 3) * CV_UROW + q * 16;
#define KFRAG(f) (*(const LAS u32x4*)(ka + (79 - (f)) * 32))
    u32x4 alo[18], ahi[18], bf[4][4];
#define CSTAGE(dl)                                                                                      \
  {                                                                                                     \
    _Pragma("unroll") for (int ks = 0; ks < 2; ++ks) {                                                  \
      bf[dl][2 * ks] = *(const LAS u32x4*)(ub + (3 - (dl)) * CV_UROW + ks * 64);                        \
      bf[dl][2 * ks + 1] = *(const LAS u32x4*)(ub + (19 - (dl)) * CV_UROW + ks * 64);                   \
    }                                                                                                   \
    _Pragma("unroll") for (int i = 0; i < 4; ++i) {                                                     \
      alo[2 + 4 * (dl) + i] = KFRAG(4 * (dl) + i);                                                      \
      ahi[2 + 4 * (dl) + i] = KFRAG(4 * (dl) + i + 64);                                                 \
    }                                                                                                   \
  }
    CSTAGE(0)
    alo[0] = KFRAG(-2); alo[1] = KFRAG(-1); ahi[0] = KFRAG(62); ahi[1] = KFRAG(63);
    SB0();
#pragma unroll
    for (int m = 0; m < 7; ++m) pack_store1(jb, lane, pk, rpk, m);
    FILL_X1(); FILL_P(0); FILL_P(1); FILL_P(2);
    SB0();
#pragma unroll
    for (int dl = 0; dl < 4; ++dl) {
      if (dl < 3) CSTAGE(dl + 1)
#pragma unroll
      for (int ks = 0; ks < 2; ++ks) {
#pragma unroll
        for (int m = 0; m < 4; ++m) {
          acc[0][m] = mfma16(alo[4 * dl + m - 2 * ks + 2], bf[dl][2 * ks], acc[0][m]);
          acc[1][m] = mfma16(alo[4 * dl + m - 2 * ks + 2], bf[dl][2 * ks + 1], acc[1][m]);
          acc[1][m] = mfma16(ahi[4 * dl + m - 2 * ks + 2], bf[dl][2 * ks], acc[1][m]);
        }
      }
      if (dl < 3) {
#pragma unroll
        for (int i = 0; i < 12; ++i) {
          __builtin_amdgcn_sched_group_barrier(0x100, 1, 0);
          __builtin_amdgcn_sched_group_barrier(0x8, 2, 0);
        }
      }
      SB0();
    }
#undef CSTAGE
    STAMP(0, 28);
#undef KFRAG

    LAS unsigned char* P = L + CV_P + w * (32 * CV_PROW);
#pragma unroll
    for (int n = 0; n < 2; ++n)
#pragma unroll
      for (int m = 0; m < 4; ++m) *(LAS f32x4*)(P + (16 * n + r16) * CV_PROW + (16 * m + 4 * q) * 4) = acc[n][m];
    }
    fv = flag_wait(rs, mgo, fv, 0u, tag32, 0u);
    const unsigned sa2 = __builtin_amdgcn_readlane(fv.x, 0), ca2 = __builtin_amdgcn_readlane(fv.x, 1), ga2 = __builtin_amdgcn_readlane(fv.x, 2);
    unsigned hd[4];
    {
      const f32x2 t2 = *(const LAS f32x2*)(L + P0_MS + (lane & 7) * 8);
      const float mk = (lane & 15) < w ? 1.0f : 0.0f;
      const float b0 = row16_sum((lane & 8) ? 0.f : t2.x * mk), b1 = row16_sum((lane & 8) ? 0.f : t2.y * mk);
#pragma unroll
      for (int k = 0; k < 4; ++k) {
        unsigned h = pk_bf16(bf_lo(hq[k]) * bf_lo(ca2), bf_hi(hq[k]) * bf_hi(ca2));
        h = pk_bf16(bf_lo(h) + bf_lo(sa2), bf_hi(h) + bf_hi(sa2));
        hd[k] = pk_bf16(bf_lo(h) * bf_lo(dsk2), bf_hi(h) * bf_hi(dsk2));
        cum0[k] = (cum0[k] + b0) * bf_lo(sa2);
        cum1[k] = (cum1[k] + b1) * bf_hi(sa2);
      }
    }
    asm volatile("" : "+v"(hd[0]), "+v"(hd[1]), "+v"(hd[2]), "+v"(hd[3]), "+v"(cum0[0]), "+v"(cum0[1]), "+v"(cum0[2]), "+v"(cum0[3]),
                 "+v"(cum1[0]), "+v"(cum1[1]), "+v"(cum1[2]), "+v"(cum1[3]));
    asm volatile("s_waitcnt vmcnt(4)" ::: "memory");
    bar_lds();
    STAMP(0, 24);
    {
      const LAS unsigned char* P = L + CV_P + (tid >> 4) * CV_PROW + (tid & 15) * 16;
      f32x4 pa[8];
#pragma unroll
      for (int k = 0; k < 8; ++k) pa[k] = *(const LAS f32x4*)(P + k * (32 * CV_PROW));
      const f32x4 y0 = (pa[0] + pa[1]) + (pa[2] + pa[3]), y1 = (pa[4] + pa[5]) + (pa[6] + pa[7]);
      u32x4 out;
#pragma unroll
      for (int k = 0; k < 4; ++k) {
        const unsigned yy = pk_bf16(y0[k] * bf_lo(ca2) + cum0[k], y1[k] * bf_hi(ca2) + cum1[k]);
        unsigned t = pk_bf16(bf_lo(yy) + bf_lo(hd[k]), bf_hi(yy) + bf_hi(hd[k]));
        t = pk_bf16(bf_lo(ga2) * bf_lo(t), bf_hi(ga2) * bf_hi(t));
        out[k] = sane(pk_bf16(bf_lo(xq[k]) + bf_lo(t), bf_hi(xq[k]) + bf_hi(t)));
      }
      ST16(WS_X2B + (tid >> 3) * (int)X2B_BLK + dp * 128 + (tid & 7) * 16, out);
    }
  } else {
    const int c = bid - 224;
    LAS float* sm = (LAS float*)(L + P0_KG);
    SB0();
    mod_load(p, c, tid, mr, mc);
    SB0();
    asm volatile("s_waitcnt vmcnt(16)" ::: "memory");
    PUT_STAT();
    mod_finish(c, tag32, tid, mr, mc, sm, rs);
    STAMP(0, 21);
    if (c >= 22) {
      mod_load(p, 10 + c, tid, mr, mc);
      bar_lds();
      mod_finish(10 + c, tag32, tid, mr, mc, sm, rs);
    }
    if (packer) {
      pack_load(jb, lane, pk);
      pack_shuffle(lane, pk, L + P0_PK + w * 8192);
#pragma unroll
      for (int m = 0; m < 7; ++m) pack_store1(jb, lane, pk, rs, m);
    }
    arrive_flag<0>(L, 2, p.ws + WS_FLAGC + c * 8, ~tag, lane);
    STAMP(0, 30);
    FILL_X1(); FILL_P(0); FILL_P(1); FILL_P(2);
    {
      u32x4 sg0 = __builtin_amdgcn_raw_buffer_load_b128(rs, sto, 0, AUX_SC1), sg1 = __builtin_amdgcn_raw_buffer_load_b128(rs, sto + 16, 0, AUX_SC1);
      stat_wait(rs, sto, sg0, sg1, tag32);
    }
    bar_lds();
  }
  STAMP(0, 25);
  if (__builtin_expect(!cw, 0)) {
    {
      const int mo = (int)WS_MODG + 672 * 8 + l56 * 64;
      u32x4 g0 = __builtin_amdgcn_raw_buffer_load_b128(rs, mo, 0, AUX_SC1), g1 = __builtin_amdgcn_raw_buffer_load_b128(rs, mo + 16, 0, AUX_SC1);
      u32x4 g2 = __builtin_amdgcn_raw_buffer_load_b128(rs, mo + 32, 0, AUX_SC1), g3 = __builtin_amdgcn_raw_buffer_load_b128(rs, mo + 48, 0, AUX_SC1);
      SB0();
      gather_x2<X2_DELAY>(p, L, rs, pn, tid);
      stat_wait(rs, mo, g0, g1, tag32);
      stat_wait(rs, mo + 32, g2, g3, tag32);
      LAS unsigned char* mp = L + G_MOD + l56 * 64;
      *(LAS f32x4*)mp = f32x4{bf_lo(g0.x), bf_hi(g0.x), bf_lo(g0.z), bf_hi(g0.z)};
      *(LAS f32x4*)(mp + 16) = f32x4{bf_lo(g1.x), bf_hi(g1.x), bf_lo(g1.z), bf_hi(g1.z)};
      *(LAS f32x4*)(mp + 32) = f32x4{bf_lo(g2.x), bf_hi(g2.x), bf_lo(g2.z), bf_hi(g2.z)};
      *(LAS f32x4*)(mp + 48) = f32x4{bf_lo(g3.x), bf_hi(g3.x), bf_lo(g3.z), bf_hi(g3.z)};
    }
    STAMP(0, 1);
    bar_lds();
    const int fo = (int)WS_FLAGC + (lane & 31) * 8;
    const u32x2 fc = flag_load(rs, fo);
    ln_phase_a(L, j, w, r16, q, [](auto) {});
    STAMP(0, 2);
    bar_lds();
    flag_wait(rs, fo, fc, ~tlo, ~thi, ~0u);
    const unsigned char* src = p.ws + WS_WP + (size_t)(j * 7) * NFRAG * 1024 + NRING * 1024 + voff;
#define DMA_HALF(h)                                                                                                         \
  _Pragma("unroll") for (int ww = 0; ww < 7; ++ww) _Pragma("unroll") for (int i = (h) * (NLDS / 2); i < ((h) + 1) * (NLDS / 2); ++i) \
      __builtin_amdgcn_global_load_lds((const GAS unsigned*)(src + (size_t)ww * NFRAG * 1024 + i * 1024),                     \
                                       (LAS unsigned*)(L + G_WL + (ww * NLDS + i) * 1024), 16, 0, AUX_SC1);
    DMA_HALF(0)
    bar_lds();
    STAMP(0, 4);
    bar_lds();
    DMA_HALF(1)
#undef DMA_HALF
    bar_lds();
    {
      int gof[3];
      u32x4 xr[3];
      x1_offsets(pn, j, w, r16, q, gof);
      poll_load<3>(rs, gof, xr);
      ln_phase_c(L, xr, j, w, r16, q);
    }
    asm volatile("s_waitcnt vmcnt(0)" ::: "memory");
    STAMP(0, 7);
    bar_lds();
    return;
  }
  __builtin_amdgcn_s_setprio(1);
  SB0();
  gather_x2<X2_DELAY>(p, L, rs, pn, tid);
  STAMP(0, 1);
  bar_lds();
  STAMP(0, 27);
  u32x2 h2r[2], x2r[2], x1r[2];
  const int ncol = j * 112 + w * 16 + q * 4;
  const int gbo = (int)WS_MODG + (5 * 224 + (ncol >> 1)) * 8;
  u32x4 gbg = __builtin_amdgcn_raw_buffer_load_b128(rs, gbo, 0, AUX_SC1);
#pragma unroll
  for (int T = 0; T < 2; ++T) {
    const int row = 16 * T + r16;
    x2r[T] = *(const LAS u32x2*)(L + G_X + row * 1024 + (((ncol >> 3) ^ (row >> 2)) * 16) + (ncol & 4) * 2);
  }
  SB0();
  ring_fill<0, 8>(ring, rw, voff);
  ln_phase_a(L, j, w, r16, q, [&](auto k) { SB0(); ring_fill<8 + 6 * decltype(k)::value, 6>(ring, rw, voff); });
  SB0();
  ring_fill<26, 6>(ring, rw, voff);
  STAMP(0, 2);
  bar_lds();
  u32x2 gbp;
  {
    u32x4 dup = gbg;
    if (__builtin_expect(__any(((gbg.y ^ tag32) | (gbg.w ^ tag32)) != 0u), 0)) {
      for (unsigned spins = 0; spins < SPIN_CAP; ++spins) {
        asm volatile("" ::: "memory");
        dup = __builtin_amdgcn_raw_buffer_load_b128(rs, gbo, 0, AUX_SC1);
        if (!__any(((dup.y ^ tag32) | (dup.w ^ tag32)) != 0u)) break;
      }
    }
    gbp = u32x2{dup.x, dup.z};
  }
#pragma unroll
  for (int T = 0; T < 2; ++T) h2r[T] = *(const LAS u32x2*)(L + G_HQ + (16 * T + r16) * 240 + (w * 16 + q * 4) * 2);
  const LAS unsigned char* ap0 = L + G_T0 + r16 * AS + q * 16;
  const LAS unsigned char* ap1 = L + G_T1 + r16 * AS + q * 16;
  const int tok = (q & 1) * 16 + r16, octc = 2 * w + (q >> 1);
  {
    f32x4 acc[1][2];
    STAMP(0, 16);
    gemm14<1, 0, 32, 7, 2, 5>(ap0, ring, rw, voff, acc);
    STAMP(0, 17);
    const u32x4 own = make_piece(gelu4(acc[0][0]), gelu4(acc[0][1]));
    __builtin_amdgcn_raw_buffer_store_b128(own, rs, (int)WS_G + (pn * 4 + j) * 7168 + pslot, 0, AUX_SC1);
    SB0();
    STAMP(0, 18);
    ring_fill<39, 4>(ring, rw, voff);
    int goff[4], loff[4];
    bool wrap0;
#pragma unroll
    for (int i = 0; i < 4; ++i) {
      const int jq = (j + i) & 3;
      const int sh = (jq & 1) ? -1 - (jq >> 1) : 1 + (jq >> 1);
      const int it = tok + sh;
      const int sp = pn + (it >= 32 ? -1 : (it < 0 ? 1 : 0));
      if (i == 0) wrap0 = sp != pn;
      goff[i] = (sp < 0 || sp > 63 || (i == 0 && sp == pn)) ? (int)WS_END : (int)WS_G + (sp * 4 + jq) * 7168 + pslot;
      loff[i] = G_T1 + (it & 31) * AS + (jq * 14 + octc) * 16;
    }
    STAMP(0, 3);
    bar_lds();
    u32x4 v[4];
    poll_issue<4>(rs, goff, v);
    __builtin_amdgcn_s_sleep(3);
    ring_fill<43, 5>(ring, rw, voff);
    poll_finish<4>(rs, goff, v);
    *(LAS u32x4*)(L + loff[0]) = wrap0 ? v[0] : own;
#pragma unroll
    for (int i = 1; i < 4; ++i) *(LAS u32x4*)(L + loff[i]) = v[i];
  }
  STAMP(0, 4);
  bar_lds();
  {
    f32x4 acc[1][2];
    gemm14<1, 14, 48, 7, 2, 5>(ap1, ring, rw, voff, acc);
#pragma unroll
    for (int T = 0; T < 2; ++T) {
      const unsigned a = pk_bf16(acc[0][T][0], acc[0][T][1]), b = pk_bf16(acc[0][T][2], acc[0][T][3]);
      x1r[T].x = pk_bf16(bf_lo(h2r[T].x) + bf_lo(a), bf_hi(h2r[T].x) + bf_hi(a));
      x1r[T].y = pk_bf16(bf_lo(h2r[T].y) + bf_lo(b), bf_hi(h2r[T].y) + bf_hi(b));
    }
    const u32x4 own = make_piece(x1r[0], x1r[1]);
    __builtin_amdgcn_raw_buffer_store_b128(own, rs, (int)WS_X1 + (pn * 4 + j) * 7168 + (w * 64 + (tok >> 2) * 8 + (q >> 1) * 4 + (tok & 3)) * 16, 0, AUX_SC1);
    *(LAS u32x4*)(L + G_HQ + tok * 240 + octc * 16) = own;
    SB0();
    STAMP(0, 19);
    ring_fill<55, 4>(ring, rw, voff);
    int gof[3];
    u32x4 xr[3];
    x1_offsets(pn, j, w, r16, q, gof);
    STAMP(0, 5);
    bar_lds();
    poll_issue<3>(rs, gof, xr);
    __builtin_amdgcn_s_sleep(3);
    ring_fill<59, 5>(ring, rw, voff);
    poll_finish<3>(rs, gof, xr);
    STAMP(0, 6);
    ring_fill<64, 4>(ring, rw, voff);
    ln_phase_c(L, xr, j, w, r16, q);
    SB0();
    ring_fill<68, 4>(ring, rw, voff);
  }
  STAMP(0, 7);
  bar_lds();
  {
    {
      f32x4 ca[2][2], cb2[2][2], acc[3][2];
      LAS unsigned char* hp = L + G_T1 + r16 * AS + (w * 64 + q * 4) * 2;
      unsigned glo;
      STAMP(0, 12);
      gemm_p<2, 28, 0, 1, 14, 72, 19, 3, true>(ap0, ring, rw, voff, ca, [](auto) {});
      gemm_p<2, 56, 0, 1, 14, 91, 19, 3, true>(ap0, ring, rw, voff, cb2, [&](auto stc) {
        constexpr int st = decltype(stc)::value;
        if constexpr (st % 3 == 1 && st / 3 < 8) {
          constexpr int c = st / 3, i = c >> 2, T = (c >> 1) & 1;
          if constexpr ((c & 1) == 0) glo = gelu2(ca[i][T][0], ca[i][T][1]);
          else *(LAS u32x2*)(hp + T * 16 * AS + i * 32) = u32x2{glo, gelu2(ca[i][T][2], ca[i][T][3])};
        }
      });
      STAMP(0, 13);
      bar_lds();
      gemm_p<3, 84, 0, 2, 7, 110, 18, 3, true>(ap1, ring, rw, voff, acc, [&](auto stc) {
        constexpr int st = decltype(stc)::value;
        if constexpr (st % 2 == 1 && st / 2 < 8) {
          constexpr int c = st / 2, i = c >> 2, T = (c >> 1) & 1;
          if constexpr ((c & 1) == 0) glo = gelu2(cb2[i][T][0], cb2[i][T][1]);
          else *(LAS u32x2*)(hp + T * 16 * AS + (2 + i) * 32) = u32x2{glo, gelu2(cb2[i][T][2], cb2[i][T][3])};
        }
      });
      STAMP(0, 8);
      bar_lds();
      STAMP(0, 14);
      gemm_p<3, 105, 1, 2, 7, 0, 0, 3, false>(ap1, ring, rw, voff, acc, [](auto) {});
      STAMP(0, 15);
#pragma unroll
      for (int i = 0; i < 3; ++i) {
        const int jd = (j + 1 + i) & 3;
        const u32x4 o = {sane(pk_bf16(acc[i][0][0], acc[i][0][1])), sane(pk_bf16(acc[i][0][2], acc[i][0][3])),
                         sane(pk_bf16(acc[i][1][0], acc[i][1][1])), sane(pk_bf16(acc[i][1][2], acc[i][1][3]))};
        __builtin_amdgcn_raw_buffer_store_b128(o, rs, (int)WS_P + ((pn * 4 + jd) * 3 + (2 - i)) * 7168 + pslot, 0, AUX_SC1);
      }
      SB0();
    }
    STAMP(0, 9);
    f32x4 acc0 = {0.f, 0.f, 0.f, 0.f}, acc1 = acc0;
    u32x4 v[3];
    const int go = (int)WS_P + (pn * 4 + j) * 3 * 7168 + pslot;
    const int gof[3] = {go, go + 7168, go + 2 * 7168};
    {
      const LAS unsigned char* wl = L + G_WL + w * (NLDS * 1024) + voff;
      u32x4 wa[14], b0[14], b1[14];
#pragma unroll
      for (int ks = 0; ks < 14; ++ks) {
        const int f = 126 + ks;
        wa[ks] = f < NRING ? ring[f % RING] : *(const LAS u32x4*)(wl + (f - NRING) * 1024);
        b0[ks] = *(const LAS u32x4*)(ap1 + ks * 64);
        b1[ks] = *(const LAS u32x4*)(ap1 + 16 * AS + ks * 64);
      }
      SB0();
#pragma unroll
      for (int ks = 0; ks < 14; ++ks) {
        acc0 = mfma16(wa[ks], b0[ks], acc0);
        acc1 = mfma16(wa[ks], b1[ks], acc1);
      }
      SB0();
    }
    STAMP(0, 10);
    poll_load<3>(rs, gof, v);
    STAMP(0, 11);
#pragma unroll
    for (int T = 0; T < 2; ++T) {
      const f32x4 ac = T ? acc1 : acc0;
      float hh[4];
#pragma unroll
      for (int k = 0; k < 2; ++k) {
        hh[2 * k] = ac[2 * k] + ((bf_lo(v[0][2 * T + k]) + bf_lo(v[1][2 * T + k])) + bf_lo(v[2][2 * T + k]));
        hh[2 * k + 1] = ac[2 * k + 1] + ((bf_hi(v[0][2 * T + k]) + bf_hi(v[1][2 * T + k])) + bf_hi(v[2][2 * T + k]));
      }
      unsigned a = pk_bf16(hh[0], hh[1]), b = pk_bf16(hh[2], hh[3]);
      a = pk_bf16(bf_lo(x1r[T].x) + bf_lo(a), bf_hi(x1r[T].x) + bf_hi(a));
      b = pk_bf16(bf_lo(x1r[T].y) + bf_lo(b), bf_hi(x1r[T].y) + bf_hi(b));
      a = pk_bf16(bf_lo(gbp.x) * bf_lo(a), bf_hi(gbp.x) * bf_hi(a));
      b = pk_bf16(bf_lo(gbp.y) * bf_lo(b), bf_hi(gbp.y) * bf_hi(b));
      u32x2 o;
      o.x = pk_bf16(bf_lo(x2r[T].x) + bf_lo(a), bf_hi(x2r[T].x) + bf_hi(a));
      o.y = pk_bf16(bf_lo(x2r[T].y) + bf_lo(b), bf_hi(x2r[T].y) + bf_hi(b));
      *(u32x2*)(p.out + (size_t)(pn * 32 + 16 * T + r16) * D_ + ncol) = o;
    }
  }
  STAMP(0, 20);
}

__global__ __launch_bounds__(512, 2) void k_all(GArgs p) {
  extern __shared__ __attribute__((aligned(16))) unsigned char smem[];
  const uintptr_t code0 = (uintptr_t)__builtin_amdgcn_s_getpc() & ~(uintptr_t)127;
  body(p, (LAS unsigned char*)smem, code0);
}

}

extern "C" void kernel_launch(void* const* d_in, const int* in_sizes, int n_in, void* d_out, int out_size, void* d_ws,
                              size_t ws_size, hipStream_t stream) {
  (void)in_sizes; (void)n_in; (void)out_size; (void)ws_size;
  GArgs a;
  a.x = (const unsigned short*)d_in[0];
  a.a_re = (const unsigned short*)d_in[1];
  a.a_im = (const unsigned short*)d_in[2];
  a.b_re = (const unsigned short*)d_in[3];
  a.b_im = (const unsigned short*)d_in[4];
  a.c_re = (const unsigned short*)d_in[5];
  a.c_im = (const unsigned short*)d_in[6];
  a.log_dt = (const unsigned short*)d_in[7];
  a.dskip = (const unsigned short*)d_in[8];
  a.w1 = (const unsigned short*)d_in[10];
  a.w2 = (const unsigned short*)d_in[11];
  a.w3 = (const unsigned short*)d_in[12];
  a.w4 = (const unsigned short*)d_in[13];
  a.cvec = (const unsigned short*)d_in[14];
  a.wmod = (const unsigned short*)d_in[15];
  a.ws = (unsigned char*)d_ws;
  a.out = (unsigned short*)d_out;
  (void)hipFuncSetAttribute((const void*)k_all, hipFuncAttributeMaxDynamicSharedMemorySize, G_LDS);
  k_all<<<256, 512, G_LDS, stream>>>(a);
}
```

```cpp
#include <hip/hip_runtime.h>
#include <hip/hip_bf16.h>
#include <stdint.h>
#include <type_traits>
#include <utility>

namespace {

typedef __attribute__((ext_vector_type(8))) short bf16x8;
typedef __attribute__((ext_vector_type(8))) _Float16 f16x8;
typedef __attribute__((ext_vector_type(2))) _Float16 f16x2;
typedef __attribute__((ext_vector_type(4))) float f32x4;
typedef __attribute__((ext_vector_type(2))) float f32x2;
typedef __attribute__((ext_vector_type(4))) unsigned u32x4;
typedef __attribute__((ext_vector_type(2))) unsigned u32x2;
typedef __attribute__((ext_vector_type(2))) __bf16 bf16x2v;
#define GAS __attribute__((address_space(1)))
#define LAS __attribute__((address_space(3)))

constexpr int L_ = 2048, D_ = 448, NS_ = 256;

constexpr size_t WS_EPOCH = 0;
constexpr size_t WS_FLAGC = 4608;
constexpr size_t WS_MODG = 16384;
constexpr size_t WS_STG = 32768;
constexpr int NFRAG = 140;
constexpr size_t WS_XG = 65536;
constexpr size_t XG_BLK = 224 * 64;
constexpr size_t WS_WP = WS_XG + 256 * XG_BLK;
constexpr size_t WS_X2B = WS_WP + (size_t)28 * NFRAG * 1024;
constexpr size_t X2B_BLK = 224 * 128;
constexpr size_t WS_G = WS_X2B + 64 * X2B_BLK;
constexpr size_t WS_X1 = WS_G + (size_t)64 * 4 * 7168;
constexpr size_t WS_P = WS_X1 + (size_t)64 * 4 * 7168;
constexpr size_t WS_END = WS_P + (size_t)64 * 12 * 7168;

#define STAMP(base, k) do { } while (0)

__device__ __forceinline__ float bf_lo(unsigned u) { return __builtin_bit_cast(float, u << 16); }
__device__ __forceinline__ float bf_hi(unsigned u) { return __builtin_bit_cast(float, u & 0xffff0000u); }
__device__ __forceinline__ float bf2f(unsigned short h) { return __builtin_bit_cast(float, (unsigned)h << 16); }
__device__ __forceinline__ unsigned pk_bf16(float a, float b) {
  f32x2 v = {a, b};
  bf16x2v r = __builtin_convertvector(v, bf16x2v);
  return __builtin_bit_cast(unsigned, r);
}
__device__ __forceinline__ unsigned pk_f16(f32x2 v) {
  f16x2 r = __builtin_convertvector(v, f16x2);
  return __builtin_bit_cast(unsigned, r);
}
__device__ __forceinline__ float rbf(float a) { return bf_lo(pk_bf16(a, a)); }
__device__ __forceinline__ f32x4 mfma16(u32x4 a, u32x4 b, f32x4 c) {
  return __builtin_amdgcn_mfma_f32_16x16x32_bf16(__builtin_bit_cast(bf16x8, a), __builtin_bit_cast(bf16x8, b), c, 0, 0, 0);
}
__device__ __forceinline__ f32x4 mfma16h(u32x4 a, u32x4 b, f32x4 c) {
  return __builtin_amdgcn_mfma_f32_16x16x32_f16(__builtin_bit_cast(f16x8, a), __builtin_bit_cast(f16x8, b), c, 0, 0, 0);
}
template <int CTRL>
__device__ __forceinline__ float dpp_f(float v) {
  return __builtin_bit_cast(float, __builtin_amdgcn_update_dpp(0, __builtin_bit_cast(int, v), CTRL, 0xf, 0xf, true));
}
__device__ __forceinline__ float row16_sum(float v) {
  v += dpp_f<0xB1>(v);
  v += dpp_f<0x4E>(v);
  v += dpp_f<0x141>(v);
  v += dpp_f<0x140>(v);
  return v;
}
__device__ __forceinline__ float row16_max(float v) {
  v = fmaxf(v, dpp_f<0xB1>(v));
  v = fmaxf(v, dpp_f<0x4E>(v));
  v = fmaxf(v, dpp_f<0x141>(v));
  v = fmaxf(v, dpp_f<0x140>(v));
  return v;
}
__device__ __forceinline__ void swap16(float v, float& a, float& b) {
  const unsigned u = __builtin_bit_cast(unsigned, v), u2 = u;
  auto r = __builtin_amdgcn_permlane16_swap(u, u2, false, false);
  const unsigned r0 = r[0], r1 = r[1];
  a = __builtin_bit_cast(float, r0); b = __builtin_bit_cast(float, r1);
}
__device__ __forceinline__ void swap32(float v, float& a, float& b) {
  const unsigned u = __builtin_bit_cast(unsigned, v), u2 = u;
  auto r = __builtin_amdgcn_permlane32_swap(u, u2, false, false);
  const unsigned r0 = r[0], r1 = r[1];
  a = __builtin_bit_cast(float, r0); b = __builtin_bit_cast(float, r1);
}
__device__ __forceinline__ float rows_sum(float v) {
  float a, b;
  swap16(v, a, b); v = a + b;
  swap32(v, a, b);
  return a + b;
}
__device__ __forceinline__ float rows_max(float v) {
  float a, b;
  swap16(v, a, b); v = fmaxf(a, b);
  swap32(v, a, b);
  return fmaxf(a, b);
}
template <int CTRL, int RMASK>
__device__ __forceinline__ float dpp_z(float v) {
  return __builtin_bit_cast(float, __builtin_amdgcn_update_dpp(0, __builtin_bit_cast(int, v), CTRL, RMASK, 0xf, false));
}
__device__ __forceinline__ float wave_scan(float v) {
  v += dpp_z<0x111, 0xf>(v);
  v += dpp_z<0x112, 0xf>(v);
  v += dpp_z<0x114, 0xf>(v);
  v += dpp_z<0x118, 0xf>(v);
  v += dpp_z<0x142, 0xa>(v);
  v += dpp_z<0x143, 0xc>(v);
  return v;
}
__device__ __forceinline__ float wave_sum(float v) { return rows_sum(row16_sum(v)); }
__device__ __forceinline__ float gelu_tanh(float x) {
  const float c1 = -2.0f * 0.7978845608028654f * 1.4426950408889634f;
  const float c2 = c1 * 0.044715f;
  float t = x * (c1 + c2 * x * x);
  float e = __builtin_amdgcn_exp2f(t);
  return x * __builtin_amdgcn_rcpf(1.0f + e);
}
__device__ __forceinline__ float ln_sd(float var) { return rbf(__builtin_amdgcn_sqrtf(rbf(rbf(var) + 1e-5f))); }
__device__ __forceinline__ void lds_fence() { asm volatile("" ::: "memory"); }
__device__ __forceinline__ void bar_lds() {
  asm volatile("s_waitcnt lgkmcnt(0)" ::: "memory");
  __builtin_amdgcn_s_barrier();
  asm volatile("" ::: "memory");
}

__device__ __forceinline__ void transpose8x8(const u32x4 (&r)[8], u32x4 (&o)[8]) {
#pragma unroll
  for (int c2 = 0; c2 < 4; ++c2) {
#pragma unroll
    for (int rp = 0; rp < 4; ++rp) {
      unsigned a = r[2 * rp][c2], b = r[2 * rp + 1][c2];
      o[2 * c2][rp] = __builtin_amdgcn_perm(b, a, 0x05040100u);
      o[2 * c2 + 1][rp] = __builtin_amdgcn_perm(b, a, 0x07060302u);
    }
  }
}
__device__ __forceinline__ f32x2 cmul(f32x2 a, f32x2 b) {
  f32x2 r;
  asm("v_pk_mul_f32 %0, %1, %2 op_sel_hi:[0,1]\n\tv_pk_fma_f32 %0, %1, %2, %0 op_sel:[1,1,0] op_sel_hi:[1,0,1] neg_lo:[1,0,0]"
      : "=&v"(r) : "v"(a), "v"(b));
  return r;
}
__device__ __forceinline__ f32x2 cexp_tab(float a, float b, float l) {
  const float e = __builtin_amdgcn_exp2f(a * l), ph = __builtin_amdgcn_fractf(b * l);
  f32x2 r;
  r.x = e * __builtin_amdgcn_cosf(ph);
  r.y = e * __builtin_amdgcn_sinf(ph);
  return r;
}

constexpr int CV_KCOPY = 8 * 272 * 16;
constexpr int CV_UROW = 160;
constexpr int CV_U = 48 * CV_UROW;
constexpr int CV_CH = CV_KCOPY + CV_U;
constexpr int CV_LDS = 2 * CV_CH;
constexpr int CV_PROW = 272;
static_assert(4 * 32 * CV_PROW <= CV_KCOPY, "partials alias the kernel copies");

struct GArgs {
  const unsigned short* x;
  const unsigned short* a_re;
  const unsigned short* a_im;
  const unsigned short* b_re;
  const unsigned short* b_im;
  const unsigned short* c_re;
  const unsigned short* c_im;
  const unsigned short* log_dt;
  const unsigned short* dskip;
  const unsigned short* w1;
  const unsigned short* w2;
  const unsigned short* w3;
  const unsigned short* w4;
  const unsigned short* cvec;
  const unsigned short* wmod;
  unsigned char* ws;
  unsigned short* out;
};
constexpr int AS = 928;
constexpr int G_T0 = 0;
constexpr int G_T1 = G_T0 + 32 * AS;
constexpr int G_X = G_T1;
constexpr int G_HQ = G_X + 32768;
constexpr int G_WL = G_HQ + 32 * 240;
constexpr int NLDS = 12;
constexpr int NRING = NFRAG - NLDS;
constexpr int G_MOD = G_WL + 7 * NLDS * 1024;
constexpr int G_CNT = G_MOD + 2 * 448 * 4;
constexpr int P0_MS = G_CNT + 16;
constexpr int G_LDS = P0_MS + 256;
constexpr int KG_ROW = 288;
constexpr int KG_BUF = 16 * KG_ROW;
constexpr int P0_KG = CV_LDS;
constexpr int P0_LN = 0;
constexpr int LNS = 928;
constexpr int P0_PK = 16384;
static_assert(P0_KG + 16 * KG_BUF <= G_CNT, "tables");
static_assert(G_MOD >= CV_LDS && G_LDS <= 163840, "LDS map");
constexpr int RING = 44;
constexpr int CV_P = CV_LDS;
static_assert(CV_P + 8 * 32 * CV_PROW <= G_MOD, "partials");
constexpr unsigned SPIN_CAP = 1u << 17;
constexpr int AUX_SC1 = 16;
constexpr int CODE_LINES = 320;
#define PRETOUCH 1
#define X2_DELAY 20
#define POLL_SLEEP 1

__device__ __forceinline__ unsigned sane(unsigned v) { return v < 0xFFFFFFFEu ? v : 0xFFFFFFFEu; }
__device__ __forceinline__ unsigned umax4(const u32x4& v) { return max(max(v.x, v.y), max(v.z, v.w)); }
__device__ __forceinline__ bool bad4(const u32x4& v) { return umax4(v) == ~0u; }
__device__ __forceinline__ u32x4 make_piece(u32x2 a, u32x2 b) {
  auto rx = __builtin_amdgcn_permlane16_swap(a.x, b.x, false, false);
  auto ry = __builtin_amdgcn_permlane16_swap(a.y, b.y, false, false);
  return u32x4{sane(rx[0]), sane(ry[0]), sane(rx[1]), sane(ry[1])};
}
template <int N>
__device__ __forceinline__ void poll_issue(__amdgpu_buffer_rsrc_t r, const int (&off)[N], u32x4 (&v)[N]) {
#pragma unroll
  for (int i = 0; i < N; ++i) v[i] = __builtin_amdgcn_raw_buffer_load_b128(r, off[i], 0, AUX_SC1);
  __builtin_amdgcn_sched_barrier(0);
}
template <int N>
__device__ __forceinline__ void poll_finish(__amdgpu_buffer_rsrc_t r, const int (&off)[N], u32x4 (&v)[N]) {
  unsigned m = 0u;
#pragma unroll
  for (int i = 0; i < N; ++i) m = max(m, umax4(v[i]));
  if (__builtin_expect(__any(m == ~0u), 0)) {
    for (unsigned spins = 0; spins < SPIN_CAP; ++spins) {
      __builtin_amdgcn_s_sleep(POLL_SLEEP);
      asm volatile("" ::: "memory");
#pragma unroll
      for (int i = 0; i < N; ++i)
        if (bad4(v[i])) v[i] = __builtin_amdgcn_raw_buffer_load_b128(r, off[i], 0, AUX_SC1);
      m = 0u;
#pragma unroll
      for (int i = 0; i < N; ++i) m = max(m, umax4(v[i]));
      if (!__any(m == ~0u)) break;
    }
  }
}
template <int N>
__device__ __forceinline__ void poll_load(__amdgpu_buffer_rsrc_t r, const int (&off)[N], u32x4 (&v)[N]) {
  poll_issue<N>(r, off, v);
  poll_finish<N>(r, off, v);
}
__device__ __forceinline__ float dot2bf(unsigned a, unsigned b, float c) {
  return __builtin_amdgcn_fdot2_f32_bf16(__builtin_bit_cast(bf16x2v, a), __builtin_bit_cast(bf16x2v, b), c, false);
}
__device__ __forceinline__ void ln_stats(const unsigned (&x)[16], float lmsk, float& r, float& mr) {
  float s0 = 0.f, s1 = 0.f, q0 = 0.f, q1 = 0.f;
#pragma unroll
  for (int k = 0; k < 16; k += 2) {
    s0 = dot2bf(x[k], 0x3f803f80u, s0); q0 = dot2bf(x[k], x[k], q0);
    s1 = dot2bf(x[k + 1], 0x3f803f80u, s1); q1 = dot2bf(x[k + 1], x[k + 1], q1);
  }
  const float mean = row16_sum((s0 + s1) * lmsk) * (1.0f / D_);
  const float msq = row16_sum((q0 + q1) * lmsk) * (1.0f / D_);
  const float var = fmaxf(msq - mean * mean, 0.f);
  r = __builtin_amdgcn_rcpf(ln_sd(var));
  mr = -rbf(mean) * r;
}

template <class F, int... I>
__device__ __forceinline__ void static_for_impl(F f, std::integer_sequence<int, I...>) { (f(std::integral_constant<int, I>{}), ...); }
template <int N, class F>
__device__ __forceinline__ void static_for(F f) { static_for_impl(f, std::make_integer_sequence<int, N>{}); }
#define SB0() __builtin_amdgcn_sched_barrier(0)
#define WLOAD(f) __builtin_amdgcn_raw_buffer_load_b128(rw, voff + ((f) & 3) * 1024, ((f) >> 2) * 4096, AUX_SC1)
template <int NT, int F0, int LSTART, int LNUM, int LEVERY, int PD>
__device__ __forceinline__ void gemm14(const LAS unsigned char* ap, u32x4 (&ring)[RING], __amdgpu_buffer_rsrc_t rw, int voff, f32x4 (&acc)[NT][2]) {
  u32x4 bq[PD][2];
#pragma unroll
  for (int i = 0; i < PD; ++i) { bq[i][0] = *(const LAS u32x4*)(ap + i * 64); bq[i][1] = *(const LAS u32x4*)(ap + 16 * AS + i * 64); }
#pragma unroll
  for (int i = 0; i < NT; ++i) { acc[i][0] = f32x4{0.f, 0.f, 0.f, 0.f}; acc[i][1] = acc[i][0]; }
  SB0();
#pragma unroll
  for (int ks = 0; ks < 14; ++ks) {
#pragma unroll
    for (int i = 0; i < NT; ++i) {
      const int st = ks * NT + i, f = F0 + st;
      acc[i][0] = mfma16(ring[f % RING], bq[ks % PD][0], acc[i][0]);
      acc[i][1] = mfma16(ring[f % RING], bq[ks % PD][1], acc[i][1]);
      if (st % LEVERY == 0 && st / LEVERY < LNUM) {
        const int g = LSTART + st / LEVERY;
        ring[g % RING] = WLOAD(g);
      }
      if (i == NT - 1 && ks + PD < 14) {
        bq[ks % PD][0] = *(const LAS u32x4*)(ap + (ks + PD) * 64);
        bq[ks % PD][1] = *(const LAS u32x4*)(ap + 16 * AS + (ks + PD) * 64);
      }
      SB0();
    }
  }
}
template <int NT, int F0, int KS0, int KSTEP, int NKS, int LSTART, int LNUM, int PD, bool INIT, class S>
__device__ __forceinline__ void gemm_p(const LAS unsigned char* ap, u32x4 (&ring)[RING], __amdgpu_buffer_rsrc_t rw, int voff, f32x4 (&acc)[NT][2], S side) {
  u32x4 bq[PD][2];
#pragma unroll
  for (int i = 0; i < PD; ++i) {
    bq[i][0] = *(const LAS u32x4*)(ap + (KS0 + i * KSTEP) * 64);
    bq[i][1] = *(const LAS u32x4*)(ap + 16 * AS + (KS0 + i * KSTEP) * 64);
  }
  if (INIT) {
#pragma unroll
    for (int i = 0; i < NT; ++i) { acc[i][0] = f32x4{0.f, 0.f, 0.f, 0.f}; acc[i][1] = acc[i][0]; }
  }
  SB0();
  static_for<NKS * NT>([&](auto stc) {
    constexpr int st = decltype(stc)::value, k = st / NT, i = st % NT, f = F0 + st;
    acc[i][0] = mfma16(ring[f % RING], bq[k % PD][0], acc[i][0]);
    acc[i][1] = mfma16(ring[f % RING], bq[k % PD][1], acc[i][1]);
    constexpr int l0 = st * LNUM / (NKS * NT), l1 = (st + 1) * LNUM / (NKS * NT);
    if constexpr (l1 > l0) {
      static_assert(LSTART + l0 - RING <= f, "slot still in use");
      ring[(LSTART + l0) % RING] = WLOAD(LSTART + l0);
    }
    if constexpr (i == NT - 1 && k + PD < NKS) {
      bq[k % PD][0] = *(const LAS u32x4*)(ap + (KS0 + (k + PD) * KSTEP) * 64);
      bq[k % PD][1] = *(const LAS u32x4*)(ap + 16 * AS + (KS0 + (k + PD) * KSTEP) * 64);
    }
    side(stc);
    SB0();
  });
}
template <int F0, int N>
__device__ __forceinline__ void ring_fill(u32x4 (&ring)[RING], __amdgpu_buffer_rsrc_t rw, int voff) {
#pragma unroll
  for (int f = F0; f < F0 + N; ++f) { ring[f % RING] = WLOAD(f); SB0(); }
}

template <int DELAY>
__device__ __forceinline__ void gather_x2(const GArgs& p, LAS unsigned char* L, __amdgpu_buffer_rsrc_t rs, int pn, int tid) {
  const int lane = tid & 63, w = tid >> 6; (void)lane; (void)w; (void)p;
  u32x4 xin[4];
  int xo[4];
#pragma unroll
  for (int i = 0; i < 4; ++i) {
    const int pce = tid + 512 * i;
    xo[i] = pce < 1792 ? (int)WS_X2B + pn * (int)X2B_BLK + pce * 16 : (int)WS_END;
  }
  if (DELAY) __builtin_amdgcn_s_sleep(DELAY);
  poll_issue<4>(rs, xo, xin);
  poll_finish<4>(rs, xo, xin);
  STAMP(0, 26);
#pragma unroll
  for (int i = 0; i < 4; ++i) {
    const int pce = tid + 512 * i;
    if (i < 3 || pce < 1792) {
      const int dpi = pce >> 3, tq = pce & 7;
      LAS unsigned char* dst = L + G_X + tq * 4096 + (((dpi >> 2) ^ tq) * 16) + (dpi & 3) * 4;
#pragma unroll
      for (int k = 0; k < 4; ++k) *(LAS unsigned*)(dst + k * 1024) = xin[i][k];
    }
  }
}
template <class H>
__device__ __forceinline__ void ln_phase_a(LAS unsigned char* L, int j, int w, int r16, int q, H hook) {
  const int c16 = r16 < 14 ? r16 : 0;
  const float lmsk = r16 < 14 ? 1.0f : 0.0f;
  const int row = 4 * w + q;
  unsigned x[16];
#pragma unroll
  for (int jq = 0; jq < 4; ++jq) {
    const u32x4 t = *(const LAS u32x4*)(L + G_X + row * 1024 + (((((jq + j) & 3) * 14 + c16) ^ w) * 16));
    x[4 * jq] = t.x; x[4 * jq + 1] = t.y; x[4 * jq + 2] = t.z; x[4 * jq + 3] = t.w;
  }
  float r, mr;
  ln_stats(x, lmsk, r, mr);
  hook(std::integral_constant<int, 0>{});
  f32x2 h[16];
#pragma unroll
  for (int jq = 0; jq < 4; ++jq) {
    const LAS unsigned char* mp = L + G_MOD + (((jq + j) & 3) * 112 + c16 * 8) * 4;
    const f32x4 sb0 = *(const LAS f32x4*)mp, sb1 = *(const LAS f32x4*)(mp + 16);
    const f32x4 c10 = *(const LAS f32x4*)(mp + 1792), c11 = *(const LAS f32x4*)(mp + 1792 + 16);
#pragma unroll
    for (int k = 0; k < 4; ++k) {
      const f32x2 xv2 = {bf_lo(x[4 * jq + k]), bf_hi(x[4 * jq + k])};
      const f32x2 c2 = k < 2 ? f32x2{c10[2 * k], c10[2 * k + 1]} : f32x2{c11[2 * k - 4], c11[2 * k - 3]};
      const f32x2 sb2 = k < 2 ? f32x2{sb0[2 * k], sb0[2 * k + 1]} : f32x2{sb1[2 * k - 4], sb1[2 * k - 3]};
      const f32x2 n = xv2 * r + mr;
      h[4 * jq + k] = n * c2 + sb2;
      x[4 * jq + k] = pk_bf16(h[4 * jq + k].x, h[4 * jq + k].y);
    }
  }
  *(LAS u32x4*)(L + G_HQ + row * 240 + (r16 < 14 ? r16 : 14) * 16) = u32x4{x[0], x[1], x[2], x[3]};
  hook(std::integral_constant<int, 1>{});
  ln_stats(x, lmsk, r, mr);
  hook(std::integral_constant<int, 2>{});
#pragma unroll
  for (int jq = 0; jq < 4; ++jq) {
    u32x4 o;
#pragma unroll
    for (int k = 0; k < 4; ++k) {
      const f32x2 n = h[4 * jq + k] * r + mr;
      o[k] = pk_bf16(n.x, n.y);
    }
    *(LAS u32x4*)(L + G_T0 + row * AS + (r16 < 14 ? (((jq + j) & 3) * 14 + r16) * 16 : 896)) = o;
  }
}
__device__ __forceinline__ void x1_offsets(int pn, int j, int w, int r16, int q, int (&gof)[3]) {
  const int c16 = r16 < 14 ? r16 : 0;
  const int go = (int)WS_X1 + pn * 4 * 7168 + ((c16 >> 1) * 64 + w * 8 + (c16 & 1) * 4 + q) * 16;
#pragma unroll
  for (int i = 0; i < 3; ++i) gof[i] = go + ((j + 1 + i) & 3) * 7168;
}
__device__ __forceinline__ void ln_phase_c(LAS unsigned char* L, const u32x4 (&xr)[3], int j, int w, int r16, int q) {
  const int c16 = r16 < 14 ? r16 : 0;
  const float lmsk = r16 < 14 ? 1.0f : 0.0f;
  const int row = 4 * w + q;
  const u32x4 own = *(const LAS u32x4*)(L + G_HQ + row * 240 + c16 * 16);
  unsigned x[16];
#pragma unroll
  for (int k = 0; k < 4; ++k) x[k] = own[k];
#pragma unroll
  for (int k = 4; k < 16; ++k) x[k] = xr[(k >> 2) - 1][k & 3];
  float r, mr;
  ln_stats(x, lmsk, r, mr);
#pragma unroll
  for (int jq = 0; jq < 4; ++jq) {
    u32x4 o;
#pragma unroll
    for (int k = 0; k < 4; ++k) {
      const f32x2 n = f32x2{bf_lo(x[4 * jq + k]), bf_hi(x[4 * jq + k])} * r + mr;
      o[k] = pk_bf16(n.x, n.y);
    }
    *(LAS u32x4*)(L + G_T0 + row * AS + (r16 < 14 ? (((jq + j) & 3) * 14 + r16) * 16 : 896)) = o;
  }
}
__device__ __forceinline__ unsigned gelu2(float a, float b) {
  return pk_bf16(gelu_tanh(a), gelu_tanh(b));
}
__device__ __forceinline__ u32x2 gelu4(f32x4 a) { return u32x2{gelu2(a[0], a[1]), gelu2(a[2], a[3])}; }

#define ST16(off, v) __builtin_amdgcn_raw_buffer_store_b128(v, rs, (int)(off), 0, AUX_SC1)
typedef unsigned long long u64;

struct PackJob { const GAS unsigned short* src; int ldn, mat, ks, cb, na, nb, fb; };
__device__ __forceinline__ PackJob pack_job(const GArgs& p, int g) {
  PackJob jb;
  const uintptr_t a1 = (uintptr_t)p.w1, a2 = (uintptr_t)p.w2, a3 = (uintptr_t)p.w3, a4 = (uintptr_t)p.w4;
  uintptr_t lo = g < 56 ? a1 : a2, hi = g < 336 ? a3 : a4;
  asm volatile("" : "+s"(lo), "+s"(hi));
  jb.src = (const GAS unsigned short*)(g < 112 ? lo : hi);
  jb.mat = (g >= 56) + (g >= 112) + (g >= 336);
  jb.ldn = jb.mat == 2 ? 1792 : 448;
  g -= g < 112 ? (g < 56 ? 0 : 56) : (g < 336 ? 112 : 336);
  const int sh = jb.mat == 2 ? 4 : 2;
  jb.ks = g >> sh;
  jb.cb = g & ((1 << sh) - 1);
  const int j3 = (jb.ks * 4682) >> 16, ksl = jb.ks - j3 * 14, sl = (jb.cb - j3 - 1) & 3;
  const int is2 = jb.mat == 2, is3 = jb.mat == 3;
  jb.na = is2 * jb.cb * 7 + (1 - is2) * (is3 * j3 + (1 - is3) * jb.cb) * 28;
  jb.nb = 4 - 3 * is2;
  const int own = sl == 3;
  const int f3 = (1 - own) * (84 + (ksl & 1) * 21 + (ksl >> 1) * 3 + sl) + own * (126 + ksl);
  jb.fb = is2 * (28 + jb.ks * 2) + is3 * f3 + (1 - is2 - is3) * (jb.mat * 14 + jb.ks);
  jb.na = __builtin_amdgcn_readfirstlane(jb.na); jb.nb = __builtin_amdgcn_readfirstlane(jb.nb); jb.fb = __builtin_amdgcn_readfirstlane(jb.fb);
  return jb;
}
__device__ __forceinline__ void pack_load(const PackJob& jb, int lane, u32x4 (&r)[8]) {
  const int jn = lane & 15, q = lane >> 4, jl = jn < 14 ? jn : 0;
#pragma unroll
  for (int i = 0; i < 8; ++i) r[i] = *(const GAS u32x4*)(jb.src + (size_t)(jb.ks * 32 + q * 8 + i) * jb.ldn + jb.cb * 112 + jl * 8);
}
__device__ __forceinline__ void pack_shuffle(int lane, u32x4 (&r)[8], LAS unsigned char* Lw) {
  const int jn = lane & 15, q = lane >> 4;
  u32x4 o[8];
  transpose8x8(r, o);
#pragma unroll
  for (int i = 0; i < 8; ++i) *(LAS u32x4*)(Lw + (jn >> 1) * 1024 + (q * 16 + (jn & 1) * 8 + (i ^ (jn >> 1))) * 16) = o[i];
  lds_fence();
#pragma unroll
  for (int m = 0; m < 7; ++m) r[m] = *(const LAS u32x4*)(Lw + m * 1024 + (lane ^ m) * 16);
  lds_fence();
}
__device__ __forceinline__ void pack_store1(const PackJob& jb, int lane, const u32x4 (&r)[8], __amdgpu_buffer_rsrc_t rs, int m) {
  const int nt = jb.na + m * jb.nb;
  __builtin_amdgcn_raw_buffer_store_b128(r[m], rs, lane * 16, (int)WS_WP + ((nt >> 2) * NFRAG + jb.fb + ((nt >> 1) & 1) * 28 + (nt & 1)) * 1024, AUX_SC1);
}
__device__ __forceinline__ void mod_load(const GArgs& p, int u, int tid, u32x4 (&r)[7], float (&sc)[7]) {
  const int rg = tid >> 3, pc = tid & 7;
#pragma unroll
  for (int i = 0; i < 7; ++i) r[i] = *(const u32x4*)(p.wmod + (size_t)(rg + 64 * i) * 2688 + u * 64 + pc * 8);
#pragma unroll
  for (int i = 0; i < 7; ++i) sc[i] = bf2f(p.cvec[rg + 64 * i]);
}
__device__ __forceinline__ void mod_finish(int u, unsigned tag32, int tid, const u32x4 (&r)[7], const float (&cv)[7], LAS float* sm, __amdgpu_buffer_rsrc_t rs) {
  const int rg = tid >> 3, pc = tid & 7;
  float acc[8];
#pragma unroll
  for (int k = 0; k < 8; ++k) acc[k] = 0.f;
#pragma unroll
  for (int i = 0; i < 7; ++i) {
    const float sc = rbf(cv[i] * __builtin_amdgcn_rcpf(1.0f + __builtin_amdgcn_exp2f(-1.4426950408889634f * cv[i])));
#pragma unroll
    for (int k = 0; k < 4; ++k) { acc[2 * k] += sc * bf_lo(r[i][k]); acc[2 * k + 1] += sc * bf_hi(r[i][k]); }
  }
  *(LAS f32x4*)(sm + rg * 68 + pc * 8) = f32x4{acc[0], acc[1], acc[2], acc[3]};
  *(LAS f32x4*)(sm + rg * 68 + pc * 8 + 4) = f32x4{acc[4], acc[5], acc[6], acc[7]};
  bar_lds();
  {
    const int col = tid & 63, part = tid >> 6;
    float s = 0.f;
#pragma unroll
    for (int k = 0; k < 8; ++k) s += sm[(part * 8 + k) * 68 + col];
    sm[64 * 68 + part * 64 + col] = s;
  }
  bar_lds();
  if (tid < 32) {
    f32x2 s = {0.f, 0.f};
#pragma unroll
    for (int k = 0; k < 8; ++k) s += *(const LAS f32x2*)(sm + 64 * 68 + k * 64 + 2 * tid);
    unsigned v = pk_bf16(s.x, s.y);
    const int sec = u / 7;
    if (sec == 1 || sec == 4) v = pk_bf16(bf_lo(v) + 1.0f, bf_hi(v) + 1.0f);
    __builtin_amdgcn_raw_buffer_store_b64(u32x2{v, tag32}, rs, (int)WS_MODG + (u * 32 + tid) * 8, 0, AUX_SC1);
  }
}
template <int YOUNGER>
__device__ __forceinline__ void arrive_flag(LAS unsigned char* L, int cnt_slot, unsigned char* flag, u64 tag, int lane) {
  asm volatile("s_waitcnt vmcnt(%0)" ::"i"(YOUNGER) : "memory");
  if (lane == 0) {
    const unsigned old = __hip_atomic_fetch_add((LAS unsigned*)(L + G_CNT) + cnt_slot, 1u, __ATOMIC_RELAXED, __HIP_MEMORY_SCOPE_WORKGROUP);
    if (old == 7u) __hip_atomic_store((GAS u64*)flag, tag, __ATOMIC_RELAXED, __HIP_MEMORY_SCOPE_AGENT);
  }
}
__device__ __forceinline__ u32x2 flag_load(__amdgpu_buffer_rsrc_t rs, int off) { return __builtin_amdgcn_raw_buffer_load_b64(rs, off, 0, AUX_SC1); }
__device__ __forceinline__ u32x2 flag_wait(__amdgpu_buffer_rsrc_t rs, int off, u32x2 v, unsigned lo, unsigned hi, unsigned lomask) {
  if (__builtin_expect(__any((((v.x ^ lo) & lomask) | (v.y ^ hi)) != 0u), 0)) {
    for (unsigned spins = 0; spins < SPIN_CAP; ++spins) {
      __builtin_amdgcn_s_sleep(2);
      asm volatile("" ::: "memory");
      v = flag_load(rs, off);
      if (!__any((((v.x ^ lo) & lomask) | (v.y ^ hi)) != 0u)) break;
    }
  }
  __builtin_amdgcn_fence(__ATOMIC_ACQUIRE, "wavefront");
  return v;
}
__device__ __forceinline__ void stat_wait(__amdgpu_buffer_rsrc_t rs, int off, u32x4& a, u32x4& b, unsigned tag32) {
  if (__builtin_expect(__any((((a.y ^ tag32) | (a.w ^ tag32)) | ((b.y ^ tag32) | (b.w ^ tag32))) != 0u), 0)) {
    for (unsigned spins = 0; spins < SPIN_CAP; ++spins) {
      __builtin_amdgcn_s_sleep(2);
      asm volatile("" ::: "memory");
      a = __builtin_amdgcn_raw_buffer_load_b128(rs, off, 0, AUX_SC1);
      b = __builtin_amdgcn_raw_buffer_load_b128(rs, off + 16, 0, AUX_SC1);
      if (!__any((((a.y ^ tag32) | (a.w ^ tag32)) | ((b.y ^ tag32) | (b.w ^ tag32))) != 0u)) break;
    }
  }
}
#define FILL_P(i) do { SB0(); __builtin_amdgcn_raw_buffer_store_b128(ONES, rst, (int)WS_P + ((pn * 4 + j) * 3 + (i)) * 7168 + pslot, 0, AUX_SC1); SB0(); } while (0)
#define FILL_X1() do { SB0(); __builtin_amdgcn_raw_buffer_store_b128(ONES, rst, (int)WS_X1 + (pn * 4 + j) * 7168 + pslot, 0, AUX_SC1); SB0(); } while (0)
__device__ __forceinline__ void body(const GArgs& p, LAS unsigned char* L, uintptr_t code0) {
  const bool CONV = blockIdx.x < 224;
  const int tid = threadIdx.x, lane = tid & 63;
  const int w = __builtin_amdgcn_readfirstlane(tid >> 6);
  const int bid = blockIdx.x;
  const int j = bid & 3, pn = (bid >> 3) * 2 + ((bid >> 2) & 1);
  const int r16 = lane & 15, q = lane >> 4;
  const bool cw = w < 7;
  const int wc = cw ? w : 6;
  const int dp = (bid & 7) * 28 + (CONV ? bid >> 3 : 0);
  const __amdgpu_buffer_rsrc_t rs = __builtin_amdgcn_make_buffer_rsrc((void*)p.ws, 0, (int)WS_END, 0x00020000);
  const __amdgpu_buffer_rsrc_t rst = __builtin_amdgcn_make_buffer_rsrc((void*)p.ws, 0, cw ? (int)WS_END : 0, 0x00020000);
  const __amdgpu_buffer_rsrc_t rw =
      __builtin_amdgcn_make_buffer_rsrc((void*)(p.ws + WS_WP + (size_t)(j * 7 + wc) * NFRAG * 1024), 0, cw ? NFRAG * 1024 : 0, 0x00020000);
  const int voff = lane * 16;
  const int pslot = (wc * 64 + lane) * 16;
  const u32x4 ONES = {~0u, ~0u, ~0u, ~0u};
  const unsigned long long t_begin = __builtin_amdgcn_s_memrealtime();
#define HOLD(x) while (__builtin_amdgcn_s_memrealtime() - t_begin < (x)) __builtin_amdgcn_s_sleep(1)
  const int l56 = lane < 56 ? lane : 0;
  const u32x4 xrow = *(const u32x4*)(p.x + (size_t)(bid * 8 + w) * D_ + l56 * 8);
  const u32x2 ep = flag_load(rs, (int)WS_EPOCH);
  const int cl = (bid >> 3) * 16 + (lane & 15);
  const unsigned touch = *(const GAS unsigned*)(code0 + (size_t)(cl < CODE_LINES ? cl : 0) * 128);
  float are, aim, b_re, b_im, cre, cim, ldt;
  u32x4 mr[7], pk[8];
  float mc[7];
  {
    const size_t o = (size_t)(2 * dp + (w >> 2)) * NS_ + (w & 3) * 64 + lane;
    SB0();
    are = bf2f(p.a_re[o]); aim = bf2f(p.a_im[o]); b_re = bf2f(p.b_re[o]); b_im = bf2f(p.b_im[o]);
    cre = bf2f(p.c_re[o]); cim = bf2f(p.c_im[o]); ldt = bf2f(p.log_dt[2 * dp + (w >> 2)]);
    SB0();
  }
  __builtin_amdgcn_s_barrier();
  __builtin_amdgcn_raw_buffer_store_b128(ONES, rst, (int)WS_X2B + bid * 7168 + pslot, 0, AUX_SC1);
  __builtin_amdgcn_raw_buffer_store_b128(ONES, rst, (int)WS_G + bid * 7168 + pslot, 0, AUX_SC1);
  SB0();
  const int pg = (bid - 224) * 6 + w;
  const bool packer = CONV ? w < 2 : (w < 6 && pg < 112);
  const PackJob jb = pack_job(p, CONV ? bid * 2 + (w & 1) : (packer ? 448 + pg : 0));
  if (tid < 4) *((LAS unsigned*)(L + G_CNT) + tid) = 0u;
  u64 tag;
  {
    const u64 e = ((u64)__builtin_amdgcn_readfirstlane(ep.y) << 32) | (unsigned)__builtin_amdgcn_readfirstlane(ep.x);
    u64 z = e + 0x9E3779B97F4A7C15ull;
    z = (z ^ (z >> 30)) * 0xBF58476D1CE4E5B9ull;
    z = (z ^ (z >> 27)) * 0x94D049BB133111EBull;
    tag = z ^ (z >> 31);
  }
  const unsigned tag32 = ((unsigned)__builtin_amdgcn_readfirstlane(ep.x) + 1u) * 0x9E3779B1u ^ 0x5bd1e995u;
  const unsigned tlo = (unsigned)tag, thi = (unsigned)(tag >> 32);
  unsigned stv;
#define PUT_STAT() do { if (lane == 0) __builtin_amdgcn_raw_buffer_store_b64(u32x2{stv, tag32}, rs, (int)WS_STG + (bid * 8 + w) * 8, 0, AUX_SC1); } while (0)
  {
    const float msk = lane < 56 ? 1.0f : 0.0f;
    float v[8];
#pragma unroll
    for (int k = 0; k < 4; ++k) { v[2 * k] = bf_lo(xrow[k]); v[2 * k + 1] = bf_hi(xrow[k]); }
    const float mean = wave_sum((((v[0] + v[1]) + (v[2] + v[3])) + ((v[4] + v[5]) + (v[6] + v[7]))) * msk) * (1.0f / D_);
    float qq = 0.f;
#pragma unroll
    for (int k = 0; k < 8; ++k) { const float d = v[k] - mean; qq += d * d; }
    const float sd = ln_sd(wave_sum(qq * msk) * (1.0f / D_));
    stv = pk_bf16(mean, sd);
    if (lane < 56) *(LAS u32x4*)(L + P0_LN + w * LNS + lane * 16) = xrow;
    bar_lds();
#pragma unroll
    for (int u = 0; u < 2; ++u) {
      const int pce = (wc * 2 + u) * 64 + lane;
      const LAS unsigned* a = (const LAS unsigned*)(L + P0_LN + (pce & 3) * (2 * LNS) + (pce >> 2) * 4);
      __builtin_amdgcn_raw_buffer_store_b128(u32x4{a[0], tag32, a[LNS / 4], tag32}, rst, (int)WS_XG + bid * (int)XG_BLK + pce * 16, 0, AUX_SC1);
    }
  }
  asm volatile("" ::"v"(touch));
  STAMP(0, 29);
  const int sto = (int)WS_STG + tid * 32;
  u32x4 ring[RING];
  if (__builtin_expect(CONV, 1)) {
    const float dt = rbf(expf(ldt));
    const float ea = (dt * are) * 1.4426950408889634f, eb = (dt * aim) * 0.15915494309189535f;
    const float bre = dt * b_re, bim = dt * b_im;
    f32x2 wc;
    wc.x = cre * bre - cim * bim;
    wc.y = -(cre * bim + cim * bre);
    asm volatile("" ::"v"(ea), "v"(eb), "v"(wc.x), "v"(wc.y));
    const float mx = rows_max(row16_max(fmaxf(fabsf(wc.x), fabsf(wc.y))));
    int ex = mx > 0.f ? __builtin_amdgcn_frexp_expf(mx) : 0;
    ex = ex < -120 ? -120 : (ex > 120 ? 120 : ex);
    const float scale = __builtin_amdgcn_ldexpf(1.0f, ex), iscale = __builtin_amdgcn_ldexpf(1.0f, -ex);
    wc.x *= iscale; wc.y *= iscale;
    LAS unsigned char* B0 = L + P0_KG + w * (2 * KG_BUF);
    LAS unsigned char* B1 = B0 + KG_BUF;
    const int wo = lane * 4;
    const int ro = r16 * KG_ROW + q * 16;
    f32x2 z8c = cexp_tab(ea, eb, 8.0f);
    z8c.y = -z8c.y;
    const f32x2 z1 = cexp_tab(ea, eb, 1.0f), z128 = cexp_tab(ea, eb, 128.0f), z1024 = cexp_tab(ea, eb, 1024.0f);
    {
      const f32x2 z16c = cmul(z8c, z8c);
      f32x2 t0 = wc, t1 = cmul(wc, z8c);
#pragma unroll
      for (int c = 0; c < 16; c += 2) {
        *(LAS unsigned*)(B0 + c * KG_ROW + wo) = pk_f16(t0);
        *(LAS unsigned*)(B0 + (c + 1) * KG_ROW + wo) = pk_f16(t1);
        if (c < 14) { t0 = cmul(t0, z16c); t1 = cmul(t1, z16c); }
      }
    }
    SB0();
    asm volatile("s_waitcnt vmcnt(2)" ::: "memory");
    PUT_STAT();
    SB0();
    f32x2 ze[8];
    ze[0] = f32x2{1.0f, 0.0f}; ze[1] = z1; ze[2] = cmul(z1, z1); ze[3] = cmul(ze[2], z1); ze[4] = cmul(ze[2], ze[2]);
    ze[5] = cmul(ze[4], z1); ze[6] = cmul(ze[4], ze[2]); ze[7] = cmul(ze[4], ze[3]);
#pragma unroll
    for (int e = 0; e < 8; ++e) {
      *(LAS unsigned*)(B1 + e * KG_ROW + wo) = pk_f16(ze[e]);
      *(LAS unsigned*)(B1 + (8 + e) * KG_ROW + wo) = pk_f16(e == 0 ? z128 : cmul(z128, ze[e]));
    }
    lds_fence();
    u32x4 tf[4];
#pragma unroll
    for (int ks = 0; ks < 4; ++ks) tf[ks] = *(const LAS u32x4*)(B0 + ro + ks * 64);
    u32x4 rf[4];
#pragma unroll
    for (int ks = 0; ks < 4; ++ks) rf[ks] = *(const LAS u32x4*)(B1 + ro + ks * 64);
    lds_fence();
    SB0();
    if (packer) pack_load(jb, lane, pk);
    SB0();
    STAMP(0, 30);
    const f32x2 p2 = cmul(z128, z128), p3 = cmul(p2, z128), p4 = cmul(p2, p2), p5 = cmul(p4, z128), p6 = cmul(p4, p2), p7 = cmul(p4, p3);
    {
#pragma unroll
      for (int e = 0; e < 4; ++e) {
        *(LAS unsigned*)(B0 + e * KG_ROW + wo) = pk_f16(e == 0 ? p2 : cmul(p2, ze[2 * e]));
        *(LAS unsigned*)(B0 + (4 + e) * KG_ROW + wo) = pk_f16(e == 0 ? p3 : cmul(p3, ze[2 * e]));
      }
      *(LAS unsigned*)(B0 + 8 * KG_ROW + wo) = pk_f16(p4);
      *(LAS unsigned*)(B0 + 9 * KG_ROW + wo) = pk_f16(cmul(p4, ze[4]));
      *(LAS unsigned*)(B0 + 10 * KG_ROW + wo) = pk_f16(p5);
      *(LAS unsigned*)(B0 + 11 * KG_ROW + wo) = pk_f16(cmul(p5, ze[4]));
      *(LAS unsigned*)(B0 + 12 * KG_ROW + wo) = pk_f16(p6);
      *(LAS unsigned*)(B0 + 13 * KG_ROW + wo) = pk_f16(cmul(p6, ze[4]));
      *(LAS unsigned*)(B0 + 14 * KG_ROW + wo) = pk_f16(p7);
      *(LAS unsigned*)(B0 + 15 * KG_ROW + wo) = pk_f16(cmul(p7, ze[4]));
    }
    f32x4 acc0 = {0.f, 0.f, 0.f, 0.f}, acc1 = acc0, acc2 = acc0;
#pragma unroll
    for (int ks = 0; ks < 4; ++ks) acc0 = mfma16h(rf[ks], tf[ks], acc0);
    {
      *(LAS unsigned*)(B1 + 0 * KG_ROW + wo) = pk_f16(z1024);
      *(LAS unsigned*)(B1 + 1 * KG_ROW + wo) = pk_f16(cmul(z1024, z128));
      *(LAS unsigned*)(B1 + 2 * KG_ROW + wo) = pk_f16(cmul(z1024, p2));
      *(LAS unsigned*)(B1 + 3 * KG_ROW + wo) = pk_f16(cmul(z1024, p3));
      *(LAS unsigned*)(B1 + 4 * KG_ROW + wo) = pk_f16(cmul(z1024, p4));
      *(LAS unsigned*)(B1 + 5 * KG_ROW + wo) = pk_f16(cmul(z1024, p5));
      *(LAS unsigned*)(B1 + 6 * KG_ROW + wo) = pk_f16(cmul(z1024, p6));
      *(LAS unsigned*)(B1 + 7 * KG_ROW + wo) = pk_f16(cmul(z1024, p7));
      *(LAS unsigned*)(B1 + 8 * KG_ROW + wo) = pk_f16(cmul(z1024, z1024));
    }
    lds_fence();
#pragma unroll
    for (int ks = 0; ks < 4; ++ks) acc1 = mfma16h(*(const LAS u32x4*)(B0 + ro + ks * 64), tf[ks], acc1);
#pragma unroll
    for (int ks = 0; ks < 4; ++ks) acc2 = mfma16h(*(const LAS u32x4*)(B1 + ro + ks * 64), tf[ks], acc2);
    lds_fence();
    {
      LAS float* red = (LAS float*)B0;
      acc0 *= scale; acc1 *= scale; acc2 *= scale;
      *(LAS f32x4*)(red + (q >> 1) * 128 + 8 * r16 + (q & 1) * 4) = acc0;
      const int i1 = q < 2 ? 256 + q * 64 + 4 * r16 : 384 + (q - 2) * 64 + 2 * r16, d1 = q < 2 ? 2 : 32;
      *(LAS f32x2*)(red + i1) = f32x2{acc1[0], acc1[1]};
      *(LAS f32x2*)(red + i1 + d1) = f32x2{acc1[2], acc1[3]};
      LAS float* r2 = red + 512 + q * 64 + r16;
      r2[0] = acc2[0]; r2[16] = acc2[1]; r2[32] = acc2[2]; r2[48] = acc2[3];
    }
    STAMP(0, 21);
    bar_lds();
    const int mgo = (int)WS_MODG + ((lane < 3 ? lane : 0) * 224 + dp) * 8;
    SB0();
    u32x4 sg0 = __builtin_amdgcn_raw_buffer_load_b128(rs, sto, 0, AUX_SC1), sg1 = __builtin_amdgcn_raw_buffer_load_b128(rs, sto + 16, 0, AUX_SC1);
    const int xgo = (int)WS_XG + (tid >> 1) * (int)XG_BLK + dp * 64 + (tid & 1) * 32;
    u32x4 xg0 = __builtin_amdgcn_raw_buffer_load_b128(rs, xgo, 0, AUX_SC1), xg1 = __builtin_amdgcn_raw_buffer_load_b128(rs, xgo + 16, 0, AUX_SC1);
    const unsigned dsk2 = *(const unsigned*)(p.dskip + 2 * dp);
    SB0();
    const int kc = tid >> 8, kt = tid & 255;
    LAS float* kd = (LAS float*)(L + P0_KG + kc * (8 * KG_BUF) + KG_BUF);
#pragma unroll
    for (int i = 0; i < 3; ++i) {
      const int oo = kt + 256 * i;
      if (i < 2 || oo < 648) {
        const LAS float* s = (const LAS float*)(L + P0_KG + kc * (8 * KG_BUF)) + oo;
        kd[oo] = (s[0] + s[2 * KG_BUF / 4]) + (s[4 * KG_BUF / 4] + s[6 * KG_BUF / 4]);
      }
    }
    if (kt == 0) kd[648] = 0.f;
    const unsigned OFFS = 0x087EDA52u;
    {
      int off = CV_KCOPY + kt * 16;
      if (kt >= 160) { const int k = kt - 160, s = k >> 3, ch = (k & 7) == 7 ? 6 : (k & 7); off = (272 * (s & 7) + ((OFFS >> (4 * (s & 7))) & 15) + 257 + ch) * 16; }
      *(LAS u32x4*)(L + kc * CV_CH + off) = u32x4{0u, 0u, 0u, 0u};
    }
    bar_lds();
    LAS unsigned char* base = L + kc * CV_CH;
    u32x4 k0;
    {
      float v[8];
#pragma unroll
      for (int i = 0; i < 8; i += 2) {
        const int l0 = 2048 - (kt * 8 + i), l1 = l0 - 1;
        const unsigned bits = pk_bf16((float)l0, (float)l1);
        int i0 = l0 < 128 ? l0 : (int)(bits & 0xffffu) - 17024;
        const int i1 = l1 < 128 ? l1 : (int)(bits >> 16) - 17024;
        if (i == 0) i0 = kt == 0 ? 648 : i0;
        v[i] = kd[i0];
        v[i + 1] = kd[i1];
      }
      k0 = u32x4{pk_bf16(v[0], v[1]), pk_bf16(v[2], v[3]), pk_bf16(v[4], v[5]), pk_bf16(v[6], v[7])};
      *(LAS u32x4*)(base + (2 + kt) * 16) = k0;
      if (kt < 8) {
        const unsigned k00 = pk_bf16(kd[0], 0.f);
        *(LAS u32x4*)(base + (272 * kt + ((OFFS >> (4 * kt)) & 15) + 256) * 16) = u32x4{kt == 0 ? k00 : 0u, 0u, 0u, 0u};
      }
    }
    bar_lds();
    {
      const u32x4 k1 = *(const LAS u32x4*)(base + (2 + kt + 1) * 16);
      unsigned wv[8] = {k0.x, k0.y, k0.z, k0.w, k1.x, k1.y, k1.z, k1.w};
      unsigned vv[7];
#pragma unroll
      for (int i = 0; i < 7; ++i) vv[i] = __builtin_amdgcn_alignbit(wv[i + 1], wv[i], 16);
#pragma unroll
      for (int s = 1; s < 8; ++s) {
        const int k = s >> 1;
        u32x4 o = (s & 1) ? u32x4{vv[k], vv[k + 1], vv[k + 2], vv[k + 3]} : u32x4{wv[k], wv[k + 1], wv[k + 2], wv[k + 3]};
        *(LAS u32x4*)(base + (272 * s + ((OFFS >> (4 * s)) & 15) + kt) * 16) = o;
      }
    }
    float cum0[4], cum1[4];
    {
      const LAS float* kd0 = (const LAS float*)(L + P0_KG + KG_BUF);
      const LAS float* kd1 = (const LAS float*)(L + P0_KG + 8 * KG_BUF + KG_BUF);
#pragma unroll
      for (int i = 0; i < 4; i += 2) {
        const int l0 = 4 * tid + i;
        const unsigned bits = pk_bf16((float)l0, (float)(l0 + 1));
        const int i0 = l0 < 128 ? l0 : (int)(bits & 0xffffu) - 17024, i1 = l0 < 128 ? l0 + 1 : (int)(bits >> 16) - 17024;
        cum0[i] = kd0[i0]; cum0[i + 1] = kd0[i1]; cum1[i] = kd1[i0]; cum1[i + 1] = kd1[i1];
      }
#pragma unroll
      for (int i = 1; i < 4; ++i) { cum0[i] += cum0[i - 1]; cum1[i] += cum1[i - 1]; }
      const float inc0 = wave_scan(cum0[3]), inc1 = wave_scan(cum1[3]);
      const float e0 = inc0 - cum0[3], e1 = inc1 - cum1[3];
#pragma unroll
      for (int i = 0; i < 4; ++i) { cum0[i] += e0; cum1[i] += e1; }
      if (lane == 63) *(LAS f32x2*)(L + P0_MS + w * 8) = f32x2{inc0, inc1};
    }
#define GRAN_BAD() __any(((((sg0.y ^ tag32) | (sg0.w ^ tag32)) | ((sg1.y ^ tag32) | (sg1.w ^ tag32))) | (((xg0.y ^ tag32) | (xg0.w ^ tag32)) | ((xg1.y ^ tag32) | (xg1.w ^ tag32)))) != 0u)
    if (__builtin_expect(GRAN_BAD(), 0)) {
      for (unsigned spins = 0; spins < SPIN_CAP; ++spins) {
        __builtin_amdgcn_s_sleep(2);
        asm volatile("" ::: "memory");
        sg0 = __builtin_amdgcn_raw_buffer_load_b128(rs, sto, 0, AUX_SC1); sg1 = __builtin_amdgcn_raw_buffer_load_b128(rs, sto + 16, 0, AUX_SC1);
        xg0 = __builtin_amdgcn_raw_buffer_load_b128(rs, xgo, 0, AUX_SC1); xg1 = __builtin_amdgcn_raw_buffer_load_b128(rs, xgo + 16, 0, AUX_SC1);
        if (!GRAN_BAD()) break;
      }
    }
#undef GRAN_BAD
    const unsigned xq[4] = {xg0.x, xg0.z, xg1.x, xg1.z};
    STAMP(0, 22);
    unsigned hq[4];
    {
      const unsigned sv[4] = {sg0.x, sg0.z, sg1.x, sg1.z};
#pragma unroll
      for (int k = 0; k < 4; ++k) {
        const float m = bf_lo(sv[k]), r = __builtin_amdgcn_rcpf(bf_hi(sv[k]));
        unsigned t = pk_bf16(bf_lo(xq[k]) - m, bf_hi(xq[k]) - m);
        hq[k] = pk_bf16(bf_lo(t) * r, bf_hi(t) * r);
      }
      LAS unsigned char* U = L + CV_KCOPY + (16 + (tid >> 4)) * CV_UROW + (tid & 15) * 8;
      *(LAS u32x2*)U = u32x2{__builtin_amdgcn_perm(hq[1], hq[0], 0x05040100u), __builtin_amdgcn_perm(hq[3], hq[2], 0x05040100u)};
      *(LAS u32x2*)(U + CV_CH) = u32x2{__builtin_amdgcn_perm(hq[1], hq[0], 0x07060302u), __builtin_amdgcn_perm(hq[3], hq[2], 0x07060302u)};
    }
    if (packer) pack_shuffle(lane, pk, L + P0_KG + (w + 1) * (2 * KG_BUF));
    HOLD(900);
    bar_lds();
    if (bid == 0 && tid == 0) {
      const u64 e = ((u64)ep.y << 32) | ep.x;
      __hip_atomic_store((GAS u64*)(p.ws + WS_EPOCH), e + 1ull, __ATOMIC_RELAXED, __HIP_MEMORY_SCOPE_AGENT);
    }
    u32x2 fv = flag_load(rs, mgo);
    const __amdgpu_buffer_rsrc_t rpk = __builtin_amdgcn_make_buffer_rsrc((void*)p.ws, 0, packer ? (int)WS_END : 0, 0x00020000);
    SB0();
  {
    const int ch = w >> 2, wq = w & 3;
    f32x4 acc[2][4];
#pragma unroll
    for (int n = 0; n < 2; ++n)
#pragma unroll
      for (int m = 0; m < 4; ++m) acc[n][m] = f32x4{0.f, 0.f, 0.f, 0.f};
    const int s = (8 - r16) & 7, u = (r16 + 7) >> 3;
    const LAS unsigned char* ka = L + ch * CV_CH + (272 * s + ((OFFS >> (4 * s)) & 15) + 256 + q - u - 2 * (16 * wq) - 2 * 79) * 16;
    const LAS unsigned char* ub = L + ch * CV_CH + CV_KCOPY + (16 + r16 - 4 * wq - 3) * CV_UROW + q * 16;
#define KFRAG(f) (*(const LAS u32x4*)(ka + (79 - (f)) * 32))
    u32x4 alo[18], ahi[18], bf[4][4];
#define CSTAGE(dl)                                                                                      \
  {                                                                                                     \
    _Pragma("unroll") for (int ks = 0; ks < 2; ++ks) {                                                  \
      bf[dl][2 * ks] = *(const LAS u32x4*)(ub + (3 - (dl)) * CV_UROW + ks * 64);                        \
      bf[dl][2 * ks + 1] = *(const LAS u32x4*)(ub + (19 - (dl)) * CV_UROW + ks * 64);                   \
    }                                                                                                   \
    _Pragma("unroll") for (int i = 0; i < 4; ++i) {                                                     \
      alo[2 + 4 * (dl) + i] = KFRAG(4 * (dl) + i);                                                      \
      ahi[2 + 4 * (dl) + i] = KFRAG(4 * (dl) + i + 64);                                                 \
    }                                                                                                   \
  }
    CSTAGE(0)
    alo[0] = KFRAG(-2); alo[1] = KFRAG(-1); ahi[0] = KFRAG(62); ahi[1] = KFRAG(63);
    SB0();
#pragma unroll
    for (int m = 0; m < 7; ++m) pack_store1(jb, lane, pk, rpk, m);
    FILL_X1(); FILL_P(0); FILL_P(1); FILL_P(2);
    SB0();
#pragma unroll
    for (int dl = 0; dl < 4; ++dl) {
      if (dl < 3) CSTAGE(dl + 1)
#pragma unroll
      for (int ks = 0; ks < 2; ++ks) {
#pragma unroll
        for (int m = 0; m < 4; ++m) {
          acc[0][m] = mfma16(alo[4 * dl + m - 2 * ks + 2], bf[dl][2 * ks], acc[0][m]);
          acc[1][m] = mfma16(alo[4 * dl + m - 2 * ks + 2], bf[dl][2 * ks + 1], acc[1][m]);
          acc[1][m] = mfma16(ahi[4 * dl + m - 2 * ks + 2], bf[dl][2 * ks], acc[1][m]);
        }
      }
      if (dl < 3) {
#pragma unroll
        for (int i = 0; i < 12; ++i) {
          __builtin_amdgcn_sched_group_barrier(0x100, 1, 0);
          __builtin_amdgcn_sched_group_barrier(0x8, 2, 0);
        }
      }
      SB0();
    }
#undef CSTAGE
    STAMP(0, 28);
#undef KFRAG

    LAS unsigned char* P = L + CV_P + w * (32 * CV_PROW);
#pragma unroll
    for (int n = 0; n < 2; ++n)
#pragma unroll
      for (int m = 0; m < 4; ++m) *(LAS f32x4*)(P + (16 * n + r16) * CV_PROW + (16 * m + 4 * q) * 4) = acc[n][m];
    }
    fv = flag_wait(rs, mgo, fv, 0u, tag32, 0u);
    const unsigned sa2 = __builtin_amdgcn_readlane(fv.x, 0), ca2 = __builtin_amdgcn_readlane(fv.x, 1), ga2 = __builtin_amdgcn_readlane(fv.x, 2);
    unsigned hd[4];
    {
      const f32x2 t2 = *(const LAS f32x2*)(L + P0_MS + (lane & 7) * 8);
      const float mk = (lane & 15) < w ? 1.0f : 0.0f;
      const float b0 = row16_sum((lane & 8) ? 0.f : t2.x * mk), b1 = row16_sum((lane & 8) ? 0.f : t2.y * mk);
#pragma unroll
      for (int k = 0; k < 4; ++k) {
        unsigned h = pk_bf16(bf_lo(hq[k]) * bf_lo(ca2), bf_hi(hq[k]) * bf_hi(ca2));
        h = pk_bf16(bf_lo(h) + bf_lo(sa2), bf_hi(h) + bf_hi(sa2));
        hd[k] = pk_bf16(bf_lo(h) * bf_lo(dsk2), bf_hi(h) * bf_hi(dsk2));
        cum0[k] = (cum0[k] + b0) * bf_lo(sa2);
        cum1[k] = (cum1[k] + b1) * bf_hi(sa2);
      }
    }
    asm volatile("" : "+v"(hd[0]), "+v"(hd[1]), "+v"(hd[2]), "+v"(hd[3]), "+v"(cum0[0]), "+v"(cum0[1]), "+v"(cum0[2]), "+v"(cum0[3]),
                 "+v"(cum1[0]), "+v"(cum1[1]), "+v"(cum1[2]), "+v"(cum1[3]));
    asm volatile("s_waitcnt vmcnt(4)" ::: "memory");
    bar_lds();
    STAMP(0, 24);
    {
      const LAS unsigned char* P = L + CV_P + (tid >> 4) * CV_PROW + (tid & 15) * 16;
      f32x4 pa[8];
#pragma unroll
      for (int k = 0; k < 8; ++k) pa[k] = *(const LAS f32x4*)(P + k * (32 * CV_PROW));
      const f32x4 y0 = (pa[0] + pa[1]) + (pa[2] + pa[3]), y1 = (pa[4] + pa[5]) + (pa[6] + pa[7]);
      u32x4 out;
#pragma unroll
      for (int k = 0; k < 4; ++k) {
        const unsigned yy = pk_bf16(y0[k] * bf_lo(ca2) + cum0[k], y1[k] * bf_hi(ca2) + cum1[k]);
        unsigned t = pk_bf16(bf_lo(yy) + bf_lo(hd[k]), bf_hi(yy) + bf_hi(hd[k]));
        t = pk_bf16(bf_lo(ga2) * bf_lo(t), bf_hi(ga2) * bf_hi(t));
        out[k] = sane(pk_bf16(bf_lo(xq[k]) + bf_lo(t), bf_hi(xq[k]) + bf_hi(t)));
      }
      ST16(WS_X2B + (tid >> 3) * (int)X2B_BLK + dp * 128 + (tid & 7) * 16, out);
    }
  } else {
    const int c = bid - 224;
    LAS float* sm = (LAS float*)(L + P0_KG);
    SB0();
    mod_load(p, c, tid, mr, mc);
    SB0();
    asm volatile("s_waitcnt vmcnt(16)" ::: "memory");
    PUT_STAT();
    mod_finish(c, tag32, tid, mr, mc, sm, rs);
    STAMP(0, 21);
    if (c >= 22) {
      mod_load(p, 10 + c, tid, mr, mc);
      bar_lds();
      mod_finish(10 + c, tag32, tid, mr, mc, sm, rs);
    }
    if (packer) {
      pack_load(jb, lane, pk);
      pack_shuffle(lane, pk, L + P0_PK + w * 8192);
#pragma unroll
      for (int m = 0; m < 7; ++m) pack_store1(jb, lane, pk, rs, m);
    }
    arrive_flag<0>(L, 2, p.ws + WS_FLAGC + c * 8, ~tag, lane);
    STAMP(0, 30);
    FILL_X1(); FILL_P(0); FILL_P(1); FILL_P(2);
    {
      u32x4 sg0 = __builtin_amdgcn_raw_buffer_load_b128(rs, sto, 0, AUX_SC1), sg1 = __builtin_amdgcn_raw_buffer_load_b128(rs, sto + 16, 0, AUX_SC1);
      stat_wait(rs, sto, sg0, sg1, tag32);
    }
    bar_lds();
  }
  STAMP(0, 25);
  if (__builtin_expect(!cw, 0)) {
    {
      const int mo = (int)WS_MODG + 672 * 8 + l56 * 64;
      u32x4 g0 = __builtin_amdgcn_raw_buffer_load_b128(rs, mo, 0, AUX_SC1), g1 = __builtin_amdgcn_raw_buffer_load_b128(rs, mo + 16, 0, AUX_SC1);
      u32x4 g2 = __builtin_amdgcn_raw_buffer_load_b128(rs, mo + 32, 0, AUX_SC1), g3 = __builtin_amdgcn_raw_buffer_load_b128(rs, mo + 48, 0, AUX_SC1);
      SB0();
      gather_x2<X2_DELAY>(p, L, rs, pn, tid);
      stat_wait(rs, mo, g0, g1, tag32);
      stat_wait(rs, mo + 32, g2, g3, tag32);
      LAS unsigned char* mp = L + G_MOD + l56 * 64;
      *(LAS f32x4*)mp = f32x4{bf_lo(g0.x), bf_hi(g0.x), bf_lo(g0.z), bf_hi(g0.z)};
      *(LAS f32x4*)(mp + 16) = f32x4{bf_lo(g1.x), bf_hi(g1.x), bf_lo(g1.z), bf_hi(g1.z)};
      *(LAS f32x4*)(mp + 32) = f32x4{bf_lo(g2.x), bf_hi(g2.x), bf_lo(g2.z), bf_hi(g2.z)};
      *(LAS f32x4*)(mp + 48) = f32x4{bf_lo(g3.x), bf_hi(g3.x), bf_lo(g3.z), bf_hi(g3.z)};
    }
    STAMP(0, 1);
    bar_lds();
    const int fo = (int)WS_FLAGC + (lane & 31) * 8;
    const u32x2 fc = flag_load(rs, fo);
    ln_phase_a(L, j, w, r16, q, [](auto) {});
    STAMP(0, 2);
    bar_lds();
    flag_wait(rs, fo, fc, ~tlo, ~thi, ~0u);
    const unsigned char* src = p.ws + WS_WP + (size_t)(j * 7) * NFRAG * 1024 + NRING * 1024 + voff;
#define DMA_HALF(h)                                                                                                         \
  _Pragma("unroll") for (int ww = 0; ww < 7; ++ww) _Pragma("unroll") for (int i = (h) * (NLDS / 2); i < ((h) + 1) * (NLDS / 2); ++i) \
      __builtin_amdgcn_global_load_lds((const GAS unsigned*)(src + (size_t)ww * NFRAG * 1024 + i * 1024),                     \
                                       (LAS unsigned*)(L + G_WL + (ww * NLDS + i) * 1024), 16, 0, AUX_SC1);
    DMA_HALF(0)
    bar_lds();
    STAMP(0, 4);
    bar_lds();
    DMA_HALF(1)
#undef DMA_HALF
    bar_lds();
    {
      int gof[3];
      u32x4 xr[3];
      x1_offsets(pn, j, w, r16, q, gof);
      poll_load<3>(rs, gof, xr);
      ln_phase_c(L, xr, j, w, r16, q);
    }
    asm volatile("s_waitcnt vmcnt(0)" ::: "memory");
    STAMP(0, 7);
    bar_lds();
    return;
  }
  __builtin_amdgcn_s_setprio(1);
  SB0();
  gather_x2<X2_DELAY>(p, L, rs, pn, tid);
  STAMP(0, 1);
  bar_lds();
  STAMP(0, 27);
  u32x2 h2r[2], x2r[2], x1r[2];
  const int ncol = j * 112 + w * 16 + q * 4;
  const int gbo = (int)WS_MODG + (5 * 224 + (ncol >> 1)) * 8;
  u32x4 gbg = __builtin_amdgcn_raw_buffer_load_b128(rs, gbo, 0, AUX_SC1);
#pragma unroll
  for (int T = 0; T < 2; ++T) {
    const int row = 16 * T + r16;
    x2r[T] = *(const LAS u32x2*)(L + G_X + row * 1024 + (((ncol >> 3) ^ (row >> 2)) * 16) + (ncol & 4) * 2);
  }
  SB0();
  ring_fill<0, 8>(ring, rw, voff);
  ln_phase_a(L, j, w, r16, q, [&](auto k) { SB0(); ring_fill<8 + 6 * decltype(k)::value, 6>(ring, rw, voff); });
  SB0();
  ring_fill<26, 6>(ring, rw, voff);
  STAMP(0, 2);
  bar_lds();
  u32x2 gbp;
  {
    u32x4 dup = gbg;
    if (__builtin_expect(__any(((gbg.y ^ tag32) | (gbg.w ^ tag32)) != 0u), 0)) {
      for (unsigned spins = 0; spins < SPIN_CAP; ++spins) {
        asm volatile("" ::: "memory");
        dup = __builtin_amdgcn_raw_buffer_load_b128(rs, gbo, 0, AUX_SC1);
        if (!__any(((dup.y ^ tag32) | (dup.w ^ tag32)) != 0u)) break;
      }
    }
    gbp = u32x2{dup.x, dup.z};
  }
#pragma unroll
  for (int T = 0; T < 2; ++T) h2r[T] = *(const LAS u32x2*)(L + G_HQ + (16 * T + r16) * 240 + (w * 16 + q * 4) * 2);
  const LAS unsigned char* ap0 = L + G_T0 + r16 * AS + q * 16;
  const LAS unsigned char* ap1 = L + G_T1 + r16 * AS + q * 16;
  const int tok = (q & 1) * 16 + r16, octc = 2 * w + (q >> 1);
  {
    f32x4 acc[1][2];
    STAMP(0, 16);
    gemm14<1, 0, 32, 7, 2, 5>(ap0, ring, rw, voff, acc);
    STAMP(0, 17);
    const u32x4 own = make_piece(gelu4(acc[0][0]), gelu4(acc[0][1]));
    __builtin_amdgcn_raw_buffer_store_b128(own, rs, (int)WS_G + (pn * 4 + j) * 7168 + pslot, 0, AUX_SC1);
    SB0();
    STAMP(0, 18);
    ring_fill<39, 4>(ring, rw, voff);
    int goff[4], loff[4];
    bool wrap0;
#pragma unroll
    for (int i = 0; i < 4; ++i) {
      const int jq = (j + i) & 3;
      const int sh = (jq & 1) ? -1 - (jq >> 1) : 1 + (jq >> 1);
      const int it = tok + sh;
      const int sp = pn + (it >= 32 ? -1 : (it < 0 ? 1 : 0));
      if (i == 0) wrap0 = sp != pn;
      goff[i] = (sp < 0 || sp > 63 || (i == 0 && sp == pn)) ? (int)WS_END : (int)WS_G + (sp * 4 + jq) * 7168 + pslot;
      loff[i] = G_T1 + (it & 31) * AS + (jq * 14 + octc) * 16;
    }
    STAMP(0, 3);
    bar_lds();
    u32x4 v[4];
    poll_issue<4>(rs, goff, v);
    __builtin_amdgcn_s_sleep(3);
    ring_fill<43, 5>(ring, rw, voff);
    poll_finish<4>(rs, goff, v);
    *(LAS u32x4*)(L + loff[0]) = wrap0 ? v[0] : own;
#pragma unroll
    for (int i = 1; i < 4; ++i) *(LAS u32x4*)(L + loff[i]) = v[i];
  }
  STAMP(0, 4);
  bar_lds();
  {
    f32x4 acc[1][2];
    gemm14<1, 14, 48, 7, 2, 5>(ap1, ring, rw, voff, acc);
#pragma unroll
    for (int T = 0; T < 2; ++T) {
      const unsigned a = pk_bf16(acc[0][T][0], acc[0][T][1]), b = pk_bf16(acc[0][T][2], acc[0][T][3]);
      x1r[T].x = pk_bf16(bf_lo(h2r[T].x) + bf_lo(a), bf_hi(h2r[T].x) + bf_hi(a));
      x1r[T].y = pk_bf16(bf_lo(h2r[T].y) + bf_lo(b), bf_hi(h2r[T].y) + bf_hi(b));
    }
    const u32x4 own = make_piece(x1r[0], x1r[1]);
    __builtin_amdgcn_raw_buffer_store_b128(own, rs, (int)WS_X1 + (pn * 4 + j) * 7168 + (w * 64 + (tok >> 2) * 8 + (q >> 1) * 4 + (tok & 3)) * 16, 0, AUX_SC1);
    *(LAS u32x4*)(L + G_HQ + tok * 240 + octc * 16) = own;
    SB0();
    STAMP(0, 19);
    ring_fill<55, 4>(ring, rw, voff);
    int gof[3];
    u32x4 xr[3];
    x1_offsets(pn, j, w, r16, q, gof);
    STAMP(0, 5);
    bar_lds();
    poll_issue<3>(rs, gof, xr);
    __builtin_amdgcn_s_sleep(3);
    ring_fill<59, 5>(ring, rw, voff);
    poll_finish<3>(rs, gof, xr);
    STAMP(0, 6);
    ring_fill<64, 4>(ring, rw, voff);
    ln_phase_c(L, xr, j, w, r16, q);
    SB0();
    ring_fill<68, 4>(ring, rw, voff);
  }
  STAMP(0, 7);
  bar_lds();
  {
    {
      f32x4 ca[2][2], cb2[2][2], acc[3][2];
      LAS unsigned char* hp = L + G_T1 + r16 * AS + (w * 64 + q * 4) * 2;
      unsigned glo;
      STAMP(0, 12);
      gemm_p<2, 28, 0, 1, 14, 72, 19, 3, true>(ap0, ring, rw, voff, ca, [](auto) {});
      gemm_p<2, 56, 0, 1, 14, 91, 19, 3, true>(ap0, ring, rw, voff, cb2, [&](auto stc) {
        constexpr int st = decltype(stc)::value;
        if constexpr (st % 3 == 1 && st / 3 < 8) {
          constexpr int c = st / 3, i = c >> 2, T = (c >> 1) & 1;
          if constexpr ((c & 1) == 0) glo = gelu2(ca[i][T][0], ca[i][T][1]);
          else *(LAS u32x2*)(hp + T * 16 * AS + i * 32) = u32x2{glo, gelu2(ca[i][T][2], ca[i][T][3])};
        }
      });
      STAMP(0, 13);
      bar_lds();
      gemm_p<3, 84, 0, 2, 7, 110, 18, 3, true>(ap1, ring, rw, voff, acc, [&](auto stc) {
        constexpr int st = decltype(stc)::value;
        if constexpr (st % 2 == 1 && st / 2 < 8) {
          constexpr int c = st / 2, i = c >> 2, T = (c >> 1) & 1;
          if constexpr ((c & 1) == 0) glo = gelu2(cb2[i][T][0], cb2[i][T][1]);
          else *(LAS u32x2*)(hp + T * 16 * AS + (2 + i) * 32) = u32x2{glo, gelu2(cb2[i][T][2], cb2[i][T][3])};
        }
      });
      STAMP(0, 8);
      bar_lds();
      STAMP(0, 14);
      gemm_p<3, 105, 1, 2, 7, 0, 0, 3, false>(ap1, ring, rw, voff, acc, [](auto) {});
      STAMP(0, 15);
#pragma unroll
      for (int i = 0; i < 3; ++i) {
        const int jd = (j + 1 + i) & 3;
        const u32x4 o = {sane(pk_bf16(acc[i][0][0], acc[i][0][1])), sane(pk_bf16(acc[i][0][2], acc[i][0][3])),
                         sane(pk_bf16(acc[i][1][0], acc[i][1][1])), sane(pk_bf16(acc[i][1][2], acc[i][1][3]))};
        __builtin_amdgcn_raw_buffer_store_b128(o, rs, (int)WS_P + ((pn * 4 + jd) * 3 + (2 - i)) * 7168 + pslot, 0, AUX_SC1);
      }
      SB0();
    }
    STAMP(0, 9);
    f32x4 acc0 = {0.f, 0.f, 0.f, 0.f}, acc1 = acc0;
    u32x4 v[3];
    const int go = (int)WS_P + (pn * 4 + j) * 3 * 7168 + pslot;
    const int gof[3] = {go, go + 7168, go + 2 * 7168};
    {
      const LAS unsigned char* wl = L + G_WL + w * (NLDS * 1024) + voff;
      u32x4 wa[14], b0[14], b1[14];
#pragma unroll
      for (int ks = 0; ks < 14; ++ks) {
        const int f = 126 + ks;
        wa[ks] = f < NRING ? ring[f % RING] : *(const LAS u32x4*)(wl + (f - NRING) * 1024);
        b0[ks] = *(const LAS u32x4*)(ap1 + ks * 64);
        b1[ks] = *(const LAS u32x4*)(ap1 + 16 * AS + ks * 64);
      }
      SB0();
#pragma unroll
      for (int ks = 0; ks < 14; ++ks) {
        acc0 = mfma16(wa[ks], b0[ks], acc0);
        acc1 = mfma16(wa[ks], b1[ks], acc1);
      }
      SB0();
    }
    STAMP(0, 10);
    poll_load<3>(rs, gof, v);
    STAMP(0, 11);
#pragma unroll
    for (int T = 0; T < 2; ++T) {
      const f32x4 ac = T ? acc1 : acc0;
      float hh[4];
#pragma unroll
      for (int k = 0; k < 2; ++k) {
        hh[2 * k] = ac[2 * k] + ((bf_lo(v[0][2 * T + k]) + bf_lo(v[1][2 * T + k])) + bf_lo(v[2][2 * T + k]));
        hh[2 * k + 1] = ac[2 * k + 1] + ((bf_hi(v[0][2 * T + k]) + bf_hi(v[1][2 * T + k])) + bf_hi(v[2][2 * T + k]));
      }
      unsigned a = pk_bf16(hh[0], hh[1]), b = pk_bf16(hh[2], hh[3]);
      a = pk_bf16(bf_lo(x1r[T].x) + bf_lo(a), bf_hi(x1r[T].x) + bf_hi(a));
      b = pk_bf16(bf_lo(x1r[T].y) + bf_lo(b), bf_hi(x1r[T].y) + bf_hi(b));
      a = pk_bf16(bf_lo(gbp.x) * bf_lo(a), bf_hi(gbp.x) * bf_hi(a));
      b = pk_bf16(bf_lo(gbp.y) * bf_lo(b), bf_hi(gbp.y) * bf_hi(b));
      u32x2 o;
      o.x = pk_bf16(bf_lo(x2r[T].x) + bf_lo(a), bf_hi(x2r[T].x) + bf_hi(a));
      o.y = pk_bf16(bf_lo(x2r[T].y) + bf_lo(b), bf_hi(x2r[T].y) + bf_hi(b));
      *(u32x2*)(p.out + (size_t)(pn * 32 + 16 * T + r16) * D_ + ncol) = o;
    }
  }
  STAMP(0, 20);
}

__global__ __launch_bounds__(512, 2) void k_all(GArgs p) {
  extern __shared__ __attribute__((aligned(16))) unsigned char smem[];
  const uintptr_t code0 = (uintptr_t)__builtin_amdgcn_s_getpc() & ~(uintptr_t)127;
  body(p, (LAS unsigned char*)smem, code0);
}

}

extern "C" void kernel_launch(void* const* d_in, const int* in_sizes, int n_in, void* d_out, int out_size, void* d_ws,
                              size_t ws_size, hipStream_t stream) {
  (void)in_sizes; (void)n_in; (void)out_size; (void)ws_size;
  GArgs a;
  a.x = (const unsigned short*)d_in[0];
  a.a_re = (const unsigned short*)d_in[1];
  a.a_im = (const unsigned short*)d_in[2];
  a.b_re = (const unsigned short*)d_in[3];
  a.b_im = (const unsigned short*)d_in[4];
  a.c_re = (const unsigned short*)d_in[5];
  a.c_im = (const unsigned short*)d_in[6];
  a.log_dt = (const unsigned short*)d_in[7];
  a.dskip = (const unsigned short*)d_in[8];
  a.w1 = (const unsigned short*)d_in[10];
  a.w2 = (const unsigned short*)d_in[11];
  a.w3 = (const unsigned short*)d_in[12];
  a.w4 = (const unsigned short*)d_in[13];
  a.cvec = (const unsigned short*)d_in[14];
  a.wmod = (const unsigned short*)d_in[15];
  a.ws = (unsigned char*)d_ws;
  a.out = (unsigned short*)d_out;
  (void)hipFuncSetAttribute((const void*)k_all, hipFuncAttributeMaxDynamicSharedMemorySize, G_LDS);
  k_all<<<256, 512, G_LDS, stream>>>(a);
}
```

```cpp
#include <hip/hip_runtime.h>
#include <hip/hip_bf16.h>
#include <stdint.h>
#include <type_traits>
#include <utility>

namespace {

typedef __attribute__((ext_vector_type(8))) short bf16x8;
typedef __attribute__((ext_vector_type(8))) _Float16 f16x8;
typedef __attribute__((ext_vector_type(2))) _Float16 f16x2;
typedef __attribute__((ext_vector_type(4))) float f32x4;
typedef __attribute__((ext_vector_type(2))) float f32x2;
typedef __attribute__((ext_vector_type(4))) unsigned u32x4;
typedef __attribute__((ext_vector_type(2))) unsigned u32x2;
typedef __attribute__((ext_vector_type(2))) __bf16 bf16x2v;
#define GAS __attribute__((address_space(1)))
#define LAS __attribute__((address_space(3)))

constexpr int L_ = 2048, D_ = 448, NS_ = 256;

constexpr size_t WS_EPOCH = 0;
constexpr size_t WS_FLAGC = 4608;
constexpr size_t WS_MODG = 16384;
constexpr size_t WS_STG = 32768;
constexpr int NFRAG = 140;
constexpr size_t WS_XG = 65536;
constexpr size_t XG_BLK = 224 * 64;
constexpr size_t WS_WP = WS_XG + 256 * XG_BLK;
constexpr size_t WS_X2B = WS_WP + (size_t)28 * NFRAG * 1024;
constexpr size_t X2B_BLK = 224 * 128;
constexpr size_t WS_G = WS_X2B + 64 * X2B_BLK;
constexpr size_t WS_X1 = WS_G + (size_t)64 * 4 * 7168;
constexpr size_t WS_P = WS_X1 + (size_t)64 * 4 * 7168;
constexpr size_t WS_END = WS_P + (size_t)64 * 12 * 7168;

#define STAMP(base, k) do { } while (0)

__device__ __forceinline__ float bf_lo(unsigned u) { return __builtin_bit_cast(float, u << 16); }
__device__ __forceinline__ float bf_hi(unsigned u) { return __builtin_bit_cast(float, u & 0xffff0000u); }
__device__ __forceinline__ float bf2f(unsigned short h) { return __builtin_bit_cast(float, (unsigned)h << 16); }
__device__ __forceinline__ unsigned pk_bf16(float a, float b) {
  f32x2 v = {a, b};
  bf16x2v r = __builtin_convertvector(v, bf16x2v);
  return __builtin_bit_cast(unsigned, r);
}
__device__ __forceinline__ unsigned pk_f16(f32x2 v) {
  f16x2 r = __builtin_convertvector(v, f16x2);
  return __builtin_bit_cast(unsigned, r);
}
__device__ __forceinline__ float rbf(float a) { return bf_lo(pk_bf16(a, a)); }
__device__ __forceinline__ f32x4 mfma16(u32x4 a, u32x4 b, f32x4 c) {
  return __builtin_amdgcn_mfma_f32_16x16x32_bf16(__builtin_bit_cast(bf16x8, a), __builtin_bit_cast(bf16x8, b), c, 0, 0, 0);
}
__device__ __forceinline__ f32x4 mfma16h(u32x4 a, u32x4 b, f32x4 c) {
  return __builtin_amdgcn_mfma_f32_16x16x32_f16(__builtin_bit_cast(f16x8, a), __builtin_bit_cast(f16x8, b), c, 0, 0, 0);
}
template <int CTRL>
__device__ __forceinline__ float dpp_f(float v) {
  return __builtin_bit_cast(float, __builtin_amdgcn_update_dpp(0, __builtin_bit_cast(int, v), CTRL, 0xf, 0xf, true));
}
__device__ __forceinline__ float row16_sum(float v) {
  v += dpp_f<0xB1>(v);
  v += dpp_f<0x4E>(v);
  v += dpp_f<0x141>(v);
  v += dpp_f<0x140>(v);
  return v;
}
__device__ __forceinline__ float row16_max(float v) {
  v = fmaxf(v, dpp_f<0xB1>(v));
  v = fmaxf(v, dpp_f<0x4E>(v));
  v = fmaxf(v, dpp_f<0x141>(v));
  v = fmaxf(v, dpp_f<0x140>(v));
  return v;
}
__device__ __forceinline__ void swap16(float v, float& a, float& b) {
  const unsigned u = __builtin_bit_cast(unsigned, v), u2 = u;
  auto r = __builtin_amdgcn_permlane16_swap(u, u2, false, false);
  const unsigned r0 = r[0], r1 = r[1];
  a = __builtin_bit_cast(float, r0); b = __builtin_bit_cast(float, r1);
}
__device__ __forceinline__ void swap32(float v, float& a, float& b) {
  const unsigned u = __builtin_bit_cast(unsigned, v), u2 = u;
  auto r = __builtin_amdgcn_permlane32_swap(u, u2, false, false);
  const unsigned r0 = r[0], r1 = r[1];
  a = __builtin_bit_cast(float, r0); b = __builtin_bit_cast(float, r1);
}
__device__ __forceinline__ float rows_sum(float v) {
  float a, b;
  swap16(v, a, b); v = a + b;
  swap32(v, a, b);
  return a + b;
}
__device__ __forceinline__ float rows_max(float v) {
  float a, b;
  swap16(v, a, b); v = fmaxf(a, b);
  swap32(v, a, b);
  return fmaxf(a, b);
}
template <int CTRL, int RMASK>
__device__ __forceinline__ float dpp_z(float v) {
  return __builtin_bit_cast(float, __builtin_amdgcn_update_dpp(0, __builtin_bit_cast(int, v), CTRL, RMASK, 0xf, false));
}
__device__ __forceinline__ float wave_scan(float v) {
  v += dpp_z<0x111, 0xf>(v);
  v += dpp_z<0x112, 0xf>(v);
  v += dpp_z<0x114, 0xf>(v);
  v += dpp_z<0x118, 0xf>(v);
  v += dpp_z<0x142, 0xa>(v);
  v += dpp_z<0x143, 0xc>(v);
  return v;
}
__device__ __forceinline__ float wave_sum(float v) { return rows_sum(row16_sum(v)); }
__device__ __forceinline__ float gelu_tanh(float x) {
  const float c1 = -2.0f * 0.7978845608028654f * 1.4426950408889634f;
  const float c2 = c1 * 0.044715f;
  float t = x * (c1 + c2 * x * x);
  float e = __builtin_amdgcn_exp2f(t);
  return x * __builtin_amdgcn_rcpf(1.0f + e);
}
__device__ __forceinline__ float ln_sd(float var) { return rbf(__builtin_amdgcn_sqrtf(rbf(rbf(var) + 1e-5f))); }
__device__ __forceinline__ void lds_fence() { asm volatile("" ::: "memory"); }
__device__ __forceinline__ void bar_lds() {
  asm volatile("s_waitcnt lgkmcnt(0)" ::: "memory");
  __builtin_amdgcn_s_barrier();
  asm volatile("" ::: "memory");
}

__device__ __forceinline__ void transpose8x8(const u32x4 (&r)[8], u32x4 (&o)[8]) {
#pragma unroll
  for (int c2 = 0; c2 < 4; ++c2) {
#pragma unroll
    for (int rp = 0; rp < 4; ++rp) {
      unsigned a = r[2 * rp][c2], b = r[2 * rp + 1][c2];
      o[2 * c2][rp] = __builtin_amdgcn_perm(b, a, 0x05040100u);
      o[2 * c2 + 1][rp] = __builtin_amdgcn_perm(b, a, 0x07060302u);
    }
  }
}
__device__ __forceinline__ f32x2 cmul(f32x2 a, f32x2 b) {
  f32x2 r;
  asm("v_pk_mul_f32 %0, %1, %2 op_sel_hi:[0,1]\n\tv_pk_fma_f32 %0, %1, %2, %0 op_sel:[1,1,0] op_sel_hi:[1,0,1] neg_lo:[1,0,0]"
      : "=&v"(r) : "v"(a), "v"(b));
  return r;
}
__device__ __forceinline__ f32x2 cexp_tab(float a, float b, float l) {
  const float e = __builtin_amdgcn_exp2f(a * l), ph = __builtin_amdgcn_fractf(b * l);
  f32x2 r;
  r.x = e * __builtin_amdgcn_cosf(ph);
  r.y = e * __builtin_amdgcn_sinf(ph);
  return r;
}

constexpr int CV_KCOPY = 8 * 272 * 16;
constexpr int CV_UROW = 160;
constexpr int CV_U = 48 * CV_UROW;
constexpr int CV_CH = CV_KCOPY + CV_U;
constexpr int CV_LDS = 2 * CV_CH;
constexpr int CV_PROW = 272;
static_assert(4 * 32 * CV_PROW <= CV_KCOPY, "partials alias the kernel copies");

struct GArgs {
  const unsigned short* x;
  const unsigned short* a_re;
  const unsigned short* a_im;
  const unsigned short* b_re;
  const unsigned short* b_im;
  const unsigned short* c_re;
  const unsigned short* c_im;
  const unsigned short* log_dt;
  const unsigned short* dskip;
  const unsigned short* w1;
  const unsigned short* w2;
  const unsigned short* w3;
  const unsigned short* w4;
  const unsigned short* cvec;
  const unsigned short* wmod;
  unsigned char* ws;
  unsigned short* out;
};
constexpr int AS = 928;
constexpr int G_T0 = 0;
constexpr int G_T1 = G_T0 + 32 * AS;
constexpr int G_X = G_T1;
constexpr int G_HQ = G_X + 32768;
constexpr int G_WL = G_HQ + 32 * 240;
constexpr int NLDS = 12;
constexpr int NRING = NFRAG - NLDS;
constexpr int G_MOD = G_WL + 7 * NLDS * 1024;
constexpr int G_CNT = G_MOD + 2 * 448 * 4;
constexpr int P0_MS = G_CNT + 16;
constexpr int G_LDS = P0_MS + 256;
constexpr int KG_ROW = 288;
constexpr int KG_BUF = 16 * KG_ROW;
constexpr int P0_KG = CV_LDS;
constexpr int P0_LN = 0;
constexpr int LNS = 928;
constexpr int P0_PK = 16384;
static_assert(P0_KG + 16 * KG_BUF <= G_CNT, "tables");
static_assert(G_MOD >= CV_LDS && G_LDS <= 163840, "LDS map");
constexpr int RING = 44;
constexpr int CV_P = CV_LDS;
static_assert(CV_P + 8 * 32 * CV_PROW <= G_MOD, "partials");
constexpr unsigned SPIN_CAP = 1u << 17;
constexpr int AUX_SC1 = 16;
constexpr int CODE_LINES = 320;
#define PRETOUCH 1
#define X2_DELAY 20
#define POLL_SLEEP 1

__device__ __forceinline__ unsigned sane(unsigned v) { return v < 0xFFFFFFFEu ? v : 0xFFFFFFFEu; }
__device__ __forceinline__ unsigned umax4(const u32x4& v) { return max(max(v.x, v.y), max(v.z, v.w)); }
__device__ __forceinline__ bool bad4(const u32x4& v) { return umax4(v) == ~0u; }
__device__ __forceinline__ u32x4 make_piece(u32x2 a, u32x2 b) {
  auto rx = __builtin_amdgcn_permlane16_swap(a.x, b.x, false, false);
  auto ry = __builtin_amdgcn_permlane16_swap(a.y, b.y, false, false);
  return u32x4{sane(rx[0]), sane(ry[0]), sane(rx[1]), sane(ry[1])};
}
template <int N>
__device__ __forceinline__ void poll_issue(__amdgpu_buffer_rsrc_t r, const int (&off)[N], u32x4 (&v)[N]) {
#pragma unroll
  for (int i = 0; i < N; ++i) v[i] = __builtin_amdgcn_raw_buffer_load_b128(r, off[i], 0, AUX_SC1);
  __builtin_amdgcn_sched_barrier(0);
}
template <int N>
__device__ __forceinline__ void poll_finish(__amdgpu_buffer_rsrc_t r, const int (&off)[N], u32x4 (&v)[N]) {
  unsigned m = 0u;
#pragma unroll
  for (int i = 0; i < N; ++i) m = max(m, umax4(v[i]));
  if (__builtin_expect(__any(m == ~0u), 0)) {
    for (unsigned spins = 0; spins < SPIN_CAP; ++spins) {
      __builtin_amdgcn_s_sleep(POLL_SLEEP);
      asm volatile("" ::: "memory");
#pragma unroll
      for (int i = 0; i < N; ++i)
        if (bad4(v[i])) v[i] = __builtin_amdgcn_raw_buffer_load_b128(r, off[i], 0, AUX_SC1);
      m = 0u;
#pragma unroll
      for (int i = 0; i < N; ++i) m = max(m, umax4(v[i]));
      if (!__any(m == ~0u)) break;
    }
  }
}
template <int N>
__device__ __forceinline__ void poll_load(__amdgpu_buffer_rsrc_t r, const int (&off)[N], u32x4 (&v)[N]) {
  poll_issue<N>(r, off, v);
  poll_finish<N>(r, off, v);
}
__device__ __forceinline__ float dot2bf(unsigned a, unsigned b, float c) {
  return __builtin_amdgcn_fdot2_f32_bf16(__builtin_bit_cast(bf16x2v, a), __builtin_bit_cast(bf16x2v, b), c, false);
}
__device__ __forceinline__ void ln_stats(const unsigned (&x)[16], float lmsk, float& r, float& mr) {
  float s0 = 0.f, s1 = 0.f, q0 = 0.f, q1 = 0.f;
#pragma unroll
  for (int k = 0; k < 16; k += 2) {
    s0 = dot2bf(x[k], 0x3f803f80u, s0); q0 = dot2bf(x[k], x[k], q0);
    s1 = dot2bf(x[k + 1], 0x3f803f80u, s1); q1 = dot2bf(x[k + 1], x[k + 1], q1);
  }
  const float mean = row16_sum((s0 + s1) * lmsk) * (1.0f / D_);
  const float msq = row16_sum((q0 + q1) * lmsk) * (1.0f / D_);
  const float var = fmaxf(msq - mean * mean, 0.f);
  r = __builtin_amdgcn_rcpf(ln_sd(var));
  mr = -rbf(mean) * r;
}

template <class F, int... I>
__device__ __forceinline__ void static_for_impl(F f, std::integer_sequence<int, I...>) { (f(std::integral_constant<int, I>{}), ...); }
template <int N, class F>
__device__ __forceinline__ void static_for(F f) { static_for_impl(f, std::make_integer_sequence<int, N>{}); }
#define SB0() __builtin_amdgcn_sched_barrier(0)
#define WLOAD(f) __builtin_amdgcn_raw_buffer_load_b128(rw, voff + ((f) & 3) * 1024, ((f) >> 2) * 4096, AUX_SC1)
template <int NT, int F0, int LSTART, int LNUM, int LEVERY, int PD>
__device__ __forceinline__ void gemm14(const LAS unsigned char* ap, u32x4 (&ring)[RING], __amdgpu_buffer_rsrc_t rw, int voff, f32x4 (&acc)[NT][2]) {
  u32x4 bq[PD][2];
#pragma unroll
  for (int i = 0; i < PD; ++i) { bq[i][0] = *(const LAS u32x4*)(ap + i * 64); bq[i][1] = *(const LAS u32x4*)(ap + 16 * AS + i * 64); }
#pragma unroll
  for (int i = 0; i < NT; ++i) { acc[i][0] = f32x4{0.f, 0.f, 0.f, 0.f}; acc[i][1] = acc[i][0]; }
  SB0();
#pragma unroll
  for (int ks = 0; ks < 14; ++ks) {
#pragma unroll
    for (int i = 0; i < NT; ++i) {
      const int st = ks * NT + i, f = F0 + st;
      acc[i][0] = mfma16(ring[f % RING], bq[ks % PD][0], acc[i][0]);
      acc[i][1] = mfma16(ring[f % RING], bq[ks % PD][1], acc[i][1]);
      if (st % LEVERY == 0 && st / LEVERY < LNUM) {
        const int g = LSTART + st / LEVERY;
        ring[g % RING] = WLOAD(g);
      }
      if (i == NT - 1 && ks + PD < 14) {
        bq[ks % PD][0] = *(const LAS u32x4*)(ap + (ks + PD) * 64);
        bq[ks % PD][1] = *(const LAS u32x4*)(ap + 16 * AS + (ks + PD) * 64);
      }
      SB0();
    }
  }
}
template <int NT, int F0, int KS0, int KSTEP, int NKS, int LSTART, int LNUM, int PD, bool INIT, class S>
__device__ __forceinline__ void gemm_p(const LAS unsigned char* ap, u32x4 (&ring)[RING], __amdgpu_buffer_rsrc_t rw, int voff, f32x4 (&acc)[NT][2], S side) {
  u32x4 bq[PD][2];
#pragma unroll
  for (int i = 0; i < PD; ++i) {
    bq[i][0] = *(const LAS u32x4*)(ap + (KS0 + i * KSTEP) * 64);
    bq[i][1] = *(const LAS u32x4*)(ap + 16 * AS + (KS0 + i * KSTEP) * 64);
  }
  if (INIT) {
#pragma unroll
    for (int i = 0; i < NT; ++i) { acc[i][0] = f32x4{0.f, 0.f, 0.f, 0.f}; acc[i][1] = acc[i][0]; }
  }
  SB0();
  static_for<NKS * NT>([&](auto stc) {
    constexpr int st = decltype(stc)::value, k = st / NT, i = st % NT, f = F0 + st;
    acc[i][0] = mfma16(ring[f % RING], bq[k % PD][0], acc[i][0]);
    acc[i][1] = mfma16(ring[f % RING], bq[k % PD][1], acc[i][1]);
    constexpr int l0 = st * LNUM / (NKS * NT), l1 = (st + 1) * LNUM / (NKS * NT);
    if constexpr (l1 > l0) {
      static_assert(LSTART + l0 - RING <= f, "slot still in use");
      ring[(LSTART + l0) % RING] = WLOAD(LSTART + l0);
    }
    if constexpr (i == NT - 1 && k + PD < NKS) {
      bq[k % PD][0] = *(const LAS u32x4*)(ap + (KS0 + (k + PD) * KSTEP) * 64);
      bq[k % PD][1] = *(const LAS u32x4*)(ap + 16 * AS + (KS0 + (k + PD) * KSTEP) * 64);
    }
    side(stc);
    SB0();
  });
}
template <int F0, int N>
__device__ __forceinline__ void ring_fill(u32x4 (&ring)[RING], __amdgpu_buffer_rsrc_t rw, int voff) {
#pragma unroll
  for (int f = F0; f < F0 + N; ++f) { ring[f % RING] = WLOAD(f); SB0(); }
}

template <int DELAY>
__device__ __forceinline__ void gather_x2(const GArgs& p, LAS unsigned char* L, __amdgpu_buffer_rsrc_t rs, int pn, int tid) {
  const int lane = tid & 63, w = tid >> 6; (void)lane; (void)w; (void)p;
  u32x4 xin[4];
  int xo[4];
#pragma unroll
  for (int i = 0; i < 4; ++i) {
    const int pce = tid + 512 * i;
    xo[i] = pce < 1792 ? (int)WS_X2B + pn * (int)X2B_BLK + pce * 16 : (int)WS_END;
  }
  if (DELAY) __builtin_amdgcn_s_sleep(DELAY);
  poll_issue<4>(rs, xo, xin);
  poll_finish<4>(rs, xo, xin);
  STAMP(0, 26);
#pragma unroll
  for (int i = 0; i < 4; ++i) {
    const int pce = tid + 512 * i;
    if (i < 3 || pce < 1792) {
      const int dpi = pce >> 3, tq = pce & 7;
      LAS unsigned char* dst = L + G_X + tq * 4096 + (((dpi >> 2) ^ tq) * 16) + (dpi & 3) * 4;
#pragma unroll
      for (int k = 0; k < 4; ++k) *(LAS unsigned*)(dst + k * 1024) = xin[i][k];
    }
  }
}
template <class H>
__device__ __forceinline__ void ln_phase_a(LAS unsigned char* L, int j, int w, int r16, int q, H hook) {
  const int c16 = r16 < 14 ? r16 : 0;
  const float lmsk = r16 < 14 ? 1.0f : 0.0f;
  const int row = 4 * w + q;
  unsigned x[16];
#pragma unroll
  for (int jq = 0; jq < 4; ++jq) {
    const u32x4 t = *(const LAS u32x4*)(L + G_X + row * 1024 + (((((jq + j) & 3) * 14 + c16) ^ w) * 16));
    x[4 * jq] = t.x; x[4 * jq + 1] = t.y; x[4 * jq + 2] = t.z; x[4 * jq + 3] = t.w;
  }
  float r, mr;
  ln_stats(x, lmsk, r, mr);
  hook(std::integral_constant<int, 0>{});
  f32x2 h[16];
#pragma unroll
  for (int jq = 0; jq < 4; ++jq) {
    const LAS unsigned char* mp = L + G_MOD + (((jq + j) & 3) * 112 + c16 * 8) * 4;
    const f32x4 sb0 = *(const LAS f32x4*)mp, sb1 = *(const LAS f32x4*)(mp + 16);
    const f32x4 c10 = *(const LAS f32x4*)(mp + 1792), c11 = *(const LAS f32x4*)(mp + 1792 + 16);
#pragma unroll
    for (int k = 0; k < 4; ++k) {
      const f32x2 xv2 = {bf_lo(x[4 * jq + k]), bf_hi(x[4 * jq + k])};
      const f32x2 c2 = k < 2 ? f32x2{c10[2 * k], c10[2 * k + 1]} : f32x2{c11[2 * k - 4], c11[2 * k - 3]};
      const f32x2 sb2 = k < 2 ? f32x2{sb0[2 * k], sb0[2 * k + 1]} : f32x2{sb1[2 * k - 4], sb1[2 * k - 3]};
      const f32x2 n = xv2 * r + mr;
      h[4 * jq + k] = n * c2 + sb2;
      x[4 * jq + k] = pk_bf16(h[4 * jq + k].x, h[4 * jq + k].y);
    }
  }
  *(LAS u32x4*)(L + G_HQ + row * 240 + (r16 < 14 ? r16 : 14) * 16) = u32x4{x[0], x[1], x[2], x[3]};
  hook(std::integral_constant<int, 1>{});
  ln_stats(x, lmsk, r, mr);
  hook(std::integral_constant<int, 2>{});
#pragma unroll
  for (int jq = 0; jq < 4; ++jq) {
    u32x4 o;
#pragma unroll
    for (int k = 0; k < 4; ++k) {
      const f32x2 n = h[4 * jq + k] * r + mr;
      o[k] = pk_bf16(n.x, n.y);
    }
    *(LAS u32x4*)(L + G_T0 + row * AS + (r16 < 14 ? (((jq + j) & 3) * 14 + r16) * 16 : 896)) = o;
  }
}
__device__ __forceinline__ void x1_offsets(int pn, int j, int w, int r16, int q, int (&gof)[3]) {
  const int c16 = r16 < 14 ? r16 : 0;
  const int go = (int)WS_X1 + pn * 4 * 7168 + ((c16 >> 1) * 64 + w * 8 + (c16 & 1) * 4 + q) * 16;
#pragma unroll
  for (int i = 0; i < 3; ++i) gof[i] = go + ((j + 1 + i) & 3) * 7168;
}
__device__ __forceinline__ void ln_phase_c(LAS unsigned char* L, const u32x4 (&xr)[3], int j, int w, int r16, int q) {
  const int c16 = r16 < 14 ? r16 : 0;
  const float lmsk = r16 < 14 ? 1.0f : 0.0f;
  const int row = 4 * w + q;
  const u32x4 own = *(const LAS u32x4*)(L + G_HQ + row * 240 + c16 * 16);
  unsigned x[16];
#pragma unroll
  for (int k = 0; k < 4; ++k) x[k] = own[k];
#pragma unroll
  for (int k = 4; k < 16; ++k) x[k] = xr[(k >> 2) - 1][k & 3];
  float r, mr;
  ln_stats(x, lmsk, r, mr);
#pragma unroll
  for (int jq = 0; jq < 4; ++jq) {
    u32x4 o;
#pragma unroll
    for (int k = 0; k < 4; ++k) {
      const f32x2 n = f32x2{bf_lo(x[4 * jq + k]), bf_hi(x[4 * jq + k])} * r + mr;
      o[k] = pk_bf16(n.x, n.y);
    }
    *(LAS u32x4*)(L + G_T0 + row * AS + (r16 < 14 ? (((jq + j) & 3) * 14 + r16) * 16 : 896)) = o;
  }
}
__device__ __forceinline__ unsigned gelu2(float a, float b) {
  return pk_bf16(gelu_tanh(a), gelu_tanh(b));
}
__device__ __forceinline__ u32x2 gelu4(f32x4 a) { return u32x2{gelu2(a[0], a[1]), gelu2(a[2], a[3])}; }

#define ST16(off, v) __builtin_amdgcn_raw_buffer_store_b128(v, rs, (int)(off), 0, AUX_SC1)
typedef unsigned long long u64;

struct PackJob { const GAS unsigned short* src; int ldn, mat, ks, cb, na, nb, fb; };
__device__ __forceinline__ PackJob pack_job(const GArgs& p, int g) {
  PackJob jb;
  const uintptr_t a1 = (uintptr_t)p.w1, a2 = (uintptr_t)p.w2, a3 = (uintptr_t)p.w3, a4 = (uintptr_t)p.w4;
  uintptr_t lo = g < 56 ? a1 : a2, hi = g < 336 ? a3 : a4;
  asm volatile("" : "+s"(lo), "+s"(hi));
  jb.src = (const GAS unsigned short*)(g < 112 ? lo : hi);
  jb.mat = (g >= 56) + (g >= 112) + (g >= 336);
  jb.ldn = jb.mat == 2 ? 1792 : 448;
  g -= g < 112 ? (g < 56 ? 0 : 56) : (g < 336 ? 112 : 336);
  const int sh = jb.mat == 2 ? 4 : 2;
  jb.ks = g >> sh;
  jb.cb = g & ((1 << sh) - 1);
  const int j3 = (jb.ks * 4682) >> 16, ksl = jb.ks - j3 * 14, sl = (jb.cb - j3 - 1) & 3;
  const int is2 = jb.mat == 2, is3 = jb.mat == 3;
  jb.na = is2 * jb.cb * 7 + (1 - is2) * (is3 * j3 + (1 - is3) * jb.cb) * 28;
  jb.nb = 4 - 3 * is2;
  const int own = sl == 3;
  const int f3 = (1 - own) * (84 + (ksl & 1) * 21 + (ksl >> 1) * 3 + sl) + own * (126 + ksl);
  jb.fb = is2 * (28 + jb.ks * 2) + is3 * f3 + (1 - is2 - is3) * (jb.mat * 14 + jb.ks);
  jb.na = __builtin_amdgcn_readfirstlane(jb.na); jb.nb = __builtin_amdgcn_readfirstlane(jb.nb); jb.fb = __builtin_amdgcn_readfirstlane(jb.fb);
  return jb;
}
__device__ __forceinline__ void pack_load(const PackJob& jb, int lane, u32x4 (&r)[8]) {
  const int jn = lane & 15, q = lane >> 4, jl = jn < 14 ? jn : 0;
#pragma unroll
  for (int i = 0; i < 8; ++i) r[i] = *(const GAS u32x4*)(jb.src + (size_t)(jb.ks * 32 + q * 8 + i) * jb.ldn + jb.cb * 112 + jl * 8);
}
__device__ __forceinline__ void pack_shuffle(int lane, u32x4 (&r)[8], LAS unsigned char* Lw) {
  const int jn = lane & 15, q = lane >> 4;
  u32x4 o[8];
  transpose8x8(r, o);
#pragma unroll
  for (int i = 0; i < 8; ++i) *(LAS u32x4*)(Lw + (jn >> 1) * 1024 + (q * 16 + (jn & 1) * 8 + (i ^ (jn >> 1))) * 16) = o[i];
  lds_fence();
#pragma unroll
  for (int m = 0; m < 7; ++m) r[m] = *(const LAS u32x4*)(Lw + m * 1024 + (lane ^ m) * 16);
  lds_fence();
}
__device__ __forceinline__ void pack_store1(const PackJob& jb, int lane, const u32x4 (&r)[8], __amdgpu_buffer_rsrc_t rs, int m) {
  const int nt = jb.na + m * jb.nb;
  __builtin_amdgcn_raw_buffer_store_b128(r[m], rs, lane * 16, (int)WS_WP + ((nt >> 2) * NFRAG + jb.fb + ((nt >> 1) & 1) * 28 + (nt & 1)) * 1024, AUX_SC1);
}
__device__ __forceinline__ void mod_load(const GArgs& p, int u, int tid, u32x4 (&r)[7], float (&sc)[7]) {
  const int rg = tid >> 3, pc = tid & 7;
#pragma unroll
  for (int i = 0; i < 7; ++i) r[i] = *(const u32x4*)(p.wmod + (size_t)(rg + 64 * i) * 2688 + u * 64 + pc * 8);
#pragma unroll
  for (int i = 0; i < 7; ++i) sc[i] = bf2f(p.cvec[rg + 64 * i]);
}
__device__ __forceinline__ void mod_finish(int u, unsigned tag32, int tid, const u32x4 (&r)[7], const float (&cv)[7], LAS float* sm, __amdgpu_buffer_rsrc_t rs) {
  const int rg = tid >> 3, pc = tid & 7;
  float acc[8];
#pragma unroll
  for (int k = 0; k < 8; ++k) acc[k] = 0.f;
#pragma unroll
  for (int i = 0; i < 7; ++i) {
    const float sc = rbf(cv[i] * __builtin_amdgcn_rcpf(1.0f + __builtin_amdgcn_exp2f(-1.4426950408889634f * cv[i])));
#pragma unroll
    for (int k = 0; k < 4; ++k) { acc[2 * k] += sc * bf_lo(r[i][k]); acc[2 * k + 1] += sc * bf_hi(r[i][k]); }
  }
  *(LAS f32x4*)(sm + rg * 68 + pc * 8) = f32x4{acc[0], acc[1], acc[2], acc[3]};
  *(LAS f32x4*)(sm + rg * 68 + pc * 8 + 4) = f32x4{acc[4], acc[5], acc[6], acc[7]};
  bar_lds();
  {
    const int col = tid & 63, part = tid >> 6;
    float s = 0.f;
#pragma unroll
    for (int k = 0; k < 8; ++k) s += sm[(part * 8 + k) * 68 + col];
    sm[64 * 68 + part * 64 + col] = s;
  }
  bar_lds();
  if (tid < 32) {
    f32x2 s = {0.f, 0.f};
#pragma unroll
    for (int k = 0; k < 8; ++k) s += *(const LAS f32x2*)(sm + 64 * 68 + k * 64 + 2 * tid);
    unsigned v = pk_bf16(s.x, s.y);
    const int sec = u / 7;
    if (sec == 1 || sec == 4) v = pk_bf16(bf_lo(v) + 1.0f, bf_hi(v) + 1.0f);
    __builtin_amdgcn_raw_buffer_store_b64(u32x2{v, tag32}, rs, (int)WS_MODG + (u * 32 + tid) * 8, 0, AUX_SC1);
  }
}
template <int YOUNGER>
__device__ __forceinline__ void arrive_flag(LAS unsigned char* L, int cnt_slot, unsigned char* flag, u64 tag, int lane) {
  asm volatile("s_waitcnt vmcnt(%0)" ::"i"(YOUNGER) : "memory");
  if (lane == 0) {
    const unsigned old = __hip_atomic_fetch_add((LAS unsigned*)(L + G_CNT) + cnt_slot, 1u, __ATOMIC_RELAXED, __HIP_MEMORY_SCOPE_WORKGROUP);
    if (old == 7u) __hip_atomic_store((GAS u64*)flag, tag, __ATOMIC_RELAXED, __HIP_MEMORY_SCOPE_AGENT);
  }
}
__device__ __forceinline__ u32x2 flag_load(__amdgpu_buffer_rsrc_t rs, int off) { return __builtin_amdgcn_raw_buffer_load_b64(rs, off, 0, AUX_SC1); }
__device__ __forceinline__ u32x2 flag_wait(__amdgpu_buffer_rsrc_t rs, int off, u32x2 v, unsigned lo, unsigned hi, unsigned lomask) {
  if (__builtin_expect(__any((((v.x ^ lo) & lomask) | (v.y ^ hi)) != 0u), 0)) {
    for (unsigned spins = 0; spins < SPIN_CAP; ++spins) {
      __builtin_amdgcn_s_sleep(2);
      asm volatile("" ::: "memory");
      v = flag_load(rs, off);
      if (!__any((((v.x ^ lo) & lomask) | (v.y ^ hi)) != 0u)) break;
    }
  }
  __builtin_amdgcn_fence(__ATOMIC_ACQUIRE, "wavefront");
  return v;
}
__device__ __forceinline__ void stat_wait(__amdgpu_buffer_rsrc_t rs, int off, u32x4& a, u32x4& b, unsigned tag32) {
  if (__builtin_expect(__any((((a.y ^ tag32) | (a.w ^ tag32)) | ((b.y ^ tag32) | (b.w ^ tag32))) != 0u), 0)) {
    for (unsigned spins = 0; spins < SPIN_CAP; ++spins) {
      __builtin_amdgcn_s_sleep(2);
      asm volatile("" ::: "memory");
      a = __builtin_amdgcn_raw_buffer_load_b128(rs, off, 0, AUX_SC1);
      b = __builtin_amdgcn_raw_buffer_load_b128(rs, off + 16, 0, AUX_SC1);
      if (!__any((((a.y ^ tag32) | (a.w ^ tag32)) | ((b.y ^ tag32) | (b.w ^ tag32))) != 0u)) break;
    }
  }
}
#define FILL_P(i) do { SB0(); __builtin_amdgcn_raw_buffer_store_b128(ONES, rst, (int)WS_P + ((pn * 4 + j) * 3 + (i)) * 7168 + pslot, 0, AUX_SC1); SB0(); } while (0)
#define FILL_X1() do { SB0(); __builtin_amdgcn_raw_buffer_store_b128(ONES, rst, (int)WS_X1 + (pn * 4 + j) * 7168 + pslot, 0, AUX_SC1); SB0(); } while (0)
__device__ __forceinline__ void body(const GArgs& p, LAS unsigned char* L, uintptr_t code0) {
  const bool CONV = blockIdx.x < 224;
  const int tid = threadIdx.x, lane = tid & 63;
  const int w = __builtin_amdgcn_readfirstlane(tid >> 6);
  const int bid = blockIdx.x;
  const int j = bid & 3, pn = (bid >> 3) * 2 + ((bid >> 2) & 1);
  const int r16 = lane & 15, q = lane >> 4;
  const bool cw = w < 7;
  const int wc = cw ? w : 6;
  const int dp = (bid & 7) * 28 + (CONV ? bid >> 3 : 0);
  const __amdgpu_buffer_rsrc_t rs = __builtin_amdgcn_make_buffer_rsrc((void*)p.ws, 0, (int)WS_END, 0x00020000);
  const __amdgpu_buffer_rsrc_t rst = __builtin_amdgcn_make_buffer_rsrc((void*)p.ws, 0, cw ? (int)WS_END : 0, 0x00020000);
  const __amdgpu_buffer_rsrc_t rw =
      __builtin_amdgcn_make_buffer_rsrc((void*)(p.ws + WS_WP + (size_t)(j * 7 + wc) * NFRAG * 1024), 0, cw ? NFRAG * 1024 : 0, 0x00020000);
  const int voff = lane * 16;
  const int pslot = (wc * 64 + lane) * 16;
  const u32x4 ONES = {~0u, ~0u, ~0u, ~0u};
  STAMP(0, 0);
  const int l56 = lane < 56 ? lane : 0;
  const u32x4 xrow = *(const u32x4*)(p.x + (size_t)(bid * 8 + w) * D_ + l56 * 8);
  const u32x2 ep = flag_load(rs, (int)WS_EPOCH);
  const int cl = ((bid >> 3) & 3) * 80 + w * 10 + (lane < 10 ? lane : 0);
  const unsigned touch = *(const GAS unsigned*)(code0 + (size_t)(cl < CODE_LINES ? cl : 0) * 128);
  float are, aim, b_re, b_im, cre, cim, ldt;
  u32x4 mr[7], pk[8];
  float mc[7];
  {
    const size_t o = (size_t)(2 * dp + (w >> 2)) * NS_ + (w & 3) * 64 + lane;
    SB0();
    are = bf2f(p.a_re[o]); aim = bf2f(p.a_im[o]); b_re = bf2f(p.b_re[o]); b_im = bf2f(p.b_im[o]);
    cre = bf2f(p.c_re[o]); cim = bf2f(p.c_im[o]); ldt = bf2f(p.log_dt[2 * dp + (w >> 2)]);
    SB0();
  }
  __builtin_amdgcn_s_barrier();
  __builtin_amdgcn_raw_buffer_store_b128(ONES, rst, (int)WS_X2B + bid * 7168 + pslot, 0, AUX_SC1);
  __builtin_amdgcn_raw_buffer_store_b128(ONES, rst, (int)WS_G + bid * 7168 + pslot, 0, AUX_SC1);
  SB0();
  const int pg = (bid - 224) * 6 + w;
  const bool packer = CONV ? w < 2 : (w < 6 && pg < 112);
  const PackJob jb = pack_job(p, CONV ? bid * 2 + (w & 1) : (packer ? 448 + pg : 0));
  if (tid < 4) *((LAS unsigned*)(L + G_CNT) + tid) = 0u;
  u64 tag;
  {
    const u64 e = ((u64)__builtin_amdgcn_readfirstlane(ep.y) << 32) | (unsigned)__builtin_amdgcn_readfirstlane(ep.x);
    u64 z = e + 0x9E3779B97F4A7C15ull;
    z = (z ^ (z >> 30)) * 0xBF58476D1CE4E5B9ull;
    z = (z ^ (z >> 27)) * 0x94D049BB133111EBull;
    tag = z ^ (z >> 31);
  }
  const unsigned tag32 = ((unsigned)__builtin_amdgcn_readfirstlane(ep.x) + 1u) * 0x9E3779B1u ^ 0x5bd1e995u;
  const unsigned tlo = (unsigned)tag, thi = (unsigned)(tag >> 32);
  unsigned stv;
#define PUT_STAT() do { if (lane == 0) __builtin_amdgcn_raw_buffer_store_b64(u32x2{stv, tag32}, rs, (int)WS_STG + (bid * 8 + w) * 8, 0, AUX_SC1); } while (0)
  {
    const float msk = lane < 56 ? 1.0f : 0.0f;
    float v[8];
#pragma unroll
    for (int k = 0; k < 4; ++k) { v[2 * k] = bf_lo(xrow[k]); v[2 * k + 1] = bf_hi(xrow[k]); }
    const float mean = wave_sum((((v[0] + v[1]) + (v[2] + v[3])) + ((v[4] + v[5]) + (v[6] + v[7]))) * msk) * (1.0f / D_);
    float qq = 0.f;
#pragma unroll
    for (int k = 0; k < 8; ++k) { const float d = v[k] - mean; qq += d * d; }
    const float sd = ln_sd(wave_sum(qq * msk) * (1.0f / D_));
    stv = pk_bf16(mean, sd);
    if (lane < 56) *(LAS u32x4*)(L + P0_LN + w * LNS + lane * 16) = xrow;
    bar_lds();
#pragma unroll
    for (int u = 0; u < 2; ++u) {
      const int pce = (wc * 2 + u) * 64 + lane;
      const LAS unsigned* a = (const LAS unsigned*)(L + P0_LN + (pce & 3) * (2 * LNS) + (pce >> 2) * 4);
      __builtin_amdgcn_raw_buffer_store_b128(u32x4{a[0], tag32, a[LNS / 4], tag32}, rst, (int)WS_XG + bid * (int)XG_BLK + pce * 16, 0, AUX_SC1);
    }
  }
  asm volatile("" ::"v"(touch));
  STAMP(0, 29);
  const int sto = (int)WS_STG + tid * 32;
  u32x4 ring[RING];
  if (__builtin_expect(CONV, 1)) {
    const float dt = rbf(expf(ldt));
    const float ea = (dt * are) * 1.4426950408889634f, eb = (dt * aim) * 0.15915494309189535f;
    const float bre = dt * b_re, bim = dt * b_im;
    f32x2 wc;
    wc.x = cre * bre - cim * bim;
    wc.y = -(cre * bim + cim * bre);
    asm volatile("" ::"v"(ea), "v"(eb), "v"(wc.x), "v"(wc.y));
    const float mx = rows_max(row16_max(fmaxf(fabsf(wc.x), fabsf(wc.y))));
    int ex = mx > 0.f ? __builtin_amdgcn_frexp_expf(mx) : 0;
    ex = ex < -120 ? -120 : (ex > 120 ? 120 : ex);
    const float scale = __builtin_amdgcn_ldexpf(1.0f, ex), iscale = __builtin_amdgcn_ldexpf(1.0f, -ex);
    wc.x *= iscale; wc.y *= iscale;
    LAS unsigned char* B0 = L + P0_KG + w * (2 * KG_BUF);
    LAS unsigned char* B1 = B0 + KG_BUF;
    const int wo = lane * 4;
    const int ro = r16 * KG_ROW + q * 16;
    f32x2 z8c = cexp_tab(ea, eb, 8.0f);
    z8c.y = -z8c.y;
    const f32x2 z1 = cexp_tab(ea, eb, 1.0f), z128 = cexp_tab(ea, eb, 128.0f), z1024 = cexp_tab(ea, eb, 1024.0f);
    {
      const f32x2 z16c = cmul(z8c, z8c);
      f32x2 t0 = wc, t1 = cmul(wc, z8c);
#pragma unroll
      for (int c = 0; c < 16; c += 2) {
        *(LAS unsigned*)(B0 + c * KG_ROW + wo) = pk_f16(t0);
        *(LAS unsigned*)(B0 + (c + 1) * KG_ROW + wo) = pk_f16(t1);
        if (c < 14) { t0 = cmul(t0, z16c); t1 = cmul(t1, z16c); }
      }
    }
    SB0();
    asm volatile("s_waitcnt vmcnt(2)" ::: "memory");
    PUT_STAT();
    SB0();
    f32x2 ze[8];
    ze[0] = f32x2{1.0f, 0.0f}; ze[1] = z1; ze[2] = cmul(z1, z1); ze[3] = cmul(ze[2], z1); ze[4] = cmul(ze[2], ze[2]);
    ze[5] = cmul(ze[4], z1); ze[6] = cmul(ze[4], ze[2]); ze[7] = cmul(ze[4], ze[3]);
#pragma unroll
    for (int e = 0; e < 8; ++e) {
      *(LAS unsigned*)(B1 + e * KG_ROW + wo) = pk_f16(ze[e]);
      *(LAS unsigned*)(B1 + (8 + e) * KG_ROW + wo) = pk_f16(e == 0 ? z128 : cmul(z128, ze[e]));
    }
    lds_fence();
    u32x4 tf[4];
#pragma unroll
    for (int ks = 0; ks < 4; ++ks) tf[ks] = *(const LAS u32x4*)(B0 + ro + ks * 64);
    u32x4 rf[4];
#pragma unroll
    for (int ks = 0; ks < 4; ++ks) rf[ks] = *(const LAS u32x4*)(B1 + ro + ks * 64);
    lds_fence();
    SB0();
    if (packer) pack_load(jb, lane, pk);
    SB0();
    STAMP(0, 30);
    const f32x2 p2 = cmul(z128, z128), p3 = cmul(p2, z128), p4 = cmul(p2, p2), p5 = cmul(p4, z128), p6 = cmul(p4, p2), p7 = cmul(p4, p3);
    {
#pragma unroll
      for (int e = 0; e < 4; ++e) {
        *(LAS unsigned*)(B0 + e * KG_ROW + wo) = pk_f16(e == 0 ? p2 : cmul(p2, ze[2 * e]));
        *(LAS unsigned*)(B0 + (4 + e) * KG_ROW + wo) = pk_f16(e == 0 ? p3 : cmul(p3, ze[2 * e]));
      }
      *(LAS unsigned*)(B0 + 8 * KG_ROW + wo) = pk_f16(p4);
      *(LAS unsigned*)(B0 + 9 * KG_ROW + wo) = pk_f16(cmul(p4, ze[4]));
      *(LAS unsigned*)(B0 + 10 * KG_ROW + wo) = pk_f16(p5);
      *(LAS unsigned*)(B0 + 11 * KG_ROW + wo) = pk_f16(cmul(p5, ze[4]));
      *(LAS unsigned*)(B0 + 12 * KG_ROW + wo) = pk_f16(p6);
      *(LAS unsigned*)(B0 + 13 * KG_ROW + wo) = pk_f16(cmul(p6, ze[4]));
      *(LAS unsigned*)(B0 + 14 * KG_ROW + wo) = pk_f16(p7);
      *(LAS unsigned*)(B0 + 15 * KG_ROW + wo) = pk_f16(cmul(p7, ze[4]));
    }
    f32x4 acc0 = {0.f, 0.f, 0.f, 0.f}, acc1 = acc0, acc2 = acc0;
#pragma unroll
    for (int ks = 0; ks < 4; ++ks) acc0 = mfma16h(rf[ks], tf[ks], acc0);
    {
      *(LAS unsigned*)(B1 + 0 * KG_ROW + wo) = pk_f16(z1024);
      *(LAS unsigned*)(B1 + 1 * KG_ROW + wo) = pk_f16(cmul(z1024, z128));
      *(LAS unsigned*)(B1 + 2 * KG_ROW + wo) = pk_f16(cmul(z1024, p2));
      *(LAS unsigned*)(B1 + 3 * KG_ROW + wo) = pk_f16(cmul(z1024, p3));
      *(LAS unsigned*)(B1 + 4 * KG_ROW + wo) = pk_f16(cmul(z1024, p4));
      *(LAS unsigned*)(B1 + 5 * KG_ROW + wo) = pk_f16(cmul(z1024, p5));
      *(LAS unsigned*)(B1 + 6 * KG_ROW + wo) = pk_f16(cmul(z1024, p6));
      *(LAS unsigned*)(B1 + 7 * KG_ROW + wo) = pk_f16(cmul(z1024, p7));
      *(LAS unsigned*)(B1 + 8 * KG_ROW + wo) = pk_f16(cmul(z1024, z1024));
    }
    lds_fence();
#pragma unroll
    for (int ks = 0; ks < 4; ++ks) acc1 = mfma16h(*(const LAS u32x4*)(B0 + ro + ks * 64), tf[ks], acc1);
#pragma unroll
    for (int ks = 0; ks < 4; ++ks) acc2 = mfma16h(*(const LAS u32x4*)(B1 + ro + ks * 64), tf[ks], acc2);
    lds_fence();
    {
      LAS float* red = (LAS float*)B0;
      acc0 *= scale; acc1 *= scale; acc2 *= scale;
      *(LAS f32x4*)(red + (q >> 1) * 128 + 8 * r16 + (q & 1) * 4) = acc0;
      const int i1 = q < 2 ? 256 + q * 64 + 4 * r16 : 384 + (q - 2) * 64 + 2 * r16, d1 = q < 2 ? 2 : 32;
      *(LAS f32x2*)(red + i1) = f32x2{acc1[0], acc1[1]};
      *(LAS f32x2*)(red + i1 + d1) = f32x2{acc1[2], acc1[3]};
      LAS float* r2 = red + 512 + q * 64 + r16;
      r2[0] = acc2[0]; r2[16] = acc2[1]; r2[32] = acc2[2]; r2[48] = acc2[3];
    }
    STAMP(0, 21);
    bar_lds();
    const int mgo = (int)WS_MODG + ((lane < 3 ? lane : 0) * 224 + dp) * 8;
    SB0();
    u32x4 sg0 = __builtin_amdgcn_raw_buffer_load_b128(rs, sto, 0, AUX_SC1), sg1 = __builtin_amdgcn_raw_buffer_load_b128(rs, sto + 16, 0, AUX_SC1);
    const int xgo = (int)WS_XG + (tid >> 1) * (int)XG_BLK + dp * 64 + (tid & 1) * 32;
    u32x4 xg0 = __builtin_amdgcn_raw_buffer_load_b128(rs, xgo, 0, AUX_SC1), xg1 = __builtin_amdgcn_raw_buffer_load_b128(rs, xgo + 16, 0, AUX_SC1);
    const unsigned dsk2 = *(const unsigned*)(p.dskip + 2 * dp);
    SB0();
    const int kc = tid >> 8, kt = tid & 255;
    LAS float* kd = (LAS float*)(L + P0_KG + kc * (8 * KG_BUF) + KG_BUF);
#pragma unroll
    for (int i = 0; i < 3; ++i) {
      const int oo = kt + 256 * i;
      if (i < 2 || oo < 648) {
        const LAS float* s = (const LAS float*)(L + P0_KG + kc * (8 * KG_BUF)) + oo;
        kd[oo] = (s[0] + s[2 * KG_BUF / 4]) + (s[4 * KG_BUF / 4] + s[6 * KG_BUF / 4]);
      }
    }
    if (kt == 0) kd[648] = 0.f;
    const unsigned OFFS = 0x087EDA52u;
    {
      int off = CV_KCOPY + kt * 16;
      if (kt >= 160) { const int k = kt - 160, s = k >> 3, ch = (k & 7) == 7 ? 6 : (k & 7); off = (272 * (s & 7) + ((OFFS >> (4 * (s & 7))) & 15) + 257 + ch) * 16; }
      *(LAS u32x4*)(L + kc * CV_CH + off) = u32x4{0u, 0u, 0u, 0u};
    }
    bar_lds();
    LAS unsigned char* base = L + kc * CV_CH;
    u32x4 k0;
    {
      float v[8];
#pragma unroll
      for (int i = 0; i < 8; i += 2) {
        const int l0 = 2048 - (kt * 8 + i), l1 = l0 - 1;
        const unsigned bits = pk_bf16((float)l0, (float)l1);
        int i0 = l0 < 128 ? l0 : (int)(bits & 0xffffu) - 17024;
        const int i1 = l1 < 128 ? l1 : (int)(bits >> 16) - 17024;
        if (i == 0) i0 = kt == 0 ? 648 : i0;
        v[i] = kd[i0];
        v[i + 1] = kd[i1];
      }
      k0 = u32x4{pk_bf16(v[0], v[1]), pk_bf16(v[2], v[3]), pk_bf16(v[4], v[5]), pk_bf16(v[6], v[7])};
      *(LAS u32x4*)(base + (2 + kt) * 16) = k0;
      if (kt < 8) {
        const unsigned k00 = pk_bf16(kd[0], 0.f);
        *(LAS u32x4*)(base + (272 * kt + ((OFFS >> (4 * kt)) & 15) + 256) * 16) = u32x4{kt == 0 ? k00 : 0u, 0u, 0u, 0u};
      }
    }
    bar_lds();
    {
      const u32x4 k1 = *(const LAS u32x4*)(base + (2 + kt + 1) * 16);
      unsigned wv[8] = {k0.x, k0.y, k0.z, k0.w, k1.x, k1.y, k1.z, k1.w};
      unsigned vv[7];
#pragma unroll
      for (int i = 0; i < 7; ++i) vv[i] = __builtin_amdgcn_alignbit(wv[i + 1], wv[i], 16);
#pragma unroll
      for (int s = 1; s < 8; ++s) {
        const int k = s >> 1;
        u32x4 o = (s & 1) ? u32x4{vv[k], vv[k + 1], vv[k + 2], vv[k + 3]} : u32x4{wv[k], wv[k + 1], wv[k + 2], wv[k + 3]};
        *(LAS u32x4*)(base + (272 * s + ((OFFS >> (4 * s)) & 15) + kt) * 16) = o;
      }
    }
    float cum0[4], cum1[4];
    {
      const LAS float* kd0 = (const LAS float*)(L + P0_KG + KG_BUF);
      const LAS float* kd1 = (const LAS float*)(L + P0_KG + 8 * KG_BUF + KG_BUF);
#pragma unroll
      for (int i = 0; i < 4; i += 2) {
        const int l0 = 4 * tid + i;
        const unsigned bits = pk_bf16((float)l0, (float)(l0 + 1));
        const int i0 = l0 < 128 ? l0 : (int)(bits & 0xffffu) - 17024, i1 = l0 < 128 ? l0 + 1 : (int)(bits >> 16) - 17024;
        cum0[i] = kd0[i0]; cum0[i + 1] = kd0[i1]; cum1[i] = kd1[i0]; cum1[i + 1] = kd1[i1];
      }
#pragma unroll
      for (int i = 1; i < 4; ++i) { cum0[i] += cum0[i - 1]; cum1[i] += cum1[i - 1]; }
      const float inc0 = wave_scan(cum0[3]), inc1 = wave_scan(cum1[3]);
      const float e0 = inc0 - cum0[3], e1 = inc1 - cum1[3];
#pragma unroll
      for (int i = 0; i < 4; ++i) { cum0[i] += e0; cum1[i] += e1; }
      if (lane == 63) *(LAS f32x2*)(L + P0_MS + w * 8) = f32x2{inc0, inc1};
    }
#define GRAN_BAD() __any(((((sg0.y ^ tag32) | (sg0.w ^ tag32)) | ((sg1.y ^ tag32) | (sg1.w ^ tag32))) | (((xg0.y ^ tag32) | (xg0.w ^ tag32)) | ((xg1.y ^ tag32) | (xg1.w ^ tag32)))) != 0u)
    if (__builtin_expect(GRAN_BAD(), 0)) {
      for (unsigned spins = 0; spins < SPIN_CAP; ++spins) {
        __builtin_amdgcn_s_sleep(2);
        asm volatile("" ::: "memory");
        sg0 = __builtin_amdgcn_raw_buffer_load_b128(rs, sto, 0, AUX_SC1); sg1 = __builtin_amdgcn_raw_buffer_load_b128(rs, sto + 16, 0, AUX_SC1);
        xg0 = __builtin_amdgcn_raw_buffer_load_b128(rs, xgo, 0, AUX_SC1); xg1 = __builtin_amdgcn_raw_buffer_load_b128(rs, xgo + 16, 0, AUX_SC1);
        if (!GRAN_BAD()) break;
      }
    }
#undef GRAN_BAD
    const unsigned xq[4] = {xg0.x, xg0.z, xg1.x, xg1.z};
    STAMP(0, 22);
    unsigned hq[4];
    {
      const unsigned sv[4] = {sg0.x, sg0.z, sg1.x, sg1.z};
#pragma unroll
      for (int k = 0; k < 4; ++k) {
        const float m = bf_lo(sv[k]), r = __builtin_amdgcn_rcpf(bf_hi(sv[k]));
        unsigned t = pk_bf16(bf_lo(xq[k]) - m, bf_hi(xq[k]) - m);
        hq[k] = pk_bf16(bf_lo(t) * r, bf_hi(t) * r);
      }
      LAS unsigned char* U = L + CV_KCOPY + (16 + (tid >> 4)) * CV_UROW + (tid & 15) * 8;
      *(LAS u32x2*)U = u32x2{__builtin_amdgcn_perm(hq[1], hq[0], 0x05040100u), __builtin_amdgcn_perm(hq[3], hq[2], 0x05040100u)};
      *(LAS u32x2*)(U + CV_CH) = u32x2{__builtin_amdgcn_perm(hq[1], hq[0], 0x07060302u), __builtin_amdgcn_perm(hq[3], hq[2], 0x07060302u)};
    }
    if (packer) pack_shuffle(lane, pk, L + P0_KG + (w + 1) * (2 * KG_BUF));
    STAMP(0, 23);
    bar_lds();
    if (bid == 0 && tid == 0) {
      const u64 e = ((u64)ep.y << 32) | ep.x;
      __hip_atomic_store((GAS u64*)(p.ws + WS_EPOCH), e + 1ull, __ATOMIC_RELAXED, __HIP_MEMORY_SCOPE_AGENT);
    }
    u32x2 fv = flag_load(rs, mgo);
    const __amdgpu_buffer_rsrc_t rpk = __builtin_amdgcn_make_buffer_rsrc((void*)p.ws, 0, packer ? (int)WS_END : 0, 0x00020000);
    SB0();
  {
    const int ch = w >> 2, wq = w & 3;
    f32x4 acc[2][4];
#pragma unroll
    for (int n = 0; n < 2; ++n)
#pragma unroll
      for (int m = 0; m < 4; ++m) acc[n][m] = f32x4{0.f, 0.f, 0.f, 0.f};
    const int s = (8 - r16) & 7, u = (r16 + 7) >> 3;
    const LAS unsigned char* ka = L + ch * CV_CH + (272 * s + ((OFFS >> (4 * s)) & 15) + 256 + q - u - 2 * (16 * wq) - 2 * 79) * 16;
    const LAS unsigned char* ub = L + ch * CV_CH + CV_KCOPY + (16 + r16 - 4 * wq - 3) * CV_UROW + q * 16;
#define KFRAG(f) (*(const LAS u32x4*)(ka + (79 - (f)) * 32))
    u32x4 alo[18], ahi[18], bf[4][4];
#define CSTAGE(dl)                                                                                      \
  {                                                                                                     \
    _Pragma("unroll") for (int ks = 0; ks < 2; ++ks) {                                                  \
      bf[dl][2 * ks] = *(const LAS u32x4*)(ub + (3 - (dl)) * CV_UROW + ks * 64);                        \
      bf[dl][2 * ks + 1] = *(const LAS u32x4*)(ub + (19 - (dl)) * CV_UROW + ks * 64);                   \
    }                                                                                                   \
    _Pragma("unroll") for (int i = 0; i < 4; ++i) {                                                     \
      alo[2 + 4 * (dl) + i] = KFRAG(4 * (dl) + i);                                                      \
      ahi[2 + 4 * (dl) + i] = KFRAG(4 * (dl) + i + 64);                                                 \
    }                                                                                                   \
  }
    CSTAGE(0)
    alo[0] = KFRAG(-2); alo[1] = KFRAG(-1); ahi[0] = KFRAG(62); ahi[1] = KFRAG(63);
    SB0();
#pragma unroll
    for (int m = 0; m < 7; ++m) pack_store1(jb, lane, pk, rpk, m);
    FILL_X1(); FILL_P(0); FILL_P(1); FILL_P(2);
    SB0();
#pragma unroll
    for (int dl = 0; dl < 4; ++dl) {
      if (dl < 3) CSTAGE(dl + 1)
#pragma unroll
      for (int ks = 0; ks < 2; ++ks) {
#pragma unroll
        for (int m = 0; m < 4; ++m) {
          acc[0][m] = mfma16(alo[4 * dl + m - 2 * ks + 2], bf[dl][2 * ks], acc[0][m]);
          acc[1][m] = mfma16(alo[4 * dl + m - 2 * ks + 2], bf[dl][2 * ks + 1], acc[1][m]);
          acc[1][m] = mfma16(ahi[4 * dl + m - 2 * ks + 2], bf[dl][2 * ks], acc[1][m]);
        }
      }
      if (dl < 3) {
#pragma unroll
        for (int i = 0; i < 12; ++i) {
          __builtin_amdgcn_sched_group_barrier(0x100, 1, 0);
          __builtin_amdgcn_sched_group_barrier(0x8, 2, 0);
        }
      }
      SB0();
    }
#undef CSTAGE
    STAMP(0, 28);
#undef KFRAG

    LAS unsigned char* P = L + CV_P + w * (32 * CV_PROW);
#pragma unroll
    for (int n = 0; n < 2; ++n)
#pragma unroll
      for (int m = 0; m < 4; ++m) *(LAS f32x4*)(P + (16 * n + r16) * CV_PROW + (16 * m + 4 * q) * 4) = acc[n][m];
    }
    fv = flag_wait(rs, mgo, fv, 0u, tag32, 0u);
    const unsigned sa2 = __builtin_amdgcn_readlane(fv.x, 0), ca2 = __builtin_amdgcn_readlane(fv.x, 1), ga2 = __builtin_amdgcn_readlane(fv.x, 2);
    unsigned hd[4];
    {
      const f32x2 t2 = *(const LAS f32x2*)(L + P0_MS + (lane & 7) * 8);
      const float mk = (lane & 15) < w ? 1.0f : 0.0f;
      const float b0 = row16_sum((lane & 8) ? 0.f : t2.x * mk), b1 = row16_sum((lane & 8) ? 0.f : t2.y * mk);
#pragma unroll
      for (int k = 0; k < 4; ++k) {
        unsigned h = pk_bf16(bf_lo(hq[k]) * bf_lo(ca2), bf_hi(hq[k]) * bf_hi(ca2));
        h = pk_bf16(bf_lo(h) + bf_lo(sa2), bf_hi(h) + bf_hi(sa2));
        hd[k] = pk_bf16(bf_lo(h) * bf_lo(dsk2), bf_hi(h) * bf_hi(dsk2));
        cum0[k] = (cum0[k] + b0) * bf_lo(sa2);
        cum1[k] = (cum1[k] + b1) * bf_hi(sa2);
      }
    }
    asm volatile("" : "+v"(hd[0]), "+v"(hd[1]), "+v"(hd[2]), "+v"(hd[3]), "+v"(cum0[0]), "+v"(cum0[1]), "+v"(cum0[2]), "+v"(cum0[3]),
                 "+v"(cum1[0]), "+v"(cum1[1]), "+v"(cum1[2]), "+v"(cum1[3]));
    asm volatile("s_waitcnt vmcnt(4)" ::: "memory");
    bar_lds();
    STAMP(0, 24);
    {
      const LAS unsigned char* P = L + CV_P + (tid >> 4) * CV_PROW + (tid & 15) * 16;
      f32x4 pa[8];
#pragma unroll
      for (int k = 0; k < 8; ++k) pa[k] = *(const LAS f32x4*)(P + k * (32 * CV_PROW));
      const f32x4 y0 = (pa[0] + pa[1]) + (pa[2] + pa[3]), y1 = (pa[4] + pa[5]) + (pa[6] + pa[7]);
      u32x4 out;
#pragma unroll
      for (int k = 0; k < 4; ++k) {
        const unsigned yy = pk_bf16(y0[k] * bf_lo(ca2) + cum0[k], y1[k] * bf_hi(ca2) + cum1[k]);
        unsigned t = pk_bf16(bf_lo(yy) + bf_lo(hd[k]), bf_hi(yy) + bf_hi(hd[k]));
        t = pk_bf16(bf_lo(ga2) * bf_lo(t), bf_hi(ga2) * bf_hi(t));
        out[k] = sane(pk_bf16(bf_lo(xq[k]) + bf_lo(t), bf_hi(xq[k]) + bf_hi(t)));
      }
      ST16(WS_X2B + (tid >> 3) * (int)X2B_BLK + dp * 128 + (tid & 7) * 16, out);
    }
  } else {
    const int c = bid - 224;
    LAS float* sm = (LAS float*)(L + P0_KG);
    SB0();
    mod_load(p, c, tid, mr, mc);
    SB0();
    asm volatile("s_waitcnt vmcnt(16)" ::: "memory");
    PUT_STAT();
    mod_finish(c, tag32, tid, mr, mc, sm, rs);
    STAMP(0, 21);
    if (c >= 22) {
      mod_load(p, 10 + c, tid, mr, mc);
      bar_lds();
      mod_finish(10 + c, tag32, tid, mr, mc, sm, rs);
    }
    if (packer) {
      pack_load(jb, lane, pk);
      pack_shuffle(lane, pk, L + P0_PK + w * 8192);
#pragma unroll
      for (int m = 0; m < 7; ++m) pack_store1(jb, lane, pk, rs, m);
    }
    arrive_flag<0>(L, 2, p.ws + WS_FLAGC + c * 8, ~tag, lane);
    STAMP(0, 30);
    FILL_X1(); FILL_P(0); FILL_P(1); FILL_P(2);
    {
      u32x4 sg0 = __builtin_amdgcn_raw_buffer_load_b128(rs, sto, 0, AUX_SC1), sg1 = __builtin_amdgcn_raw_buffer_load_b128(rs, sto + 16, 0, AUX_SC1);
      stat_wait(rs, sto, sg0, sg1, tag32);
    }
    bar_lds();
  }
  STAMP(0, 25);
  if (__builtin_expect(!cw, 0)) {
    {
      const int mo = (int)WS_MODG + 672 * 8 + l56 * 64;
      u32x4 g0 = __builtin_amdgcn_raw_buffer_load_b128(rs, mo, 0, AUX_SC1), g1 = __builtin_amdgcn_raw_buffer_load_b128(rs, mo + 16, 0, AUX_SC1);
      u32x4 g2 = __builtin_amdgcn_raw_buffer_load_b128(rs, mo + 32, 0, AUX_SC1), g3 = __builtin_amdgcn_raw_buffer_load_b128(rs, mo + 48, 0, AUX_SC1);
      SB0();
      gather_x2<X2_DELAY>(p, L, rs, pn, tid);
      stat_wait(rs, mo, g0, g1, tag32);
      stat_wait(rs, mo + 32, g2, g3, tag32);
      LAS unsigned char* mp = L + G_MOD + l56 * 64;
      *(LAS f32x4*)mp = f32x4{bf_lo(g0.x), bf_hi(g0.x), bf_lo(g0.z), bf_hi(g0.z)};
      *(LAS f32x4*)(mp + 16) = f32x4{bf_lo(g1.x), bf_hi(g1.x), bf_lo(g1.z), bf_hi(g1.z)};
      *(LAS f32x4*)(mp + 32) = f32x4{bf_lo(g2.x), bf_hi(g2.x), bf_lo(g2.z), bf_hi(g2.z)};
      *(LAS f32x4*)(mp + 48) = f32x4{bf_lo(g3.x), bf_hi(g3.x), bf_lo(g3.z), bf_hi(g3.z)};
    }
    STAMP(0, 1);
    bar_lds();
    const int fo = (int)WS_FLAGC + (lane & 31) * 8;
    const u32x2 fc = flag_load(rs, fo);
    ln_phase_a(L, j, w, r16, q, [](auto) {});
    STAMP(0, 2);
    bar_lds();
    flag_wait(rs, fo, fc, ~tlo, ~thi, ~0u);
    const unsigned char* src = p.ws + WS_WP + (size_t)(j * 7) * NFRAG * 1024 + NRING * 1024 + voff;
#define DMA_HALF(h)                                                                                                         \
  _Pragma("unroll") for (int ww = 0; ww < 7; ++ww) _Pragma("unroll") for (int i = (h) * (NLDS / 2); i < ((h) + 1) * (NLDS / 2); ++i) \
      __builtin_amdgcn_global_load_lds((const GAS unsigned*)(src + (size_t)ww * NFRAG * 1024 + i * 1024),                     \
                                       (LAS unsigned*)(L + G_WL + (ww * NLDS + i) * 1024), 16, 0, AUX_SC1);
    DMA_HALF(0)
    bar_lds();
    STAMP(0, 4);
    bar_lds();
    DMA_HALF(1)
#undef DMA_HALF
    bar_lds();
    {
      int gof[3];
      u32x4 xr[3];
      x1_offsets(pn, j, w, r16, q, gof);
      poll_load<3>(rs, gof, xr);
      ln_phase_c(L, xr, j, w, r16, q);
    }
    asm volatile("s_waitcnt vmcnt(0)" ::: "memory");
    STAMP(0, 7);
    bar_lds();
    return;
  }
  __builtin_amdgcn_s_setprio(1);
  SB0();
  gather_x2<X2_DELAY>(p, L, rs, pn, tid);
  STAMP(0, 1);
  bar_lds();
  STAMP(0, 27);
  u32x2 h2r[2], x2r[2], x1r[2];
  const int ncol = j * 112 + w * 16 + q * 4;
  const int gbo = (int)WS_MODG + (5 * 224 + (ncol >> 1)) * 8;
  u32x4 gbg = __builtin_amdgcn_raw_buffer_load_b128(rs, gbo, 0, AUX_SC1);
#pragma unroll
  for (int T = 0; T < 2; ++T) {
    const int row = 16 * T + r16;
    x2r[T] = *(const LAS u32x2*)(L + G_X + row * 1024 + (((ncol >> 3) ^ (row >> 2)) * 16) + (ncol & 4) * 2);
  }
  SB0();
  ring_fill<0, 8>(ring, rw, voff);
  ln_phase_a(L, j, w, r16, q, [&](auto k) { SB0(); ring_fill<8 + 6 * decltype(k)::value, 6>(ring, rw, voff); });
  SB0();
  ring_fill<26, 6>(ring, rw, voff);
  STAMP(0, 2);
  bar_lds();
  u32x2 gbp;
  {
    u32x4 dup = gbg;
    if (__builtin_expect(__any(((gbg.y ^ tag32) | (gbg.w ^ tag32)) != 0u), 0)) {
      for (unsigned spins = 0; spins < SPIN_CAP; ++spins) {
        asm volatile("" ::: "memory");
        dup = __builtin_amdgcn_raw_buffer_load_b128(rs, gbo, 0, AUX_SC1);
        if (!__any(((dup.y ^ tag32) | (dup.w ^ tag32)) != 0u)) break;
      }
    }
    gbp = u32x2{dup.x, dup.z};
  }
#pragma unroll
  for (int T = 0; T < 2; ++T) h2r[T] = *(const LAS u32x2*)(L + G_HQ + (16 * T + r16) * 240 + (w * 16 + q * 4) * 2);
  const LAS unsigned char* ap0 = L + G_T0 + r16 * AS + q * 16;
  const LAS unsigned char* ap1 = L + G_T1 + r16 * AS + q * 16;
  const int tok = (q & 1) * 16 + r16, octc = 2 * w + (q >> 1);
  {
    f32x4 acc[1][2];
    STAMP(0, 16);
    gemm14<1, 0, 32, 7, 2, 5>(ap0, ring, rw, voff, acc);
    STAMP(0, 17);
    const u32x4 own = make_piece(gelu4(acc[0][0]), gelu4(acc[0][1]));
    __builtin_amdgcn_raw_buffer_store_b128(own, rs, (int)WS_G + (pn * 4 + j) * 7168 + pslot, 0, AUX_SC1);
    SB0();
    STAMP(0, 18);
    ring_fill<39, 4>(ring, rw, voff);
    int goff[4], loff[4];
    bool wrap0;
#pragma unroll
    for (int i = 0; i < 4; ++i) {
      const int jq = (j + i) & 3;
      const int sh = (jq & 1) ? -1 - (jq >> 1) : 1 + (jq >> 1);
      const int it = tok + sh;
      const int sp = pn + (it >= 32 ? -1 : (it < 0 ? 1 : 0));
      if (i == 0) wrap0 = sp != pn;
      goff[i] = (sp < 0 || sp > 63 || (i == 0 && sp == pn)) ? (int)WS_END : (int)WS_G + (sp * 4 + jq) * 7168 + pslot;
      loff[i] = G_T1 + (it & 31) * AS + (jq * 14 + octc) * 16;
    }
    STAMP(0, 3);
    bar_lds();
    u32x4 v[4];
    poll_issue<4>(rs, goff, v);
    __builtin_amdgcn_s_sleep(3);
    ring_fill<43, 5>(ring, rw, voff);
    poll_finish<4>(rs, goff, v);
    *(LAS u32x4*)(L + loff[0]) = wrap0 ? v[0] : own;
#pragma unroll
    for (int i = 1; i < 4; ++i) *(LAS u32x4*)(L + loff[i]) = v[i];
  }
  STAMP(0, 4);
  bar_lds();
  {
    f32x4 acc[1][2];
    gemm14<1, 14, 48, 7, 2, 5>(ap1, ring, rw, voff, acc);
#pragma unroll
    for (int T = 0; T < 2; ++T) {
      const unsigned a = pk_bf16(acc[0][T][0], acc[0][T][1]), b = pk_bf16(acc[0][T][2], acc[0][T][3]);
      x1r[T].x = pk_bf16(bf_lo(h2r[T].x) + bf_lo(a), bf_hi(h2r[T].x) + bf_hi(a));
      x1r[T].y = pk_bf16(bf_lo(h2r[T].y) + bf_lo(b), bf_hi(h2r[T].y) + bf_hi(b));
    }
    const u32x4 own = make_piece(x1r[0], x1r[1]);
    __builtin_amdgcn_raw_buffer_store_b128(own, rs, (int)WS_X1 + (pn * 4 + j) * 7168 + (w * 64 + (tok >> 2) * 8 + (q >> 1) * 4 + (tok & 3)) * 16, 0, AUX_SC1);
    *(LAS u32x4*)(L + G_HQ + tok * 240 + octc * 16) = own;
    SB0();
    STAMP(0, 19);
    ring_fill<55, 4>(ring, rw, voff);
    int gof[3];
    u32x4 xr[3];
    x1_offsets(pn, j, w, r16, q, gof);
    STAMP(0, 5);
    bar_lds();
    poll_issue<3>(rs, gof, xr);
    __builtin_amdgcn_s_sleep(3);
    ring_fill<59, 5>(ring, rw, voff);
    poll_finish<3>(rs, gof, xr);
    STAMP(0, 6);
    ring_fill<64, 4>(ring, rw, voff);
    ln_phase_c(L, xr, j, w, r16, q);
    SB0();
    ring_fill<68, 4>(ring, rw, voff);
  }
  STAMP(0, 7);
  bar_lds();
  {
    {
      f32x4 ca[2][2], cb2[2][2], acc[3][2];
      LAS unsigned char* hp = L + G_T1 + r16 * AS + (w * 64 + q * 4) * 2;
      unsigned glo;
      STAMP(0, 12);
      gemm_p<2, 28, 0, 1, 14, 72, 19, 3, true>(ap0, ring, rw, voff, ca, [](auto) {});
      gemm_p<2, 56, 0, 1, 14, 91, 19, 3, true>(ap0, ring, rw, voff, cb2, [&](auto stc) {
        constexpr int st = decltype(stc)::value;
        if constexpr (st % 3 == 1 && st / 3 < 8) {
          constexpr int c = st / 3, i = c >> 2, T = (c >> 1) & 1;
          if constexpr ((c & 1) == 0) glo = gelu2(ca[i][T][0], ca[i][T][1]);
          else *(LAS u32x2*)(hp + T * 16 * AS + i * 32) = u32x2{glo, gelu2(ca[i][T][2], ca[i][T][3])};
        }
      });
      STAMP(0, 13);
      bar_lds();
      gemm_p<3, 84, 0, 2, 7, 110, 18, 3, true>(ap1, ring, rw, voff, acc, [&](auto stc) {
        constexpr int st = decltype(stc)::value;
        if constexpr (st % 2 == 1 && st / 2 < 8) {
          constexpr int c = st / 2, i = c >> 2, T = (c >> 1) & 1;
          if constexpr ((c & 1) == 0) glo = gelu2(cb2[i][T][0], cb2[i][T][1]);
          else *(LAS u32x2*)(hp + T * 16 * AS + (2 + i) * 32) = u32x2{glo, gelu2(cb2[i][T][2], cb2[i][T][3])};
        }
      });
      STAMP(0, 8);
      bar_lds();
      STAMP(0, 14);
      gemm_p<3, 105, 1, 2, 7, 0, 0, 3, false>(ap1, ring, rw, voff, acc, [](auto) {});
      STAMP(0, 15);
#pragma unroll
      for (int i = 0; i < 3; ++i) {
        const int jd = (j + 1 + i) & 3;
        const u32x4 o = {sane(pk_bf16(acc[i][0][0], acc[i][0][1])), sane(pk_bf16(acc[i][0][2], acc[i][0][3])),
                         sane(pk_bf16(acc[i][1][0], acc[i][1][1])), sane(pk_bf16(acc[i][1][2], acc[i][1][3]))};
        __builtin_amdgcn_raw_buffer_store_b128(o, rs, (int)WS_P + ((pn * 4 + jd) * 3 + (2 - i)) * 7168 + pslot, 0, AUX_SC1);
      }
      SB0();
    }
    STAMP(0, 9);
    f32x4 acc0 = {0.f, 0.f, 0.f, 0.f}, acc1 = acc0;
    u32x4 v[3];
    const int go = (int)WS_P + (pn * 4 + j) * 3 * 7168 + pslot;
    const int gof[3] = {go, go + 7168, go + 2 * 7168};
    {
      const LAS unsigned char* wl = L + G_WL + w * (NLDS * 1024) + voff;
      u32x4 wa[14], b0[14], b1[14];
#pragma unroll
      for (int ks = 0; ks < 14; ++ks) {
        const int f = 126 + ks;
        wa[ks] = f < NRING ? ring[f % RING] : *(const LAS u32x4*)(wl + (f - NRING) * 1024);
        b0[ks] = *(const LAS u32x4*)(ap1 + ks * 64);
        b1[ks] = *(const LAS u32x4*)(ap1 + 16 * AS + ks * 64);
      }
      SB0();
#pragma unroll
      for (int ks = 0; ks < 14; ++ks) {
        acc0 = mfma16(wa[ks], b0[ks], acc0);
        acc1 = mfma16(wa[ks], b1[ks], acc1);
      }
      SB0();
    }
    STAMP(0, 10);
    poll_load<3>(rs, gof, v);
    STAMP(0, 11);
    u32x2 ov[2];
#pragma unroll
    for (int T = 0; T < 2; ++T) {
      const f32x4 ac = T ? acc1 : acc0;
      float hh[4];
#pragma unroll
      for (int k = 0; k < 2; ++k) {
        hh[2 * k] = ac[2 * k] + ((bf_lo(v[0][2 * T + k]) + bf_lo(v[1][2 * T + k])) + bf_lo(v[2][2 * T + k]));
        hh[2 * k + 1] = ac[2 * k + 1] + ((bf_hi(v[0][2 * T + k]) + bf_hi(v[1][2 * T + k])) + bf_hi(v[2][2 * T + k]));
      }
      unsigned a = pk_bf16(hh[0], hh[1]), b = pk_bf16(hh[2], hh[3]);
      a = pk_bf16(bf_lo(x1r[T].x) + bf_lo(a), bf_hi(x1r[T].x) + bf_hi(a));
      b = pk_bf16(bf_lo(x1r[T].y) + bf_lo(b), bf_hi(x1r[T].y) + bf_hi(b));
      a = pk_bf16(bf_lo(gbp.x) * bf_lo(a), bf_hi(gbp.x) * bf_hi(a));
      b = pk_bf16(bf_lo(gbp.y) * bf_lo(b), bf_hi(gbp.y) * bf_hi(b));
      u32x2 o;
      o.x = pk_bf16(bf_lo(x2r[T].x) + bf_lo(a), bf_hi(x2r[T].x) + bf_hi(a));
      o.y = pk_bf16(bf_lo(x2r[T].y) + bf_lo(b), bf_hi(x2r[T].y) + bf_hi(b));
      ov[T] = o;
    }
    {
      auto rx = __builtin_amdgcn_permlane16_swap(ov[0].x, ov[1].x, false, false);
      auto ry = __builtin_amdgcn_permlane16_swap(ov[0].y, ov[1].y, false, false);
      const __amdgpu_buffer_rsrc_t rout = __builtin_amdgcn_make_buffer_rsrc((void*)p.out, 0, L_ * D_ * 2, 0x00020000);
      __builtin_amdgcn_raw_buffer_store_b128(u32x4{rx[0], ry[0], rx[1], ry[1]}, rout, ((pn * 32 + tok) * D_ + j * 112 + octc * 8) * 2, 0, 16);
    }
  }
  STAMP(0, 20);
}

__global__ __launch_bounds__(512, 2) void k_all(GArgs p) {
  extern __shared__ __attribute__((aligned(16))) unsigned char smem[];
  const uintptr_t code0 = (uintptr_t)__builtin_amdgcn_s_getpc() & ~(uintptr_t)127;
  body(p, (LAS unsigned char*)smem, code0);
}

}

extern "C" void kernel_launch(void* const* d_in, const int* in_sizes, int n_in, void* d_out, int out_size, void* d_ws,
                              size_t ws_size, hipStream_t stream) {
  (void)in_sizes; (void)n_in; (void)out_size; (void)ws_size;
  GArgs a;
  a.x = (const unsigned short*)d_in[0];
  a.a_re = (const unsigned short*)d_in[1];
  a.a_im = (const unsigned short*)d_in[2];
  a.b_re = (const unsigned short*)d_in[3];
  a.b_im = (const unsigned short*)d_in[4];
  a.c_re = (const unsigned short*)d_in[5];
  a.c_im = (const unsigned short*)d_in[6];
  a.log_dt = (const unsigned short*)d_in[7];
  a.dskip = (const unsigned short*)d_in[8];
  a.w1 = (const unsigned short*)d_in[10];
  a.w2 = (const unsigned short*)d_in[11];
  a.w3 = (const unsigned short*)d_in[12];
  a.w4 = (const unsigned short*)d_in[13];
  a.cvec = (const unsigned short*)d_in[14];
  a.wmod = (const unsigned short*)d_in[15];
  a.ws = (unsigned char*)d_ws;
  a.out = (unsigned short*)d_out;
  (void)hipFuncSetAttribute((const void*)k_all, hipFuncAttributeMaxDynamicSharedMemorySize, G_LDS);
  k_all<<<256, 512, G_LDS, stream>>>(a);
}
```

```cpp
#include <hip/hip_runtime.h>
#include <hip/hip_bf16.h>
#include <stdint.h>
#include <type_traits>
#include <utility>

namespace {

typedef __attribute__((ext_vector_type(8))) short bf16x8;
typedef __attribute__((ext_vector_type(8))) _Float16 f16x8;
typedef __attribute__((ext_vector_type(2))) _Float16 f16x2;
typedef __attribute__((ext_vector_type(4))) float f32x4;
typedef __attribute__((ext_vector_type(2))) float f32x2;
typedef __attribute__((ext_vector_type(4))) unsigned u32x4;
typedef __attribute__((ext_vector_type(2))) unsigned u32x2;
typedef __attribute__((ext_vector_type(2))) __bf16 bf16x2v;
#define GAS __attribute__((address_space(1)))
#define LAS __attribute__((address_space(3)))

constexpr int L_ = 2048, D_ = 448, NS_ = 256;

constexpr size_t WS_EPOCH = 0;
constexpr size_t WS_FLAGC = 4608;
constexpr size_t WS_MODG = 16384;
constexpr size_t WS_STG = 32768;
constexpr int NFRAG = 140;
constexpr size_t WS_XG = 65536;
constexpr size_t XG_BLK = 224 * 64;
constexpr size_t WS_WP = WS_XG + 256 * XG_BLK;
constexpr size_t WS_X2B = WS_WP + (size_t)28 * NFRAG * 1024;
constexpr size_t X2B_BLK = 224 * 128;
constexpr size_t WS_G = WS_X2B + 64 * X2B_BLK;
constexpr size_t WS_X1 = WS_G + (size_t)64 * 4 * 7168;
constexpr size_t WS_P = WS_X1 + (size_t)64 * 4 * 7168;
constexpr size_t WS_END = WS_P + (size_t)64 * 12 * 7168;

#define STAMP(base, k) do { } while (0)

__device__ __forceinline__ float bf_lo(unsigned u) { return __builtin_bit_cast(float, u << 16); }
__device__ __forceinline__ float bf_hi(unsigned u) { return __builtin_bit_cast(float, u & 0xffff0000u); }
__device__ __forceinline__ float bf2f(unsigned short h) { return __builtin_bit_cast(float, (unsigned)h << 16); }
__device__ __forceinline__ unsigned pk_bf16(float a, float b) {
  f32x2 v = {a, b};
  bf16x2v r = __builtin_convertvector(v, bf16x2v);
  return __builtin_bit_cast(unsigned, r);
}
__device__ __forceinline__ unsigned pk_f16(f32x2 v) {
  f16x2 r = __builtin_convertvector(v, f16x2);
  return __builtin_bit_cast(unsigned, r);
}
__device__ __forceinline__ float rbf(float a) { return bf_lo(pk_bf16(a, a)); }
__device__ __forceinline__ f32x4 mfma16(u32x4 a, u32x4 b, f32x4 c) {
  return __builtin_amdgcn_mfma_f32_16x16x32_bf16(__builtin_bit_cast(bf16x8, a), __builtin_bit_cast(bf16x8, b), c, 0, 0, 0);
}
__device__ __forceinline__ f32x4 mfma16h(u32x4 a, u32x4 b, f32x4 c) {
  return __builtin_amdgcn_mfma_f32_16x16x32_f16(__builtin_bit_cast(f16x8, a), __builtin_bit_cast(f16x8, b), c, 0, 0, 0);
}
template <int CTRL>
__device__ __forceinline__ float dpp_f(float v) {
  return __builtin_bit_cast(float, __builtin_amdgcn_update_dpp(0, __builtin_bit_cast(int, v), CTRL, 0xf, 0xf, true));
}
__device__ __forceinline__ float row16_sum(float v) {
  v += dpp_f<0xB1>(v);
  v += dpp_f<0x4E>(v);
  v += dpp_f<0x141>(v);
  v += dpp_f<0x140>(v);
  return v;
}
__device__ __forceinline__ float row16_max(float v) {
  v = fmaxf(v, dpp_f<0xB1>(v));
  v = fmaxf(v, dpp_f<0x4E>(v));
  v = fmaxf(v, dpp_f<0x141>(v));
  v = fmaxf(v, dpp_f<0x140>(v));
  return v;
}
__device__ __forceinline__ void swap16(float v, float& a, float& b) {
  const unsigned u = __builtin_bit_cast(unsigned, v), u2 = u;
  auto r = __builtin_amdgcn_permlane16_swap(u, u2, false, false);
  const unsigned r0 = r[0], r1 = r[1];
  a = __builtin_bit_cast(float, r0); b = __builtin_bit_cast(float, r1);
}
__device__ __forceinline__ void swap32(float v, float& a, float& b) {
  const unsigned u = __builtin_bit_cast(unsigned, v), u2 = u;
  auto r = __builtin_amdgcn_permlane32_swap(u, u2, false, false);
  const unsigned r0 = r[0], r1 = r[1];
  a = __builtin_bit_cast(float, r0); b = __builtin_bit_cast(float, r1);
}
__device__ __forceinline__ float rows_sum(float v) {
  float a, b;
  swap16(v, a, b); v = a + b;
  swap32(v, a, b);
  return a + b;
}
__device__ __forceinline__ float rows_max(float v) {
  float a, b;
  swap16(v, a, b); v = fmaxf(a, b);
  swap32(v, a, b);
  return fmaxf(a, b);
}
template <int CTRL, int RMASK>
__device__ __forceinline__ float dpp_z(float v) {
  return __builtin_bit_cast(float, __builtin_amdgcn_update_dpp(0, __builtin_bit_cast(int, v), CTRL, RMASK, 0xf, false));
}
__device__ __forceinline__ float wave_scan(float v) {
  v += dpp_z<0x111, 0xf>(v);
  v += dpp_z<0x112, 0xf>(v);
  v += dpp_z<0x114, 0xf>(v);
  v += dpp_z<0x118, 0xf>(v);
  v += dpp_z<0x142, 0xa>(v);
  v += dpp_z<0x143, 0xc>(v);
  return v;
}
__device__ __forceinline__ float wave_sum(float v) { return rows_sum(row16_sum(v)); }
__device__ __forceinline__ float gelu_tanh(float x) {
  const float c1 = -2.0f * 0.7978845608028654f * 1.4426950408889634f;
  const float c2 = c1 * 0.044715f;
  float t = x * (c1 + c2 * x * x);
  float e = __builtin_amdgcn_exp2f(t);
  return x * __builtin_amdgcn_rcpf(1.0f + e);
}
__device__ __forceinline__ float ln_sd(float var) { return rbf(__builtin_amdgcn_sqrtf(rbf(rbf(var) + 1e-5f))); }
__device__ __forceinline__ void lds_fence() { asm volatile("" ::: "memory"); }
__device__ __forceinline__ void bar_lds() {
  asm volatile("s_waitcnt lgkmcnt(0)" ::: "memory");
  __builtin_amdgcn_s_barrier();
  asm volatile("" ::: "memory");
}

__device__ __forceinline__ void transpose8x8(const u32x4 (&r)[8], u32x4 (&o)[8]) {
#pragma unroll
  for (int c2 = 0; c2 < 4; ++c2) {
#pragma unroll
    for (int rp = 0; rp < 4; ++rp) {
      unsigned a = r[2 * rp][c2], b = r[2 * rp + 1][c2];
      o[2 * c2][rp] = __builtin_amdgcn_perm(b, a, 0x05040100u);
      o[2 * c2 + 1][rp] = __builtin_amdgcn_perm(b, a, 0x07060302u);
    }
  }
}
__device__ __forceinline__ f32x2 cmul(f32x2 a, f32x2 b) {
  f32x2 r;
  asm("v_pk_mul_f32 %0, %1, %2 op_sel_hi:[0,1]\n\tv_pk_fma_f32 %0, %1, %2, %0 op_sel:[1,1,0] op_sel_hi:[1,0,1] neg_lo:[1,0,0]"
      : "=&v"(r) : "v"(a), "v"(b));
  return r;
}
__device__ __forceinline__ f32x2 cexp_tab(float a, float b, float l) {
  const float e = __builtin_amdgcn_exp2f(a * l), ph = __builtin_amdgcn_fractf(b * l);
  f32x2 r;
  r.x = e * __builtin_amdgcn_cosf(ph);
  r.y = e * __builtin_amdgcn_sinf(ph);
  return r;
}

constexpr int CV_KCOPY = 8 * 272 * 16;
constexpr int CV_UROW = 160;
constexpr int CV_U = 48 * CV_UROW;
constexpr int CV_CH = CV_KCOPY + CV_U;
constexpr int CV_LDS = 2 * CV_CH;
constexpr int CV_PROW = 272;
static_assert(4 * 32 * CV_PROW <= CV_KCOPY, "partials alias the kernel copies");

struct GArgs {
  const unsigned short* x;
  const unsigned short* a_re;
  const unsigned short* a_im;
  const unsigned short* b_re;
  const unsigned short* b_im;
  const unsigned short* c_re;
  const unsigned short* c_im;
  const unsigned short* log_dt;
  const unsigned short* dskip;
  const unsigned short* w1;
  const unsigned short* w2;
  const unsigned short* w3;
  const unsigned short* w4;
  const unsigned short* cvec;
  const unsigned short* wmod;
  unsigned char* ws;
  unsigned short* out;
};
constexpr int AS = 928;
constexpr int G_T0 = 0;
constexpr int G_T1 = G_T0 + 32 * AS;
constexpr int G_X = G_T1;
constexpr int G_HQ = G_X + 32768;
constexpr int G_WL = G_HQ + 32 * 240;
constexpr int NLDS = 12;
constexpr int NRING = NFRAG - NLDS;
constexpr int G_MOD = G_WL + 7 * NLDS * 1024;
constexpr int G_CNT = G_MOD + 2 * 448 * 4;
constexpr int P0_MS = G_CNT + 16;
constexpr int G_LDS = P0_MS + 256;
constexpr int KG_ROW = 288;
constexpr int KG_BUF = 16 * KG_ROW;
constexpr int P0_KG = CV_LDS;
constexpr int P0_LN = 0;
constexpr int LNS = 928;
constexpr int P0_PK = 16384;
static_assert(P0_KG + 16 * KG_BUF <= G_CNT, "tables");
static_assert(G_MOD >= CV_LDS && G_LDS <= 163840, "LDS map");
constexpr int RING = 44;
constexpr int CV_P = CV_LDS;
static_assert(CV_P + 8 * 32 * CV_PROW <= G_MOD, "partials");
constexpr unsigned SPIN_CAP = 1u << 17;
constexpr int AUX_SC1 = 16;
constexpr int CODE_LINES = 320;
#define PRETOUCH 1
#define X2_DELAY 20
#define POLL_SLEEP 1

__device__ __forceinline__ unsigned sane(unsigned v) { return v < 0xFFFFFFFEu ? v : 0xFFFFFFFEu; }
__device__ __forceinline__ unsigned umax4(const u32x4& v) { return max(max(v.x, v.y), max(v.z, v.w)); }
__device__ __forceinline__ bool bad4(const u32x4& v) { return umax4(v) == ~0u; }
__device__ __forceinline__ u32x4 make_piece(u32x2 a, u32x2 b) {
  auto rx = __builtin_amdgcn_permlane16_swap(a.x, b.x, false, false);
  auto ry = __builtin_amdgcn_permlane16_swap(a.y, b.y, false, false);
  return u32x4{sane(rx[0]), sane(ry[0]), sane(rx[1]), sane(ry[1])};
}
template <int N>
__device__ __forceinline__ void poll_issue(__amdgpu_buffer_rsrc_t r, const int (&off)[N], u32x4 (&v)[N]) {
#pragma unroll
  for (int i = 0; i < N; ++i) v[i] = __builtin_amdgcn_raw_buffer_load_b128(r, off[i], 0, AUX_SC1);
  __builtin_amdgcn_sched_barrier(0);
}
template <int N>
__device__ __forceinline__ void poll_finish(__amdgpu_buffer_rsrc_t r, const int (&off)[N], u32x4 (&v)[N]) {
  unsigned m = 0u;
#pragma unroll
  for (int i = 0; i < N; ++i) m = max(m, umax4(v[i]));
  if (__builtin_expect(__any(m == ~0u), 0)) {
    for (unsigned spins = 0; spins < SPIN_CAP; ++spins) {
      __builtin_amdgcn_s_sleep(POLL_SLEEP);
      asm volatile("" ::: "memory");
#pragma unroll
      for (int i = 0; i < N; ++i)
        if (bad4(v[i])) v[i] = __builtin_amdgcn_raw_buffer_load_b128(r, off[i], 0, AUX_SC1);
      m = 0u;
#pragma unroll
      for (int i = 0; i < N; ++i) m = max(m, umax4(v[i]));
      if (!__any(m == ~0u)) break;
    }
  }
}
template <int N>
__device__ __forceinline__ void poll_load(__amdgpu_buffer_rsrc_t r, const int (&off)[N], u32x4 (&v)[N]) {
  poll_issue<N>(r, off, v);
  poll_finish<N>(r, off, v);
}
__device__ __forceinline__ float dot2bf(unsigned a, unsigned b, float c) {
  return __builtin_amdgcn_fdot2_f32_bf16(__builtin_bit_cast(bf16x2v, a), __builtin_bit_cast(bf16x2v, b), c, false);
}
__device__ __forceinline__ void ln_stats(const unsigned (&x)[16], float lmsk, float& r, float& mr) {
  float s0 = 0.f, s1 = 0.f, q0 = 0.f, q1 = 0.f;
#pragma unroll
  for (int k = 0; k < 16; k += 2) {
    s0 = dot2bf(x[k], 0x3f803f80u, s0); q0 = dot2bf(x[k], x[k], q0);
    s1 = dot2bf(x[k + 1], 0x3f803f80u, s1); q1 = dot2bf(x[k + 1], x[k + 1], q1);
  }
  const float mean = row16_sum((s0 + s1) * lmsk) * (1.0f / D_);
  const float msq = row16_sum((q0 + q1) * lmsk) * (1.0f / D_);
  const float var = fmaxf(msq - mean * mean, 0.f);
  r = __builtin_amdgcn_rcpf(ln_sd(var));
  mr = -rbf(mean) * r;
}

template <class F, int... I>
__device__ __forceinline__ void static_for_impl(F f, std::integer_sequence<int, I...>) { (f(std::integral_constant<int, I>{}), ...); }
template <int N, class F>
__device__ __forceinline__ void static_for(F f) { static_for_impl(f, std::make_integer_sequence<int, N>{}); }
#define SB0() __builtin_amdgcn_sched_barrier(0)
#define WLOAD(f) __builtin_amdgcn_raw_buffer_load_b128(rw, voff + ((f) & 3) * 1024, ((f) >> 2) * 4096, AUX_SC1)
template <int NT, int F0, int LSTART, int LNUM, int LEVERY, int PD>
__device__ __forceinline__ void gemm14(const LAS unsigned char* ap, u32x4 (&ring)[RING], __amdgpu_buffer_rsrc_t rw, int voff, f32x4 (&acc)[NT][2]) {
  u32x4 bq[PD][2];
#pragma unroll
  for (int i = 0; i < PD; ++i) { bq[i][0] = *(const LAS u32x4*)(ap + i * 64); bq[i][1] = *(const LAS u32x4*)(ap + 16 * AS + i * 64); }
#pragma unroll
  for (int i = 0; i < NT; ++i) { acc[i][0] = f32x4{0.f, 0.f, 0.f, 0.f}; acc[i][1] = acc[i][0]; }
  SB0();
#pragma unroll
  for (int ks = 0; ks < 14; ++ks) {
#pragma unroll
    for (int i = 0; i < NT; ++i) {
      const int st = ks * NT + i, f = F0 + st;
      acc[i][0] = mfma16(ring[f % RING], bq[ks % PD][0], acc[i][0]);
      acc[i][1] = mfma16(ring[f % RING], bq[ks % PD][1], acc[i][1]);
      if (st % LEVERY == 0 && st / LEVERY < LNUM) {
        const int g = LSTART + st / LEVERY;
        ring[g % RING] = WLOAD(g);
      }
      if (i == NT - 1 && ks + PD < 14) {
        bq[ks % PD][0] = *(const LAS u32x4*)(ap + (ks + PD) * 64);
        bq[ks % PD][1] = *(const LAS u32x4*)(ap + 16 * AS + (ks + PD) * 64);
      }
      SB0();
    }
  }
}
template <int NT, int F0, int KS0, int KSTEP, int NKS, int LSTART, int LNUM, int PD, bool INIT, class S>
__device__ __forceinline__ void gemm_p(const LAS unsigned char* ap, u32x4 (&ring)[RING], __amdgpu_buffer_rsrc_t rw, int voff, f32x4 (&acc)[NT][2], S side) {
  u32x4 bq[PD][2];
#pragma unroll
  for (int i = 0; i < PD; ++i) {
    bq[i][0] = *(const LAS u32x4*)(ap + (KS0 + i * KSTEP) * 64);
    bq[i][1] = *(const LAS u32x4*)(ap + 16 * AS + (KS0 + i * KSTEP) * 64);
  }
  if (INIT) {
#pragma unroll
    for (int i = 0; i < NT; ++i) { acc[i][0] = f32x4{0.f, 0.f, 0.f, 0.f}; acc[i][1] = acc[i][0]; }
  }
  SB0();
  static_for<NKS * NT>([&](auto stc) {
    constexpr int st = decltype(stc)::value, k = st / NT, i = st % NT, f = F0 + st;
    acc[i][0] = mfma16(ring[f % RING], bq[k % PD][0], acc[i][0]);
    acc[i][1] = mfma16(ring[f % RING], bq[k % PD][1], acc[i][1]);
    constexpr int l0 = st * LNUM / (NKS * NT), l1 = (st + 1) * LNUM / (NKS * NT);
    if constexpr (l1 > l0) {
      static_assert(LSTART + l0 - RING <= f, "slot still in use");
      ring[(LSTART + l0) % RING] = WLOAD(LSTART + l0);
    }
    if constexpr (i == NT - 1 && k + PD < NKS) {
      bq[k % PD][0] = *(const LAS u32x4*)(ap + (KS0 + (k + PD) * KSTEP) * 64);
      bq[k % PD][1] = *(const LAS u32x4*)(ap + 16 * AS + (KS0 + (k + PD) * KSTEP) * 64);
    }
    side(stc);
    SB0();
  });
}
template <int F0, int N>
__device__ __forceinline__ void ring_fill(u32x4 (&ring)[RING], __amdgpu_buffer_rsrc_t rw, int voff) {
#pragma unroll
  for (int f = F0; f < F0 + N; ++f) { ring[f % RING] = WLOAD(f); SB0(); }
}

template <int DELAY>
__device__ __forceinline__ void gather_x2(const GArgs& p, LAS unsigned char* L, __amdgpu_buffer_rsrc_t rs, int pn, int tid) {
  const int lane = tid & 63, w = tid >> 6; (void)lane; (void)w; (void)p;
  u32x4 xin[4];
  int xo[4];
#pragma unroll
  for (int i = 0; i < 4; ++i) {
    const int pce = tid + 512 * i;
    xo[i] = pce < 1792 ? (int)WS_X2B + pn * (int)X2B_BLK + pce * 16 : (int)WS_END;
  }
  if (DELAY) __builtin_amdgcn_s_sleep(DELAY);
  poll_issue<4>(rs, xo, xin);
  poll_finish<4>(rs, xo, xin);
  STAMP(0, 26);
#pragma unroll
  for (int i = 0; i < 4; ++i) {
    const int pce = tid + 512 * i;
    if (i < 3 || pce < 1792) {
      const int dpi = pce >> 3, tq = pce & 7;
      LAS unsigned char* dst = L + G_X + tq * 4096 + (((dpi >> 2) ^ tq) * 16) + (dpi & 3) * 4;
#pragma unroll
      for (int k = 0; k < 4; ++k) *(LAS unsigned*)(dst + k * 1024) = xin[i][k];
    }
  }
}
template <class H>
__device__ __forceinline__ void ln_phase_a(LAS unsigned char* L, int j, int w, int r16, int q, H hook) {
  const int c16 = r16 < 14 ? r16 : 0;
  const float lmsk = r16 < 14 ? 1.0f : 0.0f;
  const int row = 4 * w + q;
  unsigned x[16];
#pragma unroll
  for (int jq = 0; jq < 4; ++jq) {
    const u32x4 t = *(const LAS u32x4*)(L + G_X + row * 1024 + (((((jq + j) & 3) * 14 + c16) ^ w) * 16));
    x[4 * jq] = t.x; x[4 * jq + 1] = t.y; x[4 * jq + 2] = t.z; x[4 * jq + 3] = t.w;
  }
  float r, mr;
  ln_stats(x, lmsk, r, mr);
  hook(std::integral_constant<int, 0>{});
  f32x2 h[16];
#pragma unroll
  for (int jq = 0; jq < 4; ++jq) {
    const LAS unsigned char* mp = L + G_MOD + (((jq + j) & 3) * 112 + c16 * 8) * 4;
    const f32x4 sb0 = *(const LAS f32x4*)mp, sb1 = *(const LAS f32x4*)(mp + 16);
    const f32x4 c10 = *(const LAS f32x4*)(mp + 1792), c11 = *(const LAS f32x4*)(mp + 1792 + 16);
#pragma unroll
    for (int k = 0; k < 4; ++k) {
      const f32x2 xv2 = {bf_lo(x[4 * jq + k]), bf_hi(x[4 * jq + k])};
      const f32x2 c2 = k < 2 ? f32x2{c10[2 * k], c10[2 * k + 1]} : f32x2{c11[2 * k - 4], c11[2 * k - 3]};
      const f32x2 sb2 = k < 2 ? f32x2{sb0[2 * k], sb0[2 * k + 1]} : f32x2{sb1[2 * k - 4], sb1[2 * k - 3]};
      const f32x2 n = xv2 * r + mr;
      h[4 * jq + k] = n * c2 + sb2;
      x[4 * jq + k] = pk_bf16(h[4 * jq + k].x, h[4 * jq + k].y);
    }
  }
  *(LAS u32x4*)(L + G_HQ + row * 240 + (r16 < 14 ? r16 : 14) * 16) = u32x4{x[0], x[1], x[2], x[3]};
  hook(std::integral_constant<int, 1>{});
  ln_stats(x, lmsk, r, mr);
  hook(std::integral_constant<int, 2>{});
#pragma unroll
  for (int jq = 0; jq < 4; ++jq) {
    u32x4 o;
#pragma unroll
    for (int k = 0; k < 4; ++k) {
      const f32x2 n = h[4 * jq + k] * r + mr;
      o[k] = pk_bf16(n.x, n.y);
    }
    *(LAS u32x4*)(L + G_T0 + row * AS + (r16 < 14 ? (((jq + j) & 3) * 14 + r16) * 16 : 896)) = o;
  }
}
__device__ __forceinline__ void x1_offsets(int pn, int j, int w, int r16, int q, int (&gof)[3]) {
  const int c16 = r16 < 14 ? r16 : 0;
  const int go = (int)WS_X1 + pn * 4 * 7168 + ((c16 >> 1) * 64 + w * 8 + (c16 & 1) * 4 + q) * 16;
#pragma unroll
  for (int i = 0; i < 3; ++i) gof[i] = go + ((j + 1 + i) & 3) * 7168;
}
__device__ __forceinline__ void ln_phase_c(LAS unsigned char* L, const u32x4 (&xr)[3], int j, int w, int r16, int q) {
  const int c16 = r16 < 14 ? r16 : 0;
  const float lmsk = r16 < 14 ? 1.0f : 0.0f;
  const int row = 4 * w + q;
  const u32x4 own = *(const LAS u32x4*)(L + G_HQ + row * 240 + c16 * 16);
  unsigned x[16];
#pragma unroll
  for (int k = 0; k < 4; ++k) x[k] = own[k];
#pragma unroll
  for (int k = 4; k < 16; ++k) x[k] = xr[(k >> 2) - 1][k & 3];
  float r, mr;
  ln_stats(x, lmsk, r, mr);
#pragma unroll
  for (int jq = 0; jq < 4; ++jq) {
    u32x4 o;
#pragma unroll
    for (int k = 0; k < 4; ++k) {
      const f32x2 n = f32x2{bf_lo(x[4 * jq + k]), bf_hi(x[4 * jq + k])} * r + mr;
      o[k] = pk_bf16(n.x, n.y);
    }
    *(LAS u32x4*)(L + G_T0 + row * AS + (r16 < 14 ? (((jq + j) & 3) * 14 + r16) * 16 : 896)) = o;
  }
}
__device__ __forceinline__ unsigned gelu2(float a, float b) {
  return pk_bf16(gelu_tanh(a), gelu_tanh(b));
}
__device__ __forceinline__ u32x2 gelu4(f32x4 a) { return u32x2{gelu2(a[0], a[1]), gelu2(a[2], a[3])}; }

#define ST16(off, v) __builtin_amdgcn_raw_buffer_store_b128(v, rs, (int)(off), 0, AUX_SC1)
typedef unsigned long long u64;

struct PackJob { const GAS unsigned short* src; int ldn, mat, ks, cb, na, nb, fb; };
__device__ __forceinline__ PackJob pack_job(const GArgs& p, int g) {
  PackJob jb;
  const uintptr_t a1 = (uintptr_t)p.w1, a2 = (uintptr_t)p.w2, a3 = (uintptr_t)p.w3, a4 = (uintptr_t)p.w4;
  uintptr_t lo = g < 56 ? a1 : a2, hi = g < 336 ? a3 : a4;
  asm volatile("" : "+s"(lo), "+s"(hi));
  jb.src = (const GAS unsigned short*)(g < 112 ? lo : hi);
  jb.mat = (g >= 56) + (g >= 112) + (g >= 336);
  jb.ldn = jb.mat == 2 ? 1792 : 448;
  g -= g < 112 ? (g < 56 ? 0 : 56) : (g < 336 ? 112 : 336);
  const int sh = jb.mat == 2 ? 4 : 2;
  jb.ks = g >> sh;
  jb.cb = g & ((1 << sh) - 1);
  const int j3 = (jb.ks * 4682) >> 16, ksl = jb.ks - j3 * 14, sl = (jb.cb - j3 - 1) & 3;
  const int is2 = jb.mat == 2, is3 = jb.mat == 3;
  jb.na = is2 * jb.cb * 7 + (1 - is2) * (is3 * j3 + (1 - is3) * jb.cb) * 28;
  jb.nb = 4 - 3 * is2;
  const int own = sl == 3;
  const int f3 = (1 - own) * (84 + (ksl & 1) * 21 + (ksl >> 1) * 3 + sl) + own * (126 + ksl);
  jb.fb = is2 * (28 + jb.ks * 2) + is3 * f3 + (1 - is2 - is3) * (jb.mat * 14 + jb.ks);
  jb.na = __builtin_amdgcn_readfirstlane(jb.na); jb.nb = __builtin_amdgcn_readfirstlane(jb.nb); jb.fb = __builtin_amdgcn_readfirstlane(jb.fb);
  return jb;
}
__device__ __forceinline__ void pack_load(const PackJob& jb, int lane, u32x4 (&r)[8]) {
  const int jn = lane & 15, q = lane >> 4, jl = jn < 14 ? jn : 0;
#pragma unroll
  for (int i = 0; i < 8; ++i) r[i] = *(const GAS u32x4*)(jb.src + (size_t)(jb.ks * 32 + q * 8 + i) * jb.ldn + jb.cb * 112 + jl * 8);
}
__device__ __forceinline__ void pack_shuffle(int lane, u32x4 (&r)[8], LAS unsigned char* Lw) {
  const int jn = lane & 15, q = lane >> 4;
  u32x4 o[8];
  transpose8x8(r, o);
#pragma unroll
  for (int i = 0; i < 8; ++i) *(LAS u32x4*)(Lw + (jn >> 1) * 1024 + (q * 16 + (jn & 1) * 8 + (i ^ (jn >> 1))) * 16) = o[i];
  lds_fence();
#pragma unroll
  for (int m = 0; m < 7; ++m) r[m] = *(const LAS u32x4*)(Lw + m * 1024 + (lane ^ m) * 16);
  lds_fence();
}
__device__ __forceinline__ void pack_store1(const PackJob& jb, int lane, const u32x4 (&r)[8], __amdgpu_buffer_rsrc_t rs, int m) {
  const int nt = jb.na + m * jb.nb;
  __builtin_amdgcn_raw_buffer_store_b128(r[m], rs, lane * 16, (int)WS_WP + ((nt >> 2) * NFRAG + jb.fb + ((nt >> 1) & 1) * 28 + (nt & 1)) * 1024, AUX_SC1);
}
__device__ __forceinline__ void mod_load(const GArgs& p, int u, int tid, u32x4 (&r)[7], float (&sc)[7]) {
  const int rg = tid >> 3, pc = tid & 7;
#pragma unroll
  for (int i = 0; i < 7; ++i) r[i] = *(const u32x4*)(p.wmod + (size_t)(rg + 64 * i) * 2688 + u * 64 + pc * 8);
#pragma unroll
  for (int i = 0; i < 7; ++i) sc[i] = bf2f(p.cvec[rg + 64 * i]);
}
__device__ __forceinline__ void mod_finish(int u, unsigned tag32, int tid, const u32x4 (&r)[7], const float (&cv)[7], LAS float* sm, __amdgpu_buffer_rsrc_t rs) {
  const int rg = tid >> 3, pc = tid & 7;
  float acc[8];
#pragma unroll
  for (int k = 0; k < 8; ++k) acc[k] = 0.f;
#pragma unroll
  for (int i = 0; i < 7; ++i) {
    const float sc = rbf(cv[i] * __builtin_amdgcn_rcpf(1.0f + __builtin_amdgcn_exp2f(-1.4426950408889634f * cv[i])));
#pragma unroll
    for (int k = 0; k < 4; ++k) { acc[2 * k] += sc * bf_lo(r[i][k]); acc[2 * k + 1] += sc * bf_hi(r[i][k]); }
  }
  *(LAS f32x4*)(sm + rg * 68 + pc * 8) = f32x4{acc[0], acc[1], acc[2], acc[3]};
  *(LAS f32x4*)(sm + rg * 68 + pc * 8 + 4) = f32x4{acc[4], acc[5], acc[6], acc[7]};
  bar_lds();
  {
    const int col = tid & 63, part = tid >> 6;
    float s = 0.f;
#pragma unroll
    for (int k = 0; k < 8; ++k) s += sm[(part * 8 + k) * 68 + col];
    sm[64 * 68 + part * 64 + col] = s;
  }
  bar_lds();
  if (tid < 32) {
    f32x2 s = {0.f, 0.f};
#pragma unroll
    for (int k = 0; k < 8; ++k) s += *(const LAS f32x2*)(sm + 64 * 68 + k * 64 + 2 * tid);
    unsigned v = pk_bf16(s.x, s.y);
    const int sec = u / 7;
    if (sec == 1 || sec == 4) v = pk_bf16(bf_lo(v) + 1.0f, bf_hi(v) + 1.0f);
    __builtin_amdgcn_raw_buffer_store_b64(u32x2{v, tag32}, rs, (int)WS_MODG + (u * 32 + tid) * 8, 0, AUX_SC1);
  }
}
template <int YOUNGER>
__device__ __forceinline__ void arrive_flag(LAS unsigned char* L, int cnt_slot, unsigned char* flag, u64 tag, int lane) {
  asm volatile("s_waitcnt vmcnt(%0)" ::"i"(YOUNGER) : "memory");
  if (lane == 0) {
    const unsigned old = __hip_atomic_fetch_add((LAS unsigned*)(L + G_CNT) + cnt_slot, 1u, __ATOMIC_RELAXED, __HIP_MEMORY_SCOPE_WORKGROUP);
    if (old == 7u) __hip_atomic_store((GAS u64*)flag, tag, __ATOMIC_RELAXED, __HIP_MEMORY_SCOPE_AGENT);
  }
}
__device__ __forceinline__ u32x2 flag_load(__amdgpu_buffer_rsrc_t rs, int off) { return __builtin_amdgcn_raw_buffer_load_b64(rs, off, 0, AUX_SC1); }
__device__ __forceinline__ u32x2 flag_wait(__amdgpu_buffer_rsrc_t rs, int off, u32x2 v, unsigned lo, unsigned hi, unsigned lomask) {
  if (__builtin_expect(__any((((v.x ^ lo) & lomask) | (v.y ^ hi)) != 0u), 0)) {
    for (unsigned spins = 0; spins < SPIN_CAP; ++spins) {
      __builtin_amdgcn_s_sleep(2);
      asm volatile("" ::: "memory");
      v = flag_load(rs, off);
      if (!__any((((v.x ^ lo) & lomask) | (v.y ^ hi)) != 0u)) break;
    }
  }
  __builtin_amdgcn_fence(__ATOMIC_ACQUIRE, "wavefront");
  return v;
}
__device__ __forceinline__ void stat_wait(__amdgpu_buffer_rsrc_t rs, int off, u32x4& a, u32x4& b, unsigned tag32) {
  if (__builtin_expect(__any((((a.y ^ tag32) | (a.w ^ tag32)) | ((b.y ^ tag32) | (b.w ^ tag32))) != 0u), 0)) {
    for (unsigned spins = 0; spins < SPIN_CAP; ++spins) {
      __builtin_amdgcn_s_sleep(2);
      asm volatile("" ::: "memory");
      a = __builtin_amdgcn_raw_buffer_load_b128(rs, off, 0, AUX_SC1);
      b = __builtin_amdgcn_raw_buffer_load_b128(rs, off + 16, 0, AUX_SC1);
      if (!__any((((a.y ^ tag32) | (a.w ^ tag32)) | ((b.y ^ tag32) | (b.w ^ tag32))) != 0u)) break;
    }
  }
}
#define FILL_P(i) do { SB0(); __builtin_amdgcn_raw_buffer_store_b128(ONES, rst, (int)WS_P + ((pn * 4 + j) * 3 + (i)) * 7168 + pslot, 0, AUX_SC1); SB0(); } while (0)
#define FILL_X1() do { SB0(); __builtin_amdgcn_raw_buffer_store_b128(ONES, rst, (int)WS_X1 + (pn * 4 + j) * 7168 + pslot, 0, AUX_SC1); SB0(); } while (0)
__device__ __forceinline__ void body(const GArgs& p, LAS unsigned char* L, uintptr_t code0) {
  const bool CONV = blockIdx.x < 224;
  const int tid = threadIdx.x, lane = tid & 63;
  const int w = __builtin_amdgcn_readfirstlane(tid >> 6);
  const int bid = blockIdx.x;
  const int j = (bid >> 3) & 3, pn = (bid & 7) * 8 + (bid >> 5);
  const int r16 = lane & 15, q = lane >> 4;
  const bool cw = w < 7;
  const int wc = cw ? w : 6;
  const int dp = (bid & 7) * 28 + (CONV ? bid >> 3 : 0);
  const __amdgpu_buffer_rsrc_t rs = __builtin_amdgcn_make_buffer_rsrc((void*)p.ws, 0, (int)WS_END, 0x00020000);
  const __amdgpu_buffer_rsrc_t rst = __builtin_amdgcn_make_buffer_rsrc((void*)p.ws, 0, cw ? (int)WS_END : 0, 0x00020000);
  const __amdgpu_buffer_rsrc_t rw =
      __builtin_amdgcn_make_buffer_rsrc((void*)(p.ws + WS_WP + (size_t)(j * 7 + wc) * NFRAG * 1024), 0, cw ? NFRAG * 1024 : 0, 0x00020000);
  const int voff = lane * 16;
  const int pslot = (wc * 64 + lane) * 16;
  const u32x4 ONES = {~0u, ~0u, ~0u, ~0u};
  STAMP(0, 0);
  const int l56 = lane < 56 ? lane : 0;
  const u32x4 xrow = *(const u32x4*)(p.x + (size_t)(bid * 8 + w) * D_ + l56 * 8);
  const u32x2 ep = flag_load(rs, (int)WS_EPOCH);
  const int cl = ((bid >> 3) & 3) * 80 + w * 10 + (lane < 10 ? lane : 0);
  const unsigned touch = *(const GAS unsigned*)(code0 + (size_t)(cl < CODE_LINES ? cl : 0) * 128);
  float are, aim, b_re, b_im, cre, cim, ldt;
  u32x4 mr[7], pk[8];
  float mc[7];
  {
    const size_t o = (size_t)(2 * dp + (w >> 2)) * NS_ + (w & 3) * 64 + lane;
    SB0();
    are = bf2f(p.a_re[o]); aim = bf2f(p.a_im[o]); b_re = bf2f(p.b_re[o]); b_im = bf2f(p.b_im[o]);
    cre = bf2f(p.c_re[o]); cim = bf2f(p.c_im[o]); ldt = bf2f(p.log_dt[2 * dp + (w >> 2)]);
    SB0();
  }
  __builtin_amdgcn_s_barrier();
  __builtin_amdgcn_raw_buffer_store_b128(ONES, rst, (int)WS_X2B + bid * 7168 + pslot, 0, AUX_SC1);
  __builtin_amdgcn_raw_buffer_store_b128(ONES, rst, (int)WS_G + bid * 7168 + pslot, 0, AUX_SC1);
  SB0();
  const int pg = (bid - 224) * 6 + w;
  const bool packer = CONV ? w < 2 : (w < 6 && pg < 112);
  const PackJob jb = pack_job(p, CONV ? bid * 2 + (w & 1) : (packer ? 448 + pg : 0));
  if (tid < 4) *((LAS unsigned*)(L + G_CNT) + tid) = 0u;
  u64 tag;
  {
    const u64 e = ((u64)__builtin_amdgcn_readfirstlane(ep.y) << 32) | (unsigned)__builtin_amdgcn_readfirstlane(ep.x);
    u64 z = e + 0x9E3779B97F4A7C15ull;
    z = (z ^ (z >> 30)) * 0xBF58476D1CE4E5B9ull;
    z = (z ^ (z >> 27)) * 0x94D049BB133111EBull;
    tag = z ^ (z >> 31);
  }
  const unsigned tag32 = ((unsigned)__builtin_amdgcn_readfirstlane(ep.x) + 1u) * 0x9E3779B1u ^ 0x5bd1e995u;
  const unsigned tlo = (unsigned)tag, thi = (unsigned)(tag >> 32);
  unsigned stv;
#define PUT_STAT() do { if (lane == 0) __builtin_amdgcn_raw_buffer_store_b64(u32x2{stv, tag32}, rs, (int)WS_STG + (bid * 8 + w) * 8, 0, AUX_SC1); } while (0)
  {
    const float msk = lane < 56 ? 1.0f : 0.0f;
    float v[8];
#pragma unroll
    for (int k = 0; k < 4; ++k) { v[2 * k] = bf_lo(xrow[k]); v[2 * k + 1] = bf_hi(xrow[k]); }
    const float mean = wave_sum((((v[0] + v[1]) + (v[2] + v[3])) + ((v[4] + v[5]) + (v[6] + v[7]))) * msk) * (1.0f / D_);
    float qq = 0.f;
#pragma unroll
    for (int k = 0; k < 8; ++k) { const float d = v[k] - mean; qq += d * d; }
    const float sd = ln_sd(wave_sum(qq * msk) * (1.0f / D_));
    stv = pk_bf16(mean, sd);
    if (lane < 56) *(LAS u32x4*)(L + P0_LN + w * LNS + lane * 16) = xrow;
    bar_lds();
#pragma unroll
    for (int u = 0; u < 2; ++u) {
      const int pce = (wc * 2 + u) * 64 + lane;
      const LAS unsigned* a = (const LAS unsigned*)(L + P0_LN + (pce & 3) * (2 * LNS) + (pce >> 2) * 4);
      __builtin_amdgcn_raw_buffer_store_b128(u32x4{a[0], tag32, a[LNS / 4], tag32}, rst, (int)WS_XG + bid * (int)XG_BLK + pce * 16, 0, AUX_SC1);
    }
  }
  asm volatile("" ::"v"(touch));
  STAMP(0, 29);
  const int sto = (int)WS_STG + tid * 32;
  u32x4 ring[RING];
  if (__builtin_expect(CONV, 1)) {
    const float dt = rbf(expf(ldt));
    const float ea = (dt * are) * 1.4426950408889634f, eb = (dt * aim) * 0.15915494309189535f;
    const float bre = dt * b_re, bim = dt * b_im;
    f32x2 wc;
    wc.x = cre * bre - cim * bim;
    wc.y = -(cre * bim + cim * bre);
    asm volatile("" ::"v"(ea), "v"(eb), "v"(wc.x), "v"(wc.y));
    const float mx = rows_max(row16_max(fmaxf(fabsf(wc.x), fabsf(wc.y))));
    int ex = mx > 0.f ? __builtin_amdgcn_frexp_expf(mx) : 0;
    ex = ex < -120 ? -120 : (ex > 120 ? 120 : ex);
    const float scale = __builtin_amdgcn_ldexpf(1.0f, ex), iscale = __builtin_amdgcn_ldexpf(1.0f, -ex);
    wc.x *= iscale; wc.y *= iscale;
    LAS unsigned char* B0 = L + P0_KG + w * (2 * KG_BUF);
    LAS unsigned char* B1 = B0 + KG_BUF;
    const int wo = lane * 4;
    const int ro = r16 * KG_ROW + q * 16;
    f32x2 z8c = cexp_tab(ea, eb, 8.0f);
    z8c.y = -z8c.y;
    const f32x2 z1 = cexp_tab(ea, eb, 1.0f), z128 = cexp_tab(ea, eb, 128.0f), z1024 = cexp_tab(ea, eb, 1024.0f);
    {
      const f32x2 z16c = cmul(z8c, z8c);
      f32x2 t0 = wc, t1 = cmul(wc, z8c);
#pragma unroll
      for (int c = 0; c < 16; c += 2) {
        *(LAS unsigned*)(B0 + c * KG_ROW + wo) = pk_f16(t0);
        *(LAS unsigned*)(B0 + (c + 1) * KG_ROW + wo) = pk_f16(t1);
        if (c < 14) { t0 = cmul(t0, z16c); t1 = cmul(t1, z16c); }
      }
    }
    SB0();
    asm volatile("s_waitcnt vmcnt(2)" ::: "memory");
    PUT_STAT();
    SB0();
    f32x2 ze[8];
    ze[0] = f32x2{1.0f, 0.0f}; ze[1] = z1; ze[2] = cmul(z1, z1); ze[3] = cmul(ze[2], z1); ze[4] = cmul(ze[2], ze[2]);
    ze[5] = cmul(ze[4], z1); ze[6] = cmul(ze[4], ze[2]); ze[7] = cmul(ze[4], ze[3]);
#pragma unroll
    for (int e = 0; e < 8; ++e) {
      *(LAS unsigned*)(B1 + e * KG_ROW + wo) = pk_f16(ze[e]);
      *(LAS unsigned*)(B1 + (8 + e) * KG_ROW + wo) = pk_f16(e == 0 ? z128 : cmul(z128, ze[e]));
    }
    lds_fence();
    u32x4 tf[4];
#pragma unroll
    for (int ks = 0; ks < 4; ++ks) tf[ks] = *(const LAS u32x4*)(B0 + ro + ks * 64);
    u32x4 rf[4];
#pragma unroll
    for (int ks = 0; ks < 4; ++ks) rf[ks] = *(const LAS u32x4*)(B1 + ro + ks * 64);
    lds_fence();
    SB0();
    if (packer) pack_load(jb, lane, pk);
    SB0();
    STAMP(0, 30);
    const f32x2 p2 = cmul(z128, z128), p3 = cmul(p2, z128), p4 = cmul(p2, p2), p5 = cmul(p4, z128), p6 = cmul(p4, p2), p7 = cmul(p4, p3);
    {
#pragma unroll
      for (int e = 0; e < 4; ++e) {
        *(LAS unsigned*)(B0 + e * KG_ROW + wo) = pk_f16(e == 0 ? p2 : cmul(p2, ze[2 * e]));
        *(LAS unsigned*)(B0 + (4 + e) * KG_ROW + wo) = pk_f16(e == 0 ? p3 : cmul(p3, ze[2 * e]));
      }
      *(LAS unsigned*)(B0 + 8 * KG_ROW + wo) = pk_f16(p4);
      *(LAS unsigned*)(B0 + 9 * KG_ROW + wo) = pk_f16(cmul(p4, ze[4]));
      *(LAS unsigned*)(B0 + 10 * KG_ROW + wo) = pk_f16(p5);
      *(LAS unsigned*)(B0 + 11 * KG_ROW + wo) = pk_f16(cmul(p5, ze[4]));
      *(LAS unsigned*)(B0 + 12 * KG_ROW + wo) = pk_f16(p6);
      *(LAS unsigned*)(B0 + 13 * KG_ROW + wo) = pk_f16(cmul(p6, ze[4]));
      *(LAS unsigned*)(B0 + 14 * KG_ROW + wo) = pk_f16(p7);
      *(LAS unsigned*)(B0 + 15 * KG_ROW + wo) = pk_f16(cmul(p7, ze[4]));
    }
    f32x4 acc0 = {0.f, 0.f, 0.f, 0.f}, acc1 = acc0, acc2 = acc0;
#pragma unroll
    for (int ks = 0; ks < 4; ++ks) acc0 = mfma16h(rf[ks], tf[ks], acc0);
    {
      *(LAS unsigned*)(B1 + 0 * KG_ROW + wo) = pk_f16(z1024);
      *(LAS unsigned*)(B1 + 1 * KG_ROW + wo) = pk_f16(cmul(z1024, z128));
      *(LAS unsigned*)(B1 + 2 * KG_ROW + wo) = pk_f16(cmul(z1024, p2));
      *(LAS unsigned*)(B1 + 3 * KG_ROW + wo) = pk_f16(cmul(z1024, p3));
      *(LAS unsigned*)(B1 + 4 * KG_ROW + wo) = pk_f16(cmul(z1024, p4));
      *(LAS unsigned*)(B1 + 5 * KG_ROW + wo) = pk_f16(cmul(z1024, p5));
      *(LAS unsigned*)(B1 + 6 * KG_ROW + wo) = pk_f16(cmul(z1024, p6));
      *(LAS unsigned*)(B1 + 7 * KG_ROW + wo) = pk_f16(cmul(z1024, p7));
      *(LAS unsigned*)(B1 + 8 * KG_ROW + wo) = pk_f16(cmul(z1024, z1024));
    }
    lds_fence();
#pragma unroll
    for (int ks = 0; ks < 4; ++ks) acc1 = mfma16h(*(const LAS u32x4*)(B0 + ro + ks * 64), tf[ks], acc1);
#pragma unroll
    for (int ks = 0; ks < 4; ++ks) acc2 = mfma16h(*(const LAS u32x4*)(B1 + ro + ks * 64), tf[ks], acc2);
    lds_fence();
    {
      LAS float* red = (LAS float*)B0;
      acc0 *= scale; acc1 *= scale; acc2 *= scale;
      *(LAS f32x4*)(red + (q >> 1) * 128 + 8 * r16 + (q & 1) * 4) = acc0;
      const int i1 = q < 2 ? 256 + q * 64 + 4 * r16 : 384 + (q - 2) * 64 + 2 * r16, d1 = q < 2 ? 2 : 32;
      *(LAS f32x2*)(red + i1) = f32x2{acc1[0], acc1[1]};
      *(LAS f32x2*)(red + i1 + d1) = f32x2{acc1[2], acc1[3]};
      LAS float* r2 = red + 512 + q * 64 + r16;
      r2[0] = acc2[0]; r2[16] = acc2[1]; r2[32] = acc2[2]; r2[48] = acc2[3];
    }
    STAMP(0, 21);
    bar_lds();
    const int mgo = (int)WS_MODG + ((lane < 3 ? lane : 0) * 224 + dp) * 8;
    SB0();
    u32x4 sg0 = __builtin_amdgcn_raw_buffer_load_b128(rs, sto, 0, AUX_SC1), sg1 = __builtin_amdgcn_raw_buffer_load_b128(rs, sto + 16, 0, AUX_SC1);
    const int xgo = (int)WS_XG + (tid >> 1) * (int)XG_BLK + dp * 64 + (tid & 1) * 32;
    u32x4 xg0 = __builtin_amdgcn_raw_buffer_load_b128(rs, xgo, 0, AUX_SC1), xg1 = __builtin_amdgcn_raw_buffer_load_b128(rs, xgo + 16, 0, AUX_SC1);
    const unsigned dsk2 = *(const unsigned*)(p.dskip + 2 * dp);
    SB0();
    const int kc = tid >> 8, kt = tid & 255;
    LAS float* kd = (LAS float*)(L + P0_KG + kc * (8 * KG_BUF) + KG_BUF);
#pragma unroll
    for (int i = 0; i < 3; ++i) {
      const int oo = kt + 256 * i;
      if (i < 2 || oo < 648) {
        const LAS float* s = (const LAS float*)(L + P0_KG + kc * (8 * KG_BUF)) + oo;
        kd[oo] = (s[0] + s[2 * KG_BUF / 4]) + (s[4 * KG_BUF / 4] + s[6 * KG_BUF / 4]);
      }
    }
    if (kt == 0) kd[648] = 0.f;
    const unsigned OFFS = 0x087EDA52u;
    {
      int off = CV_KCOPY + kt * 16;
      if (kt >= 160) { const int k = kt - 160, s = k >> 3, ch = (k & 7) == 7 ? 6 : (k & 7); off = (272 * (s & 7) + ((OFFS >> (4 * (s & 7))) & 15) + 257 + ch) * 16; }
      *(LAS u32x4*)(L + kc * CV_CH + off) = u32x4{0u, 0u, 0u, 0u};
    }
    bar_lds();
    LAS unsigned char* base = L + kc * CV_CH;
    u32x4 k0;
    {
      float v[8];
#pragma unroll
      for (int i = 0; i < 8; i += 2) {
        const int l0 = 2048 - (kt * 8 + i), l1 = l0 - 1;
        const unsigned bits = pk_bf16((float)l0, (float)l1);
        int i0 = l0 < 128 ? l0 : (int)(bits & 0xffffu) - 17024;
        const int i1 = l1 < 128 ? l1 : (int)(bits >> 16) - 17024;
        if (i == 0) i0 = kt == 0 ? 648 : i0;
        v[i] = kd[i0];
        v[i + 1] = kd[i1];
      }
      k0 = u32x4{pk_bf16(v[0], v[1]), pk_bf16(v[2], v[3]), pk_bf16(v[4], v[5]), pk_bf16(v[6], v[7])};
      *(LAS u32x4*)(base + (2 + kt) * 16) = k0;
      if (kt < 8) {
        const unsigned k00 = pk_bf16(kd[0], 0.f);
        *(LAS u32x4*)(base + (272 * kt + ((OFFS >> (4 * kt)) & 15) + 256) * 16) = u32x4{kt == 0 ? k00 : 0u, 0u, 0u, 0u};
      }
    }
    bar_lds();
    {
      const u32x4 k1 = *(const LAS u32x4*)(base + (2 + kt + 1) * 16);
      unsigned wv[8] = {k0.x, k0.y, k0.z, k0.w, k1.x, k1.y, k1.z, k1.w};
      unsigned vv[7];
#pragma unroll
      for (int i = 0; i < 7; ++i) vv[i] = __builtin_amdgcn_alignbit(wv[i + 1], wv[i], 16);
#pragma unroll
      for (int s = 1; s < 8; ++s) {
        const int k = s >> 1;
        u32x4 o = (s & 1) ? u32x4{vv[k], vv[k + 1], vv[k + 2], vv[k + 3]} : u32x4{wv[k], wv[k + 1], wv[k + 2], wv[k + 3]};
        *(LAS u32x4*)(base + (272 * s + ((OFFS >> (4 * s)) & 15) + kt) * 16) = o;
      }
    }
    float cum0[4], cum1[4];
    {
      const LAS float* kd0 = (const LAS float*)(L + P0_KG + KG_BUF);
      const LAS float* kd1 = (const LAS float*)(L + P0_KG + 8 * KG_BUF + KG_BUF);
#pragma unroll
      for (int i = 0; i < 4; i += 2) {
        const int l0 = 4 * tid + i;
        const unsigned bits = pk_bf16((float)l0, (float)(l0 + 1));
        const int i0 = l0 < 128 ? l0 : (int)(bits & 0xffffu) - 17024, i1 = l0 < 128 ? l0 + 1 : (int)(bits >> 16) - 17024;
        cum0[i] = kd0[i0]; cum0[i + 1] = kd0[i1]; cum1[i] = kd1[i0]; cum1[i + 1] = kd1[i1];
      }
#pragma unroll
      for (int i = 1; i < 4; ++i) { cum0[i] += cum0[i - 1]; cum1[i] += cum1[i - 1]; }
      const float inc0 = wave_scan(cum0[3]), inc1 = wave_scan(cum1[3]);
      const float e0 = inc0 - cum0[3], e1 = inc1 - cum1[3];
#pragma unroll
      for (int i = 0; i < 4; ++i) { cum0[i] += e0; cum1[i] += e1; }
      if (lane == 63) *(LAS f32x2*)(L + P0_MS + w * 8) = f32x2{inc0, inc1};
    }
#define GRAN_BAD() __any(((((sg0.y ^ tag32) | (sg0.w ^ tag32)) | ((sg1.y ^ tag32) | (sg1.w ^ tag32))) | (((xg0.y ^ tag32) | (xg0.w ^ tag32)) | ((xg1.y ^ tag32) | (xg1.w ^ tag32)))) != 0u)
    if (__builtin_expect(GRAN_BAD(), 0)) {
      for (unsigned spins = 0; spins < SPIN_CAP; ++spins) {
        __builtin_amdgcn_s_sleep(2);
        asm volatile("" ::: "memory");
        sg0 = __builtin_amdgcn_raw_buffer_load_b128(rs, sto, 0, AUX_SC1); sg1 = __builtin_amdgcn_raw_buffer_load_b128(rs, sto + 16, 0, AUX_SC1);
        xg0 = __builtin_amdgcn_raw_buffer_load_b128(rs, xgo, 0, AUX_SC1); xg1 = __builtin_amdgcn_raw_buffer_load_b128(rs, xgo + 16, 0, AUX_SC1);
        if (!GRAN_BAD()) break;
      }
    }
#undef GRAN_BAD
    const unsigned xq[4] = {xg0.x, xg0.z, xg1.x, xg1.z};
    STAMP(0, 22);
    unsigned hq[4];
    {
      const unsigned sv[4] = {sg0.x, sg0.z, sg1.x, sg1.z};
#pragma unroll
      for (int k = 0; k < 4; ++k) {
        const float m = bf_lo(sv[k]), r = __builtin_amdgcn_rcpf(bf_hi(sv[k]));
        unsigned t = pk_bf16(bf_lo(xq[k]) - m, bf_hi(xq[k]) - m);
        hq[k] = pk_bf16(bf_lo(t) * r, bf_hi(t) * r);
      }
      LAS unsigned char* U = L + CV_KCOPY + (16 + (tid >> 4)) * CV_UROW + (tid & 15) * 8;
      *(LAS u32x2*)U = u32x2{__builtin_amdgcn_perm(hq[1], hq[0], 0x05040100u), __builtin_amdgcn_perm(hq[3], hq[2], 0x05040100u)};
      *(LAS u32x2*)(U + CV_CH) = u32x2{__builtin_amdgcn_perm(hq[1], hq[0], 0x07060302u), __builtin_amdgcn_perm(hq[3], hq[2], 0x07060302u)};
    }
    if (packer) pack_shuffle(lane, pk, L + P0_KG + (w + 1) * (2 * KG_BUF));
    STAMP(0, 23);
    bar_lds();
    if (bid == 0 && tid == 0) {
      const u64 e = ((u64)ep.y << 32) | ep.x;
      __hip_atomic_store((GAS u64*)(p.ws + WS_EPOCH), e + 1ull, __ATOMIC_RELAXED, __HIP_MEMORY_SCOPE_AGENT);
    }
    u32x2 fv = flag_load(rs, mgo);
    const __amdgpu_buffer_rsrc_t rpk = __builtin_amdgcn_make_buffer_rsrc((void*)p.ws, 0, packer ? (int)WS_END : 0, 0x00020000);
    SB0();
  {
    const int ch = w >> 2, wq = w & 3;
    f32x4 acc[2][4];
#pragma unroll
    for (int n = 0; n < 2; ++n)
#pragma unroll
      for (int m = 0; m < 4; ++m) acc[n][m] = f32x4{0.f, 0.f, 0.f, 0.f};
    const int s = (8 - r16) & 7, u = (r16 + 7) >> 3;
    const LAS unsigned char* ka = L + ch * CV_CH + (272 * s + ((OFFS >> (4 * s)) & 15) + 256 + q - u - 2 * (16 * wq) - 2 * 79) * 16;
    const LAS unsigned char* ub = L + ch * CV_CH + CV_KCOPY + (16 + r16 - 4 * wq - 3) * CV_UROW + q * 16;
#define KFRAG(f) (*(const LAS u32x4*)(ka + (79 - (f)) * 32))
    u32x4 alo[18], ahi[18], bf[4][4];
#define CSTAGE(dl)                                                                                      \
  {                                                                                                     \
    _Pragma("unroll") for (int ks = 0; ks < 2; ++ks) {                                                  \
      bf[dl][2 * ks] = *(const LAS u32x4*)(ub + (3 - (dl)) * CV_UROW + ks * 64);                        \
      bf[dl][2 * ks + 1] = *(const LAS u32x4*)(ub + (19 - (dl)) * CV_UROW + ks * 64);                   \
    }                                                                                                   \
    _Pragma("unroll") for (int i = 0; i < 4; ++i) {                                                     \
      alo[2 + 4 * (dl) + i] = KFRAG(4 * (dl) + i);                                                      \
      ahi[2 + 4 * (dl) + i] = KFRAG(4 * (dl) + i + 64);                                                 \
    }                                                                                                   \
  }
    CSTAGE(0)
    alo[0] = KFRAG(-2); alo[1] = KFRAG(-1); ahi[0] = KFRAG(62); ahi[1] = KFRAG(63);
    SB0();
#pragma unroll
    for (int m = 0; m < 7; ++m) pack_store1(jb, lane, pk, rpk, m);
    FILL_X1(); FILL_P(0); FILL_P(1); FILL_P(2);
    SB0();
#pragma unroll
    for (int dl = 0; dl < 4; ++dl) {
      if (dl < 3) CSTAGE(dl + 1)
#pragma unroll
      for (int ks = 0; ks < 2; ++ks) {
#pragma unroll
        for (int m = 0; m < 4; ++m) {
          acc[0][m] = mfma16(alo[4 * dl + m - 2 * ks + 2], bf[dl][2 * ks], acc[0][m]);
          acc[1][m] = mfma16(alo[4 * dl + m - 2 * ks + 2], bf[dl][2 * ks + 1], acc[1][m]);
          acc[1][m] = mfma16(ahi[4 * dl + m - 2 * ks + 2], bf[dl][2 * ks], acc[1][m]);
        }
      }
      if (dl < 3) {
#pragma unroll
        for (int i = 0; i < 12; ++i) {
          __builtin_amdgcn_sched_group_barrier(0x100, 1, 0);
          __builtin_amdgcn_sched_group_barrier(0x8, 2, 0);
        }
      }
      SB0();
    }
#undef CSTAGE
    STAMP(0, 28);
#undef KFRAG

    LAS unsigned char* P = L + CV_P + w * (32 * CV_PROW);
#pragma unroll
    for (int n = 0; n < 2; ++n)
#pragma unroll
      for (int m = 0; m < 4; ++m) *(LAS f32x4*)(P + (16 * n + r16) * CV_PROW + (16 * m + 4 * q) * 4) = acc[n][m];
    }
    fv = flag_wait(rs, mgo, fv, 0u, tag32, 0u);
    const unsigned sa2 = __builtin_amdgcn_readlane(fv.x, 0), ca2 = __builtin_amdgcn_readlane(fv.x, 1), ga2 = __builtin_amdgcn_readlane(fv.x, 2);
    unsigned hd[4];
    {
      const f32x2 t2 = *(const LAS f32x2*)(L + P0_MS + (lane & 7) * 8);
      const float mk = (lane & 15) < w ? 1.0f : 0.0f;
      const float b0 = row16_sum((lane & 8) ? 0.f : t2.x * mk), b1 = row16_sum((lane & 8) ? 0.f : t2.y * mk);
#pragma unroll
      for (int k = 0; k < 4; ++k) {
        unsigned h = pk_bf16(bf_lo(hq[k]) * bf_lo(ca2), bf_hi(hq[k]) * bf_hi(ca2));
        h = pk_bf16(bf_lo(h) + bf_lo(sa2), bf_hi(h) + bf_hi(sa2));
        hd[k] = pk_bf16(bf_lo(h) * bf_lo(dsk2), bf_hi(h) * bf_hi(dsk2));
        cum0[k] = (cum0[k] + b0) * bf_lo(sa2);
        cum1[k] = (cum1[k] + b1) * bf_hi(sa2);
      }
    }
    asm volatile("" : "+v"(hd[0]), "+v"(hd[1]), "+v"(hd[2]), "+v"(hd[3]), "+v"(cum0[0]), "+v"(cum0[1]), "+v"(cum0[2]), "+v"(cum0[3]),
                 "+v"(cum1[0]), "+v"(cum1[1]), "+v"(cum1[2]), "+v"(cum1[3]));
    asm volatile("s_waitcnt vmcnt(4)" ::: "memory");
    bar_lds();
    STAMP(0, 24);
    {
      const LAS unsigned char* P = L + CV_P + (tid >> 4) * CV_PROW + (tid & 15) * 16;
      f32x4 pa[8];
#pragma unroll
      for (int k = 0; k < 8; ++k) pa[k] = *(const LAS f32x4*)(P + k * (32 * CV_PROW));
      const f32x4 y0 = (pa[0] + pa[1]) + (pa[2] + pa[3]), y1 = (pa[4] + pa[5]) + (pa[6] + pa[7]);
      u32x4 out;
#pragma unroll
      for (int k = 0; k < 4; ++k) {
        const unsigned yy = pk_bf16(y0[k] * bf_lo(ca2) + cum0[k], y1[k] * bf_hi(ca2) + cum1[k]);
        unsigned t = pk_bf16(bf_lo(yy) + bf_lo(hd[k]), bf_hi(yy) + bf_hi(hd[k]));
        t = pk_bf16(bf_lo(ga2) * bf_lo(t), bf_hi(ga2) * bf_hi(t));
        out[k] = sane(pk_bf16(bf_lo(xq[k]) + bf_lo(t), bf_hi(xq[k]) + bf_hi(t)));
      }
      ST16(WS_X2B + (tid >> 3) * (int)X2B_BLK + dp * 128 + (tid & 7) * 16, out);
    }
  } else {
    const int c = bid - 224;
    LAS float* sm = (LAS float*)(L + P0_KG);
    SB0();
    mod_load(p, c, tid, mr, mc);
    SB0();
    asm volatile("s_waitcnt vmcnt(16)" ::: "memory");
    PUT_STAT();
    mod_finish(c, tag32, tid, mr, mc, sm, rs);
    STAMP(0, 21);
    if (c >= 22) {
      mod_load(p, 10 + c, tid, mr, mc);
      bar_lds();
      mod_finish(10 + c, tag32, tid, mr, mc, sm, rs);
    }
    if (packer) {
      pack_load(jb, lane, pk);
      pack_shuffle(lane, pk, L + P0_PK + w * 8192);
#pragma unroll
      for (int m = 0; m < 7; ++m) pack_store1(jb, lane, pk, rs, m);
    }
    arrive_flag<0>(L, 2, p.ws + WS_FLAGC + c * 8, ~tag, lane);
    STAMP(0, 30);
    FILL_X1(); FILL_P(0); FILL_P(1); FILL_P(2);
    {
      u32x4 sg0 = __builtin_amdgcn_raw_buffer_load_b128(rs, sto, 0, AUX_SC1), sg1 = __builtin_amdgcn_raw_buffer_load_b128(rs, sto + 16, 0, AUX_SC1);
      stat_wait(rs, sto, sg0, sg1, tag32);
    }
    bar_lds();
  }
  STAMP(0, 25);
  if (__builtin_expect(!cw, 0)) {
    {
      const int mo = (int)WS_MODG + 672 * 8 + l56 * 64;
      u32x4 g0 = __builtin_amdgcn_raw_buffer_load_b128(rs, mo, 0, AUX_SC1), g1 = __builtin_amdgcn_raw_buffer_load_b128(rs, mo + 16, 0, AUX_SC1);
      u32x4 g2 = __builtin_amdgcn_raw_buffer_load_b128(rs, mo + 32, 0, AUX_SC1), g3 = __builtin_amdgcn_raw_buffer_load_b128(rs, mo + 48, 0, AUX_SC1);
      SB0();
      gather_x2<X2_DELAY>(p, L, rs, pn, tid);
      stat_wait(rs, mo, g0, g1, tag32);
      stat_wait(rs, mo + 32, g2, g3, tag32);
      LAS unsigned char* mp = L + G_MOD + l56 * 64;
      *(LAS f32x4*)mp = f32x4{bf_lo(g0.x), bf_hi(g0.x), bf_lo(g0.z), bf_hi(g0.z)};
      *(LAS f32x4*)(mp + 16) = f32x4{bf_lo(g1.x), bf_hi(g1.x), bf_lo(g1.z), bf_hi(g1.z)};
      *(LAS f32x4*)(mp + 32) = f32x4{bf_lo(g2.x), bf_hi(g2.x), bf_lo(g2.z), bf_hi(g2.z)};
      *(LAS f32x4*)(mp + 48) = f32x4{bf_lo(g3.x), bf_hi(g3.x), bf_lo(g3.z), bf_hi(g3.z)};
    }
    STAMP(0, 1);
    bar_lds();
    const int fo = (int)WS_FLAGC + (lane & 31) * 8;
    const u32x2 fc = flag_load(rs, fo);
    ln_phase_a(L, j, w, r16, q, [](auto) {});
    STAMP(0, 2);
    bar_lds();
    flag_wait(rs, fo, fc, ~tlo, ~thi, ~0u);
    const unsigned char* src = p.ws + WS_WP + (size_t)(j * 7) * NFRAG * 1024 + NRING * 1024 + voff;
#define DMA_HALF(h)                                                                                                         \
  _Pragma("unroll") for (int ww = 0; ww < 7; ++ww) _Pragma("unroll") for (int i = (h) * (NLDS / 2); i < ((h) + 1) * (NLDS / 2); ++i) \
      __builtin_amdgcn_global_load_lds((const GAS unsigned*)(src + (size_t)ww * NFRAG * 1024 + i * 1024),                     \
                                       (LAS unsigned*)(L + G_WL + (ww * NLDS + i) * 1024), 16, 0, AUX_SC1);
    DMA_HALF(0)
    bar_lds();
    STAMP(0, 4);
    bar_lds();
    DMA_HALF(1)
#undef DMA_HALF
    bar_lds();
    {
      int gof[3];
      u32x4 xr[3];
      x1_offsets(pn, j, w, r16, q, gof);
      poll_load<3>(rs, gof, xr);
      ln_phase_c(L, xr, j, w, r16, q);
    }
    asm volatile("s_waitcnt vmcnt(0)" ::: "memory");
    STAMP(0, 7);
    bar_lds();
    return;
  }
  __builtin_amdgcn_s_setprio(1);
  SB0();
  gather_x2<X2_DELAY>(p, L, rs, pn, tid);
  STAMP(0, 1);
  bar_lds();
  STAMP(0, 27);
  u32x2 h2r[2], x2r[2], x1r[2];
  const int ncol = j * 112 + w * 16 + q * 4;
  const int gbo = (int)WS_MODG + (5 * 224 + (ncol >> 1)) * 8;
  u32x4 gbg = __builtin_amdgcn_raw_buffer_load_b128(rs, gbo, 0, AUX_SC1);
#pragma unroll
  for (int T = 0; T < 2; ++T) {
    const int row = 16 * T + r16;
    x2r[T] = *(const LAS u32x2*)(L + G_X + row * 1024 + (((ncol >> 3) ^ (row >> 2)) * 16) + (ncol & 4) * 2);
  }
  SB0();
  ring_fill<0, 8>(ring, rw, voff);
  ln_phase_a(L, j, w, r16, q, [&](auto k) { SB0(); ring_fill<8 + 6 * decltype(k)::value, 6>(ring, rw, voff); });
  SB0();
  ring_fill<26, 6>(ring, rw, voff);
  STAMP(0, 2);
  bar_lds();
  u32x2 gbp;
  {
    u32x4 dup = gbg;
    if (__builtin_expect(__any(((gbg.y ^ tag32) | (gbg.w ^ tag32)) != 0u), 0)) {
      for (unsigned spins = 0; spins < SPIN_CAP; ++spins) {
        asm volatile("" ::: "memory");
        dup = __builtin_amdgcn_raw_buffer_load_b128(rs, gbo, 0, AUX_SC1);
        if (!__any(((dup.y ^ tag32) | (dup.w ^ tag32)) != 0u)) break;
      }
    }
    gbp = u32x2{dup.x, dup.z};
  }
#pragma unroll
  for (int T = 0; T < 2; ++T) h2r[T] = *(const LAS u32x2*)(L + G_HQ + (16 * T + r16) * 240 + (w * 16 + q * 4) * 2);
  const LAS unsigned char* ap0 = L + G_T0 + r16 * AS + q * 16;
  const LAS unsigned char* ap1 = L + G_T1 + r16 * AS + q * 16;
  const int tok = (q & 1) * 16 + r16, octc = 2 * w + (q >> 1);
  {
    f32x4 acc[1][2];
    STAMP(0, 16);
    gemm14<1, 0, 32, 7, 2, 5>(ap0, ring, rw, voff, acc);
    STAMP(0, 17);
    const u32x4 own = make_piece(gelu4(acc[0][0]), gelu4(acc[0][1]));
    __builtin_amdgcn_raw_buffer_store_b128(own, rs, (int)WS_G + (pn * 4 + j) * 7168 + pslot, 0, AUX_SC1);
    SB0();
    STAMP(0, 18);
    ring_fill<39, 4>(ring, rw, voff);
    int goff[4], loff[4];
    bool wrap0;
#pragma unroll
    for (int i = 0; i < 4; ++i) {
      const int jq = (j + i) & 3;
      const int sh = (jq & 1) ? -1 - (jq >> 1) : 1 + (jq >> 1);
      const int it = tok + sh;
      const int sp = pn + (it >= 32 ? -1 : (it < 0 ? 1 : 0));
      if (i == 0) wrap0 = sp != pn;
      goff[i] = (sp < 0 || sp > 63 || (i == 0 && sp == pn)) ? (int)WS_END : (int)WS_G + (sp * 4 + jq) * 7168 + pslot;
      loff[i] = G_T1 + (it & 31) * AS + (jq * 14 + octc) * 16;
    }
    STAMP(0, 3);
    bar_lds();
    u32x4 v[4];
    poll_issue<4>(rs, goff, v);
    __builtin_amdgcn_s_sleep(3);
    ring_fill<43, 5>(ring, rw, voff);
    poll_finish<4>(rs, goff, v);
    *(LAS u32x4*)(L + loff[0]) = wrap0 ? v[0] : own;
#pragma unroll
    for (int i = 1; i < 4; ++i) *(LAS u32x4*)(L + loff[i]) = v[i];
  }
  STAMP(0, 4);
  bar_lds();
  {
    f32x4 acc[1][2];
    gemm14<1, 14, 48, 7, 2, 5>(ap1, ring, rw, voff, acc);
#pragma unroll
    for (int T = 0; T < 2; ++T) {
      const unsigned a = pk_bf16(acc[0][T][0], acc[0][T][1]), b = pk_bf16(acc[0][T][2], acc[0][T][3]);
      x1r[T].x = pk_bf16(bf_lo(h2r[T].x) + bf_lo(a), bf_hi(h2r[T].x) + bf_hi(a));
      x1r[T].y = pk_bf16(bf_lo(h2r[T].y) + bf_lo(b), bf_hi(h2r[T].y) + bf_hi(b));
    }
    const u32x4 own = make_piece(x1r[0], x1r[1]);
    __builtin_amdgcn_raw_buffer_store_b128(own, rs, (int)WS_X1 + (pn * 4 + j) * 7168 + (w * 64 + (tok >> 2) * 8 + (q >> 1) * 4 + (tok & 3)) * 16, 0, AUX_SC1);
    *(LAS u32x4*)(L + G_HQ + tok * 240 + octc * 16) = own;
    SB0();
    STAMP(0, 19);
    ring_fill<55, 4>(ring, rw, voff);
    int gof[3];
    u32x4 xr[3];
    x1_offsets(pn, j, w, r16, q, gof);
    STAMP(0, 5);
    bar_lds();
    poll_issue<3>(rs, gof, xr);
    __builtin_amdgcn_s_sleep(3);
    ring_fill<59, 5>(ring, rw, voff);
    poll_finish<3>(rs, gof, xr);
    STAMP(0, 6);
    ring_fill<64, 4>(ring, rw, voff);
    ln_phase_c(L, xr, j, w, r16, q);
    SB0();
    ring_fill<68, 4>(ring, rw, voff);
  }
  STAMP(0, 7);
  bar_lds();
  {
    {
      f32x4 ca[2][2], cb2[2][2], acc[3][2];
      LAS unsigned char* hp = L + G_T1 + r16 * AS + (w * 64 + q * 4) * 2;
      unsigned glo;
      STAMP(0, 12);
      gemm_p<2, 28, 0, 1, 14, 72, 19, 3, true>(ap0, ring, rw, voff, ca, [](auto) {});
      gemm_p<2, 56, 0, 1, 14, 91, 19, 3, true>(ap0, ring, rw, voff, cb2, [&](auto stc) {
        constexpr int st = decltype(stc)::value;
        if constexpr (st % 3 == 1 && st / 3 < 8) {
          constexpr int c = st / 3, i = c >> 2, T = (c >> 1) & 1;
          if constexpr ((c & 1) == 0) glo = gelu2(ca[i][T][0], ca[i][T][1]);
          else *(LAS u32x2*)(hp + T * 16 * AS + i * 32) = u32x2{glo, gelu2(ca[i][T][2], ca[i][T][3])};
        }
      });
      STAMP(0, 13);
      bar_lds();
      gemm_p<3, 84, 0, 2, 7, 110, 18, 3, true>(ap1, ring, rw, voff, acc, [&](auto stc) {
        constexpr int st = decltype(stc)::value;
        if constexpr (st % 2 == 1 && st / 2 < 8) {
          constexpr int c = st / 2, i = c >> 2, T = (c >> 1) & 1;
          if constexpr ((c & 1) == 0) glo = gelu2(cb2[i][T][0], cb2[i][T][1]);
          else *(LAS u32x2*)(hp + T * 16 * AS + (2 + i) * 32) = u32x2{glo, gelu2(cb2[i][T][2], cb2[i][T][3])};
        }
      });
      STAMP(0, 8);
      bar_lds();
      STAMP(0, 14);
      gemm_p<3, 105, 1, 2, 7, 0, 0, 3, false>(ap1, ring, rw, voff, acc, [](auto) {});
      STAMP(0, 15);
#pragma unroll
      for (int i = 0; i < 3; ++i) {
        const int jd = (j + 1 + i) & 3;
        const u32x4 o = {sane(pk_bf16(acc[i][0][0], acc[i][0][1])), sane(pk_bf16(acc[i][0][2], acc[i][0][3])),
                         sane(pk_bf16(acc[i][1][0], acc[i][1][1])), sane(pk_bf16(acc[i][1][2], acc[i][1][3]))};
        __builtin_amdgcn_raw_buffer_store_b128(o, rs, (int)WS_P + ((pn * 4 + jd) * 3 + (2 - i)) * 7168 + pslot, 0, AUX_SC1);
      }
      SB0();
    }
    STAMP(0, 9);
    f32x4 acc0 = {0.f, 0.f, 0.f, 0.f}, acc1 = acc0;
    u32x4 v[3];
    const int go = (int)WS_P + (pn * 4 + j) * 3 * 7168 + pslot;
    const int gof[3] = {go, go + 7168, go + 2 * 7168};
    {
      const LAS unsigned char* wl = L + G_WL + w * (NLDS * 1024) + voff;
      u32x4 wa[14], b0[14], b1[14];
#pragma unroll
      for (int ks = 0; ks < 14; ++ks) {
        const int f = 126 + ks;
        wa[ks] = f < NRING ? ring[f % RING] : *(const LAS u32x4*)(wl + (f - NRING) * 1024);
        b0[ks] = *(const LAS u32x4*)(ap1 + ks * 64);
        b1[ks] = *(const LAS u32x4*)(ap1 + 16 * AS + ks * 64);
      }
      SB0();
#pragma unroll
      for (int ks = 0; ks < 14; ++ks) {
        acc0 = mfma16(wa[ks], b0[ks], acc0);
        acc1 = mfma16(wa[ks], b1[ks], acc1);
      }
      SB0();
    }
    STAMP(0, 10);
    poll_load<3>(rs, gof, v);
    STAMP(0, 11);
#pragma unroll
    for (int T = 0; T < 2; ++T) {
      const f32x4 ac = T ? acc1 : acc0;
      float hh[4];
#pragma unroll
      for (int k = 0; k < 2; ++k) {
        hh[2 * k] = ac[2 * k] + ((bf_lo(v[0][2 * T + k]) + bf_lo(v[1][2 * T + k])) + bf_lo(v[2][2 * T + k]));
        hh[2 * k + 1] = ac[2 * k + 1] + ((bf_hi(v[0][2 * T + k]) + bf_hi(v[1][2 * T + k])) + bf_hi(v[2][2 * T + k]));
      }
      unsigned a = pk_bf16(hh[0], hh[1]), b = pk_bf16(hh[2], hh[3]);
      a = pk_bf16(bf_lo(x1r[T].x) + bf_lo(a), bf_hi(x1r[T].x) + bf_hi(a));
      b = pk_bf16(bf_lo(x1r[T].y) + bf_lo(b), bf_hi(x1r[T].y) + bf_hi(b));
      a = pk_bf16(bf_lo(gbp.x) * bf_lo(a), bf_hi(gbp.x) * bf_hi(a));
      b = pk_bf16(bf_lo(gbp.y) * bf_lo(b), bf_hi(gbp.y) * bf_hi(b));
      u32x2 o;
      o.x = pk_bf16(bf_lo(x2r[T].x) + bf_lo(a), bf_hi(x2r[T].x) + bf_hi(a));
      o.y = pk_bf16(bf_lo(x2r[T].y) + bf_lo(b), bf_hi(x2r[T].y) + bf_hi(b));
      *(u32x2*)(p.out + (size_t)(pn * 32 + 16 * T + r16) * D_ + ncol) = o;
    }
  }
  STAMP(0, 20);
}

__global__ __launch_bounds__(512, 2) void k_all(GArgs p) {
  extern __shared__ __attribute__((aligned(16))) unsigned char smem[];
  const uintptr_t code0 = (uintptr_t)__builtin_amdgcn_s_getpc() & ~(uintptr_t)127;
  body(p, (LAS unsigned char*)smem, code0);
}

}

extern "C" void kernel_launch(void* const* d_in, const int* in_sizes, int n_in, void* d_out, int out_size, void* d_ws,
                              size_t ws_size, hipStream_t stream) {
  (void)in_sizes; (void)n_in; (void)out_size; (void)ws_size;
  GArgs a;
  a.x = (const unsigned short*)d_in[0];
  a.a_re = (const unsigned short*)d_in[1];
  a.a_im = (const unsigned short*)d_in[2];
  a.b_re = (const unsigned short*)d_in[3];
  a.b_im = (const unsigned short*)d_in[4];
  a.c_re = (const unsigned short*)d_in[5];
  a.c_im = (const unsigned short*)d_in[6];
  a.log_dt = (const unsigned short*)d_in[7];
  a.dskip = (const unsigned short*)d_in[8];
  a.w1 = (const unsigned short*)d_in[10];
  a.w2 = (const unsigned short*)d_in[11];
  a.w3 = (const unsigned short*)d_in[12];
  a.w4 = (const unsigned short*)d_in[13];
  a.cvec = (const unsigned short*)d_in[14];
  a.wmod = (const unsigned short*)d_in[15];
  a.ws = (unsigned char*)d_ws;
  a.out = (unsigned short*)d_out;
  (void)hipFuncSetAttribute((const void*)k_all, hipFuncAttributeMaxDynamicSharedMemorySize, G_LDS);
  k_all<<<256, 512, G_LDS, stream>>>(a);
}
```

```cpp
#include <hip/hip_runtime.h>
#include <hip/hip_bf16.h>
#include <stdint.h>
#include <type_traits>
#include <utility>

namespace {

typedef __attribute__((ext_vector_type(8))) short bf16x8;
typedef __attribute__((ext_vector_type(8))) _Float16 f16x8;
typedef __attribute__((ext_vector_type(2))) _Float16 f16x2;
typedef __attribute__((ext_vector_type(4))) float f32x4;
typedef __attribute__((ext_vector_type(2))) float f32x2;
typedef __attribute__((ext_vector_type(4))) unsigned u32x4;
typedef __attribute__((ext_vector_type(2))) unsigned u32x2;
typedef __attribute__((ext_vector_type(2))) __bf16 bf16x2v;
#define GAS __attribute__((address_space(1)))
#define LAS __attribute__((address_space(3)))

constexpr int L_ = 2048, D_ = 448, NS_ = 256;

constexpr size_t WS_EPOCH = 0;
constexpr size_t WS_FLAGC = 4608;
constexpr size_t WS_MODG = 16384;
constexpr size_t WS_STG = 32768;
constexpr int NFRAG = 140;
constexpr size_t WS_XG = 65536;
constexpr size_t XG_BLK = 224 * 64;
constexpr size_t WS_WP = WS_XG + 256 * XG_BLK;
constexpr size_t WS_X2B = WS_WP + (size_t)28 * NFRAG * 1024;
constexpr size_t X2B_BLK = 224 * 128;
constexpr size_t WS_G = WS_X2B + 64 * X2B_BLK;
constexpr size_t WS_X1 = WS_G + (size_t)64 * 4 * 7168;
constexpr size_t WS_P = WS_X1 + (size_t)64 * 4 * 7168;
constexpr size_t WS_END = WS_P + (size_t)64 * 12 * 7168;

#define STAMP(base, k) do { } while (0)

__device__ __forceinline__ float bf_lo(unsigned u) { return __builtin_bit_cast(float, u << 16); }
__device__ __forceinline__ float bf_hi(unsigned u) { return __builtin_bit_cast(float, u & 0xffff0000u); }
__device__ __forceinline__ float bf2f(unsigned short h) { return __builtin_bit_cast(float, (unsigned)h << 16); }
__device__ __forceinline__ unsigned pk_bf16(float a, float b) {
  f32x2 v = {a, b};
  bf16x2v r = __builtin_convertvector(v, bf16x2v);
  return __builtin_bit_cast(unsigned, r);
}
__device__ __forceinline__ unsigned pk_f16(f32x2 v) {
  f16x2 r = __builtin_convertvector(v, f16x2);
  return __builtin_bit_cast(unsigned, r);
}
__device__ __forceinline__ float rbf(float a) { return bf_lo(pk_bf16(a, a)); }
__device__ __forceinline__ f32x4 mfma16(u32x4 a, u32x4 b, f32x4 c) {
  return __builtin_amdgcn_mfma_f32_16x16x32_bf16(__builtin_bit_cast(bf16x8, a), __builtin_bit_cast(bf16x8, b), c, 0, 0, 0);
}
__device__ __forceinline__ f32x4 mfma16h(u32x4 a, u32x4 b, f32x4 c) {
  return __builtin_amdgcn_mfma_f32_16x16x32_f16(__builtin_bit_cast(f16x8, a), __builtin_bit_cast(f16x8, b), c, 0, 0, 0);
}
template <int CTRL>
__device__ __forceinline__ float dpp_f(float v) {
  return __builtin_bit_cast(float, __builtin_amdgcn_update_dpp(0, __builtin_bit_cast(int, v), CTRL, 0xf, 0xf, true));
}
__device__ __forceinline__ float row16_sum(float v) {
  v += dpp_f<0xB1>(v);
  v += dpp_f<0x4E>(v);
  v += dpp_f<0x141>(v);
  v += dpp_f<0x140>(v);
  return v;
}
__device__ __forceinline__ float row16_max(float v) {
  v = fmaxf(v, dpp_f<0xB1>(v));
  v = fmaxf(v, dpp_f<0x4E>(v));
  v = fmaxf(v, dpp_f<0x141>(v));
  v = fmaxf(v, dpp_f<0x140>(v));
  return v;
}
__device__ __forceinline__ void swap16(float v, float& a, float& b) {
  const unsigned u = __builtin_bit_cast(unsigned, v), u2 = u;
  auto r = __builtin_amdgcn_permlane16_swap(u, u2, false, false);
  const unsigned r0 = r[0], r1 = r[1];
  a = __builtin_bit_cast(float, r0); b = __builtin_bit_cast(float, r1);
}
__device__ __forceinline__ void swap32(float v, float& a, float& b) {
  const unsigned u = __builtin_bit_cast(unsigned, v), u2 = u;
  auto r = __builtin_amdgcn_permlane32_swap(u, u2, false, false);
  const unsigned r0 = r[0], r1 = r[1];
  a = __builtin_bit_cast(float, r0); b = __builtin_bit_cast(float, r1);
}
__device__ __forceinline__ float rows_sum(float v) {
  float a, b;
  swap16(v, a, b); v = a + b;
  swap32(v, a, b);
  return a + b;
}
__device__ __forceinline__ float rows_max(float v) {
  float a, b;
  swap16(v, a, b); v = fmaxf(a, b);
  swap32(v, a, b);
  return fmaxf(a, b);
}
template <int CTRL, int RMASK>
__device__ __forceinline__ float dpp_z(float v) {
  return __builtin_bit_cast(float, __builtin_amdgcn_update_dpp(0, __builtin_bit_cast(int, v), CTRL, RMASK, 0xf, false));
}
__device__ __forceinline__ float wave_scan(float v) {
  v += dpp_z<0x111, 0xf>(v);
  v += dpp_z<0x112, 0xf>(v);
  v += dpp_z<0x114, 0xf>(v);
  v += dpp_z<0x118, 0xf>(v);
  v += dpp_z<0x142, 0xa>(v);
  v += dpp_z<0x143, 0xc>(v);
  return v;
}
__device__ __forceinline__ float wave_sum(float v) { return rows_sum(row16_sum(v)); }
__device__ __forceinline__ float gelu_tanh(float x) {
  const float c1 = -2.0f * 0.7978845608028654f * 1.4426950408889634f;
  const float c2 = c1 * 0.044715f;
  float t = x * (c1 + c2 * x * x);
  float e = __builtin_amdgcn_exp2f(t);
  return x * __builtin_amdgcn_rcpf(1.0f + e);
}
__device__ __forceinline__ float ln_sd(float var) { return rbf(__builtin_amdgcn_sqrtf(rbf(rbf(var) + 1e-5f))); }
__device__ __forceinline__ void lds_fence() { asm volatile("" ::: "memory"); }
__device__ __forceinline__ void bar_lds() {
  asm volatile("s_waitcnt lgkmcnt(0)" ::: "memory");
  __builtin_amdgcn_s_barrier();
  asm volatile("" ::: "memory");
}

__device__ __forceinline__ void transpose8x8(const u32x4 (&r)[8], u32x4 (&o)[8]) {
#pragma unroll
  for (int c2 = 0; c2 < 4; ++c2) {
#pragma unroll
    for (int rp = 0; rp < 4; ++rp) {
      unsigned a = r[2 * rp][c2], b = r[2 * rp + 1][c2];
      o[2 * c2][rp] = __builtin_amdgcn_perm(b, a, 0x05040100u);
      o[2 * c2 + 1][rp] = __builtin_amdgcn_perm(b, a, 0x07060302u);
    }
  }
}
__device__ __forceinline__ f32x2 cmul(f32x2 a, f32x2 b) {
  f32x2 r;
  asm("v_pk_mul_f32 %0, %1, %2 op_sel_hi:[0,1]\n\tv_pk_fma_f32 %0, %1, %2, %0 op_sel:[1,1,0] op_sel_hi:[1,0,1] neg_lo:[1,0,0]"
      : "=&v"(r) : "v"(a), "v"(b));
  return r;
}
__device__ __forceinline__ f32x2 cexp_tab(float a, float b, float l) {
  const float e = __builtin_amdgcn_exp2f(a * l), ph = __builtin_amdgcn_fractf(b * l);
  f32x2 r;
  r.x = e * __builtin_amdgcn_cosf(ph);
  r.y = e * __builtin_amdgcn_sinf(ph);
  return r;
}

constexpr int CV_KCOPY = 8 * 272 * 16;
constexpr int CV_UROW = 160;
constexpr int CV_U = 48 * CV_UROW;
constexpr int CV_CH = CV_KCOPY + CV_U;
constexpr int CV_LDS = 2 * CV_CH;
constexpr int CV_PROW = 272;
static_assert(4 * 32 * CV_PROW <= CV_KCOPY, "partials alias the kernel copies");

struct GArgs {
  const unsigned short* x;
  const unsigned short* a_re;
  const unsigned short* a_im;
  const unsigned short* b_re;
  const unsigned short* b_im;
  const unsigned short* c_re;
  const unsigned short* c_im;
  const unsigned short* log_dt;
  const unsigned short* dskip;
  const unsigned short* w1;
  const unsigned short* w2;
  const unsigned short* w3;
  const unsigned short* w4;
  const unsigned short* cvec;
  const unsigned short* wmod;
  unsigned char* ws;
  unsigned short* out;
};
constexpr int AS = 928;
constexpr int G_T0 = 0;
constexpr int G_T1 = G_T0 + 32 * AS;
constexpr int G_X = G_T1;
constexpr int G_HQ = G_X + 32768;
constexpr int G_WL = G_HQ + 32 * 240;
constexpr int NLDS = 12;
constexpr int NRING = NFRAG - NLDS;
constexpr int G_MOD = G_WL + 7 * NLDS * 1024;
constexpr int G_CNT = G_MOD + 2 * 448 * 4;
constexpr int P0_MS = G_CNT + 16;
constexpr int G_LDS = P0_MS + 256;
constexpr int KG_ROW = 288;
constexpr int KG_BUF = 16 * KG_ROW;
constexpr int P0_KG = CV_LDS;
constexpr int P0_LN = 0;
constexpr int LNS = 928;
constexpr int P0_PK = 16384;
static_assert(P0_KG + 16 * KG_BUF <= G_CNT, "tables");
static_assert(G_MOD >= CV_LDS && G_LDS <= 163840, "LDS map");
constexpr int RING = 44;
constexpr int CV_P = CV_LDS;
static_assert(CV_P + 8 * 32 * CV_PROW <= G_MOD, "partials");
constexpr unsigned SPIN_CAP = 1u << 17;
constexpr int AUX_SC1 = 16;
constexpr int CODE_LINES = 312;
#define PRETOUCH 1
#define X2_DELAY 20
#define POLL_SLEEP 1

__device__ __forceinline__ unsigned sane(unsigned v) { return v < 0xFFFFFFFEu ? v : 0xFFFFFFFEu; }
__device__ __forceinline__ unsigned umax4(const u32x4& v) { return max(max(v.x, v.y), max(v.z, v.w)); }
__device__ __forceinline__ bool bad4(const u32x4& v) { return umax4(v) == ~0u; }
__device__ __forceinline__ u32x4 make_piece(u32x2 a, u32x2 b) {
  auto rx = __builtin_amdgcn_permlane16_swap(a.x, b.x, false, false);
  auto ry = __builtin_amdgcn_permlane16_swap(a.y, b.y, false, false);
  return u32x4{sane(rx[0]), sane(ry[0]), sane(rx[1]), sane(ry[1])};
}
template <int N>
__device__ __forceinline__ void poll_issue(__amdgpu_buffer_rsrc_t r, const int (&off)[N], u32x4 (&v)[N]) {
#pragma unroll
  for (int i = 0; i < N; ++i) v[i] = __builtin_amdgcn_raw_buffer_load_b128(r, off[i], 0, AUX_SC1);
  __builtin_amdgcn_sched_barrier(0);
}
template <int N>
__device__ __forceinline__ void poll_finish(__amdgpu_buffer_rsrc_t r, const int (&off)[N], u32x4 (&v)[N]) {
  unsigned m = 0u;
#pragma unroll
  for (int i = 0; i < N; ++i) m = max(m, umax4(v[i]));
  if (__builtin_expect(__any(m == ~0u), 0)) {
    for (unsigned spins = 0; spins < SPIN_CAP; ++spins) {
      __builtin_amdgcn_s_sleep(POLL_SLEEP);
      asm volatile("" ::: "memory");
#pragma unroll
      for (int i = 0; i < N; ++i)
        if (bad4(v[i])) v[i] = __builtin_amdgcn_raw_buffer_load_b128(r, off[i], 0, AUX_SC1);
      m = 0u;
#pragma unroll
      for (int i = 0; i < N; ++i) m = max(m, umax4(v[i]));
      if (!__any(m == ~0u)) break;
    }
  }
}
template <int N>
__device__ __forceinline__ void poll_load(__amdgpu_buffer_rsrc_t r, const int (&off)[N], u32x4 (&v)[N]) {
  poll_issue<N>(r, off, v);
  poll_finish<N>(r, off, v);
}
__device__ __forceinline__ float dot2bf(unsigned a, unsigned b, float c) {
  return __builtin_amdgcn_fdot2_f32_bf16(__builtin_bit_cast(bf16x2v, a), __builtin_bit_cast(bf16x2v, b), c, false);
}
__device__ __forceinline__ void ln_stats(const unsigned (&x)[16], float lmsk, float& r, float& mr) {
  float s0 = 0.f, s1 = 0.f, q0 = 0.f, q1 = 0.f;
#pragma unroll
  for (int k = 0; k < 16; k += 2) {
    s0 = dot2bf(x[k], 0x3f803f80u, s0); q0 = dot2bf(x[k], x[k], q0);
    s1 = dot2bf(x[k + 1], 0x3f803f80u, s1); q1 = dot2bf(x[k + 1], x[k + 1], q1);
  }
  const float mean = row16_sum((s0 + s1) * lmsk) * (1.0f / D_);
  const float msq = row16_sum((q0 + q1) * lmsk) * (1.0f / D_);
  const float var = fmaxf(msq - mean * mean, 0.f);
  r = __builtin_amdgcn_rcpf(ln_sd(var));
  mr = -rbf(mean) * r;
}

template <class F, int... I>
__device__ __forceinline__ void static_for_impl(F f, std::integer_sequence<int, I...>) { (f(std::integral_constant<int, I>{}), ...); }
template <int N, class F>
__device__ __forceinline__ void static_for(F f) { static_for_impl(f, std::make_integer_sequence<int, N>{}); }
#define SB0() __builtin_amdgcn_sched_barrier(0)
#define WLOAD(f) __builtin_amdgcn_raw_buffer_load_b128(rw, voff + ((f) & 3) * 1024, ((f) >> 2) * 4096, AUX_SC1)
template <int NT, int F0, int LSTART, int LNUM, int LEVERY, int PD>
__device__ __forceinline__ void gemm14(const LAS unsigned char* ap, u32x4 (&ring)[RING], __amdgpu_buffer_rsrc_t rw, int voff, f32x4 (&acc)[NT][2]) {
  u32x4 bq[PD][2];
#pragma unroll
  for (int i = 0; i < PD; ++i) { bq[i][0] = *(const LAS u32x4*)(ap + i * 64); bq[i][1] = *(const LAS u32x4*)(ap + 16 * AS + i * 64); }
#pragma unroll
  for (int i = 0; i < NT; ++i) { acc[i][0] = f32x4{0.f, 0.f, 0.f, 0.f}; acc[i][1] = acc[i][0]; }
  SB0();
#pragma unroll
  for (int ks = 0; ks < 14; ++ks) {
#pragma unroll
    for (int i = 0; i < NT; ++i) {
      const int st = ks * NT + i, f = F0 + st;
      acc[i][0] = mfma16(ring[f % RING], bq[ks % PD][0], acc[i][0]);
      acc[i][1] = mfma16(ring[f % RING], bq[ks % PD][1], acc[i][1]);
      if (st % LEVERY == 0 && st / LEVERY < LNUM) {
        const int g = LSTART + st / LEVERY;
        ring[g % RING] = WLOAD(g);
      }
      if (i == NT - 1 && ks + PD < 14) {
        bq[ks % PD][0] = *(const LAS u32x4*)(ap + (ks + PD) * 64);
        bq[ks % PD][1] = *(const LAS u32x4*)(ap + 16 * AS + (ks + PD) * 64);
      }
      SB0();
    }
  }
}
template <int NT, int F0, int KS0, int KSTEP, int NKS, int LSTART, int LNUM, int PD, bool INIT, class S>
__device__ __forceinline__ void gemm_p(const LAS unsigned char* ap, u32x4 (&ring)[RING], __amdgpu_buffer_rsrc_t rw, int voff, f32x4 (&acc)[NT][2], S side) {
  u32x4 bq[PD][2];
#pragma unroll
  for (int i = 0; i < PD; ++i) {
    bq[i][0] = *(const LAS u32x4*)(ap + (KS0 + i * KSTEP) * 64);
    bq[i][1] = *(const LAS u32x4*)(ap + 16 * AS + (KS0 + i * KSTEP) * 64);
  }
  if (INIT) {
#pragma unroll
    for (int i = 0; i < NT; ++i) { acc[i][0] = f32x4{0.f, 0.f, 0.f, 0.f}; acc[i][1] = acc[i][0]; }
  }
  SB0();
  static_for<NKS * NT>([&](auto stc) {
    constexpr int st = decltype(stc)::value, k = st / NT, i = st % NT, f = F0 + st;
    acc[i][0] = mfma16(ring[f % RING], bq[k % PD][0], acc[i][0]);
    acc[i][1] = mfma16(ring[f % RING], bq[k % PD][1], acc[i][1]);
    constexpr int l0 = st * LNUM / (NKS * NT), l1 = (st + 1) * LNUM / (NKS * NT);
    if constexpr (l1 > l0) {
      static_assert(LSTART + l0 - RING <= f, "slot still in use");
      ring[(LSTART + l0) % RING] = WLOAD(LSTART + l0);
    }
    if constexpr (i == NT - 1 && k + PD < NKS) {
      bq[k % PD][0] = *(const LAS u32x4*)(ap + (KS0 + (k + PD) * KSTEP) * 64);
      bq[k % PD][1] = *(const LAS u32x4*)(ap + 16 * AS + (KS0 + (k + PD) * KSTEP) * 64);
    }
    side(stc);
    SB0();
  });
}
template <int F0, int N>
__device__ __forceinline__ void ring_fill(u32x4 (&ring)[RING], __amdgpu_buffer_rsrc_t rw, int voff) {
#pragma unroll
  for (int f = F0; f < F0 + N; ++f) { ring[f % RING] = WLOAD(f); SB0(); }
}

template <int DELAY>
__device__ __forceinline__ void gather_x2(const GArgs& p, LAS unsigned char* L, __amdgpu_buffer_rsrc_t rs, int pn, int tid) {
  const int lane = tid & 63, w = tid >> 6; (void)lane; (void)w; (void)p;
  u32x4 xin[4];
  int xo[4];
#pragma unroll
  for (int i = 0; i < 4; ++i) {
    const int pce = tid + 512 * i;
    xo[i] = pce < 1792 ? (int)WS_X2B + pn * (int)X2B_BLK + pce * 16 : (int)WS_END;
  }
  if (DELAY) __builtin_amdgcn_s_sleep(DELAY);
  poll_issue<4>(rs, xo, xin);
  poll_finish<4>(rs, xo, xin);
  STAMP(0, 26);
#pragma unroll
  for (int i = 0; i < 4; ++i) {
    const int pce = tid + 512 * i;
    if (i < 3 || pce < 1792) {
      const int dpi = pce >> 3, tq = pce & 7;
      LAS unsigned char* dst = L + G_X + tq * 4096 + (((dpi >> 2) ^ tq) * 16) + (dpi & 3) * 4;
#pragma unroll
      for (int k = 0; k < 4; ++k) *(LAS unsigned*)(dst + k * 1024) = xin[i][k];
    }
  }
}
template <class H>
__device__ __forceinline__ void ln_phase_a(LAS unsigned char* L, int j, int w, int r16, int q, H hook) {
  const int c16 = r16 < 14 ? r16 : 0;
  const float lmsk = r16 < 14 ? 1.0f : 0.0f;
  const int row = 4 * w + q;
  unsigned x[16];
#pragma unroll
  for (int jq = 0; jq < 4; ++jq) {
    const u32x4 t = *(const LAS u32x4*)(L + G_X + row * 1024 + (((((jq + j) & 3) * 14 + c16) ^ w) * 16));
    x[4 * jq] = t.x; x[4 * jq + 1] = t.y; x[4 * jq + 2] = t.z; x[4 * jq + 3] = t.w;
  }
  float r, mr;
  ln_stats(x, lmsk, r, mr);
  hook(std::integral_constant<int, 0>{});
  f32x2 h[16];
#pragma unroll
  for (int jq = 0; jq < 4; ++jq) {
    const LAS unsigned char* mp = L + G_MOD + (((jq + j) & 3) * 112 + c16 * 8) * 4;
    const f32x4 sb0 = *(const LAS f32x4*)mp, sb1 = *(const LAS f32x4*)(mp + 16);
    const f32x4 c10 = *(const LAS f32x4*)(mp + 1792), c11 = *(const LAS f32x4*)(mp + 1792 + 16);
#pragma unroll
    for (int k = 0; k < 4; ++k) {
      const f32x2 xv2 = {bf_lo(x[4 * jq + k]), bf_hi(x[4 * jq + k])};
      const f32x2 c2 = k < 2 ? f32x2{c10[2 * k], c10[2 * k + 1]} : f32x2{c11[2 * k - 4], c11[2 * k - 3]};
      const f32x2 sb2 = k < 2 ? f32x2{sb0[2 * k], sb0[2 * k + 1]} : f32x2{sb1[2 * k - 4], sb1[2 * k - 3]};
      const f32x2 n = xv2 * r + mr;
      h[4 * jq + k] = n * c2 + sb2;
      x[4 * jq + k] = pk_bf16(h[4 * jq + k].x, h[4 * jq + k].y);
    }
  }
  *(LAS u32x4*)(L + G_HQ + row * 240 + (r16 < 14 ? r16 : 14) * 16) = u32x4{x[0], x[1], x[2], x[3]};
  hook(std::integral_constant<int, 1>{});
  ln_stats(x, lmsk, r, mr);
  hook(std::integral_constant<int, 2>{});
#pragma unroll
  for (int jq = 0; jq < 4; ++jq) {
    u32x4 o;
#pragma unroll
    for (int k = 0; k < 4; ++k) {
      const f32x2 n = h[4 * jq + k] * r + mr;
      o[k] = pk_bf16(n.x, n.y);
    }
    *(LAS u32x4*)(L + G_T0 + row * AS + (r16 < 14 ? (((jq + j) & 3) * 14 + r16) * 16 : 896)) = o;
  }
}
__device__ __forceinline__ void x1_offsets(int pn, int j, int w, int r16, int q, int (&gof)[3]) {
  const int c16 = r16 < 14 ? r16 : 0;
  const int go = (int)WS_X1 + pn * 4 * 7168 + ((c16 >> 1) * 64 + w * 8 + (c16 & 1) * 4 + q) * 16;
#pragma unroll
  for (int i = 0; i < 3; ++i) gof[i] = go + ((j + 1 + i) & 3) * 7168;
}
struct LnOwn { f32x2 v[4]; float s, q; };
__device__ __forceinline__ LnOwn ln_c_own(LAS unsigned char* L, int w, int r16, int q) {
  const u32x4 own = *(const LAS u32x4*)(L + G_HQ + (4 * w + q) * 240 + (r16 < 14 ? r16 : 0) * 16);
  LnOwn o;
  o.s = 0.f; o.q = 0.f;
#pragma unroll
  for (int k = 0; k < 4; ++k) {
    o.s = dot2bf(own[k], 0x3f803f80u, o.s); o.q = dot2bf(own[k], own[k], o.q);
    o.v[k] = f32x2{bf_lo(own[k]), bf_hi(own[k])};
  }
  asm volatile("" : "+v"(o.s), "+v"(o.q), "+v"(o.v[0]), "+v"(o.v[1]), "+v"(o.v[2]), "+v"(o.v[3]));
  return o;
}
__device__ __forceinline__ void ln_phase_c(LAS unsigned char* L, const LnOwn& ow, const u32x4 (&xr)[3], int j, int w, int r16, int q) {
  const float lmsk = r16 < 14 ? 1.0f : 0.0f;
  const int row = 4 * w + q;
  float s0 = ow.s, s1 = 0.f, q0 = ow.q, q1 = 0.f;
#pragma unroll
  for (int k = 0; k < 12; k += 2) {
    const unsigned x0 = xr[k >> 2][k & 3], x1 = xr[k >> 2][(k & 3) + 1];
    s0 = dot2bf(x0, 0x3f803f80u, s0); q0 = dot2bf(x0, x0, q0);
    s1 = dot2bf(x1, 0x3f803f80u, s1); q1 = dot2bf(x1, x1, q1);
  }
  const float mean = row16_sum((s0 + s1) * lmsk) * (1.0f / D_);
  const float msq = row16_sum((q0 + q1) * lmsk) * (1.0f / D_);
  const float r = __builtin_amdgcn_rcpf(ln_sd(fmaxf(msq - mean * mean, 0.f)));
  const float mr = -rbf(mean) * r;
#pragma unroll
  for (int jq = 0; jq < 4; ++jq) {
    u32x4 o;
#pragma unroll
    for (int k = 0; k < 4; ++k) {
      const f32x2 xv = jq == 0 ? ow.v[k] : f32x2{bf_lo(xr[jq ? jq - 1 : 0][k]), bf_hi(xr[jq ? jq - 1 : 0][k])};
      const f32x2 n = xv * r + mr;
      o[k] = pk_bf16(n.x, n.y);
    }
    *(LAS u32x4*)(L + G_T0 + row * AS + (r16 < 14 ? (((jq + j) & 3) * 14 + r16) * 16 : 896)) = o;
  }
}
__device__ __forceinline__ unsigned gelu2(float a, float b) {
  return pk_bf16(gelu_tanh(a), gelu_tanh(b));
}
__device__ __forceinline__ u32x2 gelu4(f32x4 a) { return u32x2{gelu2(a[0], a[1]), gelu2(a[2], a[3])}; }

#define ST16(off, v) __builtin_amdgcn_raw_buffer_store_b128(v, rs, (int)(off), 0, AUX_SC1)
typedef unsigned long long u64;

struct PackJob { const GAS unsigned short* src; int ldn, mat, ks, cb, na, nb, fb; };
__device__ __forceinline__ PackJob pack_job(const GArgs& p, int g) {
  PackJob jb;
  const uintptr_t a1 = (uintptr_t)p.w1, a2 = (uintptr_t)p.w2, a3 = (uintptr_t)p.w3, a4 = (uintptr_t)p.w4;
  uintptr_t lo = g < 56 ? a1 : a2, hi = g < 336 ? a3 : a4;
  asm volatile("" : "+s"(lo), "+s"(hi));
  jb.src = (const GAS unsigned short*)(g < 112 ? lo : hi);
  jb.mat = (g >= 56) + (g >= 112) + (g >= 336);
  jb.ldn = jb.mat == 2 ? 1792 : 448;
  g -= g < 112 ? (g < 56 ? 0 : 56) : (g < 336 ? 112 : 336);
  const int sh = jb.mat == 2 ? 4 : 2;
  jb.ks = g >> sh;
  jb.cb = g & ((1 << sh) - 1);
  const int j3 = (jb.ks * 4682) >> 16, ksl = jb.ks - j3 * 14, sl = (jb.cb - j3 - 1) & 3;
  const int is2 = jb.mat == 2, is3 = jb.mat == 3;
  jb.na = is2 * jb.cb * 7 + (1 - is2) * (is3 * j3 + (1 - is3) * jb.cb) * 28;
  jb.nb = 4 - 3 * is2;
  const int own = sl == 3;
  const int f3 = (1 - own) * (84 + (ksl & 1) * 21 + (ksl >> 1) * 3 + sl) + own * (126 + ksl);
  jb.fb = is2 * (28 + jb.ks * 2) + is3 * f3 + (1 - is2 - is3) * (jb.mat * 14 + jb.ks);
  jb.na = __builtin_amdgcn_readfirstlane(jb.na); jb.nb = __builtin_amdgcn_readfirstlane(jb.nb); jb.fb = __builtin_amdgcn_readfirstlane(jb.fb);
  return jb;
}
__device__ __forceinline__ void pack_load(const PackJob& jb, int lane, u32x4 (&r)[8]) {
  const int jn = lane & 15, q = lane >> 4, jl = jn < 14 ? jn : 0;
#pragma unroll
  for (int i = 0; i < 8; ++i) r[i] = *(const GAS u32x4*)(jb.src + (size_t)(jb.ks * 32 + q * 8 + i) * jb.ldn + jb.cb * 112 + jl * 8);
}
__device__ __forceinline__ void pack_shuffle(int lane, u32x4 (&r)[8], LAS unsigned char* Lw) {
  const int jn = lane & 15, q = lane >> 4;
  u32x4 o[8];
  transpose8x8(r, o);
#pragma unroll
  for (int i = 0; i < 8; ++i) *(LAS u32x4*)(Lw + (jn >> 1) * 1024 + (q * 16 + (jn & 1) * 8 + (i ^ (jn >> 1))) * 16) = o[i];
  lds_fence();
#pragma unroll
  for (int m = 0; m < 7; ++m) r[m] = *(const LAS u32x4*)(Lw + m * 1024 + (lane ^ m) * 16);
  lds_fence();
}
__device__ __forceinline__ void pack_store1(const PackJob& jb, int lane, const u32x4 (&r)[8], __amdgpu_buffer_rsrc_t rs, int m) {
  const int nt = jb.na + m * jb.nb;
  __builtin_amdgcn_raw_buffer_store_b128(r[m], rs, lane * 16, (int)WS_WP + ((nt >> 2) * NFRAG + jb.fb + ((nt >> 1) & 1) * 28 + (nt & 1)) * 1024, AUX_SC1);
}
__device__ __forceinline__ void mod_load(const GArgs& p, int u, int tid, u32x4 (&r)[7], float (&sc)[7]) {
  const int rg = tid >> 3, pc = tid & 7;
#pragma unroll
  for (int i = 0; i < 7; ++i) r[i] = *(const u32x4*)(p.wmod + (size_t)(rg + 64 * i) * 2688 + u * 64 + pc * 8);
#pragma unroll
  for (int i = 0; i < 7; ++i) sc[i] = bf2f(p.cvec[rg + 64 * i]);
}
__device__ __forceinline__ void mod_finish(int u, unsigned tag32, int tid, const u32x4 (&r)[7], const float (&cv)[7], LAS float* sm, __amdgpu_buffer_rsrc_t rs) {
  const int rg = tid >> 3, pc = tid & 7;
  float acc[8];
#pragma unroll
  for (int k = 0; k < 8; ++k) acc[k] = 0.f;
#pragma unroll
  for (int i = 0; i < 7; ++i) {
    const float sc = rbf(cv[i] * __builtin_amdgcn_rcpf(1.0f + __builtin_amdgcn_exp2f(-1.4426950408889634f * cv[i])));
#pragma unroll
    for (int k = 0; k < 4; ++k) { acc[2 * k] += sc * bf_lo(r[i][k]); acc[2 * k + 1] += sc * bf_hi(r[i][k]); }
  }
  *(LAS f32x4*)(sm + rg * 68 + pc * 8) = f32x4{acc[0], acc[1], acc[2], acc[3]};
  *(LAS f32x4*)(sm + rg * 68 + pc * 8 + 4) = f32x4{acc[4], acc[5], acc[6], acc[7]};
  bar_lds();
  {
    const int col = tid & 63, part = tid >> 6;
    float s = 0.f;
#pragma unroll
    for (int k = 0; k < 8; ++k) s += sm[(part * 8 + k) * 68 + col];
    sm[64 * 68 + part * 64 + col] = s;
  }
  bar_lds();
  if (tid < 32) {
    f32x2 s = {0.f, 0.f};
#pragma unroll
    for (int k = 0; k < 8; ++k) s += *(const LAS f32x2*)(sm + 64 * 68 + k * 64 + 2 * tid);
    unsigned v = pk_bf16(s.x, s.y);
    const int sec = u / 7;
    if (sec == 1 || sec == 4) v = pk_bf16(bf_lo(v) + 1.0f, bf_hi(v) + 1.0f);
    __builtin_amdgcn_raw_buffer_store_b64(u32x2{v, tag32}, rs, (int)WS_MODG + (u * 32 + tid) * 8, 0, AUX_SC1);
  }
}
template <int YOUNGER>
__device__ __forceinline__ void arrive_flag(LAS unsigned char* L, int cnt_slot, unsigned char* flag, u64 tag, int lane) {
  asm volatile("s_waitcnt vmcnt(%0)" ::"i"(YOUNGER) : "memory");
  if (lane == 0) {
    const unsigned old = __hip_atomic_fetch_add((LAS unsigned*)(L + G_CNT) + cnt_slot, 1u, __ATOMIC_RELAXED, __HIP_MEMORY_SCOPE_WORKGROUP);
    if (old == 7u) __hip_atomic_store((GAS u64*)flag, tag, __ATOMIC_RELAXED, __HIP_MEMORY_SCOPE_AGENT);
  }
}
__device__ __forceinline__ u32x2 flag_load(__amdgpu_buffer_rsrc_t rs, int off) { return __builtin_amdgcn_raw_buffer_load_b64(rs, off, 0, AUX_SC1); }
__device__ __forceinline__ u32x2 flag_wait(__amdgpu_buffer_rsrc_t rs, int off, u32x2 v, unsigned lo, unsigned hi, unsigned lomask) {
  if (__builtin_expect(__any((((v.x ^ lo) & lomask) | (v.y ^ hi)) != 0u), 0)) {
    for (unsigned spins = 0; spins < SPIN_CAP; ++spins) {
      __builtin_amdgcn_s_sleep(2);
      asm volatile("" ::: "memory");
      v = flag_load(rs, off);
      if (!__any((((v.x ^ lo) & lomask) | (v.y ^ hi)) != 0u)) break;
    }
  }
  __builtin_amdgcn_fence(__ATOMIC_ACQUIRE, "wavefront");
  return v;
}
__device__ __forceinline__ void stat_wait(__amdgpu_buffer_rsrc_t rs, int off, u32x4& a, u32x4& b, unsigned tag32) {
  if (__builtin_expect(__any((((a.y ^ tag32) | (a.w ^ tag32)) | ((b.y ^ tag32) | (b.w ^ tag32))) != 0u), 0)) {
    for (unsigned spins = 0; spins < SPIN_CAP; ++spins) {
      __builtin_amdgcn_s_sleep(2);
      asm volatile("" ::: "memory");
      a = __builtin_amdgcn_raw_buffer_load_b128(rs, off, 0, AUX_SC1);
      b = __builtin_amdgcn_raw_buffer_load_b128(rs, off + 16, 0, AUX_SC1);
      if (!__any((((a.y ^ tag32) | (a.w ^ tag32)) | ((b.y ^ tag32) | (b.w ^ tag32))) != 0u)) break;
    }
  }
}
#define FILL_P(i) do { SB0(); __builtin_amdgcn_raw_buffer_store_b128(ONES, rst, (int)WS_P + ((pn * 4 + j) * 3 + (i)) * 7168 + pslot, 0, AUX_SC1); SB0(); } while (0)
#define FILL_X1() do { SB0(); __builtin_amdgcn_raw_buffer_store_b128(ONES, rst, (int)WS_X1 + (pn * 4 + j) * 7168 + pslot, 0, AUX_SC1); SB0(); } while (0)
__device__ __forceinline__ void body(const GArgs& p, LAS unsigned char* L, uintptr_t code0) {
  const bool CONV = blockIdx.x < 224;
  const int tid = threadIdx.x, lane = tid & 63;
  const int w = __builtin_amdgcn_readfirstlane(tid >> 6);
  const int bid = blockIdx.x;
  const int j = bid & 3, pn = (bid >> 3) * 2 + ((bid >> 2) & 1);
  const int r16 = lane & 15, q = lane >> 4;
  const bool cw = w < 7;
  const int wc = cw ? w : 6;
  const int dp = (bid & 7) * 28 + (CONV ? bid >> 3 : 0);
  const __amdgpu_buffer_rsrc_t rs = __builtin_amdgcn_make_buffer_rsrc((void*)p.ws, 0, (int)WS_END, 0x00020000);
  const __amdgpu_buffer_rsrc_t rst = __builtin_amdgcn_make_buffer_rsrc((void*)p.ws, 0, cw ? (int)WS_END : 0, 0x00020000);
  const __amdgpu_buffer_rsrc_t rw =
      __builtin_amdgcn_make_buffer_rsrc((void*)(p.ws + WS_WP + (size_t)(j * 7 + wc) * NFRAG * 1024), 0, cw ? NFRAG * 1024 : 0, 0x00020000);
  const int voff = lane * 16;
  const int pslot = (wc * 64 + lane) * 16;
  const u32x4 ONES = {~0u, ~0u, ~0u, ~0u};
  STAMP(0, 0);
  const int l56 = lane < 56 ? lane : 0;
  const u32x4 xrow = *(const u32x4*)(p.x + (size_t)(bid * 8 + w) * D_ + l56 * 8);
  const u32x2 ep = flag_load(rs, (int)WS_EPOCH);
  const int cl = ((bid >> 3) & 3) * 80 + w * 10 + (lane < 10 ? lane : 0);
  const unsigned touch = *(const GAS unsigned*)(code0 + (size_t)(cl < CODE_LINES ? cl : 0) * 128);
  float are, aim, b_re, b_im, cre, cim, ldt;
  u32x4 mr[7], pk[8];
  float mc[7];
  {
    const size_t o = (size_t)(2 * dp + (w >> 2)) * NS_ + (w & 3) * 64 + lane;
    SB0();
    are = bf2f(p.a_re[o]); aim = bf2f(p.a_im[o]); b_re = bf2f(p.b_re[o]); b_im = bf2f(p.b_im[o]);
    cre = bf2f(p.c_re[o]); cim = bf2f(p.c_im[o]); ldt = bf2f(p.log_dt[2 * dp + (w >> 2)]);
    SB0();
  }
  __builtin_amdgcn_s_barrier();
  __builtin_amdgcn_raw_buffer_store_b128(ONES, rst, (int)WS_X2B + bid * 7168 + pslot, 0, AUX_SC1);
  __builtin_amdgcn_raw_buffer_store_b128(ONES, rst, (int)WS_G + bid * 7168 + pslot, 0, AUX_SC1);
  SB0();
  const int pg = (bid - 224) * 6 + w;
  const bool packer = CONV ? w < 2 : (w < 6 && pg < 112);
  const PackJob jb = pack_job(p, CONV ? bid * 2 + (w & 1) : (packer ? 448 + pg : 0));
  if (tid < 4) *((LAS unsigned*)(L + G_CNT) + tid) = 0u;
  u64 tag;
  {
    const u64 e = ((u64)__builtin_amdgcn_readfirstlane(ep.y) << 32) | (unsigned)__builtin_amdgcn_readfirstlane(ep.x);
    u64 z = e + 0x9E3779B97F4A7C15ull;
    z = (z ^ (z >> 30)) * 0xBF58476D1CE4E5B9ull;
    z = (z ^ (z >> 27)) * 0x94D049BB133111EBull;
    tag = z ^ (z >> 31);
  }
  const unsigned tag32 = ((unsigned)__builtin_amdgcn_readfirstlane(ep.x) + 1u) * 0x9E3779B1u ^ 0x5bd1e995u;
  const unsigned tlo = (unsigned)tag, thi = (unsigned)(tag >> 32);
  unsigned stv;
#define PUT_STAT() do { if (lane == 0) __builtin_amdgcn_raw_buffer_store_b64(u32x2{stv, tag32}, rs, (int)WS_STG + (bid * 8 + w) * 8, 0, AUX_SC1); } while (0)
  {
    const float msk = lane < 56 ? 1.0f : 0.0f;
    float v[8];
#pragma unroll
    for (int k = 0; k < 4; ++k) { v[2 * k] = bf_lo(xrow[k]); v[2 * k + 1] = bf_hi(xrow[k]); }
    const float mean = wave_sum((((v[0] + v[1]) + (v[2] + v[3])) + ((v[4] + v[5]) + (v[6] + v[7]))) * msk) * (1.0f / D_);
    float qq = 0.f;
#pragma unroll
    for (int k = 0; k < 8; ++k) { const float d = v[k] - mean; qq += d * d; }
    const float sd = ln_sd(wave_sum(qq * msk) * (1.0f / D_));
    stv = pk_bf16(mean, sd);
    if (lane < 56) *(LAS u32x4*)(L + P0_LN + w * LNS + lane * 16) = xrow;
    bar_lds();
#pragma unroll
    for (int u = 0; u < 2; ++u) {
      const int pce = (wc * 2 + u) * 64 + lane;
      const LAS unsigned* a = (const LAS unsigned*)(L + P0_LN + (pce & 3) * (2 * LNS) + (pce >> 2) * 4);
      __builtin_amdgcn_raw_buffer_store_b128(u32x4{a[0], tag32, a[LNS / 4], tag32}, rst, (int)WS_XG + bid * (int)XG_BLK + pce * 16, 0, AUX_SC1);
    }
  }
  asm volatile("" ::"v"(touch));
  STAMP(0, 29);
  const int sto = (int)WS_STG + tid * 32;
  u32x4 ring[RING];
  if (__builtin_expect(CONV, 1)) {
    const float dt = rbf(expf(ldt));
    const float ea = (dt * are) * 1.4426950408889634f, eb = (dt * aim) * 0.15915494309189535f;
    const float bre = dt * b_re, bim = dt * b_im;
    f32x2 wc;
    wc.x = cre * bre - cim * bim;
    wc.y = -(cre * bim + cim * bre);
    asm volatile("" ::"v"(ea), "v"(eb), "v"(wc.x), "v"(wc.y));
    const float mx = rows_max(row16_max(fmaxf(fabsf(wc.x), fabsf(wc.y))));
    int ex = mx > 0.f ? __builtin_amdgcn_frexp_expf(mx) : 0;
    ex = ex < -120 ? -120 : (ex > 120 ? 120 : ex);
    const float scale = __builtin_amdgcn_ldexpf(1.0f, ex), iscale = __builtin_amdgcn_ldexpf(1.0f, -ex);
    wc.x *= iscale; wc.y *= iscale;
    LAS unsigned char* B0 = L + P0_KG + w * (2 * KG_BUF);
    LAS unsigned char* B1 = B0 + KG_BUF;
    const int wo = lane * 4;
    const int ro = r16 * KG_ROW + q * 16;
    f32x2 z8c = cexp_tab(ea, eb, 8.0f);
    z8c.y = -z8c.y;
    const f32x2 z1 = cexp_tab(ea, eb, 1.0f), z128 = cexp_tab(ea, eb, 128.0f), z1024 = cexp_tab(ea, eb, 1024.0f);
    {
      const f32x2 z16c = cmul(z8c, z8c);
      f32x2 t0 = wc, t1 = cmul(wc, z8c);
#pragma unroll
      for (int c = 0; c < 16; c += 2) {
        *(LAS unsigned*)(B0 + c * KG_ROW + wo) = pk_f16(t0);
        *(LAS unsigned*)(B0 + (c + 1) * KG_ROW + wo) = pk_f16(t1);
        if (c < 14) { t0 = cmul(t0, z16c); t1 = cmul(t1, z16c); }
      }
    }
    SB0();
    asm volatile("s_waitcnt vmcnt(2)" ::: "memory");
    PUT_STAT();
    SB0();
    f32x2 ze[8];
    ze[0] = f32x2{1.0f, 0.0f}; ze[1] = z1; ze[2] = cmul(z1, z1); ze[3] = cmul(ze[2], z1); ze[4] = cmul(ze[2], ze[2]);
    ze[5] = cmul(ze[4], z1); ze[6] = cmul(ze[4], ze[2]); ze[7] = cmul(ze[4], ze[3]);
#pragma unroll
    for (int e = 0; e < 8; ++e) {
      *(LAS unsigned*)(B1 + e * KG_ROW + wo) = pk_f16(ze[e]);
      *(LAS unsigned*)(B1 + (8 + e) * KG_ROW + wo) = pk_f16(e == 0 ? z128 : cmul(z128, ze[e]));
    }
    lds_fence();
    u32x4 tf[4];
#pragma unroll
    for (int ks = 0; ks < 4; ++ks) tf[ks] = *(const LAS u32x4*)(B0 + ro + ks * 64);
    u32x4 rf[4];
#pragma unroll
    for (int ks = 0; ks < 4; ++ks) rf[ks] = *(const LAS u32x4*)(B1 + ro + ks * 64);
    lds_fence();
    SB0();
    if (packer) pack_load(jb, lane, pk);
    SB0();
    STAMP(0, 30);
    const f32x2 p2 = cmul(z128, z128), p3 = cmul(p2, z128), p4 = cmul(p2, p2), p5 = cmul(p4, z128), p6 = cmul(p4, p2), p7 = cmul(p4, p3);
    {
#pragma unroll
      for (int e = 0; e < 4; ++e) {
        *(LAS unsigned*)(B0 + e * KG_ROW + wo) = pk_f16(e == 0 ? p2 : cmul(p2, ze[2 * e]));
        *(LAS unsigned*)(B0 + (4 + e) * KG_ROW + wo) = pk_f16(e == 0 ? p3 : cmul(p3, ze[2 * e]));
      }
      *(LAS unsigned*)(B0 + 8 * KG_ROW + wo) = pk_f16(p4);
      *(LAS unsigned*)(B0 + 9 * KG_ROW + wo) = pk_f16(cmul(p4, ze[4]));
      *(LAS unsigned*)(B0 + 10 * KG_ROW + wo) = pk_f16(p5);
      *(LAS unsigned*)(B0 + 11 * KG_ROW + wo) = pk_f16(cmul(p5, ze[4]));
      *(LAS unsigned*)(B0 + 12 * KG_ROW + wo) = pk_f16(p6);
      *(LAS unsigned*)(B0 + 13 * KG_ROW + wo) = pk_f16(cmul(p6, ze[4]));
      *(LAS unsigned*)(B0 + 14 * KG_ROW + wo) = pk_f16(p7);
      *(LAS unsigned*)(B0 + 15 * KG_ROW + wo) = pk_f16(cmul(p7, ze[4]));
    }
    f32x4 acc0 = {0.f, 0.f, 0.f, 0.f}, acc1 = acc0, acc2 = acc0;
#pragma unroll
    for (int ks = 0; ks < 4; ++ks) acc0 = mfma16h(rf[ks], tf[ks], acc0);
    {
      *(LAS unsigned*)(B1 + 0 * KG_ROW + wo) = pk_f16(z1024);
      *(LAS unsigned*)(B1 + 1 * KG_ROW + wo) = pk_f16(cmul(z1024, z128));
      *(LAS unsigned*)(B1 + 2 * KG_ROW + wo) = pk_f16(cmul(z1024, p2));
      *(LAS unsigned*)(B1 + 3 * KG_ROW + wo) = pk_f16(cmul(z1024, p3));
      *(LAS unsigned*)(B1 + 4 * KG_ROW + wo) = pk_f16(cmul(z1024, p4));
      *(LAS unsigned*)(B1 + 5 * KG_ROW + wo) = pk_f16(cmul(z1024, p5));
      *(LAS unsigned*)(B1 + 6 * KG_ROW + wo) = pk_f16(cmul(z1024, p6));
      *(LAS unsigned*)(B1 + 7 * KG_ROW + wo) = pk_f16(cmul(z1024, p7));
      *(LAS unsigned*)(B1 + 8 * KG_ROW + wo) = pk_f16(cmul(z1024, z1024));
    }
    lds_fence();
#pragma unroll
    for (int ks = 0; ks < 4; ++ks) acc1 = mfma16h(*(const LAS u32x4*)(B0 + ro + ks * 64), tf[ks], acc1);
#pragma unroll
    for (int ks = 0; ks < 4; ++ks) acc2 = mfma16h(*(const LAS u32x4*)(B1 + ro + ks * 64), tf[ks], acc2);
    lds_fence();
    {
      LAS float* red = (LAS float*)B0;
      acc0 *= scale; acc1 *= scale; acc2 *= scale;
      *(LAS f32x4*)(red + (q >> 1) * 128 + 8 * r16 + (q & 1) * 4) = acc0;
      const int i1 = q < 2 ? 256 + q * 64 + 4 * r16 : 384 + (q - 2) * 64 + 2 * r16, d1 = q < 2 ? 2 : 32;
      *(LAS f32x2*)(red + i1) = f32x2{acc1[0], acc1[1]};
      *(LAS f32x2*)(red + i1 + d1) = f32x2{acc1[2], acc1[3]};
      LAS float* r2 = red + 512 + q * 64 + r16;
      r2[0] = acc2[0]; r2[16] = acc2[1]; r2[32] = acc2[2]; r2[48] = acc2[3];
    }
    STAMP(0, 21);
    bar_lds();
    const int mgo = (int)WS_MODG + ((lane < 3 ? lane : 0) * 224 + dp) * 8;
    SB0();
    u32x4 sg0 = __builtin_amdgcn_raw_buffer_load_b128(rs, sto, 0, AUX_SC1), sg1 = __builtin_amdgcn_raw_buffer_load_b128(rs, sto + 16, 0, AUX_SC1);
    const int xgo = (int)WS_XG + (tid >> 1) * (int)XG_BLK + dp * 64 + (tid & 1) * 32;
    u32x4 xg0 = __builtin_amdgcn_raw_buffer_load_b128(rs, xgo, 0, AUX_SC1), xg1 = __builtin_amdgcn_raw_buffer_load_b128(rs, xgo + 16, 0, AUX_SC1);
    const unsigned dsk2 = *(const unsigned*)(p.dskip + 2 * dp);
    SB0();
    const int kc = tid >> 8, kt = tid & 255;
    LAS float* kd = (LAS float*)(L + P0_KG + kc * (8 * KG_BUF) + KG_BUF);
#pragma unroll
    for (int i = 0; i < 3; ++i) {
      const int oo = kt + 256 * i;
      if (i < 2 || oo < 648) {
        const LAS float* s = (const LAS float*)(L + P0_KG + kc * (8 * KG_BUF)) + oo;
        kd[oo] = (s[0] + s[2 * KG_BUF / 4]) + (s[4 * KG_BUF / 4] + s[6 * KG_BUF / 4]);
      }
    }
    if (kt == 0) kd[648] = 0.f;
    const unsigned OFFS = 0x087EDA52u;
    {
      int off = CV_KCOPY + kt * 16;
      if (kt >= 160) { const int k = kt - 160, s = k >> 3, ch = (k & 7) == 7 ? 6 : (k & 7); off = (272 * (s & 7) + ((OFFS >> (4 * (s & 7))) & 15) + 257 + ch) * 16; }
      *(LAS u32x4*)(L + kc * CV_CH + off) = u32x4{0u, 0u, 0u, 0u};
    }
    bar_lds();
    LAS unsigned char* base = L + kc * CV_CH;
    u32x4 k0;
    {
      float v[8];
#pragma unroll
      for (int i = 0; i < 8; i += 2) {
        const int l0 = 2048 - (kt * 8 + i), l1 = l0 - 1;
        const unsigned bits = pk_bf16((float)l0, (float)l1);
        int i0 = l0 < 128 ? l0 : (int)(bits & 0xffffu) - 17024;
        const int i1 = l1 < 128 ? l1 : (int)(bits >> 16) - 17024;
        if (i == 0) i0 = kt == 0 ? 648 : i0;
        v[i] = kd[i0];
        v[i + 1] = kd[i1];
      }
      k0 = u32x4{pk_bf16(v[0], v[1]), pk_bf16(v[2], v[3]), pk_bf16(v[4], v[5]), pk_bf16(v[6], v[7])};
      *(LAS u32x4*)(base + (2 + kt) * 16) = k0;
      if (kt < 8) {
        const unsigned k00 = pk_bf16(kd[0], 0.f);
        *(LAS u32x4*)(base + (272 * kt + ((OFFS >> (4 * kt)) & 15) + 256) * 16) = u32x4{kt == 0 ? k00 : 0u, 0u, 0u, 0u};
      }
    }
    bar_lds();
    {
      const u32x4 k1 = *(const LAS u32x4*)(base + (2 + kt + 1) * 16);
      unsigned wv[8] = {k0.x, k0.y, k0.z, k0.w, k1.x, k1.y, k1.z, k1.w};
      unsigned vv[7];
#pragma unroll
      for (int i = 0; i < 7; ++i) vv[i] = __builtin_amdgcn_alignbit(wv[i + 1], wv[i], 16);
#pragma unroll
      for (int s = 1; s < 8; ++s) {
        const int k = s >> 1;
        u32x4 o = (s & 1) ? u32x4{vv[k], vv[k + 1], vv[k + 2], vv[k + 3]} : u32x4{wv[k], wv[k + 1], wv[k + 2], wv[k + 3]};
        *(LAS u32x4*)(base + (272 * s + ((OFFS >> (4 * s)) & 15) + kt) * 16) = o;
      }
    }
    float cum0[4], cum1[4];
    {
      const LAS float* kd0 = (const LAS float*)(L + P0_KG + KG_BUF);
      const LAS float* kd1 = (const LAS float*)(L + P0_KG + 8 * KG_BUF + KG_BUF);
#pragma unroll
      for (int i = 0; i < 4; i += 2) {
        const int l0 = 4 * tid + i;
        const unsigned bits = pk_bf16((float)l0, (float)(l0 + 1));
        const int i0 = l0 < 128 ? l0 : (int)(bits & 0xffffu) - 17024, i1 = l0 < 128 ? l0 + 1 : (int)(bits >> 16) - 17024;
        cum0[i] = kd0[i0]; cum0[i + 1] = kd0[i1]; cum1[i] = kd1[i0]; cum1[i + 1] = kd1[i1];
      }
#pragma unroll
      for (int i = 1; i < 4; ++i) { cum0[i] += cum0[i - 1]; cum1[i] += cum1[i - 1]; }
      const float inc0 = wave_scan(cum0[3]), inc1 = wave_scan(cum1[3]);
      const float e0 = inc0 - cum0[3], e1 = inc1 - cum1[3];
#pragma unroll
      for (int i = 0; i < 4; ++i) { cum0[i] += e0; cum1[i] += e1; }
      if (lane == 63) *(LAS f32x2*)(L + P0_MS + w * 8) = f32x2{inc0, inc1};
    }
#define GRAN_BAD() __any(((((sg0.y ^ tag32) | (sg0.w ^ tag32)) | ((sg1.y ^ tag32) | (sg1.w ^ tag32))) | (((xg0.y ^ tag32) | (xg0.w ^ tag32)) | ((xg1.y ^ tag32) | (xg1.w ^ tag32)))) != 0u)
    if (__builtin_expect(GRAN_BAD(), 0)) {
      for (unsigned spins = 0; spins < SPIN_CAP; ++spins) {
        __builtin_amdgcn_s_sleep(2);
        asm volatile("" ::: "memory");
        sg0 = __builtin_amdgcn_raw_buffer_load_b128(rs, sto, 0, AUX_SC1); sg1 = __builtin_amdgcn_raw_buffer_load_b128(rs, sto + 16, 0, AUX_SC1);
        xg0 = __builtin_amdgcn_raw_buffer_load_b128(rs, xgo, 0, AUX_SC1); xg1 = __builtin_amdgcn_raw_buffer_load_b128(rs, xgo + 16, 0, AUX_SC1);
        if (!GRAN_BAD()) break;
      }
    }
#undef GRAN_BAD
    const unsigned xq[4] = {xg0.x, xg0.z, xg1.x, xg1.z};
    STAMP(0, 22);
    unsigned hq[4];
    {
      const unsigned sv[4] = {sg0.x, sg0.z, sg1.x, sg1.z};
#pragma unroll
      for (int k = 0; k < 4; ++k) {
        const float m = bf_lo(sv[k]), r = __builtin_amdgcn_rcpf(bf_hi(sv[k]));
        unsigned t = pk_bf16(bf_lo(xq[k]) - m, bf_hi(xq[k]) - m);
        hq[k] = pk_bf16(bf_lo(t) * r, bf_hi(t) * r);
      }
      LAS unsigned char* U = L + CV_KCOPY + (16 + (tid >> 4)) * CV_UROW + (tid & 15) * 8;
      *(LAS u32x2*)U = u32x2{__builtin_amdgcn_perm(hq[1], hq[0], 0x05040100u), __builtin_amdgcn_perm(hq[3], hq[2], 0x05040100u)};
      *(LAS u32x2*)(U + CV_CH) = u32x2{__builtin_amdgcn_perm(hq[1], hq[0], 0x07060302u), __builtin_amdgcn_perm(hq[3], hq[2], 0x07060302u)};
    }
    if (packer) pack_shuffle(lane, pk, L + P0_KG + (w + 1) * (2 * KG_BUF));
    STAMP(0, 23);
    bar_lds();
    if (bid == 0 && tid == 0) {
      const u64 e = ((u64)ep.y << 32) | ep.x;
      __hip_atomic_store((GAS u64*)(p.ws + WS_EPOCH), e + 1ull, __ATOMIC_RELAXED, __HIP_MEMORY_SCOPE_AGENT);
    }
    u32x2 fv = flag_load(rs, mgo);
    const __amdgpu_buffer_rsrc_t rpk = __builtin_amdgcn_make_buffer_rsrc((void*)p.ws, 0, packer ? (int)WS_END : 0, 0x00020000);
    SB0();
  {
    const int ch = w >> 2, wq = w & 3;
    f32x4 acc[2][4];
#pragma unroll
    for (int n = 0; n < 2; ++n)
#pragma unroll
      for (int m = 0; m < 4; ++m) acc[n][m] = f32x4{0.f, 0.f, 0.f, 0.f};
    const int s = (8 - r16) & 7, u = (r16 + 7) >> 3;
    const LAS unsigned char* ka = L + ch * CV_CH + (272 * s + ((OFFS >> (4 * s)) & 15) + 256 + q - u - 2 * (16 * wq) - 2 * 79) * 16;
    const LAS unsigned char* ub = L + ch * CV_CH + CV_KCOPY + (16 + r16 - 4 * wq - 3) * CV_UROW + q * 16;
#define KFRAG(f) (*(const LAS u32x4*)(ka + (79 - (f)) * 32))
    u32x4 alo[18], ahi[18], bf[4][4];
#define CSTAGE(dl)                                                                                      \
  {                                                                                                     \
    _Pragma("unroll") for (int ks = 0; ks < 2; ++ks) {                                                  \
      bf[dl][2 * ks] = *(const LAS u32x4*)(ub + (3 - (dl)) * CV_UROW + ks * 64);                        \
      bf[dl][2 * ks + 1] = *(const LAS u32x4*)(ub + (19 - (dl)) * CV_UROW + ks * 64);                   \
    }                                                                                                   \
    _Pragma("unroll") for (int i = 0; i < 4; ++i) {                                                     \
      alo[2 + 4 * (dl) + i] = KFRAG(4 * (dl) + i);                                                      \
      ahi[2 + 4 * (dl) + i] = KFRAG(4 * (dl) + i + 64);                                                 \
    }                                                                                                   \
  }
    CSTAGE(0)
    alo[0] = KFRAG(-2); alo[1] = KFRAG(-1); ahi[0] = KFRAG(62); ahi[1] = KFRAG(63);
    SB0();
#pragma unroll
    for (int m = 0; m < 7; ++m) pack_store1(jb, lane, pk, rpk, m);
    FILL_X1(); FILL_P(0); FILL_P(1); FILL_P(2);
    SB0();
#pragma unroll
    for (int dl = 0; dl < 4; ++dl) {
      if (dl < 3) CSTAGE(dl + 1)
#pragma unroll
      for (int ks = 0; ks < 2; ++ks) {
#pragma unroll
        for (int m = 0; m < 4; ++m) {
          acc[0][m] = mfma16(alo[4 * dl + m - 2 * ks + 2], bf[dl][2 * ks], acc[0][m]);
          acc[1][m] = mfma16(alo[4 * dl + m - 2 * ks + 2], bf[dl][2 * ks + 1], acc[1][m]);
          acc[1][m] = mfma16(ahi[4 * dl + m - 2 * ks + 2], bf[dl][2 * ks], acc[1][m]);
        }
      }
      if (dl < 3) {
#pragma unroll
        for (int i = 0; i < 12; ++i) {
          __builtin_amdgcn_sched_group_barrier(0x100, 1, 0);
          __builtin_amdgcn_sched_group_barrier(0x8, 2, 0);
        }
      }
      SB0();
    }
#undef CSTAGE
    STAMP(0, 28);
#undef KFRAG

    LAS unsigned char* P = L + CV_P + w * (32 * CV_PROW);
#pragma unroll
    for (int n = 0; n < 2; ++n)
#pragma unroll
      for (int m = 0; m < 4; ++m) *(LAS f32x4*)(P + (16 * n + r16) * CV_PROW + (16 * m + 4 * q) * 4) = acc[n][m];
    }
    fv = flag_wait(rs, mgo, fv, 0u, tag32, 0u);
    const unsigned sa2 = __builtin_amdgcn_readlane(fv.x, 0), ca2 = __builtin_amdgcn_readlane(fv.x, 1), ga2 = __builtin_amdgcn_readlane(fv.x, 2);
    unsigned hd[4];
    {
      const f32x2 t2 = *(const LAS f32x2*)(L + P0_MS + (lane & 7) * 8);
      const float mk = (lane & 15) < w ? 1.0f : 0.0f;
      const float b0 = row16_sum((lane & 8) ? 0.f : t2.x * mk), b1 = row16_sum((lane & 8) ? 0.f : t2.y * mk);
#pragma unroll
      for (int k = 0; k < 4; ++k) {
        unsigned h = pk_bf16(bf_lo(hq[k]) * bf_lo(ca2), bf_hi(hq[k]) * bf_hi(ca2));
        h = pk_bf16(bf_lo(h) + bf_lo(sa2), bf_hi(h) + bf_hi(sa2));
        hd[k] = pk_bf16(bf_lo(h) * bf_lo(dsk2), bf_hi(h) * bf_hi(dsk2));
        cum0[k] = (cum0[k] + b0) * bf_lo(sa2);
        cum1[k] = (cum1[k] + b1) * bf_hi(sa2);
      }
    }
    asm volatile("" : "+v"(hd[0]), "+v"(hd[1]), "+v"(hd[2]), "+v"(hd[3]), "+v"(cum0[0]), "+v"(cum0[1]), "+v"(cum0[2]), "+v"(cum0[3]),
                 "+v"(cum1[0]), "+v"(cum1[1]), "+v"(cum1[2]), "+v"(cum1[3]));
    asm volatile("s_waitcnt vmcnt(4)" ::: "memory");
    bar_lds();
    STAMP(0, 24);
    {
      const LAS unsigned char* P = L + CV_P + (tid >> 4) * CV_PROW + (tid & 15) * 16;
      f32x4 pa[8];
#pragma unroll
      for (int k = 0; k < 8; ++k) pa[k] = *(const LAS f32x4*)(P + k * (32 * CV_PROW));
      const f32x4 y0 = (pa[0] + pa[1]) + (pa[2] + pa[3]), y1 = (pa[4] + pa[5]) + (pa[6] + pa[7]);
      u32x4 out;
#pragma unroll
      for (int k = 0; k < 4; ++k) {
        const unsigned yy = pk_bf16(y0[k] * bf_lo(ca2) + cum0[k], y1[k] * bf_hi(ca2) + cum1[k]);
        unsigned t = pk_bf16(bf_lo(yy) + bf_lo(hd[k]), bf_hi(yy) + bf_hi(hd[k]));
        t = pk_bf16(bf_lo(ga2) * bf_lo(t), bf_hi(ga2) * bf_hi(t));
        out[k] = sane(pk_bf16(bf_lo(xq[k]) + bf_lo(t), bf_hi(xq[k]) + bf_hi(t)));
      }
      ST16(WS_X2B + (tid >> 3) * (int)X2B_BLK + dp * 128 + (tid & 7) * 16, out);
    }
  } else {
    const int c = bid - 224;
    LAS float* sm = (LAS float*)(L + P0_KG);
    SB0();
    mod_load(p, c, tid, mr, mc);
    SB0();
    asm volatile("s_waitcnt vmcnt(16)" ::: "memory");
    PUT_STAT();
    mod_finish(c, tag32, tid, mr, mc, sm, rs);
    STAMP(0, 21);
    if (c >= 22) {
      mod_load(p, 10 + c, tid, mr, mc);
      bar_lds();
      mod_finish(10 + c, tag32, tid, mr, mc, sm, rs);
    }
    if (packer) {
      pack_load(jb, lane, pk);
      pack_shuffle(lane, pk, L + P0_PK + w * 8192);
#pragma unroll
      for (int m = 0; m < 7; ++m) pack_store1(jb, lane, pk, rs, m);
    }
    arrive_flag<0>(L, 2, p.ws + WS_FLAGC + c * 8, ~tag, lane);
    STAMP(0, 30);
    FILL_X1(); FILL_P(0); FILL_P(1); FILL_P(2);
    {
      u32x4 sg0 = __builtin_amdgcn_raw_buffer_load_b128(rs, sto, 0, AUX_SC1), sg1 = __builtin_amdgcn_raw_buffer_load_b128(rs, sto + 16, 0, AUX_SC1);
      stat_wait(rs, sto, sg0, sg1, tag32);
    }
    bar_lds();
  }
  STAMP(0, 25);
  if (__builtin_expect(!cw, 0)) {
    {
      const int mo = (int)WS_MODG + 672 * 8 + l56 * 64;
      u32x4 g0 = __builtin_amdgcn_raw_buffer_load_b128(rs, mo, 0, AUX_SC1), g1 = __builtin_amdgcn_raw_buffer_load_b128(rs, mo + 16, 0, AUX_SC1);
      u32x4 g2 = __builtin_amdgcn_raw_buffer_load_b128(rs, mo + 32, 0, AUX_SC1), g3 = __builtin_amdgcn_raw_buffer_load_b128(rs, mo + 48, 0, AUX_SC1);
      SB0();
      gather_x2<X2_DELAY>(p, L, rs, pn, tid);
      stat_wait(rs, mo, g0, g1, tag32);
      stat_wait(rs, mo + 32, g2, g3, tag32);
      LAS unsigned char* mp = L + G_MOD + l56 * 64;
      *(LAS f32x4*)mp = f32x4{bf_lo(g0.x), bf_hi(g0.x), bf_lo(g0.z), bf_hi(g0.z)};
      *(LAS f32x4*)(mp + 16) = f32x4{bf_lo(g1.x), bf_hi(g1.x), bf_lo(g1.z), bf_hi(g1.z)};
      *(LAS f32x4*)(mp + 32) = f32x4{bf_lo(g2.x), bf_hi(g2.x), bf_lo(g2.z), bf_hi(g2.z)};
      *(LAS f32x4*)(mp + 48) = f32x4{bf_lo(g3.x), bf_hi(g3.x), bf_lo(g3.z), bf_hi(g3.z)};
    }
    STAMP(0, 1);
    bar_lds();
    const int fo = (int)WS_FLAGC + (lane & 31) * 8;
    const u32x2 fc = flag_load(rs, fo);
    ln_phase_a(L, j, w, r16, q, [](auto) {});
    STAMP(0, 2);
    bar_lds();
    flag_wait(rs, fo, fc, ~tlo, ~thi, ~0u);
    const unsigned char* src = p.ws + WS_WP + (size_t)(j * 7) * NFRAG * 1024 + NRING * 1024 + voff;
#define DMA_HALF(h)                                                                                                         \
  _Pragma("unroll") for (int ww = 0; ww < 7; ++ww) _Pragma("unroll") for (int i = (h) * (NLDS / 2); i < ((h) + 1) * (NLDS / 2); ++i) \
      __builtin_amdgcn_global_load_lds((const GAS unsigned*)(src + (size_t)ww * NFRAG * 1024 + i * 1024),                     \
                                       (LAS unsigned*)(L + G_WL + (ww * NLDS + i) * 1024), 16, 0, AUX_SC1);
    DMA_HALF(0)
    bar_lds();
    STAMP(0, 4);
    bar_lds();
    DMA_HALF(1)
#undef DMA_HALF
    bar_lds();
    {
      int gof[3];
      u32x4 xr[3];
      x1_offsets(pn, j, w, r16, q, gof);
      poll_issue<3>(rs, gof, xr);
      const LnOwn ow = ln_c_own(L, w, r16, q);
      poll_finish<3>(rs, gof, xr);
      ln_phase_c(L, ow, xr, j, w, r16, q);
    }
    asm volatile("s_waitcnt vmcnt(0)" ::: "memory");
    STAMP(0, 7);
    bar_lds();
    return;
  }
  __builtin_amdgcn_s_setprio(1);
  SB0();
  gather_x2<X2_DELAY>(p, L, rs, pn, tid);
  STAMP(0, 1);
  bar_lds();
  STAMP(0, 27);
  u32x2 h2r[2], x2r[2], x1r[2];
  const int ncol = j * 112 + w * 16 + q * 4;
  const int gbo = (int)WS_MODG + (5 * 224 + (ncol >> 1)) * 8;
  u32x4 gbg = __builtin_amdgcn_raw_buffer_load_b128(rs, gbo, 0, AUX_SC1);
#pragma unroll
  for (int T = 0; T < 2; ++T) {
    const int row = 16 * T + r16;
    x2r[T] = *(const LAS u32x2*)(L + G_X + row * 1024 + (((ncol >> 3) ^ (row >> 2)) * 16) + (ncol & 4) * 2);
  }
  SB0();
  ring_fill<0, 8>(ring, rw, voff);
  ln_phase_a(L, j, w, r16, q, [&](auto k) { SB0(); ring_fill<8 + 6 * decltype(k)::value, 6>(ring, rw, voff); });
  SB0();
  ring_fill<26, 6>(ring, rw, voff);
  STAMP(0, 2);
  bar_lds();
  u32x2 gbp;
  {
    u32x4 dup = gbg;
    if (__builtin_expect(__any(((gbg.y ^ tag32) | (gbg.w ^ tag32)) != 0u), 0)) {
      for (unsigned spins = 0; spins < SPIN_CAP; ++spins) {
        asm volatile("" ::: "memory");
        dup = __builtin_amdgcn_raw_buffer_load_b128(rs, gbo, 0, AUX_SC1);
        if (!__any(((dup.y ^ tag32) | (dup.w ^ tag32)) != 0u)) break;
      }
    }
    gbp = u32x2{dup.x, dup.z};
  }
#pragma unroll
  for (int T = 0; T < 2; ++T) h2r[T] = *(const LAS u32x2*)(L + G_HQ + (16 * T + r16) * 240 + (w * 16 + q * 4) * 2);
  const LAS unsigned char* ap0 = L + G_T0 + r16 * AS + q * 16;
  const LAS unsigned char* ap1 = L + G_T1 + r16 * AS + q * 16;
  const int tok = (q & 1) * 16 + r16, octc = 2 * w + (q >> 1);
  {
    f32x4 acc[1][2];
    STAMP(0, 16);
    gemm14<1, 0, 32, 7, 2, 5>(ap0, ring, rw, voff, acc);
    STAMP(0, 17);
    const u32x4 own = make_piece(gelu4(acc[0][0]), gelu4(acc[0][1]));
    __builtin_amdgcn_raw_buffer_store_b128(own, rs, (int)WS_G + (pn * 4 + j) * 7168 + pslot, 0, AUX_SC1);
    SB0();
    STAMP(0, 18);
    ring_fill<39, 4>(ring, rw, voff);
    int goff[4], loff[4];
    bool wrap0;
#pragma unroll
    for (int i = 0; i < 4; ++i) {
      const int jq = (j + i) & 3;
      const int sh = (jq & 1) ? -1 - (jq >> 1) : 1 + (jq >> 1);
      const int it = tok + sh;
      const int sp = pn + (it >= 32 ? -1 : (it < 0 ? 1 : 0));
      if (i == 0) wrap0 = sp != pn;
      goff[i] = (sp < 0 || sp > 63 || (i == 0 && sp == pn)) ? (int)WS_END : (int)WS_G + (sp * 4 + jq) * 7168 + pslot;
      loff[i] = G_T1 + (it & 31) * AS + (jq * 14 + octc) * 16;
    }
    STAMP(0, 3);
    bar_lds();
    u32x4 v[4];
    poll_issue<4>(rs, goff, v);
    __builtin_amdgcn_s_sleep(3);
    ring_fill<43, 5>(ring, rw, voff);
    poll_finish<4>(rs, goff, v);
    *(LAS u32x4*)(L + loff[0]) = wrap0 ? v[0] : own;
#pragma unroll
    for (int i = 1; i < 4; ++i) *(LAS u32x4*)(L + loff[i]) = v[i];
  }
  STAMP(0, 4);
  bar_lds();
  {
    f32x4 acc[1][2];
    gemm14<1, 14, 48, 7, 2, 5>(ap1, ring, rw, voff, acc);
#pragma unroll
    for (int T = 0; T < 2; ++T) {
      const unsigned a = pk_bf16(acc[0][T][0], acc[0][T][1]), b = pk_bf16(acc[0][T][2], acc[0][T][3]);
      x1r[T].x = pk_bf16(bf_lo(h2r[T].x) + bf_lo(a), bf_hi(h2r[T].x) + bf_hi(a));
      x1r[T].y = pk_bf16(bf_lo(h2r[T].y) + bf_lo(b), bf_hi(h2r[T].y) + bf_hi(b));
    }
    const u32x4 own = make_piece(x1r[0], x1r[1]);
    __builtin_amdgcn_raw_buffer_store_b128(own, rs, (int)WS_X1 + (pn * 4 + j) * 7168 + (w * 64 + (tok >> 2) * 8 + (q >> 1) * 4 + (tok & 3)) * 16, 0, AUX_SC1);
    *(LAS u32x4*)(L + G_HQ + tok * 240 + octc * 16) = own;
    SB0();
    STAMP(0, 19);
    ring_fill<55, 4>(ring, rw, voff);
    int gof[3];
    u32x4 xr[3];
    x1_offsets(pn, j, w, r16, q, gof);
    STAMP(0, 5);
    bar_lds();
    poll_issue<3>(rs, gof, xr);
    const LnOwn ow = ln_c_own(L, w, r16, q);
    SB0();
    ring_fill<59, 5>(ring, rw, voff);
    poll_finish<3>(rs, gof, xr);
    STAMP(0, 6);
    ring_fill<64, 4>(ring, rw, voff);
    ln_phase_c(L, ow, xr, j, w, r16, q);
    SB0();
    ring_fill<68, 4>(ring, rw, voff);
  }
  STAMP(0, 7);
  bar_lds();
  {
    {
      f32x4 ca[2][2], cb2[2][2], acc[3][2];
      LAS unsigned char* hp = L + G_T1 + r16 * AS + (w * 64 + q * 4) * 2;
      unsigned glo;
      STAMP(0, 12);
      gemm_p<2, 28, 0, 1, 14, 72, 19, 3, true>(ap0, ring, rw, voff, ca, [](auto) {});
      gemm_p<2, 56, 0, 1, 14, 91, 19, 3, true>(ap0, ring, rw, voff, cb2, [&](auto stc) {
        constexpr int st = decltype(stc)::value;
        if constexpr (st % 3 == 1 && st / 3 < 8) {
          constexpr int c = st / 3, i = c >> 2, T = (c >> 1) & 1;
          if constexpr ((c & 1) == 0) glo = gelu2(ca[i][T][0], ca[i][T][1]);
          else *(LAS u32x2*)(hp + T * 16 * AS + i * 32) = u32x2{glo, gelu2(ca[i][T][2], ca[i][T][3])};
        }
      });
      STAMP(0, 13);
      bar_lds();
      gemm_p<3, 84, 0, 2, 7, 110, 18, 3, true>(ap1, ring, rw, voff, acc, [&](auto stc) {
        constexpr int st = decltype(stc)::value;
        if constexpr (st % 2 == 1 && st / 2 < 8) {
          constexpr int c = st / 2, i = c >> 2, T = (c >> 1) & 1;
          if constexpr ((c & 1) == 0) glo = gelu2(cb2[i][T][0], cb2[i][T][1]);
          else *(LAS u32x2*)(hp + T * 16 * AS + (2 + i) * 32) = u32x2{glo, gelu2(cb2[i][T][2], cb2[i][T][3])};
        }
      });
      STAMP(0, 8);
      bar_lds();
      STAMP(0, 14);
      gemm_p<3, 105, 1, 2, 7, 0, 0, 3, false>(ap1, ring, rw, voff, acc, [](auto) {});
      STAMP(0, 15);
#pragma unroll
      for (int i = 0; i < 3; ++i) {
        const int jd = (j + 1 + i) & 3;
        const u32x4 o = {sane(pk_bf16(acc[i][0][0], acc[i][0][1])), sane(pk_bf16(acc[i][0][2], acc[i][0][3])),
                         sane(pk_bf16(acc[i][1][0], acc[i][1][1])), sane(pk_bf16(acc[i][1][2], acc[i][1][3]))};
        __builtin_amdgcn_raw_buffer_store_b128(o, rs, (int)WS_P + ((pn * 4 + jd) * 3 + (2 - i)) * 7168 + pslot, 0, AUX_SC1);
      }
      SB0();
    }
    STAMP(0, 9);
    f32x4 acc0 = {0.f, 0.f, 0.f, 0.f}, acc1 = acc0;
    u32x4 v[3];
    const int go = (int)WS_P + (pn * 4 + j) * 3 * 7168 + pslot;
    const int gof[3] = {go, go + 7168, go + 2 * 7168};
    {
      const LAS unsigned char* wl = L + G_WL + w * (NLDS * 1024) + voff;
      u32x4 wa[14], b0[14], b1[14];
#pragma unroll
      for (int ks = 0; ks < 14; ++ks) {
        const int f = 126 + ks;
        wa[ks] = f < NRING ? ring[f % RING] : *(const LAS u32x4*)(wl + (f - NRING) * 1024);
        b0[ks] = *(const LAS u32x4*)(ap1 + ks * 64);
        b1[ks] = *(const LAS u32x4*)(ap1 + 16 * AS + ks * 64);
      }
      SB0();
#pragma unroll
      for (int ks = 0; ks < 14; ++ks) {
        acc0 = mfma16(wa[ks], b0[ks], acc0);
        acc1 = mfma16(wa[ks], b1[ks], acc1);
      }
      SB0();
    }
    STAMP(0, 10);
    poll_load<3>(rs, gof, v);
    STAMP(0, 11);
#pragma unroll
    for (int T = 0; T < 2; ++T) {
      const f32x4 ac = T ? acc1 : acc0;
      float hh[4];
#pragma unroll
      for (int k = 0; k < 2; ++k) {
        hh[2 * k] = ac[2 * k] + ((bf_lo(v[0][2 * T + k]) + bf_lo(v[1][2 * T + k])) + bf_lo(v[2][2 * T + k]));
        hh[2 * k + 1] = ac[2 * k + 1] + ((bf_hi(v[0][2 * T + k]) + bf_hi(v[1][2 * T + k])) + bf_hi(v[2][2 * T + k]));
      }
      unsigned a = pk_bf16(hh[0], hh[1]), b = pk_bf16(hh[2], hh[3]);
      a = pk_bf16(bf_lo(x1r[T].x) + bf_lo(a), bf_hi(x1r[T].x) + bf_hi(a));
      b = pk_bf16(bf_lo(x1r[T].y) + bf_lo(b), bf_hi(x1r[T].y) + bf_hi(b));
      a = pk_bf16(bf_lo(gbp.x) * bf_lo(a), bf_hi(gbp.x) * bf_hi(a));
      b = pk_bf16(bf_lo(gbp.y) * bf_lo(b), bf_hi(gbp.y) * bf_hi(b));
      u32x2 o;
      o.x = pk_bf16(bf_lo(x2r[T].x) + bf_lo(a), bf_hi(x2r[T].x) + bf_hi(a));
      o.y = pk_bf16(bf_lo(x2r[T].y) + bf_lo(b), bf_hi(x2r[T].y) + bf_hi(b));
      *(u32x2*)(p.out + (size_t)(pn * 32 + 16 * T + r16) * D_ + ncol) = o;
    }
  }
  STAMP(0, 20);
}

__global__ __launch_bounds__(512, 2) void k_all(GArgs p) {
  extern __shared__ __attribute__((aligned(16))) unsigned char smem[];
  const uintptr_t code0 = (uintptr_t)__builtin_amdgcn_s_getpc() & ~(uintptr_t)127;
  body(p, (LAS unsigned char*)smem, code0);
}

}

extern "C" void kernel_launch(void* const* d_in, const int* in_sizes, int n_in, void* d_out, int out_size, void* d_ws,
                              size_t ws_size, hipStream_t stream) {
  (void)in_sizes; (void)n_in; (void)out_size; (void)ws_size;
  GArgs a;
  a.x = (const unsigned short*)d_in[0];
  a.a_re = (const unsigned short*)d_in[1];
  a.a_im = (const unsigned short*)d_in[2];
  a.b_re = (const unsigned short*)d_in[3];
  a.b_im = (const unsigned short*)d_in[4];
  a.c_re = (const unsigned short*)d_in[5];
  a.c_im = (const unsigned short*)d_in[6];
  a.log_dt = (const unsigned short*)d_in[7];
  a.dskip = (const unsigned short*)d_in[8];
  a.w1 = (const unsigned short*)d_in[10];
  a.w2 = (const unsigned short*)d_in[11];
  a.w3 = (const unsigned short*)d_in[12];
  a.w4 = (const unsigned short*)d_in[13];
  a.cvec = (const unsigned short*)d_in[14];
  a.wmod = (const unsigned short*)d_in[15];
  a.ws = (unsigned char*)d_ws;
  a.out = (unsigned short*)d_out;
  (void)hipFuncSetAttribute((const void*)k_all, hipFuncAttributeMaxDynamicSharedMemorySize, G_LDS);
  k_all<<<256, 512, G_LDS, stream>>>(a);
}
```
